```python
import torch

def _softmax(x, axis=-1):
    return torch.softmax(x, dim=axis)
def _gelu(x):
    return torch.nn.functional.gelu(x, approximate="tanh")
def _silu(x):
    return torch.nn.functional.silu(x)
def _sigmoid(x):
    return torch.sigmoid(x)
def _layernorm(x, axis=-1, eps=1e-5):
    m = x.mean(dim=axis, keepdim=True)
    v = x.var(dim=axis, keepdim=True, unbiased=False)
    return (x - m) / torch.sqrt(v + eps)
def _scatter_add(tgt, idx, src, axis=0):
    return tgt.clone().index_add_(axis, idx, src)
def _segment_sum(src, seg, n):
    out = torch.zeros((n,) + src.shape[1:], dtype=src.dtype)
    return out.index_add_(0, seg, src)
def _pad(x, pw):
    flat = tuple(v for pair in reversed(pw) for v in pair)
    return torch.nn.functional.pad(x, flat)

kernel_name = 'recurrent_scan__mha_block_cached__glu_gated_swiglu__prompt'

def setup_inputs():

    _w = (lambda a: (lambda t: t.to(torch.bfloat16) if t.is_floating_point() else t)(torch.as_tensor(a)))
    def _A_setup():
        B, Sq, D = 1, 2048, 640
        H, Sk, Dh = 32, 2048, 192
        return {
            "x": _w((lambda *s: torch.randn(*s, dtype=torch.bfloat16) * 0.05)(B, Sq, D)),
            "k_cache": _w((lambda *s: torch.randn(*s, dtype=torch.bfloat16) * 1.6)(B, H, Sk, Dh)),
            "v_cache": _w((lambda *s: torch.randn(*s, dtype=torch.bfloat16) * 1.6)(B, H, Sk, Dh)),
            "wq": _w((lambda *s: torch.randn(*s, dtype=torch.bfloat16) * 1.6)(D, H * Dh) * (D ** -0.5)),
            "wo": _w((lambda *s: torch.randn(*s, dtype=torch.bfloat16) * 1.6)(H * Dh, D) * ((H * Dh) ** -0.5)),
        }
    _A_in = _A_setup()

    def _B_setup():
        B, S, D, F = 1, 2048, 640, 1536
        return {
            "x": _w((lambda *s: torch.randn(*s, dtype=torch.bfloat16) * 0.05)(B, S, D)),
            "w1": _w((lambda *s: torch.randn(*s, dtype=torch.bfloat16) * 0.2)(D, F) * (2.0 / D) ** 0.5),
            "w3": _w((lambda *s: torch.randn(*s, dtype=torch.bfloat16) * 0.2)(D, F) * (2.0 / D) ** 0.5),
            "w2": _w((lambda *s: torch.randn(*s, dtype=torch.bfloat16) * 0.2)(F, D) * (2.0 / F) ** 0.5),
        }
    _B_in = _B_setup()

    return {
        "A_x": _A_in["x"],
        "A_k_cache": _A_in["k_cache"],
        "A_v_cache": _A_in["v_cache"],
        "A_wq": _A_in["wq"],
        "A_wo": _A_in["wo"],
        "B_x": _B_in["x"],
        "B_w1": _B_in["w1"],
        "B_w3": _B_in["w3"],
        "B_w2": _B_in["w2"],
    }

def reference(A_x, A_k_cache, A_v_cache, A_wq, A_wo, B_x, B_w1, B_w3, B_w2):
    def _A_ref(x, k_cache, v_cache, wq, wo):
        B, Sq, D = x.shape
        H, Dh = 32, 192
        q = (lambda x, *d: x.permute(*d))((x @ wq).reshape(B, Sq, H, Dh), 0, 2, 1, 3)
        scores = torch.einsum("bhqd,bhkd->bhqk", q, k_cache) * (Dh ** -0.5)
        p = _softmax(scores, axis=-1)
        o = torch.einsum("bhqk,bhkd->bhqd", p, v_cache)
        return (lambda x, *d: x.permute(*d))(o, 0, 2, 1, 3).reshape(B, Sq, H * Dh) @ wo

    def _B_ref(x, w1, w3, w2):
        gate = torch.einsum("bsd,df->bsf", x, w1)
        up = torch.einsum("bsd,df->bsf", x, w3)
        h = _silu(gate) * up
        out = torch.einsum("bsf,fd->bsd", h, w2)
        return out

    x = A_x
    xn = _layernorm(x, axis=-1)
    s = _A_ref(xn, A_k_cache, A_v_cache, A_wq, A_wo)
    g = _silu(_B_ref(xn, B_w1, B_w3, B_w2))
    return x + s * g
```

```cpp
#include <hip/hip_runtime.h>
#include <hip/hip_bf16.h>
typedef __hip_bfloat16 bf16;
static __device__ __forceinline__ float b2f(bf16 x){return __bfloat162float(x);}
static __device__ __forceinline__ bf16 f2b(float x){return __float2bfloat16(x);}
constexpr int S=2048,D=640,H=32,DH=192,F=1536;

__global__ void ln_k(const bf16* x, bf16* xn){
  int row=blockIdx.x; int t=threadIdx.x;
  float v[10]; float s=0;
  for(int i=0;i<10;i++){v[i]=b2f(x[row*D+t+64*i]); s+=v[i];}
  for(int o=32;o>0;o>>=1) s+=__shfl_xor(s,o);
  float m=s/D; float q=0;
  for(int i=0;i<10;i++){float d=v[i]-m; q+=d*d;}
  for(int o=32;o>0;o>>=1) q+=__shfl_xor(q,o);
  float r=1.0f/sqrtf(q/D+1e-5f);
  for(int i=0;i<10;i++) xn[row*D+t+64*i]=f2b((v[i]-m)*r);
}
template<int MODE>
__global__ void gemm_k(const bf16* A,const bf16* B,const bf16* B2,bf16* C,int M,int N,int K,const bf16* A2,const bf16* W2,int K2,const bf16* x){
  __shared__ float As[16][65],Bs[16][65],B2s[16][65];
  int tx=threadIdx.x&15,ty=threadIdx.x>>4; int bm=blockIdx.y*64,bn=blockIdx.x*64;
  float acc[4][4]={},acc2[4][4]={};
  for(int k0=0;k0<K;k0+=16){
    for(int i=threadIdx.x;i<1024;i+=256){int r=i>>4,c=i&15; As[c][r]=b2f(A[(size_t)(bm+r)*K+k0+c]); int kr=i>>6,nc=i&63; Bs[kr][nc]=b2f(B[(size_t)(k0+kr)*N+bn+nc]); if(MODE==1) B2s[kr][nc]=b2f(B2[(size_t)(k0+kr)*N+bn+nc]);}
    __syncthreads();
    for(int k=0;k<16;k++){float a[4],b[4],b2[4]; for(int i=0;i<4;i++){a[i]=As[k][ty*4+i]; b[i]=Bs[k][tx*4+i]; if(MODE==1) b2[i]=B2s[k][tx*4+i];}
      for(int i=0;i<4;i++)for(int j=0;j<4;j++){acc[i][j]+=a[i]*b[j]; if(MODE==1) acc2[i][j]+=a[i]*b2[j];}}
    __syncthreads();
  }
  if(MODE==2){
    for(int k0=0;k0<K2;k0+=16){
      for(int i=threadIdx.x;i<1024;i+=256){int r=i>>4,c=i&15; As[c][r]=b2f(A2[(size_t)(bm+r)*K2+k0+c]); int kr=i>>6,nc=i&63; Bs[kr][nc]=b2f(W2[(size_t)(k0+kr)*N+bn+nc]);}
      __syncthreads();
      for(int k=0;k<16;k++){float a[4],b[4]; for(int i=0;i<4;i++){a[i]=As[k][ty*4+i]; b[i]=Bs[k][tx*4+i];}
        for(int i=0;i<4;i++)for(int j=0;j<4;j++){acc2[i][j]+=a[i]*b[j];}}
      __syncthreads();
    }
  }
  for(int i=0;i<4;i++)for(int j=0;j<4;j++){
    size_t o=(size_t)(bm+ty*4+i)*N+bn+tx*4+j;
    if(MODE==0) C[o]=f2b(acc[i][j]);
    if(MODE==1){float g=acc[i][j]; C[o]=f2b(g/(1.f+__expf(-g))*acc2[i][j]);}
    if(MODE==2){float f=acc2[i][j]; float g=f/(1.f+__expf(-f)); C[o]=f2b(b2f(x[o])+acc[i][j]*g);}
  }
}
__global__ __launch_bounds__(256) void attn_k(const bf16* Q,const bf16* Kc,const bf16* Vc,bf16* O){
  __shared__ float qs[16][DH+1]; __shared__ float ks[64][DH+1]; __shared__ float ps[16][65];
  int h=blockIdx.y,q0=blockIdx.x*16,t=threadIdx.x,qi=t>>4,kk=t&15;
  for(int i=t;i<16*DH;i+=256){int r=i/DH,c=i%DH; qs[r][c]=b2f(Q[(size_t)(q0+r)*(H*DH)+h*DH+c]);}
  float m=-1e30f,l=0,o[12]={};
  const float sc=0.07216878364870322f;
  for(int k0=0;k0<S;k0+=64){
    __syncthreads();
    for(int i=t;i<64*DH;i+=256){int r=i/DH,c=i%DH; ks[r][c]=b2f(Kc[((size_t)h*S+k0+r)*DH+c]);}
    __syncthreads();
    float s[4]; float mx=-1e30f;
    _Pragma("unroll") for(int j=0;j<4;j++){float a=0; _Pragma("unroll 8") for(int d=0;d<DH;d++) a+=qs[qi][d]*ks[kk+16*j][d]; s[j]=a*sc; mx=fmaxf(mx,s[j]);}
    for(int o2=8;o2>0;o2>>=1) mx=fmaxf(mx,__shfl_xor(mx,o2));
    float mn=fmaxf(m,mx),al=__expf(m-mn); float ls=0;
    _Pragma("unroll") for(int j=0;j<4;j++){float p=__expf(s[j]-mn); ls+=p; ps[qi][kk+16*j]=p;}
    for(int o2=8;o2>0;o2>>=1) ls+=__shfl_xor(ls,o2);
    l=l*al+ls; m=mn;
    __syncthreads();
    for(int i=t;i<64*DH;i+=256){int r=i/DH,c=i%DH; ks[r][c]=b2f(Vc[((size_t)h*S+k0+r)*DH+c]);}
    __syncthreads();
    _Pragma("unroll") for(int j=0;j<12;j++){float a=o[j]*al; _Pragma("unroll 8") for(int k=0;k<64;k++) a+=ps[qi][k]*ks[k][kk+16*j]; o[j]=a;}
  }
  _Pragma("unroll") for(int j=0;j<12;j++) O[(size_t)(q0+qi)*(H*DH)+h*DH+kk+16*j]=f2b(o[j]/l);
}
extern "C" void kernel_launch(void* const* d_in, const int* in_sizes, int n_in, void* d_out, int out_size, void* d_ws, size_t ws_size, hipStream_t stream){
  const bf16* x=(const bf16*)d_in[0]; const bf16* kc=(const bf16*)d_in[1]; const bf16* vc=(const bf16*)d_in[2];
  const bf16* wq=(const bf16*)d_in[3]; const bf16* wo=(const bf16*)d_in[4];
  const bf16* w1=(const bf16*)d_in[6]; const bf16* w3=(const bf16*)d_in[7]; const bf16* w2=(const bf16*)d_in[8];
  bf16* xn=(bf16*)d_ws; bf16* q=xn+S*D; bf16* o=q+(size_t)S*H*DH; bf16* h=o+(size_t)S*H*DH;
  ln_k<<<S,64,0,stream>>>(x,xn);
  gemm_k<0><<<dim3(H*DH/64,S/64),256,0,stream>>>(xn,wq,nullptr,q,S,H*DH,D,nullptr,nullptr,0,nullptr);
  gemm_k<1><<<dim3(F/64,S/64),256,0,stream>>>(xn,w1,w3,h,S,F,D,nullptr,nullptr,0,nullptr);
  attn_k<<<dim3(S/16,H),256,0,stream>>>(q,kc,vc,o);
  gemm_k<2><<<dim3(D/64,S/64),256,0,stream>>>(o,wo,nullptr,(bf16*)d_out,S,D,H*DH,h,w2,F,x);
}
```

```cpp
#include <hip/hip_runtime.h>
extern "C" __device__ unsigned long long amd_dispatch_id() __asm__("llvm.amdgcn.dispatch.id");
extern "C" __global__ __launch_bounds__(256, 1) void fused_block_kernel(const void* x, const void* k, const void* v, const void* wq, const void* wo,
                                                                        const void* w1, const void* w3, const void* w2, void* out, void* ws) {
  __shared__ __attribute__((aligned(1024))) char lds[163840];
  auto ka = __builtin_amdgcn_kernarg_segment_ptr();
  unsigned wid = __builtin_amdgcn_readfirstlane(threadIdx.x >> 6);
  unsigned bid = blockIdx.x;
  unsigned long long did = amd_dispatch_id();
  asm volatile(
"s_load_dwordx16 s[36:51], %0, 0\n"
"s_load_dwordx4 s[52:55], %0, 64\n"
"s_mov_b64 s[98:99], %4\n"
"s_xor_b32 s99, s99, 0x5eed1234\n"
"v_mbcnt_lo_u32_b32 v255, -1, 0\n"
"v_mbcnt_hi_u32_b32 v255, -1, v255\n"
"s_getpc_b64 s[28:29]\n"
"PCHERE_%=:\n"
"s_mov_b32 s25, KEND_%=-PCHERE_%=\n"
"s_lshl_b32 s26, %2, 13\n"
"v_lshlrev_b32 v250, 7, v255\n"
"v_add_u32 v250, s26, v250\n"
"v_min_u32 v251, s25, v250\n"
"global_load_dword v253, v251, s[28:29]\n"
"v_add_u32 v250, 32768, v250\n"
"v_min_u32 v251, s25, v250\n"
"global_load_dword v253, v251, s[28:29]\n"
"v_add_u32 v250, 32768, v250\n"
"v_min_u32 v251, s25, v250\n"
"global_load_dword v253, v251, s[28:29]\n"
"s_mov_b32 s22, %2\n"
"s_and_b32 s25, %1, 7\n"
"s_lshr_b32 s26, %1, 3\n"
"s_lshr_b32 s20, s26, 3\n"
"s_lshl_b32 s25, s25, 2\n"
"s_add_u32 s20, s20, s25\n"
"s_and_b32 s21, s26, 7\n"
"s_lshr_b32 s23, s20, 2\n"
"s_and_b32 s24, s20, 3\n"
"s_mov_b32 s31, 0\n"
"v_mbcnt_lo_u32_b32 v255, -1, 0\n"
"v_mbcnt_hi_u32_b32 v255, -1, v255\n"
"v_lshrrev_b32 v192, 4, v255\n"
"v_bfe_u32 v193, v255, 2, 2\n"
"v_and_b32 v194, 3, v255\n"
"v_lshl_add_u32 v195, v192, 3, v193\n"
"v_mul_u32_u24 v195, 384, v195\n"
"v_lshl_add_u32 v195, v194, 4, v195\n"
"v_bfe_u32 v196, v255, 3, 1\n"
"v_xor_b32 v197, 0, v196\n"
"v_lshl_add_u32 v236, v197, 6, v195\n"
"v_add_u32 v236, 73728, v236\n"
"v_xor_b32 v197, 1, v196\n"
"v_lshl_add_u32 v237, v197, 6, v195\n"
"v_add_u32 v237, 73728, v237\n"
"v_xor_b32 v197, 0, v196\n"
"v_lshl_add_u32 v239, v197, 6, v195\n"
"v_add_u32 v239, 110592, v239\n"
"v_xor_b32 v197, 1, v196\n"
"v_lshl_add_u32 v240, v197, 6, v195\n"
"v_add_u32 v240, 110592, v240\n"
"v_lshrrev_b32 v195, 1, v192\n"
"v_lshlrev_b32 v195, 4, v195\n"
"v_and_b32 v196, 1, v192\n"
"v_lshl_add_u32 v195, v196, 2, v195\n"
"v_add_u32 v195, v195, v193\n"
"v_mul_u32_u24 v195, 96, v195\n"
"v_lshl_add_u32 v195, v194, 3, v195\n"
"v_add_u32 v238, 98304, v195\n"
"v_add_u32 v241, 135168, v195\n"
"v_add_u32 v192, 0, v255\n"
"v_mul_u32_u24 v193, 43691, v192\n"
"v_lshrrev_b32 v193, 20, v193\n"
"v_mul_u32_u24 v194, 24, v193\n"
"v_sub_u32 v194, v192, v194\n"
"v_bfe_u32 v195, v193, 1, 1\n"
"v_lshlrev_b32 v195, 2, v195\n"
"v_xor_b32 v194, v194, v195\n"
"v_mul_u32_u24 v193, 12288, v193\n"
"v_lshl_add_u32 v193, v194, 4, v193\n"
"v_subrev_u32 v242, 0, v193\n"
"v_mul_u32_u24 v193, 10923, v192\n"
"v_lshrrev_b32 v193, 16, v193\n"
"v_mul_u32_u24 v194, 6, v193\n"
"v_sub_u32 v194, v192, v194\n"
"v_and_b32 v195, 0x13, v193\n"
"v_bfe_u32 v196, v193, 2, 1\n"
"v_lshl_add_u32 v195, v196, 3, v195\n"
"v_bfe_u32 v196, v193, 3, 1\n"
"v_lshl_add_u32 v195, v196, 2, v195\n"
"v_mul_u32_u24 v195, 3072, v195\n"
"v_lshl_add_u32 v195, v194, 4, v195\n"
"v_subrev_u32 v245, 0, v195\n"
"v_add_u32 v192, 64, v255\n"
"v_mul_u32_u24 v193, 43691, v192\n"
"v_lshrrev_b32 v193, 20, v193\n"
"v_mul_u32_u24 v194, 24, v193\n"
"v_sub_u32 v194, v192, v194\n"
"v_bfe_u32 v195, v193, 1, 1\n"
"v_lshlrev_b32 v195, 2, v195\n"
"v_xor_b32 v194, v194, v195\n"
"v_mul_u32_u24 v193, 12288, v193\n"
"v_lshl_add_u32 v193, v194, 4, v193\n"
"v_subrev_u32 v243, 1024, v193\n"
"v_mul_u32_u24 v193, 10923, v192\n"
"v_lshrrev_b32 v193, 16, v193\n"
"v_mul_u32_u24 v194, 6, v193\n"
"v_sub_u32 v194, v192, v194\n"
"v_and_b32 v195, 0x13, v193\n"
"v_bfe_u32 v196, v193, 2, 1\n"
"v_lshl_add_u32 v195, v196, 3, v195\n"
"v_bfe_u32 v196, v193, 3, 1\n"
"v_lshl_add_u32 v195, v196, 2, v195\n"
"v_mul_u32_u24 v195, 3072, v195\n"
"v_lshl_add_u32 v195, v194, 4, v195\n"
"v_subrev_u32 v246, 1024, v195\n"
"v_add_u32 v192, 128, v255\n"
"v_mul_u32_u24 v193, 43691, v192\n"
"v_lshrrev_b32 v193, 20, v193\n"
"v_mul_u32_u24 v194, 24, v193\n"
"v_sub_u32 v194, v192, v194\n"
"v_bfe_u32 v195, v193, 1, 1\n"
"v_lshlrev_b32 v195, 2, v195\n"
"v_xor_b32 v194, v194, v195\n"
"v_mul_u32_u24 v193, 12288, v193\n"
"v_lshl_add_u32 v193, v194, 4, v193\n"
"v_subrev_u32 v244, 2048, v193\n"
"v_mul_u32_u24 v193, 10923, v192\n"
"v_lshrrev_b32 v193, 16, v193\n"
"v_mul_u32_u24 v194, 6, v193\n"
"v_sub_u32 v194, v192, v194\n"
"v_and_b32 v195, 0x13, v193\n"
"v_bfe_u32 v196, v193, 2, 1\n"
"v_lshl_add_u32 v195, v196, 3, v195\n"
"v_bfe_u32 v196, v193, 3, 1\n"
"v_lshl_add_u32 v195, v196, 2, v195\n"
"v_mul_u32_u24 v195, 3072, v195\n"
"v_lshl_add_u32 v195, v194, 4, v195\n"
"v_subrev_u32 v247, 2048, v195\n"
"v_lshlrev_b32 v249, 4, v255\n"
"s_waitcnt lgkmcnt(0)\n"
"v_and_b32 v16, 7, v255\n"
"v_lshrrev_b32 v17, 3, v255\n"
"s_lshl_b32 s25, s21, 8\n"
"s_lshl_b32 s26, s20, 3\n"
"s_add_u32 s25, s25, s26\n"
"s_mul_i32 s25, s25, 1280\n"
"v_mul_u32_u24 v18, 1280, v16\n"
"v_add_u32 v18, s25, v18\n"
"s_mul_i32 s26, s22, 20\n"
"v_add_u32 v17, s26, v17\n"
"v_lshl_add_u32 v19, v17, 4, v18\n"
"global_load_dwordx4 v[0:3], v19, s[36:37]\n"
"global_load_dwordx4 v[4:7], v19, s[36:37] offset:128\n"
"global_load_ushort v12, v18, s[36:37]\n"
"v_cmp_gt_u32 vcc, 32, v255\n"
"v_add_u32 v20, 128, v19\n"
"v_add_u32 v21, 256, v19\n"
"s_nop 0\n"
"v_cndmask_b32 v20, v20, v21, vcc\n"
"global_load_dwordx4 v[8:11], v20, s[36:37]\n"
"s_mov_b32 s72, s54\n"
"s_mov_b32 s73, s55\n"
"s_and_b32 s73, s73, 0xffff\n"
"s_mov_b32 s74, 77168640\n"
"s_mov_b32 s75, 0x20000\n"
"s_mul_i32 s86, s22, 6144\n"
"s_mul_i32 s87, s22, 3072\n"
"s_add_u32 s87, s87, 24576\n"
"s_mul_i32 s88, s22, 196608\n"
"s_mul_i32 s25, s20, 384\n"
"s_add_u32 s88, s88, s25\n"
"s_add_u32 s89, s88, 98304\n"
"s_and_b32 s25, s22, 1\n"
"s_mul_i32 s90, s25, 98304\n"
"s_mul_i32 s25, s20, 96\n"
"s_add_u32 s90, s90, s25\n"
"s_lshl_b32 s25, s21, 4\n"
"s_lshl_b32 s26, s22, 2\n"
"s_add_u32 s25, s25, s26\n"
"s_mul_i32 s25, s25, 20480\n"
"s_add_u32 s91, s25, 65536\n"
"s_add_u32 s92, s25, 86016\n"
"s_add_u32 s93, s25, 106496\n"
"s_add_u32 s94, s25, 126976\n"
"s_mov_b32 s64, s42\n"
"s_mov_b32 s65, s43\n"
"s_and_b32 s65, s65, 0xffff\n"
"s_mov_b32 s66, 7864320\n"
"s_mov_b32 s67, 0x20000\n"
"s_cmp_lt_u32 s22, 2\n"
"s_cselect_b32 s68, s46, s48\n"
"s_cselect_b32 s69, s47, s49\n"
"s_and_b32 s69, s69, 0xffff\n"
"s_mov_b32 s70, 1966080\n"
"s_mov_b32 s71, 0x20000\n"
"s_mul_i32 s26, s20, 786432\n"
"s_add_u32 s56, s38, s26\n"
"s_addc_u32 s57, s39, 0\n"
"s_and_b32 s57, s57, 0xffff\n"
"s_mov_b32 s58, 786432\n"
"s_mov_b32 s59, 0x20000\n"
"s_add_u32 s60, s40, s26\n"
"s_addc_u32 s61, s41, 0\n"
"s_and_b32 s61, s61, 0xffff\n"
"s_mov_b32 s62, 786432\n"
"s_mov_b32 s63, 0x20000\n"
"s_mul_i32 s25, s22, 3072\n"
"s_add_u32 s76, s25, 0\n"
"s_add_u32 s80, s25, 49152\n"
"s_add_u32 s77, s25, 12288\n"
"s_add_u32 s81, s25, 61440\n"
"s_add_u32 s78, s25, 24576\n"
"s_add_u32 s82, s25, 73728\n"
"s_add_u32 s79, s25, 36864\n"
"s_add_u32 s83, s25, 86016\n"
"s_mov_b32 s84, 0\n"
"s_mov_b32 s85, 0\n"
"s_add_u32 m0, s87, 73728\n"
"s_nop 0\n"
"buffer_load_dwordx4 v245, s[68:71], s90 offen offset:0 lds\n"
"buffer_load_dwordx4 v246, s[68:71], s90 offen offset:1024 lds\n"
"buffer_load_dwordx4 v247, s[68:71], s90 offen offset:2048 lds\n"
"s_add_u32 s90, s90, 196608\n"
"s_add_u32 m0, s86, 73728\n"
"s_nop 0\n"
"buffer_load_dwordx4 v242, s[64:67], s88 offen offset:0 lds\n"
"buffer_load_dwordx4 v243, s[64:67], s88 offen offset:1024 lds\n"
"buffer_load_dwordx4 v244, s[64:67], s88 offen offset:2048 lds\n"
"s_add_u32 s88, s88, 786432\n"
"s_add_u32 m0, s86, 76800\n"
"s_nop 0\n"
"buffer_load_dwordx4 v242, s[64:67], s89 offen offset:0 lds\n"
"buffer_load_dwordx4 v243, s[64:67], s89 offen offset:1024 lds\n"
"buffer_load_dwordx4 v244, s[64:67], s89 offen offset:2048 lds\n"
"s_add_u32 s89, s89, 786432\n"
"s_add_u32 m0, s86, 110592\n"
"s_nop 0\n"
"buffer_load_dwordx4 v242, s[64:67], s88 offen offset:0 lds\n"
"buffer_load_dwordx4 v243, s[64:67], s88 offen offset:1024 lds\n"
"buffer_load_dwordx4 v244, s[64:67], s88 offen offset:2048 lds\n"
"s_add_u32 s88, s88, 786432\n"
"s_add_u32 m0, s86, 113664\n"
"s_nop 0\n"
"buffer_load_dwordx4 v242, s[64:67], s89 offen offset:0 lds\n"
"buffer_load_dwordx4 v243, s[64:67], s89 offen offset:1024 lds\n"
"buffer_load_dwordx4 v244, s[64:67], s89 offen offset:2048 lds\n"
"s_add_u32 s89, s89, 786432\n"
"s_add_u32 m0, s87, 110592\n"
"s_nop 0\n"
"buffer_load_dwordx4 v245, s[68:71], s90 offen offset:0 lds\n"
"buffer_load_dwordx4 v246, s[68:71], s90 offen offset:1024 lds\n"
"buffer_load_dwordx4 v247, s[68:71], s90 offen offset:2048 lds\n"
"s_add_u32 s90, s90, 196608\n"
"v_and_b32 v192, 15, v255\n"
"v_lshrrev_b32 v193, 4, v255\n"
"v_bfe_u32 v194, v255, 1, 3\n"
"v_mul_u32_u24 v195, 384, v192\n"
"v_add_u32 v196, 0, v193\n"
"v_xor_b32 v196, v196, v194\n"
"v_lshl_add_u32 v224, v196, 4, v195\n"
"v_add_u32 v196, 4, v193\n"
"v_xor_b32 v196, v196, v194\n"
"v_lshl_add_u32 v225, v196, 4, v195\n"
"v_bfe_u32 v192, v255, 2, 2\n"
"v_lshl_add_u32 v194, v193, 2, v192\n"
"v_mul_u32_u24 v195, 384, v194\n"
"v_and_b32 v196, 3, v255\n"
"v_lshl_add_u32 v195, v196, 3, v195\n"
"v_add_u32 v195, 49152, v195\n"
"v_bfe_u32 v196, v194, 1, 2\n"
"v_xor_b32 v197, 0, v196\n"
"v_lshl_add_u32 v226, v197, 5, v195\n"
"v_xor_b32 v197, 1, v196\n"
"v_lshl_add_u32 v227, v197, 5, v195\n"
"v_xor_b32 v197, 2, v196\n"
"v_lshl_add_u32 v228, v197, 5, v195\n"
"v_xor_b32 v197, 3, v196\n"
"v_lshl_add_u32 v229, v197, 5, v195\n"
"v_add_u32 v192, 0, v255\n"
"v_mul_u32_u24 v193, 43691, v192\n"
"v_lshrrev_b32 v193, 20, v193\n"
"s_lshl_b32 s25, s22, 3\n"
"v_add_u32 v193, s25, v193\n"
"v_and_b32 v194, 7, v255\n"
"v_and_b32 v195, 56, v255\n"
"s_mul_i32 s25, s22, 3072\n"
"v_bfe_u32 v196, v193, 1, 3\n"
"v_xor_b32 v196, v196, v194\n"
"v_add_u32 v196, v196, v195\n"
"v_lshlrev_b32 v196, 4, v196\n"
"v_add_u32 v230, s25, v196\n"
"v_bfe_u32 v196, v193, 1, 2\n"
"v_lshlrev_b32 v196, 1, v196\n"
"v_xor_b32 v196, v196, v194\n"
"v_add_u32 v196, v196, v195\n"
"v_lshlrev_b32 v196, 4, v196\n"
"v_add_u32 v233, s25, v196\n"
"v_add_u32 v192, 64, v255\n"
"v_mul_u32_u24 v193, 43691, v192\n"
"v_lshrrev_b32 v193, 20, v193\n"
"s_lshl_b32 s25, s22, 3\n"
"v_add_u32 v193, s25, v193\n"
"v_and_b32 v194, 7, v255\n"
"v_and_b32 v195, 56, v255\n"
"s_mul_i32 s25, s22, 3072\n"
"v_bfe_u32 v196, v193, 1, 3\n"
"v_xor_b32 v196, v196, v194\n"
"v_add_u32 v196, v196, v195\n"
"v_lshlrev_b32 v196, 4, v196\n"
"v_add_u32 v231, s25, v196\n"
"v_bfe_u32 v196, v193, 1, 2\n"
"v_lshlrev_b32 v196, 1, v196\n"
"v_xor_b32 v196, v196, v194\n"
"v_add_u32 v196, v196, v195\n"
"v_lshlrev_b32 v196, 4, v196\n"
"v_add_u32 v234, s25, v196\n"
"v_add_u32 v192, 128, v255\n"
"v_mul_u32_u24 v193, 43691, v192\n"
"v_lshrrev_b32 v193, 20, v193\n"
"s_lshl_b32 s25, s22, 3\n"
"v_add_u32 v193, s25, v193\n"
"v_and_b32 v194, 7, v255\n"
"v_and_b32 v195, 56, v255\n"
"s_mul_i32 s25, s22, 3072\n"
"v_bfe_u32 v196, v193, 1, 3\n"
"v_xor_b32 v196, v196, v194\n"
"v_add_u32 v196, v196, v195\n"
"v_lshlrev_b32 v196, 4, v196\n"
"v_add_u32 v232, s25, v196\n"
"v_bfe_u32 v196, v193, 1, 2\n"
"v_lshlrev_b32 v196, 1, v196\n"
"v_xor_b32 v196, v196, v194\n"
"v_add_u32 v196, v196, v195\n"
"v_lshlrev_b32 v196, 4, v196\n"
"v_add_u32 v235, s25, v196\n"
"s_waitcnt vmcnt(18)\n"
"v_lshlrev_b32 v12, 16, v12\n"
"v_lshlrev_b32 v32, 16, v0\n"
"v_and_b32 v33, 0xffff0000, v0\n"
"v_lshlrev_b32 v34, 16, v1\n"
"v_and_b32 v35, 0xffff0000, v1\n"
"v_lshlrev_b32 v36, 16, v2\n"
"v_and_b32 v37, 0xffff0000, v2\n"
"v_lshlrev_b32 v38, 16, v3\n"
"v_and_b32 v39, 0xffff0000, v3\n"
"v_lshlrev_b32 v40, 16, v4\n"
"v_and_b32 v41, 0xffff0000, v4\n"
"v_lshlrev_b32 v42, 16, v5\n"
"v_and_b32 v43, 0xffff0000, v5\n"
"v_lshlrev_b32 v44, 16, v6\n"
"v_and_b32 v45, 0xffff0000, v6\n"
"v_lshlrev_b32 v46, 16, v7\n"
"v_and_b32 v47, 0xffff0000, v7\n"
"v_lshlrev_b32 v48, 16, v8\n"
"v_and_b32 v49, 0xffff0000, v8\n"
"v_lshlrev_b32 v50, 16, v9\n"
"v_and_b32 v51, 0xffff0000, v9\n"
"v_lshlrev_b32 v52, 16, v10\n"
"v_and_b32 v53, 0xffff0000, v10\n"
"v_lshlrev_b32 v54, 16, v11\n"
"v_and_b32 v55, 0xffff0000, v11\n"
"v_sub_f32 v32, v32, v12\n"
"v_sub_f32 v33, v33, v12\n"
"v_sub_f32 v34, v34, v12\n"
"v_sub_f32 v35, v35, v12\n"
"v_sub_f32 v36, v36, v12\n"
"v_sub_f32 v37, v37, v12\n"
"v_sub_f32 v38, v38, v12\n"
"v_sub_f32 v39, v39, v12\n"
"v_sub_f32 v40, v40, v12\n"
"v_sub_f32 v41, v41, v12\n"
"v_sub_f32 v42, v42, v12\n"
"v_sub_f32 v43, v43, v12\n"
"v_sub_f32 v44, v44, v12\n"
"v_sub_f32 v45, v45, v12\n"
"v_sub_f32 v46, v46, v12\n"
"v_sub_f32 v47, v47, v12\n"
"v_sub_f32 v48, v48, v12\n"
"v_sub_f32 v49, v49, v12\n"
"v_sub_f32 v50, v50, v12\n"
"v_sub_f32 v51, v51, v12\n"
"v_sub_f32 v52, v52, v12\n"
"v_sub_f32 v53, v53, v12\n"
"v_sub_f32 v54, v54, v12\n"
"v_sub_f32 v55, v55, v12\n"
"v_add_f32 v24, v32, v33\n"
"v_mul_f32 v25, v32, v32\n"
"v_fmac_f32 v25, v33, v33\n"
"v_add_f32 v24, v24, v34\n"
"v_fmac_f32 v25, v34, v34\n"
"v_add_f32 v24, v24, v35\n"
"v_fmac_f32 v25, v35, v35\n"
"v_add_f32 v24, v24, v36\n"
"v_fmac_f32 v25, v36, v36\n"
"v_add_f32 v24, v24, v37\n"
"v_fmac_f32 v25, v37, v37\n"
"v_add_f32 v24, v24, v38\n"
"v_fmac_f32 v25, v38, v38\n"
"v_add_f32 v24, v24, v39\n"
"v_fmac_f32 v25, v39, v39\n"
"v_add_f32 v24, v24, v40\n"
"v_fmac_f32 v25, v40, v40\n"
"v_add_f32 v24, v24, v41\n"
"v_fmac_f32 v25, v41, v41\n"
"v_add_f32 v24, v24, v42\n"
"v_fmac_f32 v25, v42, v42\n"
"v_add_f32 v24, v24, v43\n"
"v_fmac_f32 v25, v43, v43\n"
"v_add_f32 v24, v24, v44\n"
"v_fmac_f32 v25, v44, v44\n"
"v_add_f32 v24, v24, v45\n"
"v_fmac_f32 v25, v45, v45\n"
"v_add_f32 v24, v24, v46\n"
"v_fmac_f32 v25, v46, v46\n"
"v_add_f32 v24, v24, v47\n"
"v_fmac_f32 v25, v47, v47\n"
"s_mov_b32 exec_lo, -1\n"
"s_mov_b32 exec_hi, 0\n"
"v_add_f32 v24, v24, v48\n"
"v_fmac_f32 v25, v48, v48\n"
"v_add_f32 v24, v24, v49\n"
"v_fmac_f32 v25, v49, v49\n"
"v_add_f32 v24, v24, v50\n"
"v_fmac_f32 v25, v50, v50\n"
"v_add_f32 v24, v24, v51\n"
"v_fmac_f32 v25, v51, v51\n"
"v_add_f32 v24, v24, v52\n"
"v_fmac_f32 v25, v52, v52\n"
"v_add_f32 v24, v24, v53\n"
"v_fmac_f32 v25, v53, v53\n"
"v_add_f32 v24, v24, v54\n"
"v_fmac_f32 v25, v54, v54\n"
"v_add_f32 v24, v24, v55\n"
"v_fmac_f32 v25, v55, v55\n"
"s_mov_b64 exec, -1\n"
"s_nop 1\n"
"v_add_f32_dpp v24, v24, v24 row_ror:8 row_mask:0xf bank_mask:0xf\n"
"v_mov_b32 v26, v24\n"
"s_nop 1\n"
"v_permlane16_swap_b32 v24, v26\n"
"s_nop 1\n"
"v_add_f32 v24, v24, v26\n"
"v_mov_b32 v26, v24\n"
"s_nop 1\n"
"v_permlane32_swap_b32 v24, v26\n"
"s_nop 1\n"
"v_add_f32 v24, v24, v26\n"
"s_nop 1\n"
"v_add_f32_dpp v25, v25, v25 row_ror:8 row_mask:0xf bank_mask:0xf\n"
"v_mov_b32 v26, v25\n"
"s_nop 1\n"
"v_permlane16_swap_b32 v25, v26\n"
"s_nop 1\n"
"v_add_f32 v25, v25, v26\n"
"v_mov_b32 v26, v25\n"
"s_nop 1\n"
"v_permlane32_swap_b32 v25, v26\n"
"s_nop 1\n"
"v_add_f32 v25, v25, v26\n"
"v_lshlrev_b32 v27, 5, v16\n"
"s_lshl_b32 s26, s22, 3\n"
"s_add_u32 s26, s26, 162816\n"
"v_add_u32 v28, s26, v27\n"
"v_add_u32 v27, 162816, v27\n"
"s_mov_b64 exec, 0xff\n"
"ds_write_b64 v28, v[24:25]\n"
"s_mov_b64 exec, -1\n"
"s_waitcnt lgkmcnt(0)\n"
"s_barrier\n"
"ds_read_b128 v[56:59], v27\n"
"ds_read_b128 v[60:63], v27 offset:16\n"
"s_waitcnt lgkmcnt(0)\n"
"v_add_f32 v24, v56, v58\n"
"v_add_f32 v26, v60, v62\n"
"v_add_f32 v24, v24, v26\n"
"v_add_f32 v25, v57, v59\n"
"v_add_f32 v26, v61, v63\n"
"v_add_f32 v25, v25, v26\n"
"v_mul_f32 v24, 0x3acccccd, v24\n"
"v_mul_f32 v25, 0x3acccccd, v25\n"
"v_fma_f32 v25, -v24, v24, v25\n"
"v_max_f32 v25, 0, v25\n"
"v_add_f32 v25, 0x3727c5ac, v25\n"
"v_rsq_f32 v25, v25\n"
"s_nop 0\n"
"v_mul_f32 v26, v24, v25\n"
"v_fma_f32 v32, v32, v25, -v26\n"
"v_fma_f32 v33, v33, v25, -v26\n"
"v_fma_f32 v34, v34, v25, -v26\n"
"v_fma_f32 v35, v35, v25, -v26\n"
"v_fma_f32 v36, v36, v25, -v26\n"
"v_fma_f32 v37, v37, v25, -v26\n"
"v_fma_f32 v38, v38, v25, -v26\n"
"v_fma_f32 v39, v39, v25, -v26\n"
"v_fma_f32 v40, v40, v25, -v26\n"
"v_fma_f32 v41, v41, v25, -v26\n"
"v_fma_f32 v42, v42, v25, -v26\n"
"v_fma_f32 v43, v43, v25, -v26\n"
"v_fma_f32 v44, v44, v25, -v26\n"
"v_fma_f32 v45, v45, v25, -v26\n"
"v_fma_f32 v46, v46, v25, -v26\n"
"v_fma_f32 v47, v47, v25, -v26\n"
"v_fma_f32 v48, v48, v25, -v26\n"
"v_fma_f32 v49, v49, v25, -v26\n"
"v_fma_f32 v50, v50, v25, -v26\n"
"v_fma_f32 v51, v51, v25, -v26\n"
"v_fma_f32 v52, v52, v25, -v26\n"
"v_fma_f32 v53, v53, v25, -v26\n"
"v_fma_f32 v54, v54, v25, -v26\n"
"v_fma_f32 v55, v55, v25, -v26\n"
"v_cvt_pk_bf16_f32 v0, v32, v33\n"
"v_cvt_pk_bf16_f32 v1, v34, v35\n"
"v_cvt_pk_bf16_f32 v2, v36, v37\n"
"v_cvt_pk_bf16_f32 v3, v38, v39\n"
"v_cvt_pk_bf16_f32 v4, v40, v41\n"
"v_cvt_pk_bf16_f32 v5, v42, v43\n"
"v_cvt_pk_bf16_f32 v6, v44, v45\n"
"v_cvt_pk_bf16_f32 v7, v46, v47\n"
"v_cvt_pk_bf16_f32 v8, v48, v49\n"
"v_cvt_pk_bf16_f32 v9, v50, v51\n"
"v_cvt_pk_bf16_f32 v10, v52, v53\n"
"v_cvt_pk_bf16_f32 v11, v54, v55\n"
"s_lshl_b32 s25, s21, 4\n"
"s_lshr_b32 s26, s20, 1\n"
"s_add_u32 s25, s25, s26\n"
"s_mul_i32 s25, s25, 20480\n"
"s_and_b32 s26, s20, 1\n"
"s_lshl_b32 s26, s26, 7\n"
"s_add_u32 s25, s25, s26\n"
"s_add_u32 s25, s25, 65536\n"
"v_lshlrev_b32 v20, 8, v17\n"
"v_lshl_add_u32 v20, v16, 4, v20\n"
"buffer_store_dwordx4 v[0:3], v20, s[72:75], s25 offen sc1\n"
"buffer_store_dwordx4 v[4:7], v20, s[72:75], s25 offen offset:2048 sc1\n"
"v_add_u32 v21, 4096, v20\n"
"s_mov_b32 exec_lo, -1\n"
"s_mov_b32 exec_hi, 0\n"
"buffer_store_dwordx4 v[8:11], v21, s[72:75], s25 offen sc1\n"
"s_mov_b64 exec, -1\n"
"s_or_b32 s25, s20, s22\n"
"s_cmp_lg_u32 s25, 0\n"
"s_cbranch_scc1 NOZERO_%=\n"
"s_lshl_b32 s25, s21, 3\n"
"s_lshl_b32 s26, s21, 2\n"
"v_add_u32 v20, s25, v255\n"
"v_add_u32 v21, 56, v20\n"
"v_add_u32 v22, s26, v255\n"
"v_add_u32 v23, 112, v22\n"
"v_add_u32 v22, 140, v22\n"
"v_cmp_gt_u32 vcc, 8, v255\n"
"s_nop 1\n"
"v_cndmask_b32 v21, v21, v20, vcc\n"
"v_cmp_gt_u32 vcc, 20, v255\n"
"s_nop 1\n"
"v_cndmask_b32 v22, v22, v23, vcc\n"
"v_cmp_gt_u32 vcc, 16, v255\n"
"s_nop 1\n"
"v_cndmask_b32 v20, v22, v21, vcc\n"
"v_lshlrev_b32 v20, 8, v20\n"
"v_mov_b32 v21, 0\n"
"s_mov_b64 exec, 0xffffff\n"
"global_store_dword v20, v21, s[54:55] sc1\n"
"s_mov_b64 exec, -1\n"
"NOZERO_%=:\n"
"s_waitcnt vmcnt(0)\n"
"s_barrier\n"
"s_cmp_lg_u32 s22, 0\n"
"s_cbranch_scc1 XWAIT_%=\n"
"s_lshl_b32 s25, s21, 12\n"
"s_add_u32 s25, s25, 77135872\n"
"s_lshl_b32 s26, s20, 7\n"
"s_add_u32 s26, s26, s25\n"
"v_lshlrev_b32 v20, 4, v255\n"
"v_add_u32 v20, s26, v20\n"
"v_mov_b32 v28, s98\n"
"v_mov_b32 v29, s99\n"
"v_mov_b32 v30, s98\n"
"v_mov_b32 v31, s99\n"
"s_mov_b64 exec, 0xff\n"
"global_store_dwordx4 v20, v[28:31], s[54:55] sc1\n"
"s_mov_b32 exec_lo, -1\n"
"s_mov_b32 exec_hi, 0\n"
"v_lshlrev_b32 v20, 7, v255\n"
"v_add_u32 v20, s25, v20\n"
"s_mov_b32 s27, 20000\n"
"XSPIN_%=:\n"
"global_load_dwordx2 v[24:25], v20, s[54:55] sc1\n"
"s_waitcnt vmcnt(0)\n"
"v_xor_b32 v24, v24, v28\n"
"v_xor_b32 v25, v25, v29\n"
"v_or_b32 v24, v24, v25\n"
"v_cmp_ne_u32 vcc, 0, v24\n"
"s_nop 4\n"
"s_cmp_eq_u64 vcc, 0\n"
"s_cbranch_scc1 XDONE_%=\n"
"s_sleep 2\n"
"s_sub_u32 s27, s27, 1\n"
"s_cmp_lg_u32 s27, 0\n"
"s_cbranch_scc1 XSPIN_%=\n"
"XDONE_%=:\n"
"s_mov_b64 exec, -1\n"
"XWAIT_%=:\n"
"s_barrier\n"
"buffer_load_dwordx4 a[96:99], v249, s[72:75], s91 offen offset:0 sc1\n"
"buffer_load_dwordx4 a[100:103], v249, s[72:75], s91 offen offset:1024 sc1\n"
"s_add_u32 s91, s91, 2048\n"
"buffer_load_dwordx4 a[104:107], v249, s[72:75], s92 offen offset:0 sc1\n"
"buffer_load_dwordx4 a[108:111], v249, s[72:75], s92 offen offset:1024 sc1\n"
"s_add_u32 s92, s92, 2048\n"
"buffer_load_dwordx4 a[112:115], v249, s[72:75], s93 offen offset:0 sc1\n"
"buffer_load_dwordx4 a[116:119], v249, s[72:75], s93 offen offset:1024 sc1\n"
"s_add_u32 s93, s93, 2048\n"
"buffer_load_dwordx4 a[120:123], v249, s[72:75], s94 offen offset:0 sc1\n"
"buffer_load_dwordx4 a[124:127], v249, s[72:75], s94 offen offset:1024 sc1\n"
"s_add_u32 s94, s94, 2048\n"
"s_add_u32 m0, s86, 0\n"
"s_nop 0\n"
"buffer_load_dwordx4 v242, s[64:67], s88 offen offset:0 lds\n"
"buffer_load_dwordx4 v243, s[64:67], s88 offen offset:1024 lds\n"
"buffer_load_dwordx4 v244, s[64:67], s88 offen offset:2048 lds\n"
"s_add_u32 s88, s88, 786432\n"
"s_add_u32 m0, s86, 3072\n"
"s_nop 0\n"
"buffer_load_dwordx4 v242, s[64:67], s89 offen offset:0 lds\n"
"buffer_load_dwordx4 v243, s[64:67], s89 offen offset:1024 lds\n"
"buffer_load_dwordx4 v244, s[64:67], s89 offen offset:2048 lds\n"
"s_add_u32 s89, s89, 786432\n"
"s_add_u32 m0, s87, 0\n"
"s_nop 0\n"
"buffer_load_dwordx4 v245, s[68:71], s90 offen offset:0 lds\n"
"buffer_load_dwordx4 v246, s[68:71], s90 offen offset:1024 lds\n"
"buffer_load_dwordx4 v247, s[68:71], s90 offen offset:2048 lds\n"
"s_add_u32 s90, s90, 196608\n"
"s_waitcnt vmcnt(9)\n"
"ds_read_b64_tr_b16 v[192:193], v238 offset:0\n"
"ds_read_b64_tr_b16 v[194:195], v238 offset:768\n"
"ds_read_b64_tr_b16 v[196:197], v238 offset:3072\n"
"ds_read_b64_tr_b16 v[198:199], v238 offset:3840\n"
"ds_read_b64_tr_b16 v[200:201], v238 offset:6144\n"
"ds_read_b64_tr_b16 v[202:203], v238 offset:6912\n"
"ds_read_b64_tr_b16 v[204:205], v238 offset:9216\n"
"ds_read_b64_tr_b16 v[206:207], v238 offset:9984\n"
"ds_read_b64_tr_b16 v[208:209], v238 offset:32\n"
"ds_read_b64_tr_b16 v[210:211], v238 offset:800\n"
"ds_read_b64_tr_b16 v[212:213], v238 offset:3104\n"
"ds_read_b64_tr_b16 v[214:215], v238 offset:3872\n"
"s_waitcnt lgkmcnt(2)\n"
"v_mfma_f32_16x16x32_bf16 a[160:163], v[192:195], a[96:99], 0\n"
"ds_read_b64_tr_b16 v[216:217], v238 offset:6176\n"
"v_mfma_f32_16x16x32_bf16 a[160:163], v[196:199], a[100:103], a[160:163]\n"
"v_mfma_f32_16x16x32_bf16 a[164:167], v[192:195], a[104:107], 0\n"
"ds_read_b64_tr_b16 v[218:219], v238 offset:6944\n"
"v_mfma_f32_16x16x32_bf16 a[164:167], v[196:199], a[108:111], a[164:167]\n"
"buffer_load_dwordx4 a[128:131], v249, s[72:75], s91 offen offset:0 sc1\n"
"v_mfma_f32_16x16x32_bf16 a[168:171], v[192:195], a[112:115], 0\n"
"ds_read_b64_tr_b16 v[220:221], v238 offset:9248\n"
"v_mfma_f32_16x16x32_bf16 a[168:171], v[196:199], a[116:119], a[168:171]\n"
"v_mfma_f32_16x16x32_bf16 a[172:175], v[192:195], a[120:123], 0\n"
"ds_read_b64_tr_b16 v[222:223], v238 offset:10016\n"
"v_mfma_f32_16x16x32_bf16 a[172:175], v[196:199], a[124:127], a[172:175]\n"
"v_mfma_f32_16x16x32_bf16 a[208:211], v[200:203], a[96:99], 0\n"
"ds_read_b64_tr_b16 v[192:193], v238 offset:64\n"
"v_mfma_f32_16x16x32_bf16 a[208:211], v[204:207], a[100:103], a[208:211]\n"
"v_mfma_f32_16x16x32_bf16 a[212:215], v[200:203], a[104:107], 0\n"
"ds_read_b64_tr_b16 v[194:195], v238 offset:832\n"
"v_mfma_f32_16x16x32_bf16 a[212:215], v[204:207], a[108:111], a[212:215]\n"
"buffer_load_dwordx4 a[132:135], v249, s[72:75], s91 offen offset:1024 sc1\n"
"v_mfma_f32_16x16x32_bf16 a[216:219], v[200:203], a[112:115], 0\n"
"ds_read_b64_tr_b16 v[196:197], v238 offset:3136\n"
"v_mfma_f32_16x16x32_bf16 a[216:219], v[204:207], a[116:119], a[216:219]\n"
"v_mfma_f32_16x16x32_bf16 a[220:223], v[200:203], a[120:123], 0\n"
"ds_read_b64_tr_b16 v[198:199], v238 offset:3904\n"
"v_mfma_f32_16x16x32_bf16 a[220:223], v[204:207], a[124:127], a[220:223]\n"
"v_mfma_f32_16x16x32_bf16 a[176:179], v[208:211], a[96:99], 0\n"
"ds_read_b64_tr_b16 v[200:201], v238 offset:6208\n"
"s_waitcnt lgkmcnt(3)\n"
"v_mfma_f32_16x16x32_bf16 a[176:179], v[212:215], a[100:103], a[176:179]\n"
"v_mfma_f32_16x16x32_bf16 a[180:183], v[208:211], a[104:107], 0\n"
"ds_read_b64_tr_b16 v[202:203], v238 offset:6976\n"
"s_add_u32 s91, s91, 2048\n"
"v_mfma_f32_16x16x32_bf16 a[180:183], v[212:215], a[108:111], a[180:183]\n"
"buffer_load_dwordx4 a[136:139], v249, s[72:75], s92 offen offset:0 sc1\n"
"v_mfma_f32_16x16x32_bf16 a[184:187], v[208:211], a[112:115], 0\n"
"ds_read_b64_tr_b16 v[204:205], v238 offset:9280\n"
"v_mfma_f32_16x16x32_bf16 a[184:187], v[212:215], a[116:119], a[184:187]\n"
"v_mfma_f32_16x16x32_bf16 a[188:191], v[208:211], a[120:123], 0\n"
"ds_read_b64_tr_b16 v[206:207], v238 offset:10048\n"
"v_mfma_f32_16x16x32_bf16 a[188:191], v[212:215], a[124:127], a[188:191]\n"
"s_barrier\n"
"v_mfma_f32_16x16x32_bf16 a[224:227], v[216:219], a[96:99], 0\n"
"ds_read_b64_tr_b16 v[208:209], v236 offset:0\n"
"v_mfma_f32_16x16x32_bf16 a[224:227], v[220:223], a[100:103], a[224:227]\n"
"v_mfma_f32_16x16x32_bf16 a[228:231], v[216:219], a[104:107], 0\n"
"ds_read_b64_tr_b16 v[210:211], v236 offset:1536\n"
"v_mfma_f32_16x16x32_bf16 a[228:231], v[220:223], a[108:111], a[228:231]\n"
"buffer_load_dwordx4 a[140:143], v249, s[72:75], s92 offen offset:1024 sc1\n"
"v_mfma_f32_16x16x32_bf16 a[232:235], v[216:219], a[112:115], 0\n"
"ds_read_b64_tr_b16 v[212:213], v236 offset:12288\n"
"v_mfma_f32_16x16x32_bf16 a[232:235], v[220:223], a[116:119], a[232:235]\n"
"v_mfma_f32_16x16x32_bf16 a[236:239], v[216:219], a[120:123], 0\n"
"ds_read_b64_tr_b16 v[214:215], v236 offset:13824\n"
"v_mfma_f32_16x16x32_bf16 a[236:239], v[220:223], a[124:127], a[236:239]\n"
"v_mfma_f32_16x16x32_bf16 a[192:195], v[192:195], a[96:99], 0\n"
"ds_read_b64_tr_b16 v[216:217], v236 offset:8\n"
"s_waitcnt lgkmcnt(3)\n"
"v_mfma_f32_16x16x32_bf16 a[192:195], v[196:199], a[100:103], a[192:195]\n"
"v_mfma_f32_16x16x32_bf16 a[196:199], v[192:195], a[104:107], 0\n"
"ds_read_b64_tr_b16 v[218:219], v236 offset:1544\n"
"s_add_u32 s92, s92, 2048\n"
"v_mfma_f32_16x16x32_bf16 a[196:199], v[196:199], a[108:111], a[196:199]\n"
"buffer_load_dwordx4 a[144:147], v249, s[72:75], s93 offen offset:0 sc1\n"
"v_mfma_f32_16x16x32_bf16 a[200:203], v[192:195], a[112:115], 0\n"
"ds_read_b64_tr_b16 v[220:221], v236 offset:12296\n"
"v_mfma_f32_16x16x32_bf16 a[200:203], v[196:199], a[116:119], a[200:203]\n"
"v_mfma_f32_16x16x32_bf16 a[204:207], v[192:195], a[120:123], 0\n"
"ds_read_b64_tr_b16 v[222:223], v236 offset:13832\n"
"v_mfma_f32_16x16x32_bf16 a[204:207], v[196:199], a[124:127], a[204:207]\n"
"v_mfma_f32_16x16x32_bf16 a[240:243], v[200:203], a[96:99], 0\n"
"ds_read_b64_tr_b16 v[192:193], v237 offset:0\n"
"v_mfma_f32_16x16x32_bf16 a[240:243], v[204:207], a[100:103], a[240:243]\n"
"v_mfma_f32_16x16x32_bf16 a[244:247], v[200:203], a[104:107], 0\n"
"ds_read_b64_tr_b16 v[194:195], v237 offset:1536\n"
"v_mfma_f32_16x16x32_bf16 a[244:247], v[204:207], a[108:111], a[244:247]\n"
"buffer_load_dwordx4 a[148:151], v249, s[72:75], s93 offen offset:1024 sc1\n"
"v_mfma_f32_16x16x32_bf16 a[248:251], v[200:203], a[112:115], 0\n"
"ds_read_b64_tr_b16 v[196:197], v237 offset:12288\n"
"v_mfma_f32_16x16x32_bf16 a[248:251], v[204:207], a[116:119], a[248:251]\n"
"v_mfma_f32_16x16x32_bf16 a[252:255], v[200:203], a[120:123], 0\n"
"ds_read_b64_tr_b16 v[198:199], v237 offset:13824\n"
"v_mfma_f32_16x16x32_bf16 a[252:255], v[204:207], a[124:127], a[252:255]\n"
"v_mfma_f32_16x16x32_bf16 v[0:3], v[208:211], a[96:99], 0\n"
"ds_read_b64_tr_b16 v[200:201], v237 offset:8\n"
"s_waitcnt lgkmcnt(3)\n"
"v_mfma_f32_16x16x32_bf16 v[0:3], v[212:215], a[100:103], v[0:3]\n"
"v_mfma_f32_16x16x32_bf16 v[4:7], v[208:211], a[104:107], 0\n"
"ds_read_b64_tr_b16 v[202:203], v237 offset:1544\n"
"s_add_u32 s93, s93, 2048\n"
"v_mfma_f32_16x16x32_bf16 v[4:7], v[212:215], a[108:111], v[4:7]\n"
"buffer_load_dwordx4 a[152:155], v249, s[72:75], s94 offen offset:0 sc1\n"
"v_mfma_f32_16x16x32_bf16 v[8:11], v[208:211], a[112:115], 0\n"
"ds_read_b64_tr_b16 v[204:205], v237 offset:12296\n"
"v_mfma_f32_16x16x32_bf16 v[8:11], v[212:215], a[116:119], v[8:11]\n"
"v_mfma_f32_16x16x32_bf16 v[12:15], v[208:211], a[120:123], 0\n"
"ds_read_b64_tr_b16 v[206:207], v237 offset:13832\n"
"v_mfma_f32_16x16x32_bf16 v[12:15], v[212:215], a[124:127], v[12:15]\n"
"v_mfma_f32_16x16x32_bf16 v[16:19], v[216:219], a[96:99], 0\n"
"ds_read_b64_tr_b16 v[208:209], v236 offset:128\n"
"v_mfma_f32_16x16x32_bf16 v[16:19], v[220:223], a[100:103], v[16:19]\n"
"v_mfma_f32_16x16x32_bf16 v[20:23], v[216:219], a[104:107], 0\n"
"ds_read_b64_tr_b16 v[210:211], v236 offset:1664\n"
"v_mfma_f32_16x16x32_bf16 v[20:23], v[220:223], a[108:111], v[20:23]\n"
"buffer_load_dwordx4 a[156:159], v249, s[72:75], s94 offen offset:1024 sc1\n"
"s_add_u32 s94, s94, 2048\n"
"v_mfma_f32_16x16x32_bf16 v[24:27], v[216:219], a[112:115], 0\n"
"ds_read_b64_tr_b16 v[212:213], v236 offset:12416\n"
"v_mfma_f32_16x16x32_bf16 v[24:27], v[220:223], a[116:119], v[24:27]\n"
"v_mfma_f32_16x16x32_bf16 v[28:31], v[216:219], a[120:123], 0\n"
"ds_read_b64_tr_b16 v[214:215], v236 offset:13952\n"
"v_mfma_f32_16x16x32_bf16 v[28:31], v[220:223], a[124:127], v[28:31]\n"
"v_mfma_f32_16x16x32_bf16 v[32:35], v[192:195], a[96:99], 0\n"
"ds_read_b64_tr_b16 v[216:217], v236 offset:136\n"
"s_waitcnt lgkmcnt(3)\n"
"v_mfma_f32_16x16x32_bf16 v[32:35], v[196:199], a[100:103], v[32:35]\n"
"v_mfma_f32_16x16x32_bf16 v[36:39], v[192:195], a[104:107], 0\n"
"ds_read_b64_tr_b16 v[218:219], v236 offset:1672\n"
"v_mfma_f32_16x16x32_bf16 v[36:39], v[196:199], a[108:111], v[36:39]\n"
"v_mfma_f32_16x16x32_bf16 v[40:43], v[192:195], a[112:115], 0\n"
"ds_read_b64_tr_b16 v[220:221], v236 offset:12424\n"
"v_mfma_f32_16x16x32_bf16 v[40:43], v[196:199], a[116:119], v[40:43]\n"
"v_mfma_f32_16x16x32_bf16 v[44:47], v[192:195], a[120:123], 0\n"
"ds_read_b64_tr_b16 v[222:223], v236 offset:13960\n"
"v_mfma_f32_16x16x32_bf16 v[44:47], v[196:199], a[124:127], v[44:47]\n"
"v_mfma_f32_16x16x32_bf16 v[48:51], v[200:203], a[96:99], 0\n"
"ds_read_b64_tr_b16 v[192:193], v237 offset:128\n"
"v_mfma_f32_16x16x32_bf16 v[48:51], v[204:207], a[100:103], v[48:51]\n"
"v_mfma_f32_16x16x32_bf16 v[52:55], v[200:203], a[104:107], 0\n"
"ds_read_b64_tr_b16 v[194:195], v237 offset:1664\n"
"v_mfma_f32_16x16x32_bf16 v[52:55], v[204:207], a[108:111], v[52:55]\n"
"v_mfma_f32_16x16x32_bf16 v[56:59], v[200:203], a[112:115], 0\n"
"ds_read_b64_tr_b16 v[196:197], v237 offset:12416\n"
"v_mfma_f32_16x16x32_bf16 v[56:59], v[204:207], a[116:119], v[56:59]\n"
"v_mfma_f32_16x16x32_bf16 v[60:63], v[200:203], a[120:123], 0\n"
"ds_read_b64_tr_b16 v[198:199], v237 offset:13952\n"
"v_mfma_f32_16x16x32_bf16 v[60:63], v[204:207], a[124:127], v[60:63]\n"
"v_mfma_f32_16x16x32_bf16 v[64:67], v[208:211], a[96:99], 0\n"
"ds_read_b64_tr_b16 v[200:201], v237 offset:136\n"
"s_waitcnt lgkmcnt(3)\n"
"v_mfma_f32_16x16x32_bf16 v[64:67], v[212:215], a[100:103], v[64:67]\n"
"v_mfma_f32_16x16x32_bf16 v[68:71], v[208:211], a[104:107], 0\n"
"ds_read_b64_tr_b16 v[202:203], v237 offset:1672\n"
"v_mfma_f32_16x16x32_bf16 v[68:71], v[212:215], a[108:111], v[68:71]\n"
"v_mfma_f32_16x16x32_bf16 v[72:75], v[208:211], a[112:115], 0\n"
"ds_read_b64_tr_b16 v[204:205], v237 offset:12424\n"
"v_mfma_f32_16x16x32_bf16 v[72:75], v[212:215], a[116:119], v[72:75]\n"
"v_mfma_f32_16x16x32_bf16 v[76:79], v[208:211], a[120:123], 0\n"
"ds_read_b64_tr_b16 v[206:207], v237 offset:13960\n"
"v_mfma_f32_16x16x32_bf16 v[76:79], v[212:215], a[124:127], v[76:79]\n"
"v_mfma_f32_16x16x32_bf16 v[80:83], v[216:219], a[96:99], 0\n"
"ds_read_b64_tr_b16 v[208:209], v236 offset:256\n"
"v_mfma_f32_16x16x32_bf16 v[80:83], v[220:223], a[100:103], v[80:83]\n"
"v_mfma_f32_16x16x32_bf16 v[84:87], v[216:219], a[104:107], 0\n"
"ds_read_b64_tr_b16 v[210:211], v236 offset:1792\n"
"v_mfma_f32_16x16x32_bf16 v[84:87], v[220:223], a[108:111], v[84:87]\n"
"v_mfma_f32_16x16x32_bf16 v[88:91], v[216:219], a[112:115], 0\n"
"ds_read_b64_tr_b16 v[212:213], v236 offset:12544\n"
"v_mfma_f32_16x16x32_bf16 v[88:91], v[220:223], a[116:119], v[88:91]\n"
"v_mfma_f32_16x16x32_bf16 v[92:95], v[216:219], a[120:123], 0\n"
"ds_read_b64_tr_b16 v[214:215], v236 offset:14080\n"
"v_mfma_f32_16x16x32_bf16 v[92:95], v[220:223], a[124:127], v[92:95]\n"
"v_mfma_f32_16x16x32_bf16 v[96:99], v[192:195], a[96:99], 0\n"
"ds_read_b64_tr_b16 v[216:217], v236 offset:264\n"
"s_waitcnt lgkmcnt(3)\n"
"v_mfma_f32_16x16x32_bf16 v[96:99], v[196:199], a[100:103], v[96:99]\n"
"v_mfma_f32_16x16x32_bf16 v[100:103], v[192:195], a[104:107], 0\n"
"ds_read_b64_tr_b16 v[218:219], v236 offset:1800\n"
"v_mfma_f32_16x16x32_bf16 v[100:103], v[196:199], a[108:111], v[100:103]\n"
"v_mfma_f32_16x16x32_bf16 v[104:107], v[192:195], a[112:115], 0\n"
"ds_read_b64_tr_b16 v[220:221], v236 offset:12552\n"
"v_mfma_f32_16x16x32_bf16 v[104:107], v[196:199], a[116:119], v[104:107]\n"
"v_mfma_f32_16x16x32_bf16 v[108:111], v[192:195], a[120:123], 0\n"
"ds_read_b64_tr_b16 v[222:223], v236 offset:14088\n"
"v_mfma_f32_16x16x32_bf16 v[108:111], v[196:199], a[124:127], v[108:111]\n"
"v_mfma_f32_16x16x32_bf16 v[112:115], v[200:203], a[96:99], 0\n"
"ds_read_b64_tr_b16 v[192:193], v237 offset:256\n"
"v_mfma_f32_16x16x32_bf16 v[112:115], v[204:207], a[100:103], v[112:115]\n"
"v_mfma_f32_16x16x32_bf16 v[116:119], v[200:203], a[104:107], 0\n"
"ds_read_b64_tr_b16 v[194:195], v237 offset:1792\n"
"v_mfma_f32_16x16x32_bf16 v[116:119], v[204:207], a[108:111], v[116:119]\n"
"v_mfma_f32_16x16x32_bf16 v[120:123], v[200:203], a[112:115], 0\n"
"ds_read_b64_tr_b16 v[196:197], v237 offset:12544\n"
"v_mfma_f32_16x16x32_bf16 v[120:123], v[204:207], a[116:119], v[120:123]\n"
"v_mfma_f32_16x16x32_bf16 v[124:127], v[200:203], a[120:123], 0\n"
"ds_read_b64_tr_b16 v[198:199], v237 offset:14080\n"
"v_mfma_f32_16x16x32_bf16 v[124:127], v[204:207], a[124:127], v[124:127]\n"
"v_mfma_f32_16x16x32_bf16 v[128:131], v[208:211], a[96:99], 0\n"
"ds_read_b64_tr_b16 v[200:201], v237 offset:264\n"
"s_waitcnt lgkmcnt(3)\n"
"v_mfma_f32_16x16x32_bf16 v[128:131], v[212:215], a[100:103], v[128:131]\n"
"v_mfma_f32_16x16x32_bf16 v[132:135], v[208:211], a[104:107], 0\n"
"ds_read_b64_tr_b16 v[202:203], v237 offset:1800\n"
"v_mfma_f32_16x16x32_bf16 v[132:135], v[212:215], a[108:111], v[132:135]\n"
"v_mfma_f32_16x16x32_bf16 v[136:139], v[208:211], a[112:115], 0\n"
"ds_read_b64_tr_b16 v[204:205], v237 offset:12552\n"
"v_mfma_f32_16x16x32_bf16 v[136:139], v[212:215], a[116:119], v[136:139]\n"
"v_mfma_f32_16x16x32_bf16 v[140:143], v[208:211], a[120:123], 0\n"
"ds_read_b64_tr_b16 v[206:207], v237 offset:14088\n"
"v_mfma_f32_16x16x32_bf16 v[140:143], v[212:215], a[124:127], v[140:143]\n"
"s_barrier\n"
"v_mfma_f32_16x16x32_bf16 v[144:147], v[216:219], a[96:99], 0\n"
"ds_read_b64_tr_b16 v[208:209], v241 offset:0\n"
"v_mfma_f32_16x16x32_bf16 v[144:147], v[220:223], a[100:103], v[144:147]\n"
"v_subrev_u32 v236, 73728, v236\n"
"v_mfma_f32_16x16x32_bf16 v[148:151], v[216:219], a[104:107], 0\n"
"ds_read_b64_tr_b16 v[210:211], v241 offset:768\n"
"v_mfma_f32_16x16x32_bf16 v[148:151], v[220:223], a[108:111], v[148:151]\n"
"v_subrev_u32 v237, 73728, v237\n"
"v_mfma_f32_16x16x32_bf16 v[152:155], v[216:219], a[112:115], 0\n"
"ds_read_b64_tr_b16 v[212:213], v241 offset:3072\n"
"v_mfma_f32_16x16x32_bf16 v[152:155], v[220:223], a[116:119], v[152:155]\n"
"v_subrev_u32 v238, 73728, v238\n"
"v_mfma_f32_16x16x32_bf16 v[156:159], v[216:219], a[120:123], 0\n"
"ds_read_b64_tr_b16 v[214:215], v241 offset:3840\n"
"s_add_u32 m0, s86, 36864\n"
"v_mfma_f32_16x16x32_bf16 v[156:159], v[220:223], a[124:127], v[156:159]\n"
"buffer_load_dwordx4 v242, s[64:67], s88 offen offset:0 lds\n"
"v_mfma_f32_16x16x32_bf16 v[160:163], v[192:195], a[96:99], 0\n"
"ds_read_b64_tr_b16 v[216:217], v241 offset:6144\n"
"s_waitcnt lgkmcnt(3)\n"
"v_mfma_f32_16x16x32_bf16 v[160:163], v[196:199], a[100:103], v[160:163]\n"
"v_mfma_f32_16x16x32_bf16 v[164:167], v[192:195], a[104:107], 0\n"
"ds_read_b64_tr_b16 v[218:219], v241 offset:6912\n"
"v_mfma_f32_16x16x32_bf16 v[164:167], v[196:199], a[108:111], v[164:167]\n"
"v_mfma_f32_16x16x32_bf16 v[168:171], v[192:195], a[112:115], 0\n"
"ds_read_b64_tr_b16 v[220:221], v241 offset:9216\n"
"v_mfma_f32_16x16x32_bf16 v[168:171], v[196:199], a[116:119], v[168:171]\n"
"v_mfma_f32_16x16x32_bf16 v[172:175], v[192:195], a[120:123], 0\n"
"ds_read_b64_tr_b16 v[222:223], v241 offset:9984\n"
"v_mfma_f32_16x16x32_bf16 v[172:175], v[196:199], a[124:127], v[172:175]\n"
"buffer_load_dwordx4 v243, s[64:67], s88 offen offset:1024 lds\n"
"v_mfma_f32_16x16x32_bf16 v[176:179], v[200:203], a[96:99], 0\n"
"ds_read_b64_tr_b16 v[192:193], v241 offset:32\n"
"v_mfma_f32_16x16x32_bf16 v[176:179], v[204:207], a[100:103], v[176:179]\n"
"v_mfma_f32_16x16x32_bf16 v[180:183], v[200:203], a[104:107], 0\n"
"ds_read_b64_tr_b16 v[194:195], v241 offset:800\n"
"v_mfma_f32_16x16x32_bf16 v[180:183], v[204:207], a[108:111], v[180:183]\n"
"v_mfma_f32_16x16x32_bf16 v[184:187], v[200:203], a[112:115], 0\n"
"ds_read_b64_tr_b16 v[196:197], v241 offset:3104\n"
"v_mfma_f32_16x16x32_bf16 v[184:187], v[204:207], a[116:119], v[184:187]\n"
"v_mfma_f32_16x16x32_bf16 v[188:191], v[200:203], a[120:123], 0\n"
"ds_read_b64_tr_b16 v[198:199], v241 offset:3872\n"
"v_mfma_f32_16x16x32_bf16 v[188:191], v[204:207], a[124:127], v[188:191]\n"
"buffer_load_dwordx4 v244, s[64:67], s88 offen offset:2048 lds\n"
"s_waitcnt vmcnt(10)\n"
"v_mfma_f32_16x16x32_bf16 a[160:163], v[208:211], a[128:131], a[160:163]\n"
"ds_read_b64_tr_b16 v[200:201], v241 offset:6176\n"
"s_waitcnt vmcnt(9) lgkmcnt(3)\n"
"v_mfma_f32_16x16x32_bf16 a[160:163], v[212:215], a[132:135], a[160:163]\n"
"s_waitcnt vmcnt(8)\n"
"v_mfma_f32_16x16x32_bf16 a[164:167], v[208:211], a[136:139], a[164:167]\n"
"ds_read_b64_tr_b16 v[202:203], v241 offset:6944\n"
"s_waitcnt vmcnt(7)\n"
"v_mfma_f32_16x16x32_bf16 a[164:167], v[212:215], a[140:143], a[164:167]\n"
"s_waitcnt vmcnt(6)\n"
"v_mfma_f32_16x16x32_bf16 a[168:171], v[208:211], a[144:147], a[168:171]\n"
"ds_read_b64_tr_b16 v[204:205], v241 offset:9248\n"
"s_waitcnt vmcnt(5)\n"
"v_mfma_f32_16x16x32_bf16 a[168:171], v[212:215], a[148:151], a[168:171]\n"
"s_waitcnt vmcnt(4)\n"
"v_mfma_f32_16x16x32_bf16 a[172:175], v[208:211], a[152:155], a[172:175]\n"
"ds_read_b64_tr_b16 v[206:207], v241 offset:10016\n"
"s_waitcnt vmcnt(3)\n"
"v_mfma_f32_16x16x32_bf16 a[172:175], v[212:215], a[156:159], a[172:175]\n"
"buffer_load_dwordx4 a[96:99], v249, s[72:75], s91 offen offset:0 sc1\n"
"v_mfma_f32_16x16x32_bf16 a[208:211], v[216:219], a[128:131], a[208:211]\n"
"ds_read_b64_tr_b16 v[208:209], v241 offset:64\n"
"v_mfma_f32_16x16x32_bf16 a[208:211], v[220:223], a[132:135], a[208:211]\n"
"v_mfma_f32_16x16x32_bf16 a[212:215], v[216:219], a[136:139], a[212:215]\n"
"ds_read_b64_tr_b16 v[210:211], v241 offset:832\n"
"v_mfma_f32_16x16x32_bf16 a[212:215], v[220:223], a[140:143], a[212:215]\n"
"v_mfma_f32_16x16x32_bf16 a[216:219], v[216:219], a[144:147], a[216:219]\n"
"ds_read_b64_tr_b16 v[212:213], v241 offset:3136\n"
"v_mfma_f32_16x16x32_bf16 a[216:219], v[220:223], a[148:151], a[216:219]\n"
"v_mfma_f32_16x16x32_bf16 a[220:223], v[216:219], a[152:155], a[220:223]\n"
"ds_read_b64_tr_b16 v[214:215], v241 offset:3904\n"
"v_mfma_f32_16x16x32_bf16 a[220:223], v[220:223], a[156:159], a[220:223]\n"
"buffer_load_dwordx4 a[100:103], v249, s[72:75], s91 offen offset:1024 sc1\n"
"v_mfma_f32_16x16x32_bf16 a[176:179], v[192:195], a[128:131], a[176:179]\n"
"ds_read_b64_tr_b16 v[216:217], v241 offset:6208\n"
"s_waitcnt lgkmcnt(3)\n"
"v_mfma_f32_16x16x32_bf16 a[176:179], v[196:199], a[132:135], a[176:179]\n"
"v_mfma_f32_16x16x32_bf16 a[180:183], v[192:195], a[136:139], a[180:183]\n"
"ds_read_b64_tr_b16 v[218:219], v241 offset:6976\n"
"v_mfma_f32_16x16x32_bf16 a[180:183], v[196:199], a[140:143], a[180:183]\n"
"v_mfma_f32_16x16x32_bf16 a[184:187], v[192:195], a[144:147], a[184:187]\n"
"ds_read_b64_tr_b16 v[220:221], v241 offset:9280\n"
"v_mfma_f32_16x16x32_bf16 a[184:187], v[196:199], a[148:151], a[184:187]\n"
"v_mfma_f32_16x16x32_bf16 a[188:191], v[192:195], a[152:155], a[188:191]\n"
"ds_read_b64_tr_b16 v[222:223], v241 offset:10048\n"
"s_add_u32 s91, s91, 2048\n"
"v_mfma_f32_16x16x32_bf16 a[188:191], v[196:199], a[156:159], a[188:191]\n"
"buffer_load_dwordx4 a[104:107], v249, s[72:75], s92 offen offset:0 sc1\n"
"v_mfma_f32_16x16x32_bf16 a[224:227], v[200:203], a[128:131], a[224:227]\n"
"ds_read_b64_tr_b16 v[192:193], v239 offset:0\n"
"v_mfma_f32_16x16x32_bf16 a[224:227], v[204:207], a[132:135], a[224:227]\n"
"v_mfma_f32_16x16x32_bf16 a[228:231], v[200:203], a[136:139], a[228:231]\n"
"ds_read_b64_tr_b16 v[194:195], v239 offset:1536\n"
"v_mfma_f32_16x16x32_bf16 a[228:231], v[204:207], a[140:143], a[228:231]\n"
"v_mfma_f32_16x16x32_bf16 a[232:235], v[200:203], a[144:147], a[232:235]\n"
"ds_read_b64_tr_b16 v[196:197], v239 offset:12288\n"
"v_mfma_f32_16x16x32_bf16 a[232:235], v[204:207], a[148:151], a[232:235]\n"
"v_mfma_f32_16x16x32_bf16 a[236:239], v[200:203], a[152:155], a[236:239]\n"
"ds_read_b64_tr_b16 v[198:199], v239 offset:13824\n"
"v_mfma_f32_16x16x32_bf16 a[236:239], v[204:207], a[156:159], a[236:239]\n"
"buffer_load_dwordx4 a[108:111], v249, s[72:75], s92 offen offset:1024 sc1\n"
"v_mfma_f32_16x16x32_bf16 a[192:195], v[208:211], a[128:131], a[192:195]\n"
"ds_read_b64_tr_b16 v[200:201], v239 offset:8\n"
"s_waitcnt lgkmcnt(3)\n"
"v_mfma_f32_16x16x32_bf16 a[192:195], v[212:215], a[132:135], a[192:195]\n"
"v_mfma_f32_16x16x32_bf16 a[196:199], v[208:211], a[136:139], a[196:199]\n"
"ds_read_b64_tr_b16 v[202:203], v239 offset:1544\n"
"v_mfma_f32_16x16x32_bf16 a[196:199], v[212:215], a[140:143], a[196:199]\n"
"v_mfma_f32_16x16x32_bf16 a[200:203], v[208:211], a[144:147], a[200:203]\n"
"ds_read_b64_tr_b16 v[204:205], v239 offset:12296\n"
"v_mfma_f32_16x16x32_bf16 a[200:203], v[212:215], a[148:151], a[200:203]\n"
"v_mfma_f32_16x16x32_bf16 a[204:207], v[208:211], a[152:155], a[204:207]\n"
"ds_read_b64_tr_b16 v[206:207], v239 offset:13832\n"
"s_add_u32 s92, s92, 2048\n"
"v_mfma_f32_16x16x32_bf16 a[204:207], v[212:215], a[156:159], a[204:207]\n"
"buffer_load_dwordx4 a[112:115], v249, s[72:75], s93 offen offset:0 sc1\n"
"v_mfma_f32_16x16x32_bf16 a[240:243], v[216:219], a[128:131], a[240:243]\n"
"ds_read_b64_tr_b16 v[208:209], v240 offset:0\n"
"v_mfma_f32_16x16x32_bf16 a[240:243], v[220:223], a[132:135], a[240:243]\n"
"v_mfma_f32_16x16x32_bf16 a[244:247], v[216:219], a[136:139], a[244:247]\n"
"ds_read_b64_tr_b16 v[210:211], v240 offset:1536\n"
"v_mfma_f32_16x16x32_bf16 a[244:247], v[220:223], a[140:143], a[244:247]\n"
"v_mfma_f32_16x16x32_bf16 a[248:251], v[216:219], a[144:147], a[248:251]\n"
"ds_read_b64_tr_b16 v[212:213], v240 offset:12288\n"
"v_mfma_f32_16x16x32_bf16 a[248:251], v[220:223], a[148:151], a[248:251]\n"
"v_mfma_f32_16x16x32_bf16 a[252:255], v[216:219], a[152:155], a[252:255]\n"
"ds_read_b64_tr_b16 v[214:215], v240 offset:13824\n"
"v_mfma_f32_16x16x32_bf16 a[252:255], v[220:223], a[156:159], a[252:255]\n"
"buffer_load_dwordx4 a[116:119], v249, s[72:75], s93 offen offset:1024 sc1\n"
"v_mfma_f32_16x16x32_bf16 v[0:3], v[192:195], a[128:131], v[0:3]\n"
"ds_read_b64_tr_b16 v[216:217], v240 offset:8\n"
"s_waitcnt lgkmcnt(3)\n"
"v_mfma_f32_16x16x32_bf16 v[0:3], v[196:199], a[132:135], v[0:3]\n"
"v_mfma_f32_16x16x32_bf16 v[4:7], v[192:195], a[136:139], v[4:7]\n"
"ds_read_b64_tr_b16 v[218:219], v240 offset:1544\n"
"v_mfma_f32_16x16x32_bf16 v[4:7], v[196:199], a[140:143], v[4:7]\n"
"v_mfma_f32_16x16x32_bf16 v[8:11], v[192:195], a[144:147], v[8:11]\n"
"ds_read_b64_tr_b16 v[220:221], v240 offset:12296\n"
"v_mfma_f32_16x16x32_bf16 v[8:11], v[196:199], a[148:151], v[8:11]\n"
"v_mfma_f32_16x16x32_bf16 v[12:15], v[192:195], a[152:155], v[12:15]\n"
"ds_read_b64_tr_b16 v[222:223], v240 offset:13832\n"
"s_add_u32 s93, s93, 2048\n"
"v_mfma_f32_16x16x32_bf16 v[12:15], v[196:199], a[156:159], v[12:15]\n"
"buffer_load_dwordx4 a[120:123], v249, s[72:75], s94 offen offset:0 sc1\n"
"v_mfma_f32_16x16x32_bf16 v[16:19], v[200:203], a[128:131], v[16:19]\n"
"ds_read_b64_tr_b16 v[192:193], v239 offset:128\n"
"v_mfma_f32_16x16x32_bf16 v[16:19], v[204:207], a[132:135], v[16:19]\n"
"v_mfma_f32_16x16x32_bf16 v[20:23], v[200:203], a[136:139], v[20:23]\n"
"ds_read_b64_tr_b16 v[194:195], v239 offset:1664\n"
"v_mfma_f32_16x16x32_bf16 v[20:23], v[204:207], a[140:143], v[20:23]\n"
"v_mfma_f32_16x16x32_bf16 v[24:27], v[200:203], a[144:147], v[24:27]\n"
"ds_read_b64_tr_b16 v[196:197], v239 offset:12416\n"
"v_mfma_f32_16x16x32_bf16 v[24:27], v[204:207], a[148:151], v[24:27]\n"
"v_mfma_f32_16x16x32_bf16 v[28:31], v[200:203], a[152:155], v[28:31]\n"
"ds_read_b64_tr_b16 v[198:199], v239 offset:13952\n"
"v_mfma_f32_16x16x32_bf16 v[28:31], v[204:207], a[156:159], v[28:31]\n"
"buffer_load_dwordx4 a[124:127], v249, s[72:75], s94 offen offset:1024 sc1\n"
"s_add_u32 s94, s94, 2048\n"
"v_mfma_f32_16x16x32_bf16 v[32:35], v[208:211], a[128:131], v[32:35]\n"
"ds_read_b64_tr_b16 v[200:201], v239 offset:136\n"
"s_waitcnt lgkmcnt(3)\n"
"v_mfma_f32_16x16x32_bf16 v[32:35], v[212:215], a[132:135], v[32:35]\n"
"v_mfma_f32_16x16x32_bf16 v[36:39], v[208:211], a[136:139], v[36:39]\n"
"ds_read_b64_tr_b16 v[202:203], v239 offset:1672\n"
"v_mfma_f32_16x16x32_bf16 v[36:39], v[212:215], a[140:143], v[36:39]\n"
"v_mfma_f32_16x16x32_bf16 v[40:43], v[208:211], a[144:147], v[40:43]\n"
"ds_read_b64_tr_b16 v[204:205], v239 offset:12424\n"
"v_mfma_f32_16x16x32_bf16 v[40:43], v[212:215], a[148:151], v[40:43]\n"
"v_mfma_f32_16x16x32_bf16 v[44:47], v[208:211], a[152:155], v[44:47]\n"
"ds_read_b64_tr_b16 v[206:207], v239 offset:13960\n"
"s_add_u32 s88, s88, 786432\n"
"s_add_u32 m0, s86, 39936\n"
"v_mfma_f32_16x16x32_bf16 v[44:47], v[212:215], a[156:159], v[44:47]\n"
"buffer_load_dwordx4 v242, s[64:67], s89 offen offset:0 lds\n"
"v_mfma_f32_16x16x32_bf16 v[48:51], v[216:219], a[128:131], v[48:51]\n"
"ds_read_b64_tr_b16 v[208:209], v240 offset:128\n"
"v_mfma_f32_16x16x32_bf16 v[48:51], v[220:223], a[132:135], v[48:51]\n"
"v_mfma_f32_16x16x32_bf16 v[52:55], v[216:219], a[136:139], v[52:55]\n"
"ds_read_b64_tr_b16 v[210:211], v240 offset:1664\n"
"v_mfma_f32_16x16x32_bf16 v[52:55], v[220:223], a[140:143], v[52:55]\n"
"v_mfma_f32_16x16x32_bf16 v[56:59], v[216:219], a[144:147], v[56:59]\n"
"ds_read_b64_tr_b16 v[212:213], v240 offset:12416\n"
"v_mfma_f32_16x16x32_bf16 v[56:59], v[220:223], a[148:151], v[56:59]\n"
"v_mfma_f32_16x16x32_bf16 v[60:63], v[216:219], a[152:155], v[60:63]\n"
"ds_read_b64_tr_b16 v[214:215], v240 offset:13952\n"
"v_mfma_f32_16x16x32_bf16 v[60:63], v[220:223], a[156:159], v[60:63]\n"
"buffer_load_dwordx4 v243, s[64:67], s89 offen offset:1024 lds\n"
"v_mfma_f32_16x16x32_bf16 v[64:67], v[192:195], a[128:131], v[64:67]\n"
"ds_read_b64_tr_b16 v[216:217], v240 offset:136\n"
"s_waitcnt lgkmcnt(3)\n"
"v_mfma_f32_16x16x32_bf16 v[64:67], v[196:199], a[132:135], v[64:67]\n"
"v_mfma_f32_16x16x32_bf16 v[68:71], v[192:195], a[136:139], v[68:71]\n"
"ds_read_b64_tr_b16 v[218:219], v240 offset:1672\n"
"v_mfma_f32_16x16x32_bf16 v[68:71], v[196:199], a[140:143], v[68:71]\n"
"v_mfma_f32_16x16x32_bf16 v[72:75], v[192:195], a[144:147], v[72:75]\n"
"ds_read_b64_tr_b16 v[220:221], v240 offset:12424\n"
"v_mfma_f32_16x16x32_bf16 v[72:75], v[196:199], a[148:151], v[72:75]\n"
"v_mfma_f32_16x16x32_bf16 v[76:79], v[192:195], a[152:155], v[76:79]\n"
"ds_read_b64_tr_b16 v[222:223], v240 offset:13960\n"
"v_mfma_f32_16x16x32_bf16 v[76:79], v[196:199], a[156:159], v[76:79]\n"
"buffer_load_dwordx4 v244, s[64:67], s89 offen offset:2048 lds\n"
"v_mfma_f32_16x16x32_bf16 v[80:83], v[200:203], a[128:131], v[80:83]\n"
"ds_read_b64_tr_b16 v[192:193], v239 offset:256\n"
"v_mfma_f32_16x16x32_bf16 v[80:83], v[204:207], a[132:135], v[80:83]\n"
"v_mfma_f32_16x16x32_bf16 v[84:87], v[200:203], a[136:139], v[84:87]\n"
"ds_read_b64_tr_b16 v[194:195], v239 offset:1792\n"
"v_mfma_f32_16x16x32_bf16 v[84:87], v[204:207], a[140:143], v[84:87]\n"
"v_mfma_f32_16x16x32_bf16 v[88:91], v[200:203], a[144:147], v[88:91]\n"
"ds_read_b64_tr_b16 v[196:197], v239 offset:12544\n"
"v_mfma_f32_16x16x32_bf16 v[88:91], v[204:207], a[148:151], v[88:91]\n"
"v_mfma_f32_16x16x32_bf16 v[92:95], v[200:203], a[152:155], v[92:95]\n"
"ds_read_b64_tr_b16 v[198:199], v239 offset:14080\n"
"s_add_u32 s89, s89, 786432\n"
"s_add_u32 m0, s87, 36864\n"
"v_mfma_f32_16x16x32_bf16 v[92:95], v[204:207], a[156:159], v[92:95]\n"
"buffer_load_dwordx4 v245, s[68:71], s90 offen offset:0 lds\n"
"v_mfma_f32_16x16x32_bf16 v[96:99], v[208:211], a[128:131], v[96:99]\n"
"ds_read_b64_tr_b16 v[200:201], v239 offset:264\n"
"s_waitcnt lgkmcnt(3)\n"
"v_mfma_f32_16x16x32_bf16 v[96:99], v[212:215], a[132:135], v[96:99]\n"
"v_mfma_f32_16x16x32_bf16 v[100:103], v[208:211], a[136:139], v[100:103]\n"
"ds_read_b64_tr_b16 v[202:203], v239 offset:1800\n"
"v_mfma_f32_16x16x32_bf16 v[100:103], v[212:215], a[140:143], v[100:103]\n"
"v_mfma_f32_16x16x32_bf16 v[104:107], v[208:211], a[144:147], v[104:107]\n"
"ds_read_b64_tr_b16 v[204:205], v239 offset:12552\n"
"v_mfma_f32_16x16x32_bf16 v[104:107], v[212:215], a[148:151], v[104:107]\n"
"v_mfma_f32_16x16x32_bf16 v[108:111], v[208:211], a[152:155], v[108:111]\n"
"ds_read_b64_tr_b16 v[206:207], v239 offset:14088\n"
"v_mfma_f32_16x16x32_bf16 v[108:111], v[212:215], a[156:159], v[108:111]\n"
"buffer_load_dwordx4 v246, s[68:71], s90 offen offset:1024 lds\n"
"v_mfma_f32_16x16x32_bf16 v[112:115], v[216:219], a[128:131], v[112:115]\n"
"ds_read_b64_tr_b16 v[208:209], v240 offset:256\n"
"v_mfma_f32_16x16x32_bf16 v[112:115], v[220:223], a[132:135], v[112:115]\n"
"v_mfma_f32_16x16x32_bf16 v[116:119], v[216:219], a[136:139], v[116:119]\n"
"ds_read_b64_tr_b16 v[210:211], v240 offset:1792\n"
"v_mfma_f32_16x16x32_bf16 v[116:119], v[220:223], a[140:143], v[116:119]\n"
"v_mfma_f32_16x16x32_bf16 v[120:123], v[216:219], a[144:147], v[120:123]\n"
"ds_read_b64_tr_b16 v[212:213], v240 offset:12544\n"
"v_mfma_f32_16x16x32_bf16 v[120:123], v[220:223], a[148:151], v[120:123]\n"
"v_mfma_f32_16x16x32_bf16 v[124:127], v[216:219], a[152:155], v[124:127]\n"
"ds_read_b64_tr_b16 v[214:215], v240 offset:14080\n"
"v_mfma_f32_16x16x32_bf16 v[124:127], v[220:223], a[156:159], v[124:127]\n"
"buffer_load_dwordx4 v247, s[68:71], s90 offen offset:2048 lds\n"
"s_add_u32 s90, s90, 196608\n"
"v_mfma_f32_16x16x32_bf16 v[128:131], v[192:195], a[128:131], v[128:131]\n"
"ds_read_b64_tr_b16 v[216:217], v240 offset:264\n"
"s_waitcnt lgkmcnt(3)\n"
"v_mfma_f32_16x16x32_bf16 v[128:131], v[196:199], a[132:135], v[128:131]\n"
"v_mfma_f32_16x16x32_bf16 v[132:135], v[192:195], a[136:139], v[132:135]\n"
"ds_read_b64_tr_b16 v[218:219], v240 offset:1800\n"
"v_mfma_f32_16x16x32_bf16 v[132:135], v[196:199], a[140:143], v[132:135]\n"
"v_mfma_f32_16x16x32_bf16 v[136:139], v[192:195], a[144:147], v[136:139]\n"
"ds_read_b64_tr_b16 v[220:221], v240 offset:12552\n"
"v_mfma_f32_16x16x32_bf16 v[136:139], v[196:199], a[148:151], v[136:139]\n"
"v_mfma_f32_16x16x32_bf16 v[140:143], v[192:195], a[152:155], v[140:143]\n"
"ds_read_b64_tr_b16 v[222:223], v240 offset:14088\n"
"v_mfma_f32_16x16x32_bf16 v[140:143], v[196:199], a[156:159], v[140:143]\n"
"s_barrier\n"
"v_mfma_f32_16x16x32_bf16 v[144:147], v[200:203], a[128:131], v[144:147]\n"
"ds_read_b64_tr_b16 v[192:193], v238 offset:0\n"
"v_mfma_f32_16x16x32_bf16 v[144:147], v[204:207], a[132:135], v[144:147]\n"
"v_subrev_u32 v239, 73728, v239\n"
"v_mfma_f32_16x16x32_bf16 v[148:151], v[200:203], a[136:139], v[148:151]\n"
"ds_read_b64_tr_b16 v[194:195], v238 offset:768\n"
"v_mfma_f32_16x16x32_bf16 v[148:151], v[204:207], a[140:143], v[148:151]\n"
"v_subrev_u32 v240, 73728, v240\n"
"v_mfma_f32_16x16x32_bf16 v[152:155], v[200:203], a[144:147], v[152:155]\n"
"ds_read_b64_tr_b16 v[196:197], v238 offset:3072\n"
"v_mfma_f32_16x16x32_bf16 v[152:155], v[204:207], a[148:151], v[152:155]\n"
"v_subrev_u32 v241, 73728, v241\n"
"v_mfma_f32_16x16x32_bf16 v[156:159], v[200:203], a[152:155], v[156:159]\n"
"ds_read_b64_tr_b16 v[198:199], v238 offset:3840\n"
"s_add_u32 m0, s86, 73728\n"
"v_mfma_f32_16x16x32_bf16 v[156:159], v[204:207], a[156:159], v[156:159]\n"
"buffer_load_dwordx4 v242, s[64:67], s88 offen offset:0 lds\n"
"v_mfma_f32_16x16x32_bf16 v[160:163], v[208:211], a[128:131], v[160:163]\n"
"ds_read_b64_tr_b16 v[200:201], v238 offset:6144\n"
"s_waitcnt lgkmcnt(3)\n"
"v_mfma_f32_16x16x32_bf16 v[160:163], v[212:215], a[132:135], v[160:163]\n"
"v_mfma_f32_16x16x32_bf16 v[164:167], v[208:211], a[136:139], v[164:167]\n"
"ds_read_b64_tr_b16 v[202:203], v238 offset:6912\n"
"v_mfma_f32_16x16x32_bf16 v[164:167], v[212:215], a[140:143], v[164:167]\n"
"v_mfma_f32_16x16x32_bf16 v[168:171], v[208:211], a[144:147], v[168:171]\n"
"ds_read_b64_tr_b16 v[204:205], v238 offset:9216\n"
"v_mfma_f32_16x16x32_bf16 v[168:171], v[212:215], a[148:151], v[168:171]\n"
"v_mfma_f32_16x16x32_bf16 v[172:175], v[208:211], a[152:155], v[172:175]\n"
"ds_read_b64_tr_b16 v[206:207], v238 offset:9984\n"
"v_mfma_f32_16x16x32_bf16 v[172:175], v[212:215], a[156:159], v[172:175]\n"
"buffer_load_dwordx4 v243, s[64:67], s88 offen offset:1024 lds\n"
"v_mfma_f32_16x16x32_bf16 v[176:179], v[216:219], a[128:131], v[176:179]\n"
"ds_read_b64_tr_b16 v[208:209], v238 offset:32\n"
"v_mfma_f32_16x16x32_bf16 v[176:179], v[220:223], a[132:135], v[176:179]\n"
"v_mfma_f32_16x16x32_bf16 v[180:183], v[216:219], a[136:139], v[180:183]\n"
"ds_read_b64_tr_b16 v[210:211], v238 offset:800\n"
"v_mfma_f32_16x16x32_bf16 v[180:183], v[220:223], a[140:143], v[180:183]\n"
"v_mfma_f32_16x16x32_bf16 v[184:187], v[216:219], a[144:147], v[184:187]\n"
"ds_read_b64_tr_b16 v[212:213], v238 offset:3104\n"
"v_mfma_f32_16x16x32_bf16 v[184:187], v[220:223], a[148:151], v[184:187]\n"
"v_mfma_f32_16x16x32_bf16 v[188:191], v[216:219], a[152:155], v[188:191]\n"
"ds_read_b64_tr_b16 v[214:215], v238 offset:3872\n"
"v_mfma_f32_16x16x32_bf16 v[188:191], v[220:223], a[156:159], v[188:191]\n"
"buffer_load_dwordx4 v244, s[64:67], s88 offen offset:2048 lds\n"
"s_waitcnt vmcnt(16)\n"
"v_mfma_f32_16x16x32_bf16 a[160:163], v[192:195], a[96:99], a[160:163]\n"
"ds_read_b64_tr_b16 v[216:217], v238 offset:6176\n"
"s_waitcnt vmcnt(15) lgkmcnt(3)\n"
"v_mfma_f32_16x16x32_bf16 a[160:163], v[196:199], a[100:103], a[160:163]\n"
"s_waitcnt vmcnt(14)\n"
"v_mfma_f32_16x16x32_bf16 a[164:167], v[192:195], a[104:107], a[164:167]\n"
"ds_read_b64_tr_b16 v[218:219], v238 offset:6944\n"
"s_waitcnt vmcnt(13)\n"
"v_mfma_f32_16x16x32_bf16 a[164:167], v[196:199], a[108:111], a[164:167]\n"
"s_waitcnt vmcnt(12)\n"
"v_mfma_f32_16x16x32_bf16 a[168:171], v[192:195], a[112:115], a[168:171]\n"
"ds_read_b64_tr_b16 v[220:221], v238 offset:9248\n"
"s_waitcnt vmcnt(11)\n"
"v_mfma_f32_16x16x32_bf16 a[168:171], v[196:199], a[116:119], a[168:171]\n"
"s_waitcnt vmcnt(10)\n"
"v_mfma_f32_16x16x32_bf16 a[172:175], v[192:195], a[120:123], a[172:175]\n"
"ds_read_b64_tr_b16 v[222:223], v238 offset:10016\n"
"s_waitcnt vmcnt(9)\n"
"v_mfma_f32_16x16x32_bf16 a[172:175], v[196:199], a[124:127], a[172:175]\n"
"buffer_load_dwordx4 a[128:131], v249, s[72:75], s91 offen offset:0 sc1\n"
"v_mfma_f32_16x16x32_bf16 a[208:211], v[200:203], a[96:99], a[208:211]\n"
"ds_read_b64_tr_b16 v[192:193], v238 offset:64\n"
"v_mfma_f32_16x16x32_bf16 a[208:211], v[204:207], a[100:103], a[208:211]\n"
"v_mfma_f32_16x16x32_bf16 a[212:215], v[200:203], a[104:107], a[212:215]\n"
"ds_read_b64_tr_b16 v[194:195], v238 offset:832\n"
"v_mfma_f32_16x16x32_bf16 a[212:215], v[204:207], a[108:111], a[212:215]\n"
"v_mfma_f32_16x16x32_bf16 a[216:219], v[200:203], a[112:115], a[216:219]\n"
"ds_read_b64_tr_b16 v[196:197], v238 offset:3136\n"
"v_mfma_f32_16x16x32_bf16 a[216:219], v[204:207], a[116:119], a[216:219]\n"
"v_mfma_f32_16x16x32_bf16 a[220:223], v[200:203], a[120:123], a[220:223]\n"
"ds_read_b64_tr_b16 v[198:199], v238 offset:3904\n"
"v_mfma_f32_16x16x32_bf16 a[220:223], v[204:207], a[124:127], a[220:223]\n"
"buffer_load_dwordx4 a[132:135], v249, s[72:75], s91 offen offset:1024 sc1\n"
"v_mfma_f32_16x16x32_bf16 a[176:179], v[208:211], a[96:99], a[176:179]\n"
"ds_read_b64_tr_b16 v[200:201], v238 offset:6208\n"
"s_waitcnt lgkmcnt(3)\n"
"v_mfma_f32_16x16x32_bf16 a[176:179], v[212:215], a[100:103], a[176:179]\n"
"v_mfma_f32_16x16x32_bf16 a[180:183], v[208:211], a[104:107], a[180:183]\n"
"ds_read_b64_tr_b16 v[202:203], v238 offset:6976\n"
"v_mfma_f32_16x16x32_bf16 a[180:183], v[212:215], a[108:111], a[180:183]\n"
"v_mfma_f32_16x16x32_bf16 a[184:187], v[208:211], a[112:115], a[184:187]\n"
"ds_read_b64_tr_b16 v[204:205], v238 offset:9280\n"
"v_mfma_f32_16x16x32_bf16 a[184:187], v[212:215], a[116:119], a[184:187]\n"
"v_mfma_f32_16x16x32_bf16 a[188:191], v[208:211], a[120:123], a[188:191]\n"
"ds_read_b64_tr_b16 v[206:207], v238 offset:10048\n"
"s_add_u32 s91, s91, 2048\n"
"v_mfma_f32_16x16x32_bf16 a[188:191], v[212:215], a[124:127], a[188:191]\n"
"buffer_load_dwordx4 a[136:139], v249, s[72:75], s92 offen offset:0 sc1\n"
"v_mfma_f32_16x16x32_bf16 a[224:227], v[216:219], a[96:99], a[224:227]\n"
"ds_read_b64_tr_b16 v[208:209], v236 offset:0\n"
"v_mfma_f32_16x16x32_bf16 a[224:227], v[220:223], a[100:103], a[224:227]\n"
"v_mfma_f32_16x16x32_bf16 a[228:231], v[216:219], a[104:107], a[228:231]\n"
"ds_read_b64_tr_b16 v[210:211], v236 offset:1536\n"
"v_mfma_f32_16x16x32_bf16 a[228:231], v[220:223], a[108:111], a[228:231]\n"
"v_mfma_f32_16x16x32_bf16 a[232:235], v[216:219], a[112:115], a[232:235]\n"
"ds_read_b64_tr_b16 v[212:213], v236 offset:12288\n"
"v_mfma_f32_16x16x32_bf16 a[232:235], v[220:223], a[116:119], a[232:235]\n"
"v_mfma_f32_16x16x32_bf16 a[236:239], v[216:219], a[120:123], a[236:239]\n"
"ds_read_b64_tr_b16 v[214:215], v236 offset:13824\n"
"v_mfma_f32_16x16x32_bf16 a[236:239], v[220:223], a[124:127], a[236:239]\n"
"buffer_load_dwordx4 a[140:143], v249, s[72:75], s92 offen offset:1024 sc1\n"
"v_mfma_f32_16x16x32_bf16 a[192:195], v[192:195], a[96:99], a[192:195]\n"
"ds_read_b64_tr_b16 v[216:217], v236 offset:8\n"
"s_waitcnt lgkmcnt(3)\n"
"v_mfma_f32_16x16x32_bf16 a[192:195], v[196:199], a[100:103], a[192:195]\n"
"v_mfma_f32_16x16x32_bf16 a[196:199], v[192:195], a[104:107], a[196:199]\n"
"ds_read_b64_tr_b16 v[218:219], v236 offset:1544\n"
"v_mfma_f32_16x16x32_bf16 a[196:199], v[196:199], a[108:111], a[196:199]\n"
"v_mfma_f32_16x16x32_bf16 a[200:203], v[192:195], a[112:115], a[200:203]\n"
"ds_read_b64_tr_b16 v[220:221], v236 offset:12296\n"
"v_mfma_f32_16x16x32_bf16 a[200:203], v[196:199], a[116:119], a[200:203]\n"
"v_mfma_f32_16x16x32_bf16 a[204:207], v[192:195], a[120:123], a[204:207]\n"
"ds_read_b64_tr_b16 v[222:223], v236 offset:13832\n"
"s_add_u32 s92, s92, 2048\n"
"v_mfma_f32_16x16x32_bf16 a[204:207], v[196:199], a[124:127], a[204:207]\n"
"buffer_load_dwordx4 a[144:147], v249, s[72:75], s93 offen offset:0 sc1\n"
"v_mfma_f32_16x16x32_bf16 a[240:243], v[200:203], a[96:99], a[240:243]\n"
"ds_read_b64_tr_b16 v[192:193], v237 offset:0\n"
"v_mfma_f32_16x16x32_bf16 a[240:243], v[204:207], a[100:103], a[240:243]\n"
"v_mfma_f32_16x16x32_bf16 a[244:247], v[200:203], a[104:107], a[244:247]\n"
"ds_read_b64_tr_b16 v[194:195], v237 offset:1536\n"
"v_mfma_f32_16x16x32_bf16 a[244:247], v[204:207], a[108:111], a[244:247]\n"
"v_mfma_f32_16x16x32_bf16 a[248:251], v[200:203], a[112:115], a[248:251]\n"
"ds_read_b64_tr_b16 v[196:197], v237 offset:12288\n"
"v_mfma_f32_16x16x32_bf16 a[248:251], v[204:207], a[116:119], a[248:251]\n"
"v_mfma_f32_16x16x32_bf16 a[252:255], v[200:203], a[120:123], a[252:255]\n"
"ds_read_b64_tr_b16 v[198:199], v237 offset:13824\n"
"v_mfma_f32_16x16x32_bf16 a[252:255], v[204:207], a[124:127], a[252:255]\n"
"buffer_load_dwordx4 a[148:151], v249, s[72:75], s93 offen offset:1024 sc1\n"
"v_mfma_f32_16x16x32_bf16 v[0:3], v[208:211], a[96:99], v[0:3]\n"
"ds_read_b64_tr_b16 v[200:201], v237 offset:8\n"
"s_waitcnt lgkmcnt(3)\n"
"v_mfma_f32_16x16x32_bf16 v[0:3], v[212:215], a[100:103], v[0:3]\n"
"v_mfma_f32_16x16x32_bf16 v[4:7], v[208:211], a[104:107], v[4:7]\n"
"ds_read_b64_tr_b16 v[202:203], v237 offset:1544\n"
"v_mfma_f32_16x16x32_bf16 v[4:7], v[212:215], a[108:111], v[4:7]\n"
"v_mfma_f32_16x16x32_bf16 v[8:11], v[208:211], a[112:115], v[8:11]\n"
"ds_read_b64_tr_b16 v[204:205], v237 offset:12296\n"
"v_mfma_f32_16x16x32_bf16 v[8:11], v[212:215], a[116:119], v[8:11]\n"
"v_mfma_f32_16x16x32_bf16 v[12:15], v[208:211], a[120:123], v[12:15]\n"
"ds_read_b64_tr_b16 v[206:207], v237 offset:13832\n"
"s_add_u32 s93, s93, 2048\n"
"v_mfma_f32_16x16x32_bf16 v[12:15], v[212:215], a[124:127], v[12:15]\n"
"buffer_load_dwordx4 a[152:155], v249, s[72:75], s94 offen offset:0 sc1\n"
"v_mfma_f32_16x16x32_bf16 v[16:19], v[216:219], a[96:99], v[16:19]\n"
"ds_read_b64_tr_b16 v[208:209], v236 offset:128\n"
"v_mfma_f32_16x16x32_bf16 v[16:19], v[220:223], a[100:103], v[16:19]\n"
"v_mfma_f32_16x16x32_bf16 v[20:23], v[216:219], a[104:107], v[20:23]\n"
"ds_read_b64_tr_b16 v[210:211], v236 offset:1664\n"
"v_mfma_f32_16x16x32_bf16 v[20:23], v[220:223], a[108:111], v[20:23]\n"
"v_mfma_f32_16x16x32_bf16 v[24:27], v[216:219], a[112:115], v[24:27]\n"
"ds_read_b64_tr_b16 v[212:213], v236 offset:12416\n"
"v_mfma_f32_16x16x32_bf16 v[24:27], v[220:223], a[116:119], v[24:27]\n"
"v_mfma_f32_16x16x32_bf16 v[28:31], v[216:219], a[120:123], v[28:31]\n"
"ds_read_b64_tr_b16 v[214:215], v236 offset:13952\n"
"v_mfma_f32_16x16x32_bf16 v[28:31], v[220:223], a[124:127], v[28:31]\n"
"buffer_load_dwordx4 a[156:159], v249, s[72:75], s94 offen offset:1024 sc1\n"
"s_add_u32 s94, s94, 2048\n"
"v_mfma_f32_16x16x32_bf16 v[32:35], v[192:195], a[96:99], v[32:35]\n"
"ds_read_b64_tr_b16 v[216:217], v236 offset:136\n"
"s_waitcnt lgkmcnt(3)\n"
"v_mfma_f32_16x16x32_bf16 v[32:35], v[196:199], a[100:103], v[32:35]\n"
"v_mfma_f32_16x16x32_bf16 v[36:39], v[192:195], a[104:107], v[36:39]\n"
"ds_read_b64_tr_b16 v[218:219], v236 offset:1672\n"
"v_mfma_f32_16x16x32_bf16 v[36:39], v[196:199], a[108:111], v[36:39]\n"
"v_mfma_f32_16x16x32_bf16 v[40:43], v[192:195], a[112:115], v[40:43]\n"
"ds_read_b64_tr_b16 v[220:221], v236 offset:12424\n"
"v_mfma_f32_16x16x32_bf16 v[40:43], v[196:199], a[116:119], v[40:43]\n"
"v_mfma_f32_16x16x32_bf16 v[44:47], v[192:195], a[120:123], v[44:47]\n"
"ds_read_b64_tr_b16 v[222:223], v236 offset:13960\n"
"s_add_u32 s88, s88, 786432\n"
"s_add_u32 m0, s86, 76800\n"
"v_mfma_f32_16x16x32_bf16 v[44:47], v[196:199], a[124:127], v[44:47]\n"
"buffer_load_dwordx4 v242, s[64:67], s89 offen offset:0 lds\n"
"v_mfma_f32_16x16x32_bf16 v[48:51], v[200:203], a[96:99], v[48:51]\n"
"ds_read_b64_tr_b16 v[192:193], v237 offset:128\n"
"v_mfma_f32_16x16x32_bf16 v[48:51], v[204:207], a[100:103], v[48:51]\n"
"v_mfma_f32_16x16x32_bf16 v[52:55], v[200:203], a[104:107], v[52:55]\n"
"ds_read_b64_tr_b16 v[194:195], v237 offset:1664\n"
"v_mfma_f32_16x16x32_bf16 v[52:55], v[204:207], a[108:111], v[52:55]\n"
"v_mfma_f32_16x16x32_bf16 v[56:59], v[200:203], a[112:115], v[56:59]\n"
"ds_read_b64_tr_b16 v[196:197], v237 offset:12416\n"
"v_mfma_f32_16x16x32_bf16 v[56:59], v[204:207], a[116:119], v[56:59]\n"
"v_mfma_f32_16x16x32_bf16 v[60:63], v[200:203], a[120:123], v[60:63]\n"
"ds_read_b64_tr_b16 v[198:199], v237 offset:13952\n"
"v_mfma_f32_16x16x32_bf16 v[60:63], v[204:207], a[124:127], v[60:63]\n"
"buffer_load_dwordx4 v243, s[64:67], s89 offen offset:1024 lds\n"
"v_mfma_f32_16x16x32_bf16 v[64:67], v[208:211], a[96:99], v[64:67]\n"
"ds_read_b64_tr_b16 v[200:201], v237 offset:136\n"
"s_waitcnt lgkmcnt(3)\n"
"v_mfma_f32_16x16x32_bf16 v[64:67], v[212:215], a[100:103], v[64:67]\n"
"v_mfma_f32_16x16x32_bf16 v[68:71], v[208:211], a[104:107], v[68:71]\n"
"ds_read_b64_tr_b16 v[202:203], v237 offset:1672\n"
"v_mfma_f32_16x16x32_bf16 v[68:71], v[212:215], a[108:111], v[68:71]\n"
"v_mfma_f32_16x16x32_bf16 v[72:75], v[208:211], a[112:115], v[72:75]\n"
"ds_read_b64_tr_b16 v[204:205], v237 offset:12424\n"
"v_mfma_f32_16x16x32_bf16 v[72:75], v[212:215], a[116:119], v[72:75]\n"
"v_mfma_f32_16x16x32_bf16 v[76:79], v[208:211], a[120:123], v[76:79]\n"
"ds_read_b64_tr_b16 v[206:207], v237 offset:13960\n"
"v_mfma_f32_16x16x32_bf16 v[76:79], v[212:215], a[124:127], v[76:79]\n"
"buffer_load_dwordx4 v244, s[64:67], s89 offen offset:2048 lds\n"
"v_mfma_f32_16x16x32_bf16 v[80:83], v[216:219], a[96:99], v[80:83]\n"
"ds_read_b64_tr_b16 v[208:209], v236 offset:256\n"
"v_mfma_f32_16x16x32_bf16 v[80:83], v[220:223], a[100:103], v[80:83]\n"
"v_mfma_f32_16x16x32_bf16 v[84:87], v[216:219], a[104:107], v[84:87]\n"
"ds_read_b64_tr_b16 v[210:211], v236 offset:1792\n"
"v_mfma_f32_16x16x32_bf16 v[84:87], v[220:223], a[108:111], v[84:87]\n"
"v_mfma_f32_16x16x32_bf16 v[88:91], v[216:219], a[112:115], v[88:91]\n"
"ds_read_b64_tr_b16 v[212:213], v236 offset:12544\n"
"v_mfma_f32_16x16x32_bf16 v[88:91], v[220:223], a[116:119], v[88:91]\n"
"v_mfma_f32_16x16x32_bf16 v[92:95], v[216:219], a[120:123], v[92:95]\n"
"ds_read_b64_tr_b16 v[214:215], v236 offset:14080\n"
"s_add_u32 s89, s89, 786432\n"
"s_add_u32 m0, s87, 73728\n"
"v_mfma_f32_16x16x32_bf16 v[92:95], v[220:223], a[124:127], v[92:95]\n"
"buffer_load_dwordx4 v245, s[68:71], s90 offen offset:0 lds\n"
"v_mfma_f32_16x16x32_bf16 v[96:99], v[192:195], a[96:99], v[96:99]\n"
"ds_read_b64_tr_b16 v[216:217], v236 offset:264\n"
"s_waitcnt lgkmcnt(3)\n"
"v_mfma_f32_16x16x32_bf16 v[96:99], v[196:199], a[100:103], v[96:99]\n"
"v_mfma_f32_16x16x32_bf16 v[100:103], v[192:195], a[104:107], v[100:103]\n"
"ds_read_b64_tr_b16 v[218:219], v236 offset:1800\n"
"v_mfma_f32_16x16x32_bf16 v[100:103], v[196:199], a[108:111], v[100:103]\n"
"v_mfma_f32_16x16x32_bf16 v[104:107], v[192:195], a[112:115], v[104:107]\n"
"ds_read_b64_tr_b16 v[220:221], v236 offset:12552\n"
"v_mfma_f32_16x16x32_bf16 v[104:107], v[196:199], a[116:119], v[104:107]\n"
"v_mfma_f32_16x16x32_bf16 v[108:111], v[192:195], a[120:123], v[108:111]\n"
"ds_read_b64_tr_b16 v[222:223], v236 offset:14088\n"
"v_mfma_f32_16x16x32_bf16 v[108:111], v[196:199], a[124:127], v[108:111]\n"
"buffer_load_dwordx4 v246, s[68:71], s90 offen offset:1024 lds\n"
"v_mfma_f32_16x16x32_bf16 v[112:115], v[200:203], a[96:99], v[112:115]\n"
"ds_read_b64_tr_b16 v[192:193], v237 offset:256\n"
"v_mfma_f32_16x16x32_bf16 v[112:115], v[204:207], a[100:103], v[112:115]\n"
"v_mfma_f32_16x16x32_bf16 v[116:119], v[200:203], a[104:107], v[116:119]\n"
"ds_read_b64_tr_b16 v[194:195], v237 offset:1792\n"
"v_mfma_f32_16x16x32_bf16 v[116:119], v[204:207], a[108:111], v[116:119]\n"
"v_mfma_f32_16x16x32_bf16 v[120:123], v[200:203], a[112:115], v[120:123]\n"
"ds_read_b64_tr_b16 v[196:197], v237 offset:12544\n"
"v_mfma_f32_16x16x32_bf16 v[120:123], v[204:207], a[116:119], v[120:123]\n"
"v_mfma_f32_16x16x32_bf16 v[124:127], v[200:203], a[120:123], v[124:127]\n"
"ds_read_b64_tr_b16 v[198:199], v237 offset:14080\n"
"v_mfma_f32_16x16x32_bf16 v[124:127], v[204:207], a[124:127], v[124:127]\n"
"buffer_load_dwordx4 v247, s[68:71], s90 offen offset:2048 lds\n"
"s_add_u32 s90, s90, 196608\n"
"v_mfma_f32_16x16x32_bf16 v[128:131], v[208:211], a[96:99], v[128:131]\n"
"ds_read_b64_tr_b16 v[200:201], v237 offset:264\n"
"s_waitcnt lgkmcnt(3)\n"
"v_mfma_f32_16x16x32_bf16 v[128:131], v[212:215], a[100:103], v[128:131]\n"
"v_mfma_f32_16x16x32_bf16 v[132:135], v[208:211], a[104:107], v[132:135]\n"
"ds_read_b64_tr_b16 v[202:203], v237 offset:1800\n"
"v_mfma_f32_16x16x32_bf16 v[132:135], v[212:215], a[108:111], v[132:135]\n"
"v_mfma_f32_16x16x32_bf16 v[136:139], v[208:211], a[112:115], v[136:139]\n"
"ds_read_b64_tr_b16 v[204:205], v237 offset:12552\n"
"v_mfma_f32_16x16x32_bf16 v[136:139], v[212:215], a[116:119], v[136:139]\n"
"v_mfma_f32_16x16x32_bf16 v[140:143], v[208:211], a[120:123], v[140:143]\n"
"ds_read_b64_tr_b16 v[206:207], v237 offset:14088\n"
"v_mfma_f32_16x16x32_bf16 v[140:143], v[212:215], a[124:127], v[140:143]\n"
"s_waitcnt vmcnt(17)\n"
"s_barrier\n"
"v_mfma_f32_16x16x32_bf16 v[144:147], v[216:219], a[96:99], v[144:147]\n"
"ds_read_b64_tr_b16 v[208:209], v241 offset:0\n"
"v_mfma_f32_16x16x32_bf16 v[144:147], v[220:223], a[100:103], v[144:147]\n"
"v_add_u32 v236, 73728, v236\n"
"v_mfma_f32_16x16x32_bf16 v[148:151], v[216:219], a[104:107], v[148:151]\n"
"ds_read_b64_tr_b16 v[210:211], v241 offset:768\n"
"v_mfma_f32_16x16x32_bf16 v[148:151], v[220:223], a[108:111], v[148:151]\n"
"v_add_u32 v237, 73728, v237\n"
"v_mfma_f32_16x16x32_bf16 v[152:155], v[216:219], a[112:115], v[152:155]\n"
"ds_read_b64_tr_b16 v[212:213], v241 offset:3072\n"
"v_mfma_f32_16x16x32_bf16 v[152:155], v[220:223], a[116:119], v[152:155]\n"
"v_add_u32 v238, 73728, v238\n"
"v_mfma_f32_16x16x32_bf16 v[156:159], v[216:219], a[120:123], v[156:159]\n"
"ds_read_b64_tr_b16 v[214:215], v241 offset:3840\n"
"s_add_u32 m0, s86, 110592\n"
"v_mfma_f32_16x16x32_bf16 v[156:159], v[220:223], a[124:127], v[156:159]\n"
"buffer_load_dwordx4 v242, s[64:67], s88 offen offset:0 lds\n"
"v_mfma_f32_16x16x32_bf16 v[160:163], v[192:195], a[96:99], v[160:163]\n"
"ds_read_b64_tr_b16 v[216:217], v241 offset:6144\n"
"s_waitcnt lgkmcnt(3)\n"
"v_mfma_f32_16x16x32_bf16 v[160:163], v[196:199], a[100:103], v[160:163]\n"
"v_mfma_f32_16x16x32_bf16 v[164:167], v[192:195], a[104:107], v[164:167]\n"
"ds_read_b64_tr_b16 v[218:219], v241 offset:6912\n"
"v_mfma_f32_16x16x32_bf16 v[164:167], v[196:199], a[108:111], v[164:167]\n"
"v_mfma_f32_16x16x32_bf16 v[168:171], v[192:195], a[112:115], v[168:171]\n"
"ds_read_b64_tr_b16 v[220:221], v241 offset:9216\n"
"v_mfma_f32_16x16x32_bf16 v[168:171], v[196:199], a[116:119], v[168:171]\n"
"v_mfma_f32_16x16x32_bf16 v[172:175], v[192:195], a[120:123], v[172:175]\n"
"ds_read_b64_tr_b16 v[222:223], v241 offset:9984\n"
"v_mfma_f32_16x16x32_bf16 v[172:175], v[196:199], a[124:127], v[172:175]\n"
"buffer_load_dwordx4 v243, s[64:67], s88 offen offset:1024 lds\n"
"v_mfma_f32_16x16x32_bf16 v[176:179], v[200:203], a[96:99], v[176:179]\n"
"ds_read_b64_tr_b16 v[192:193], v241 offset:32\n"
"v_mfma_f32_16x16x32_bf16 v[176:179], v[204:207], a[100:103], v[176:179]\n"
"v_mfma_f32_16x16x32_bf16 v[180:183], v[200:203], a[104:107], v[180:183]\n"
"ds_read_b64_tr_b16 v[194:195], v241 offset:800\n"
"v_mfma_f32_16x16x32_bf16 v[180:183], v[204:207], a[108:111], v[180:183]\n"
"v_mfma_f32_16x16x32_bf16 v[184:187], v[200:203], a[112:115], v[184:187]\n"
"ds_read_b64_tr_b16 v[196:197], v241 offset:3104\n"
"v_mfma_f32_16x16x32_bf16 v[184:187], v[204:207], a[116:119], v[184:187]\n"
"v_mfma_f32_16x16x32_bf16 v[188:191], v[200:203], a[120:123], v[188:191]\n"
"ds_read_b64_tr_b16 v[198:199], v241 offset:3872\n"
"v_mfma_f32_16x16x32_bf16 v[188:191], v[204:207], a[124:127], v[188:191]\n"
"buffer_load_dwordx4 v244, s[64:67], s88 offen offset:2048 lds\n"
"s_waitcnt vmcnt(16)\n"
"v_mfma_f32_16x16x32_bf16 a[160:163], v[208:211], a[128:131], a[160:163]\n"
"ds_read_b64_tr_b16 v[200:201], v241 offset:6176\n"
"s_waitcnt vmcnt(15) lgkmcnt(3)\n"
"v_mfma_f32_16x16x32_bf16 a[160:163], v[212:215], a[132:135], a[160:163]\n"
"s_waitcnt vmcnt(14)\n"
"v_mfma_f32_16x16x32_bf16 a[164:167], v[208:211], a[136:139], a[164:167]\n"
"ds_read_b64_tr_b16 v[202:203], v241 offset:6944\n"
"s_waitcnt vmcnt(13)\n"
"v_mfma_f32_16x16x32_bf16 a[164:167], v[212:215], a[140:143], a[164:167]\n"
"s_waitcnt vmcnt(12)\n"
"v_mfma_f32_16x16x32_bf16 a[168:171], v[208:211], a[144:147], a[168:171]\n"
"ds_read_b64_tr_b16 v[204:205], v241 offset:9248\n"
"s_waitcnt vmcnt(11)\n"
"v_mfma_f32_16x16x32_bf16 a[168:171], v[212:215], a[148:151], a[168:171]\n"
"s_waitcnt vmcnt(10)\n"
"v_mfma_f32_16x16x32_bf16 a[172:175], v[208:211], a[152:155], a[172:175]\n"
"ds_read_b64_tr_b16 v[206:207], v241 offset:10016\n"
"s_waitcnt vmcnt(9)\n"
"v_mfma_f32_16x16x32_bf16 a[172:175], v[212:215], a[156:159], a[172:175]\n"
"buffer_load_dwordx4 a[96:99], v249, s[72:75], s91 offen offset:0 sc1\n"
"v_mfma_f32_16x16x32_bf16 a[208:211], v[216:219], a[128:131], a[208:211]\n"
"ds_read_b64_tr_b16 v[208:209], v241 offset:64\n"
"v_mfma_f32_16x16x32_bf16 a[208:211], v[220:223], a[132:135], a[208:211]\n"
"v_mfma_f32_16x16x32_bf16 a[212:215], v[216:219], a[136:139], a[212:215]\n"
"ds_read_b64_tr_b16 v[210:211], v241 offset:832\n"
"v_mfma_f32_16x16x32_bf16 a[212:215], v[220:223], a[140:143], a[212:215]\n"
"v_mfma_f32_16x16x32_bf16 a[216:219], v[216:219], a[144:147], a[216:219]\n"
"ds_read_b64_tr_b16 v[212:213], v241 offset:3136\n"
"v_mfma_f32_16x16x32_bf16 a[216:219], v[220:223], a[148:151], a[216:219]\n"
"v_mfma_f32_16x16x32_bf16 a[220:223], v[216:219], a[152:155], a[220:223]\n"
"ds_read_b64_tr_b16 v[214:215], v241 offset:3904\n"
"v_mfma_f32_16x16x32_bf16 a[220:223], v[220:223], a[156:159], a[220:223]\n"
"buffer_load_dwordx4 a[100:103], v249, s[72:75], s91 offen offset:1024 sc1\n"
"v_mfma_f32_16x16x32_bf16 a[176:179], v[192:195], a[128:131], a[176:179]\n"
"ds_read_b64_tr_b16 v[216:217], v241 offset:6208\n"
"s_waitcnt lgkmcnt(3)\n"
"v_mfma_f32_16x16x32_bf16 a[176:179], v[196:199], a[132:135], a[176:179]\n"
"v_mfma_f32_16x16x32_bf16 a[180:183], v[192:195], a[136:139], a[180:183]\n"
"ds_read_b64_tr_b16 v[218:219], v241 offset:6976\n"
"v_mfma_f32_16x16x32_bf16 a[180:183], v[196:199], a[140:143], a[180:183]\n"
"v_mfma_f32_16x16x32_bf16 a[184:187], v[192:195], a[144:147], a[184:187]\n"
"ds_read_b64_tr_b16 v[220:221], v241 offset:9280\n"
"v_mfma_f32_16x16x32_bf16 a[184:187], v[196:199], a[148:151], a[184:187]\n"
"v_mfma_f32_16x16x32_bf16 a[188:191], v[192:195], a[152:155], a[188:191]\n"
"ds_read_b64_tr_b16 v[222:223], v241 offset:10048\n"
"s_add_u32 s91, s91, 2048\n"
"v_mfma_f32_16x16x32_bf16 a[188:191], v[196:199], a[156:159], a[188:191]\n"
"buffer_load_dwordx4 a[104:107], v249, s[72:75], s92 offen offset:0 sc1\n"
"v_mfma_f32_16x16x32_bf16 a[224:227], v[200:203], a[128:131], a[224:227]\n"
"ds_read_b64_tr_b16 v[192:193], v239 offset:0\n"
"v_mfma_f32_16x16x32_bf16 a[224:227], v[204:207], a[132:135], a[224:227]\n"
"v_mfma_f32_16x16x32_bf16 a[228:231], v[200:203], a[136:139], a[228:231]\n"
"ds_read_b64_tr_b16 v[194:195], v239 offset:1536\n"
"v_mfma_f32_16x16x32_bf16 a[228:231], v[204:207], a[140:143], a[228:231]\n"
"v_mfma_f32_16x16x32_bf16 a[232:235], v[200:203], a[144:147], a[232:235]\n"
"ds_read_b64_tr_b16 v[196:197], v239 offset:12288\n"
"v_mfma_f32_16x16x32_bf16 a[232:235], v[204:207], a[148:151], a[232:235]\n"
"v_mfma_f32_16x16x32_bf16 a[236:239], v[200:203], a[152:155], a[236:239]\n"
"ds_read_b64_tr_b16 v[198:199], v239 offset:13824\n"
"v_mfma_f32_16x16x32_bf16 a[236:239], v[204:207], a[156:159], a[236:239]\n"
"buffer_load_dwordx4 a[108:111], v249, s[72:75], s92 offen offset:1024 sc1\n"
"v_mfma_f32_16x16x32_bf16 a[192:195], v[208:211], a[128:131], a[192:195]\n"
"ds_read_b64_tr_b16 v[200:201], v239 offset:8\n"
"s_waitcnt lgkmcnt(3)\n"
"v_mfma_f32_16x16x32_bf16 a[192:195], v[212:215], a[132:135], a[192:195]\n"
"v_mfma_f32_16x16x32_bf16 a[196:199], v[208:211], a[136:139], a[196:199]\n"
"ds_read_b64_tr_b16 v[202:203], v239 offset:1544\n"
"v_mfma_f32_16x16x32_bf16 a[196:199], v[212:215], a[140:143], a[196:199]\n"
"v_mfma_f32_16x16x32_bf16 a[200:203], v[208:211], a[144:147], a[200:203]\n"
"ds_read_b64_tr_b16 v[204:205], v239 offset:12296\n"
"v_mfma_f32_16x16x32_bf16 a[200:203], v[212:215], a[148:151], a[200:203]\n"
"v_mfma_f32_16x16x32_bf16 a[204:207], v[208:211], a[152:155], a[204:207]\n"
"ds_read_b64_tr_b16 v[206:207], v239 offset:13832\n"
"s_add_u32 s92, s92, 2048\n"
"v_mfma_f32_16x16x32_bf16 a[204:207], v[212:215], a[156:159], a[204:207]\n"
"buffer_load_dwordx4 a[112:115], v249, s[72:75], s93 offen offset:0 sc1\n"
"v_mfma_f32_16x16x32_bf16 a[240:243], v[216:219], a[128:131], a[240:243]\n"
"ds_read_b64_tr_b16 v[208:209], v240 offset:0\n"
"v_mfma_f32_16x16x32_bf16 a[240:243], v[220:223], a[132:135], a[240:243]\n"
"v_mfma_f32_16x16x32_bf16 a[244:247], v[216:219], a[136:139], a[244:247]\n"
"ds_read_b64_tr_b16 v[210:211], v240 offset:1536\n"
"v_mfma_f32_16x16x32_bf16 a[244:247], v[220:223], a[140:143], a[244:247]\n"
"v_mfma_f32_16x16x32_bf16 a[248:251], v[216:219], a[144:147], a[248:251]\n"
"ds_read_b64_tr_b16 v[212:213], v240 offset:12288\n"
"v_mfma_f32_16x16x32_bf16 a[248:251], v[220:223], a[148:151], a[248:251]\n"
"v_mfma_f32_16x16x32_bf16 a[252:255], v[216:219], a[152:155], a[252:255]\n"
"ds_read_b64_tr_b16 v[214:215], v240 offset:13824\n"
"v_mfma_f32_16x16x32_bf16 a[252:255], v[220:223], a[156:159], a[252:255]\n"
"buffer_load_dwordx4 a[116:119], v249, s[72:75], s93 offen offset:1024 sc1\n"
"v_mfma_f32_16x16x32_bf16 v[0:3], v[192:195], a[128:131], v[0:3]\n"
"ds_read_b64_tr_b16 v[216:217], v240 offset:8\n"
"s_waitcnt lgkmcnt(3)\n"
"v_mfma_f32_16x16x32_bf16 v[0:3], v[196:199], a[132:135], v[0:3]\n"
"v_mfma_f32_16x16x32_bf16 v[4:7], v[192:195], a[136:139], v[4:7]\n"
"ds_read_b64_tr_b16 v[218:219], v240 offset:1544\n"
"v_mfma_f32_16x16x32_bf16 v[4:7], v[196:199], a[140:143], v[4:7]\n"
"v_mfma_f32_16x16x32_bf16 v[8:11], v[192:195], a[144:147], v[8:11]\n"
"ds_read_b64_tr_b16 v[220:221], v240 offset:12296\n"
"v_mfma_f32_16x16x32_bf16 v[8:11], v[196:199], a[148:151], v[8:11]\n"
"v_mfma_f32_16x16x32_bf16 v[12:15], v[192:195], a[152:155], v[12:15]\n"
"ds_read_b64_tr_b16 v[222:223], v240 offset:13832\n"
"s_add_u32 s93, s93, 2048\n"
"v_mfma_f32_16x16x32_bf16 v[12:15], v[196:199], a[156:159], v[12:15]\n"
"buffer_load_dwordx4 a[120:123], v249, s[72:75], s94 offen offset:0 sc1\n"
"v_mfma_f32_16x16x32_bf16 v[16:19], v[200:203], a[128:131], v[16:19]\n"
"ds_read_b64_tr_b16 v[192:193], v239 offset:128\n"
"v_mfma_f32_16x16x32_bf16 v[16:19], v[204:207], a[132:135], v[16:19]\n"
"v_mfma_f32_16x16x32_bf16 v[20:23], v[200:203], a[136:139], v[20:23]\n"
"ds_read_b64_tr_b16 v[194:195], v239 offset:1664\n"
"v_mfma_f32_16x16x32_bf16 v[20:23], v[204:207], a[140:143], v[20:23]\n"
"v_mfma_f32_16x16x32_bf16 v[24:27], v[200:203], a[144:147], v[24:27]\n"
"ds_read_b64_tr_b16 v[196:197], v239 offset:12416\n"
"v_mfma_f32_16x16x32_bf16 v[24:27], v[204:207], a[148:151], v[24:27]\n"
"v_mfma_f32_16x16x32_bf16 v[28:31], v[200:203], a[152:155], v[28:31]\n"
"ds_read_b64_tr_b16 v[198:199], v239 offset:13952\n"
"v_mfma_f32_16x16x32_bf16 v[28:31], v[204:207], a[156:159], v[28:31]\n"
"buffer_load_dwordx4 a[124:127], v249, s[72:75], s94 offen offset:1024 sc1\n"
"s_add_u32 s94, s94, 2048\n"
"v_mfma_f32_16x16x32_bf16 v[32:35], v[208:211], a[128:131], v[32:35]\n"
"ds_read_b64_tr_b16 v[200:201], v239 offset:136\n"
"s_waitcnt lgkmcnt(3)\n"
"v_mfma_f32_16x16x32_bf16 v[32:35], v[212:215], a[132:135], v[32:35]\n"
"v_mfma_f32_16x16x32_bf16 v[36:39], v[208:211], a[136:139], v[36:39]\n"
"ds_read_b64_tr_b16 v[202:203], v239 offset:1672\n"
"v_mfma_f32_16x16x32_bf16 v[36:39], v[212:215], a[140:143], v[36:39]\n"
"v_mfma_f32_16x16x32_bf16 v[40:43], v[208:211], a[144:147], v[40:43]\n"
"ds_read_b64_tr_b16 v[204:205], v239 offset:12424\n"
"v_mfma_f32_16x16x32_bf16 v[40:43], v[212:215], a[148:151], v[40:43]\n"
"v_mfma_f32_16x16x32_bf16 v[44:47], v[208:211], a[152:155], v[44:47]\n"
"ds_read_b64_tr_b16 v[206:207], v239 offset:13960\n"
"s_add_u32 s88, s88, 786432\n"
"s_add_u32 m0, s86, 113664\n"
"v_mfma_f32_16x16x32_bf16 v[44:47], v[212:215], a[156:159], v[44:47]\n"
"buffer_load_dwordx4 v242, s[64:67], s89 offen offset:0 lds\n"
"v_mfma_f32_16x16x32_bf16 v[48:51], v[216:219], a[128:131], v[48:51]\n"
"ds_read_b64_tr_b16 v[208:209], v240 offset:128\n"
"v_mfma_f32_16x16x32_bf16 v[48:51], v[220:223], a[132:135], v[48:51]\n"
"v_mfma_f32_16x16x32_bf16 v[52:55], v[216:219], a[136:139], v[52:55]\n"
"ds_read_b64_tr_b16 v[210:211], v240 offset:1664\n"
"v_mfma_f32_16x16x32_bf16 v[52:55], v[220:223], a[140:143], v[52:55]\n"
"v_mfma_f32_16x16x32_bf16 v[56:59], v[216:219], a[144:147], v[56:59]\n"
"ds_read_b64_tr_b16 v[212:213], v240 offset:12416\n"
"v_mfma_f32_16x16x32_bf16 v[56:59], v[220:223], a[148:151], v[56:59]\n"
"v_mfma_f32_16x16x32_bf16 v[60:63], v[216:219], a[152:155], v[60:63]\n"
"ds_read_b64_tr_b16 v[214:215], v240 offset:13952\n"
"v_mfma_f32_16x16x32_bf16 v[60:63], v[220:223], a[156:159], v[60:63]\n"
"buffer_load_dwordx4 v243, s[64:67], s89 offen offset:1024 lds\n"
"v_mfma_f32_16x16x32_bf16 v[64:67], v[192:195], a[128:131], v[64:67]\n"
"ds_read_b64_tr_b16 v[216:217], v240 offset:136\n"
"s_waitcnt lgkmcnt(3)\n"
"v_mfma_f32_16x16x32_bf16 v[64:67], v[196:199], a[132:135], v[64:67]\n"
"v_mfma_f32_16x16x32_bf16 v[68:71], v[192:195], a[136:139], v[68:71]\n"
"ds_read_b64_tr_b16 v[218:219], v240 offset:1672\n"
"v_mfma_f32_16x16x32_bf16 v[68:71], v[196:199], a[140:143], v[68:71]\n"
"v_mfma_f32_16x16x32_bf16 v[72:75], v[192:195], a[144:147], v[72:75]\n"
"ds_read_b64_tr_b16 v[220:221], v240 offset:12424\n"
"v_mfma_f32_16x16x32_bf16 v[72:75], v[196:199], a[148:151], v[72:75]\n"
"v_mfma_f32_16x16x32_bf16 v[76:79], v[192:195], a[152:155], v[76:79]\n"
"ds_read_b64_tr_b16 v[222:223], v240 offset:13960\n"
"v_mfma_f32_16x16x32_bf16 v[76:79], v[196:199], a[156:159], v[76:79]\n"
"buffer_load_dwordx4 v244, s[64:67], s89 offen offset:2048 lds\n"
"v_mfma_f32_16x16x32_bf16 v[80:83], v[200:203], a[128:131], v[80:83]\n"
"ds_read_b64_tr_b16 v[192:193], v239 offset:256\n"
"v_mfma_f32_16x16x32_bf16 v[80:83], v[204:207], a[132:135], v[80:83]\n"
"v_mfma_f32_16x16x32_bf16 v[84:87], v[200:203], a[136:139], v[84:87]\n"
"ds_read_b64_tr_b16 v[194:195], v239 offset:1792\n"
"v_mfma_f32_16x16x32_bf16 v[84:87], v[204:207], a[140:143], v[84:87]\n"
"v_mfma_f32_16x16x32_bf16 v[88:91], v[200:203], a[144:147], v[88:91]\n"
"ds_read_b64_tr_b16 v[196:197], v239 offset:12544\n"
"v_mfma_f32_16x16x32_bf16 v[88:91], v[204:207], a[148:151], v[88:91]\n"
"v_mfma_f32_16x16x32_bf16 v[92:95], v[200:203], a[152:155], v[92:95]\n"
"ds_read_b64_tr_b16 v[198:199], v239 offset:14080\n"
"s_add_u32 s89, s89, 786432\n"
"s_add_u32 m0, s87, 110592\n"
"v_mfma_f32_16x16x32_bf16 v[92:95], v[204:207], a[156:159], v[92:95]\n"
"buffer_load_dwordx4 v245, s[68:71], s90 offen offset:0 lds\n"
"v_mfma_f32_16x16x32_bf16 v[96:99], v[208:211], a[128:131], v[96:99]\n"
"ds_read_b64_tr_b16 v[200:201], v239 offset:264\n"
"s_waitcnt lgkmcnt(3)\n"
"v_mfma_f32_16x16x32_bf16 v[96:99], v[212:215], a[132:135], v[96:99]\n"
"v_mfma_f32_16x16x32_bf16 v[100:103], v[208:211], a[136:139], v[100:103]\n"
"ds_read_b64_tr_b16 v[202:203], v239 offset:1800\n"
"v_mfma_f32_16x16x32_bf16 v[100:103], v[212:215], a[140:143], v[100:103]\n"
"v_mfma_f32_16x16x32_bf16 v[104:107], v[208:211], a[144:147], v[104:107]\n"
"ds_read_b64_tr_b16 v[204:205], v239 offset:12552\n"
"v_mfma_f32_16x16x32_bf16 v[104:107], v[212:215], a[148:151], v[104:107]\n"
"v_mfma_f32_16x16x32_bf16 v[108:111], v[208:211], a[152:155], v[108:111]\n"
"ds_read_b64_tr_b16 v[206:207], v239 offset:14088\n"
"v_mfma_f32_16x16x32_bf16 v[108:111], v[212:215], a[156:159], v[108:111]\n"
"buffer_load_dwordx4 v246, s[68:71], s90 offen offset:1024 lds\n"
"v_mfma_f32_16x16x32_bf16 v[112:115], v[216:219], a[128:131], v[112:115]\n"
"ds_read_b64_tr_b16 v[208:209], v240 offset:256\n"
"v_mfma_f32_16x16x32_bf16 v[112:115], v[220:223], a[132:135], v[112:115]\n"
"v_mfma_f32_16x16x32_bf16 v[116:119], v[216:219], a[136:139], v[116:119]\n"
"ds_read_b64_tr_b16 v[210:211], v240 offset:1792\n"
"v_mfma_f32_16x16x32_bf16 v[116:119], v[220:223], a[140:143], v[116:119]\n"
"v_mfma_f32_16x16x32_bf16 v[120:123], v[216:219], a[144:147], v[120:123]\n"
"ds_read_b64_tr_b16 v[212:213], v240 offset:12544\n"
"v_mfma_f32_16x16x32_bf16 v[120:123], v[220:223], a[148:151], v[120:123]\n"
"v_mfma_f32_16x16x32_bf16 v[124:127], v[216:219], a[152:155], v[124:127]\n"
"ds_read_b64_tr_b16 v[214:215], v240 offset:14080\n"
"v_mfma_f32_16x16x32_bf16 v[124:127], v[220:223], a[156:159], v[124:127]\n"
"buffer_load_dwordx4 v247, s[68:71], s90 offen offset:2048 lds\n"
"s_add_u32 s90, s90, 196608\n"
"v_mfma_f32_16x16x32_bf16 v[128:131], v[192:195], a[128:131], v[128:131]\n"
"ds_read_b64_tr_b16 v[216:217], v240 offset:264\n"
"s_waitcnt lgkmcnt(3)\n"
"v_mfma_f32_16x16x32_bf16 v[128:131], v[196:199], a[132:135], v[128:131]\n"
"v_mfma_f32_16x16x32_bf16 v[132:135], v[192:195], a[136:139], v[132:135]\n"
"ds_read_b64_tr_b16 v[218:219], v240 offset:1800\n"
"v_mfma_f32_16x16x32_bf16 v[132:135], v[196:199], a[140:143], v[132:135]\n"
"v_mfma_f32_16x16x32_bf16 v[136:139], v[192:195], a[144:147], v[136:139]\n"
"ds_read_b64_tr_b16 v[220:221], v240 offset:12552\n"
"v_mfma_f32_16x16x32_bf16 v[136:139], v[196:199], a[148:151], v[136:139]\n"
"v_mfma_f32_16x16x32_bf16 v[140:143], v[192:195], a[152:155], v[140:143]\n"
"ds_read_b64_tr_b16 v[222:223], v240 offset:14088\n"
"v_mfma_f32_16x16x32_bf16 v[140:143], v[196:199], a[156:159], v[140:143]\n"
"s_waitcnt vmcnt(17)\n"
"s_barrier\n"
"v_mfma_f32_16x16x32_bf16 v[144:147], v[200:203], a[128:131], v[144:147]\n"
"ds_read_b64_tr_b16 v[192:193], v238 offset:0\n"
"v_mfma_f32_16x16x32_bf16 v[144:147], v[204:207], a[132:135], v[144:147]\n"
"v_add_u32 v239, 73728, v239\n"
"v_mfma_f32_16x16x32_bf16 v[148:151], v[200:203], a[136:139], v[148:151]\n"
"ds_read_b64_tr_b16 v[194:195], v238 offset:768\n"
"v_mfma_f32_16x16x32_bf16 v[148:151], v[204:207], a[140:143], v[148:151]\n"
"v_add_u32 v240, 73728, v240\n"
"v_mfma_f32_16x16x32_bf16 v[152:155], v[200:203], a[144:147], v[152:155]\n"
"ds_read_b64_tr_b16 v[196:197], v238 offset:3072\n"
"v_mfma_f32_16x16x32_bf16 v[152:155], v[204:207], a[148:151], v[152:155]\n"
"v_add_u32 v241, 73728, v241\n"
"v_mfma_f32_16x16x32_bf16 v[156:159], v[200:203], a[152:155], v[156:159]\n"
"ds_read_b64_tr_b16 v[198:199], v238 offset:3840\n"
"s_add_u32 m0, s86, 0\n"
"v_mfma_f32_16x16x32_bf16 v[156:159], v[204:207], a[156:159], v[156:159]\n"
"buffer_load_dwordx4 v242, s[64:67], s88 offen offset:0 lds\n"
"v_mfma_f32_16x16x32_bf16 v[160:163], v[208:211], a[128:131], v[160:163]\n"
"ds_read_b64_tr_b16 v[200:201], v238 offset:6144\n"
"s_waitcnt lgkmcnt(3)\n"
"v_mfma_f32_16x16x32_bf16 v[160:163], v[212:215], a[132:135], v[160:163]\n"
"v_mfma_f32_16x16x32_bf16 v[164:167], v[208:211], a[136:139], v[164:167]\n"
"ds_read_b64_tr_b16 v[202:203], v238 offset:6912\n"
"v_mfma_f32_16x16x32_bf16 v[164:167], v[212:215], a[140:143], v[164:167]\n"
"v_mfma_f32_16x16x32_bf16 v[168:171], v[208:211], a[144:147], v[168:171]\n"
"ds_read_b64_tr_b16 v[204:205], v238 offset:9216\n"
"v_mfma_f32_16x16x32_bf16 v[168:171], v[212:215], a[148:151], v[168:171]\n"
"v_mfma_f32_16x16x32_bf16 v[172:175], v[208:211], a[152:155], v[172:175]\n"
"ds_read_b64_tr_b16 v[206:207], v238 offset:9984\n"
"v_mfma_f32_16x16x32_bf16 v[172:175], v[212:215], a[156:159], v[172:175]\n"
"buffer_load_dwordx4 v243, s[64:67], s88 offen offset:1024 lds\n"
"v_mfma_f32_16x16x32_bf16 v[176:179], v[216:219], a[128:131], v[176:179]\n"
"ds_read_b64_tr_b16 v[208:209], v238 offset:32\n"
"v_mfma_f32_16x16x32_bf16 v[176:179], v[220:223], a[132:135], v[176:179]\n"
"v_mfma_f32_16x16x32_bf16 v[180:183], v[216:219], a[136:139], v[180:183]\n"
"ds_read_b64_tr_b16 v[210:211], v238 offset:800\n"
"v_mfma_f32_16x16x32_bf16 v[180:183], v[220:223], a[140:143], v[180:183]\n"
"v_mfma_f32_16x16x32_bf16 v[184:187], v[216:219], a[144:147], v[184:187]\n"
"ds_read_b64_tr_b16 v[212:213], v238 offset:3104\n"
"v_mfma_f32_16x16x32_bf16 v[184:187], v[220:223], a[148:151], v[184:187]\n"
"v_mfma_f32_16x16x32_bf16 v[188:191], v[216:219], a[152:155], v[188:191]\n"
"ds_read_b64_tr_b16 v[214:215], v238 offset:3872\n"
"v_mfma_f32_16x16x32_bf16 v[188:191], v[220:223], a[156:159], v[188:191]\n"
"buffer_load_dwordx4 v244, s[64:67], s88 offen offset:2048 lds\n"
"s_waitcnt vmcnt(16)\n"
"v_mfma_f32_16x16x32_bf16 a[160:163], v[192:195], a[96:99], a[160:163]\n"
"ds_read_b64_tr_b16 v[216:217], v238 offset:6176\n"
"s_waitcnt vmcnt(15) lgkmcnt(3)\n"
"v_mfma_f32_16x16x32_bf16 a[160:163], v[196:199], a[100:103], a[160:163]\n"
"s_waitcnt vmcnt(14)\n"
"v_mfma_f32_16x16x32_bf16 a[164:167], v[192:195], a[104:107], a[164:167]\n"
"ds_read_b64_tr_b16 v[218:219], v238 offset:6944\n"
"s_waitcnt vmcnt(13)\n"
"v_mfma_f32_16x16x32_bf16 a[164:167], v[196:199], a[108:111], a[164:167]\n"
"s_waitcnt vmcnt(12)\n"
"v_mfma_f32_16x16x32_bf16 a[168:171], v[192:195], a[112:115], a[168:171]\n"
"ds_read_b64_tr_b16 v[220:221], v238 offset:9248\n"
"s_waitcnt vmcnt(11)\n"
"v_mfma_f32_16x16x32_bf16 a[168:171], v[196:199], a[116:119], a[168:171]\n"
"s_waitcnt vmcnt(10)\n"
"v_mfma_f32_16x16x32_bf16 a[172:175], v[192:195], a[120:123], a[172:175]\n"
"ds_read_b64_tr_b16 v[222:223], v238 offset:10016\n"
"s_waitcnt vmcnt(9)\n"
"v_mfma_f32_16x16x32_bf16 a[172:175], v[196:199], a[124:127], a[172:175]\n"
"buffer_load_dwordx4 a[128:131], v249, s[72:75], s91 offen offset:0 sc1\n"
"v_mfma_f32_16x16x32_bf16 a[208:211], v[200:203], a[96:99], a[208:211]\n"
"ds_read_b64_tr_b16 v[192:193], v238 offset:64\n"
"v_mfma_f32_16x16x32_bf16 a[208:211], v[204:207], a[100:103], a[208:211]\n"
"v_mfma_f32_16x16x32_bf16 a[212:215], v[200:203], a[104:107], a[212:215]\n"
"ds_read_b64_tr_b16 v[194:195], v238 offset:832\n"
"v_mfma_f32_16x16x32_bf16 a[212:215], v[204:207], a[108:111], a[212:215]\n"
"v_mfma_f32_16x16x32_bf16 a[216:219], v[200:203], a[112:115], a[216:219]\n"
"ds_read_b64_tr_b16 v[196:197], v238 offset:3136\n"
"v_mfma_f32_16x16x32_bf16 a[216:219], v[204:207], a[116:119], a[216:219]\n"
"v_mfma_f32_16x16x32_bf16 a[220:223], v[200:203], a[120:123], a[220:223]\n"
"ds_read_b64_tr_b16 v[198:199], v238 offset:3904\n"
"v_mfma_f32_16x16x32_bf16 a[220:223], v[204:207], a[124:127], a[220:223]\n"
"buffer_load_dwordx4 a[132:135], v249, s[72:75], s91 offen offset:1024 sc1\n"
"v_mfma_f32_16x16x32_bf16 a[176:179], v[208:211], a[96:99], a[176:179]\n"
"ds_read_b64_tr_b16 v[200:201], v238 offset:6208\n"
"s_waitcnt lgkmcnt(3)\n"
"v_mfma_f32_16x16x32_bf16 a[176:179], v[212:215], a[100:103], a[176:179]\n"
"v_mfma_f32_16x16x32_bf16 a[180:183], v[208:211], a[104:107], a[180:183]\n"
"ds_read_b64_tr_b16 v[202:203], v238 offset:6976\n"
"v_mfma_f32_16x16x32_bf16 a[180:183], v[212:215], a[108:111], a[180:183]\n"
"v_mfma_f32_16x16x32_bf16 a[184:187], v[208:211], a[112:115], a[184:187]\n"
"ds_read_b64_tr_b16 v[204:205], v238 offset:9280\n"
"v_mfma_f32_16x16x32_bf16 a[184:187], v[212:215], a[116:119], a[184:187]\n"
"v_mfma_f32_16x16x32_bf16 a[188:191], v[208:211], a[120:123], a[188:191]\n"
"ds_read_b64_tr_b16 v[206:207], v238 offset:10048\n"
"s_add_u32 s91, s91, 2048\n"
"v_mfma_f32_16x16x32_bf16 a[188:191], v[212:215], a[124:127], a[188:191]\n"
"buffer_load_dwordx4 a[136:139], v249, s[72:75], s92 offen offset:0 sc1\n"
"v_mfma_f32_16x16x32_bf16 a[224:227], v[216:219], a[96:99], a[224:227]\n"
"ds_read_b64_tr_b16 v[208:209], v236 offset:0\n"
"v_mfma_f32_16x16x32_bf16 a[224:227], v[220:223], a[100:103], a[224:227]\n"
"v_mfma_f32_16x16x32_bf16 a[228:231], v[216:219], a[104:107], a[228:231]\n"
"ds_read_b64_tr_b16 v[210:211], v236 offset:1536\n"
"v_mfma_f32_16x16x32_bf16 a[228:231], v[220:223], a[108:111], a[228:231]\n"
"v_mfma_f32_16x16x32_bf16 a[232:235], v[216:219], a[112:115], a[232:235]\n"
"ds_read_b64_tr_b16 v[212:213], v236 offset:12288\n"
"v_mfma_f32_16x16x32_bf16 a[232:235], v[220:223], a[116:119], a[232:235]\n"
"v_mfma_f32_16x16x32_bf16 a[236:239], v[216:219], a[120:123], a[236:239]\n"
"ds_read_b64_tr_b16 v[214:215], v236 offset:13824\n"
"v_mfma_f32_16x16x32_bf16 a[236:239], v[220:223], a[124:127], a[236:239]\n"
"buffer_load_dwordx4 a[140:143], v249, s[72:75], s92 offen offset:1024 sc1\n"
"v_mfma_f32_16x16x32_bf16 a[192:195], v[192:195], a[96:99], a[192:195]\n"
"ds_read_b64_tr_b16 v[216:217], v236 offset:8\n"
"s_waitcnt lgkmcnt(3)\n"
"v_mfma_f32_16x16x32_bf16 a[192:195], v[196:199], a[100:103], a[192:195]\n"
"v_mfma_f32_16x16x32_bf16 a[196:199], v[192:195], a[104:107], a[196:199]\n"
"ds_read_b64_tr_b16 v[218:219], v236 offset:1544\n"
"v_mfma_f32_16x16x32_bf16 a[196:199], v[196:199], a[108:111], a[196:199]\n"
"v_mfma_f32_16x16x32_bf16 a[200:203], v[192:195], a[112:115], a[200:203]\n"
"ds_read_b64_tr_b16 v[220:221], v236 offset:12296\n"
"v_mfma_f32_16x16x32_bf16 a[200:203], v[196:199], a[116:119], a[200:203]\n"
"v_mfma_f32_16x16x32_bf16 a[204:207], v[192:195], a[120:123], a[204:207]\n"
"ds_read_b64_tr_b16 v[222:223], v236 offset:13832\n"
"s_add_u32 s92, s92, 2048\n"
"v_mfma_f32_16x16x32_bf16 a[204:207], v[196:199], a[124:127], a[204:207]\n"
"buffer_load_dwordx4 a[144:147], v249, s[72:75], s93 offen offset:0 sc1\n"
"v_mfma_f32_16x16x32_bf16 a[240:243], v[200:203], a[96:99], a[240:243]\n"
"ds_read_b64_tr_b16 v[192:193], v237 offset:0\n"
"v_mfma_f32_16x16x32_bf16 a[240:243], v[204:207], a[100:103], a[240:243]\n"
"v_mfma_f32_16x16x32_bf16 a[244:247], v[200:203], a[104:107], a[244:247]\n"
"ds_read_b64_tr_b16 v[194:195], v237 offset:1536\n"
"v_mfma_f32_16x16x32_bf16 a[244:247], v[204:207], a[108:111], a[244:247]\n"
"v_mfma_f32_16x16x32_bf16 a[248:251], v[200:203], a[112:115], a[248:251]\n"
"ds_read_b64_tr_b16 v[196:197], v237 offset:12288\n"
"v_mfma_f32_16x16x32_bf16 a[248:251], v[204:207], a[116:119], a[248:251]\n"
"v_mfma_f32_16x16x32_bf16 a[252:255], v[200:203], a[120:123], a[252:255]\n"
"ds_read_b64_tr_b16 v[198:199], v237 offset:13824\n"
"v_mfma_f32_16x16x32_bf16 a[252:255], v[204:207], a[124:127], a[252:255]\n"
"buffer_load_dwordx4 a[148:151], v249, s[72:75], s93 offen offset:1024 sc1\n"
"v_mfma_f32_16x16x32_bf16 v[0:3], v[208:211], a[96:99], v[0:3]\n"
"ds_read_b64_tr_b16 v[200:201], v237 offset:8\n"
"s_waitcnt lgkmcnt(3)\n"
"v_mfma_f32_16x16x32_bf16 v[0:3], v[212:215], a[100:103], v[0:3]\n"
"v_mfma_f32_16x16x32_bf16 v[4:7], v[208:211], a[104:107], v[4:7]\n"
"ds_read_b64_tr_b16 v[202:203], v237 offset:1544\n"
"v_mfma_f32_16x16x32_bf16 v[4:7], v[212:215], a[108:111], v[4:7]\n"
"v_mfma_f32_16x16x32_bf16 v[8:11], v[208:211], a[112:115], v[8:11]\n"
"ds_read_b64_tr_b16 v[204:205], v237 offset:12296\n"
"v_mfma_f32_16x16x32_bf16 v[8:11], v[212:215], a[116:119], v[8:11]\n"
"v_mfma_f32_16x16x32_bf16 v[12:15], v[208:211], a[120:123], v[12:15]\n"
"ds_read_b64_tr_b16 v[206:207], v237 offset:13832\n"
"s_add_u32 s93, s93, 2048\n"
"v_mfma_f32_16x16x32_bf16 v[12:15], v[212:215], a[124:127], v[12:15]\n"
"buffer_load_dwordx4 a[152:155], v249, s[72:75], s94 offen offset:0 sc1\n"
"v_mfma_f32_16x16x32_bf16 v[16:19], v[216:219], a[96:99], v[16:19]\n"
"ds_read_b64_tr_b16 v[208:209], v236 offset:128\n"
"v_mfma_f32_16x16x32_bf16 v[16:19], v[220:223], a[100:103], v[16:19]\n"
"v_mfma_f32_16x16x32_bf16 v[20:23], v[216:219], a[104:107], v[20:23]\n"
"ds_read_b64_tr_b16 v[210:211], v236 offset:1664\n"
"v_mfma_f32_16x16x32_bf16 v[20:23], v[220:223], a[108:111], v[20:23]\n"
"v_mfma_f32_16x16x32_bf16 v[24:27], v[216:219], a[112:115], v[24:27]\n"
"ds_read_b64_tr_b16 v[212:213], v236 offset:12416\n"
"v_mfma_f32_16x16x32_bf16 v[24:27], v[220:223], a[116:119], v[24:27]\n"
"v_mfma_f32_16x16x32_bf16 v[28:31], v[216:219], a[120:123], v[28:31]\n"
"ds_read_b64_tr_b16 v[214:215], v236 offset:13952\n"
"v_mfma_f32_16x16x32_bf16 v[28:31], v[220:223], a[124:127], v[28:31]\n"
"buffer_load_dwordx4 a[156:159], v249, s[72:75], s94 offen offset:1024 sc1\n"
"s_add_u32 s94, s94, 2048\n"
"v_mfma_f32_16x16x32_bf16 v[32:35], v[192:195], a[96:99], v[32:35]\n"
"ds_read_b64_tr_b16 v[216:217], v236 offset:136\n"
"s_waitcnt lgkmcnt(3)\n"
"v_mfma_f32_16x16x32_bf16 v[32:35], v[196:199], a[100:103], v[32:35]\n"
"v_mfma_f32_16x16x32_bf16 v[36:39], v[192:195], a[104:107], v[36:39]\n"
"ds_read_b64_tr_b16 v[218:219], v236 offset:1672\n"
"v_mfma_f32_16x16x32_bf16 v[36:39], v[196:199], a[108:111], v[36:39]\n"
"v_mfma_f32_16x16x32_bf16 v[40:43], v[192:195], a[112:115], v[40:43]\n"
"ds_read_b64_tr_b16 v[220:221], v236 offset:12424\n"
"v_mfma_f32_16x16x32_bf16 v[40:43], v[196:199], a[116:119], v[40:43]\n"
"v_mfma_f32_16x16x32_bf16 v[44:47], v[192:195], a[120:123], v[44:47]\n"
"ds_read_b64_tr_b16 v[222:223], v236 offset:13960\n"
"s_add_u32 s88, s88, 786432\n"
"s_add_u32 m0, s86, 3072\n"
"v_mfma_f32_16x16x32_bf16 v[44:47], v[196:199], a[124:127], v[44:47]\n"
"buffer_load_dwordx4 v242, s[64:67], s89 offen offset:0 lds\n"
"v_mfma_f32_16x16x32_bf16 v[48:51], v[200:203], a[96:99], v[48:51]\n"
"ds_read_b64_tr_b16 v[192:193], v237 offset:128\n"
"v_mfma_f32_16x16x32_bf16 v[48:51], v[204:207], a[100:103], v[48:51]\n"
"v_mfma_f32_16x16x32_bf16 v[52:55], v[200:203], a[104:107], v[52:55]\n"
"ds_read_b64_tr_b16 v[194:195], v237 offset:1664\n"
"v_mfma_f32_16x16x32_bf16 v[52:55], v[204:207], a[108:111], v[52:55]\n"
"v_mfma_f32_16x16x32_bf16 v[56:59], v[200:203], a[112:115], v[56:59]\n"
"ds_read_b64_tr_b16 v[196:197], v237 offset:12416\n"
"v_mfma_f32_16x16x32_bf16 v[56:59], v[204:207], a[116:119], v[56:59]\n"
"v_mfma_f32_16x16x32_bf16 v[60:63], v[200:203], a[120:123], v[60:63]\n"
"ds_read_b64_tr_b16 v[198:199], v237 offset:13952\n"
"v_mfma_f32_16x16x32_bf16 v[60:63], v[204:207], a[124:127], v[60:63]\n"
"buffer_load_dwordx4 v243, s[64:67], s89 offen offset:1024 lds\n"
"v_mfma_f32_16x16x32_bf16 v[64:67], v[208:211], a[96:99], v[64:67]\n"
"ds_read_b64_tr_b16 v[200:201], v237 offset:136\n"
"s_waitcnt lgkmcnt(3)\n"
"v_mfma_f32_16x16x32_bf16 v[64:67], v[212:215], a[100:103], v[64:67]\n"
"v_mfma_f32_16x16x32_bf16 v[68:71], v[208:211], a[104:107], v[68:71]\n"
"ds_read_b64_tr_b16 v[202:203], v237 offset:1672\n"
"v_mfma_f32_16x16x32_bf16 v[68:71], v[212:215], a[108:111], v[68:71]\n"
"v_mfma_f32_16x16x32_bf16 v[72:75], v[208:211], a[112:115], v[72:75]\n"
"ds_read_b64_tr_b16 v[204:205], v237 offset:12424\n"
"v_mfma_f32_16x16x32_bf16 v[72:75], v[212:215], a[116:119], v[72:75]\n"
"v_mfma_f32_16x16x32_bf16 v[76:79], v[208:211], a[120:123], v[76:79]\n"
"ds_read_b64_tr_b16 v[206:207], v237 offset:13960\n"
"v_mfma_f32_16x16x32_bf16 v[76:79], v[212:215], a[124:127], v[76:79]\n"
"buffer_load_dwordx4 v244, s[64:67], s89 offen offset:2048 lds\n"
"v_mfma_f32_16x16x32_bf16 v[80:83], v[216:219], a[96:99], v[80:83]\n"
"ds_read_b64_tr_b16 v[208:209], v236 offset:256\n"
"v_mfma_f32_16x16x32_bf16 v[80:83], v[220:223], a[100:103], v[80:83]\n"
"v_mfma_f32_16x16x32_bf16 v[84:87], v[216:219], a[104:107], v[84:87]\n"
"ds_read_b64_tr_b16 v[210:211], v236 offset:1792\n"
"v_mfma_f32_16x16x32_bf16 v[84:87], v[220:223], a[108:111], v[84:87]\n"
"v_mfma_f32_16x16x32_bf16 v[88:91], v[216:219], a[112:115], v[88:91]\n"
"ds_read_b64_tr_b16 v[212:213], v236 offset:12544\n"
"v_mfma_f32_16x16x32_bf16 v[88:91], v[220:223], a[116:119], v[88:91]\n"
"v_mfma_f32_16x16x32_bf16 v[92:95], v[216:219], a[120:123], v[92:95]\n"
"ds_read_b64_tr_b16 v[214:215], v236 offset:14080\n"
"s_add_u32 s89, s89, 786432\n"
"s_add_u32 m0, s87, 0\n"
"v_mfma_f32_16x16x32_bf16 v[92:95], v[220:223], a[124:127], v[92:95]\n"
"buffer_load_dwordx4 v245, s[68:71], s90 offen offset:0 lds\n"
"v_mfma_f32_16x16x32_bf16 v[96:99], v[192:195], a[96:99], v[96:99]\n"
"ds_read_b64_tr_b16 v[216:217], v236 offset:264\n"
"s_waitcnt lgkmcnt(3)\n"
"v_mfma_f32_16x16x32_bf16 v[96:99], v[196:199], a[100:103], v[96:99]\n"
"v_mfma_f32_16x16x32_bf16 v[100:103], v[192:195], a[104:107], v[100:103]\n"
"ds_read_b64_tr_b16 v[218:219], v236 offset:1800\n"
"v_mfma_f32_16x16x32_bf16 v[100:103], v[196:199], a[108:111], v[100:103]\n"
"v_mfma_f32_16x16x32_bf16 v[104:107], v[192:195], a[112:115], v[104:107]\n"
"ds_read_b64_tr_b16 v[220:221], v236 offset:12552\n"
"v_mfma_f32_16x16x32_bf16 v[104:107], v[196:199], a[116:119], v[104:107]\n"
"v_mfma_f32_16x16x32_bf16 v[108:111], v[192:195], a[120:123], v[108:111]\n"
"ds_read_b64_tr_b16 v[222:223], v236 offset:14088\n"
"v_mfma_f32_16x16x32_bf16 v[108:111], v[196:199], a[124:127], v[108:111]\n"
"buffer_load_dwordx4 v246, s[68:71], s90 offen offset:1024 lds\n"
"v_mfma_f32_16x16x32_bf16 v[112:115], v[200:203], a[96:99], v[112:115]\n"
"ds_read_b64_tr_b16 v[192:193], v237 offset:256\n"
"v_mfma_f32_16x16x32_bf16 v[112:115], v[204:207], a[100:103], v[112:115]\n"
"v_mfma_f32_16x16x32_bf16 v[116:119], v[200:203], a[104:107], v[116:119]\n"
"ds_read_b64_tr_b16 v[194:195], v237 offset:1792\n"
"v_mfma_f32_16x16x32_bf16 v[116:119], v[204:207], a[108:111], v[116:119]\n"
"v_mfma_f32_16x16x32_bf16 v[120:123], v[200:203], a[112:115], v[120:123]\n"
"ds_read_b64_tr_b16 v[196:197], v237 offset:12544\n"
"v_mfma_f32_16x16x32_bf16 v[120:123], v[204:207], a[116:119], v[120:123]\n"
"v_mfma_f32_16x16x32_bf16 v[124:127], v[200:203], a[120:123], v[124:127]\n"
"ds_read_b64_tr_b16 v[198:199], v237 offset:14080\n"
"v_mfma_f32_16x16x32_bf16 v[124:127], v[204:207], a[124:127], v[124:127]\n"
"buffer_load_dwordx4 v247, s[68:71], s90 offen offset:2048 lds\n"
"s_add_u32 s90, s90, 196608\n"
"v_mfma_f32_16x16x32_bf16 v[128:131], v[208:211], a[96:99], v[128:131]\n"
"ds_read_b64_tr_b16 v[200:201], v237 offset:264\n"
"s_waitcnt lgkmcnt(3)\n"
"v_mfma_f32_16x16x32_bf16 v[128:131], v[212:215], a[100:103], v[128:131]\n"
"v_mfma_f32_16x16x32_bf16 v[132:135], v[208:211], a[104:107], v[132:135]\n"
"ds_read_b64_tr_b16 v[202:203], v237 offset:1800\n"
"v_mfma_f32_16x16x32_bf16 v[132:135], v[212:215], a[108:111], v[132:135]\n"
"v_mfma_f32_16x16x32_bf16 v[136:139], v[208:211], a[112:115], v[136:139]\n"
"ds_read_b64_tr_b16 v[204:205], v237 offset:12552\n"
"v_mfma_f32_16x16x32_bf16 v[136:139], v[212:215], a[116:119], v[136:139]\n"
"v_mfma_f32_16x16x32_bf16 v[140:143], v[208:211], a[120:123], v[140:143]\n"
"ds_read_b64_tr_b16 v[206:207], v237 offset:14088\n"
"v_mfma_f32_16x16x32_bf16 v[140:143], v[212:215], a[124:127], v[140:143]\n"
"s_waitcnt vmcnt(17)\n"
"s_barrier\n"
"v_mfma_f32_16x16x32_bf16 v[144:147], v[216:219], a[96:99], v[144:147]\n"
"ds_read_b64_tr_b16 v[208:209], v241 offset:0\n"
"v_mfma_f32_16x16x32_bf16 v[144:147], v[220:223], a[100:103], v[144:147]\n"
"v_subrev_u32 v236, 73728, v236\n"
"v_mfma_f32_16x16x32_bf16 v[148:151], v[216:219], a[104:107], v[148:151]\n"
"ds_read_b64_tr_b16 v[210:211], v241 offset:768\n"
"v_mfma_f32_16x16x32_bf16 v[148:151], v[220:223], a[108:111], v[148:151]\n"
"v_subrev_u32 v237, 73728, v237\n"
"v_mfma_f32_16x16x32_bf16 v[152:155], v[216:219], a[112:115], v[152:155]\n"
"ds_read_b64_tr_b16 v[212:213], v241 offset:3072\n"
"v_mfma_f32_16x16x32_bf16 v[152:155], v[220:223], a[116:119], v[152:155]\n"
"v_subrev_u32 v238, 73728, v238\n"
"v_mfma_f32_16x16x32_bf16 v[156:159], v[216:219], a[120:123], v[156:159]\n"
"ds_read_b64_tr_b16 v[214:215], v241 offset:3840\n"
"s_add_u32 m0, s86, 36864\n"
"v_mfma_f32_16x16x32_bf16 v[156:159], v[220:223], a[124:127], v[156:159]\n"
"buffer_load_dwordx4 v242, s[64:67], s88 offen offset:0 lds\n"
"v_mfma_f32_16x16x32_bf16 v[160:163], v[192:195], a[96:99], v[160:163]\n"
"ds_read_b64_tr_b16 v[216:217], v241 offset:6144\n"
"s_waitcnt lgkmcnt(3)\n"
"v_mfma_f32_16x16x32_bf16 v[160:163], v[196:199], a[100:103], v[160:163]\n"
"v_mfma_f32_16x16x32_bf16 v[164:167], v[192:195], a[104:107], v[164:167]\n"
"ds_read_b64_tr_b16 v[218:219], v241 offset:6912\n"
"v_mfma_f32_16x16x32_bf16 v[164:167], v[196:199], a[108:111], v[164:167]\n"
"v_mfma_f32_16x16x32_bf16 v[168:171], v[192:195], a[112:115], v[168:171]\n"
"ds_read_b64_tr_b16 v[220:221], v241 offset:9216\n"
"v_mfma_f32_16x16x32_bf16 v[168:171], v[196:199], a[116:119], v[168:171]\n"
"v_mfma_f32_16x16x32_bf16 v[172:175], v[192:195], a[120:123], v[172:175]\n"
"ds_read_b64_tr_b16 v[222:223], v241 offset:9984\n"
"v_mfma_f32_16x16x32_bf16 v[172:175], v[196:199], a[124:127], v[172:175]\n"
"buffer_load_dwordx4 v243, s[64:67], s88 offen offset:1024 lds\n"
"v_mfma_f32_16x16x32_bf16 v[176:179], v[200:203], a[96:99], v[176:179]\n"
"ds_read_b64_tr_b16 v[192:193], v241 offset:32\n"
"v_mfma_f32_16x16x32_bf16 v[176:179], v[204:207], a[100:103], v[176:179]\n"
"v_mfma_f32_16x16x32_bf16 v[180:183], v[200:203], a[104:107], v[180:183]\n"
"ds_read_b64_tr_b16 v[194:195], v241 offset:800\n"
"v_mfma_f32_16x16x32_bf16 v[180:183], v[204:207], a[108:111], v[180:183]\n"
"v_mfma_f32_16x16x32_bf16 v[184:187], v[200:203], a[112:115], v[184:187]\n"
"ds_read_b64_tr_b16 v[196:197], v241 offset:3104\n"
"v_mfma_f32_16x16x32_bf16 v[184:187], v[204:207], a[116:119], v[184:187]\n"
"v_mfma_f32_16x16x32_bf16 v[188:191], v[200:203], a[120:123], v[188:191]\n"
"ds_read_b64_tr_b16 v[198:199], v241 offset:3872\n"
"v_mfma_f32_16x16x32_bf16 v[188:191], v[204:207], a[124:127], v[188:191]\n"
"buffer_load_dwordx4 v244, s[64:67], s88 offen offset:2048 lds\n"
"s_waitcnt vmcnt(16)\n"
"v_mfma_f32_16x16x32_bf16 a[160:163], v[208:211], a[128:131], a[160:163]\n"
"ds_read_b64_tr_b16 v[200:201], v241 offset:6176\n"
"s_waitcnt vmcnt(15) lgkmcnt(3)\n"
"v_mfma_f32_16x16x32_bf16 a[160:163], v[212:215], a[132:135], a[160:163]\n"
"s_waitcnt vmcnt(14)\n"
"v_mfma_f32_16x16x32_bf16 a[164:167], v[208:211], a[136:139], a[164:167]\n"
"ds_read_b64_tr_b16 v[202:203], v241 offset:6944\n"
"s_waitcnt vmcnt(13)\n"
"v_mfma_f32_16x16x32_bf16 a[164:167], v[212:215], a[140:143], a[164:167]\n"
"s_waitcnt vmcnt(12)\n"
"v_mfma_f32_16x16x32_bf16 a[168:171], v[208:211], a[144:147], a[168:171]\n"
"ds_read_b64_tr_b16 v[204:205], v241 offset:9248\n"
"s_waitcnt vmcnt(11)\n"
"v_mfma_f32_16x16x32_bf16 a[168:171], v[212:215], a[148:151], a[168:171]\n"
"s_waitcnt vmcnt(10)\n"
"v_mfma_f32_16x16x32_bf16 a[172:175], v[208:211], a[152:155], a[172:175]\n"
"ds_read_b64_tr_b16 v[206:207], v241 offset:10016\n"
"s_waitcnt vmcnt(9)\n"
"v_mfma_f32_16x16x32_bf16 a[172:175], v[212:215], a[156:159], a[172:175]\n"
"buffer_load_dwordx4 a[96:99], v249, s[72:75], s91 offen offset:0 sc1\n"
"v_mfma_f32_16x16x32_bf16 a[208:211], v[216:219], a[128:131], a[208:211]\n"
"ds_read_b64_tr_b16 v[208:209], v241 offset:64\n"
"v_mfma_f32_16x16x32_bf16 a[208:211], v[220:223], a[132:135], a[208:211]\n"
"v_mfma_f32_16x16x32_bf16 a[212:215], v[216:219], a[136:139], a[212:215]\n"
"ds_read_b64_tr_b16 v[210:211], v241 offset:832\n"
"v_mfma_f32_16x16x32_bf16 a[212:215], v[220:223], a[140:143], a[212:215]\n"
"v_mfma_f32_16x16x32_bf16 a[216:219], v[216:219], a[144:147], a[216:219]\n"
"ds_read_b64_tr_b16 v[212:213], v241 offset:3136\n"
"v_mfma_f32_16x16x32_bf16 a[216:219], v[220:223], a[148:151], a[216:219]\n"
"v_mfma_f32_16x16x32_bf16 a[220:223], v[216:219], a[152:155], a[220:223]\n"
"ds_read_b64_tr_b16 v[214:215], v241 offset:3904\n"
"v_mfma_f32_16x16x32_bf16 a[220:223], v[220:223], a[156:159], a[220:223]\n"
"buffer_load_dwordx4 a[100:103], v249, s[72:75], s91 offen offset:1024 sc1\n"
"v_mfma_f32_16x16x32_bf16 a[176:179], v[192:195], a[128:131], a[176:179]\n"
"ds_read_b64_tr_b16 v[216:217], v241 offset:6208\n"
"s_waitcnt lgkmcnt(3)\n"
"v_mfma_f32_16x16x32_bf16 a[176:179], v[196:199], a[132:135], a[176:179]\n"
"v_mfma_f32_16x16x32_bf16 a[180:183], v[192:195], a[136:139], a[180:183]\n"
"ds_read_b64_tr_b16 v[218:219], v241 offset:6976\n"
"v_mfma_f32_16x16x32_bf16 a[180:183], v[196:199], a[140:143], a[180:183]\n"
"v_mfma_f32_16x16x32_bf16 a[184:187], v[192:195], a[144:147], a[184:187]\n"
"ds_read_b64_tr_b16 v[220:221], v241 offset:9280\n"
"v_mfma_f32_16x16x32_bf16 a[184:187], v[196:199], a[148:151], a[184:187]\n"
"v_mfma_f32_16x16x32_bf16 a[188:191], v[192:195], a[152:155], a[188:191]\n"
"ds_read_b64_tr_b16 v[222:223], v241 offset:10048\n"
"s_add_u32 s91, s91, 2048\n"
"v_mfma_f32_16x16x32_bf16 a[188:191], v[196:199], a[156:159], a[188:191]\n"
"buffer_load_dwordx4 a[104:107], v249, s[72:75], s92 offen offset:0 sc1\n"
"v_mfma_f32_16x16x32_bf16 a[224:227], v[200:203], a[128:131], a[224:227]\n"
"ds_read_b64_tr_b16 v[192:193], v239 offset:0\n"
"v_mfma_f32_16x16x32_bf16 a[224:227], v[204:207], a[132:135], a[224:227]\n"
"v_mfma_f32_16x16x32_bf16 a[228:231], v[200:203], a[136:139], a[228:231]\n"
"ds_read_b64_tr_b16 v[194:195], v239 offset:1536\n"
"v_mfma_f32_16x16x32_bf16 a[228:231], v[204:207], a[140:143], a[228:231]\n"
"v_mfma_f32_16x16x32_bf16 a[232:235], v[200:203], a[144:147], a[232:235]\n"
"ds_read_b64_tr_b16 v[196:197], v239 offset:12288\n"
"v_mfma_f32_16x16x32_bf16 a[232:235], v[204:207], a[148:151], a[232:235]\n"
"v_mfma_f32_16x16x32_bf16 a[236:239], v[200:203], a[152:155], a[236:239]\n"
"ds_read_b64_tr_b16 v[198:199], v239 offset:13824\n"
"v_mfma_f32_16x16x32_bf16 a[236:239], v[204:207], a[156:159], a[236:239]\n"
"buffer_load_dwordx4 a[108:111], v249, s[72:75], s92 offen offset:1024 sc1\n"
"v_mfma_f32_16x16x32_bf16 a[192:195], v[208:211], a[128:131], a[192:195]\n"
"ds_read_b64_tr_b16 v[200:201], v239 offset:8\n"
"s_waitcnt lgkmcnt(3)\n"
"v_mfma_f32_16x16x32_bf16 a[192:195], v[212:215], a[132:135], a[192:195]\n"
"v_mfma_f32_16x16x32_bf16 a[196:199], v[208:211], a[136:139], a[196:199]\n"
"ds_read_b64_tr_b16 v[202:203], v239 offset:1544\n"
"v_mfma_f32_16x16x32_bf16 a[196:199], v[212:215], a[140:143], a[196:199]\n"
"v_mfma_f32_16x16x32_bf16 a[200:203], v[208:211], a[144:147], a[200:203]\n"
"ds_read_b64_tr_b16 v[204:205], v239 offset:12296\n"
"v_mfma_f32_16x16x32_bf16 a[200:203], v[212:215], a[148:151], a[200:203]\n"
"v_mfma_f32_16x16x32_bf16 a[204:207], v[208:211], a[152:155], a[204:207]\n"
"ds_read_b64_tr_b16 v[206:207], v239 offset:13832\n"
"s_add_u32 s92, s92, 2048\n"
"v_mfma_f32_16x16x32_bf16 a[204:207], v[212:215], a[156:159], a[204:207]\n"
"buffer_load_dwordx4 a[112:115], v249, s[72:75], s93 offen offset:0 sc1\n"
"v_mfma_f32_16x16x32_bf16 a[240:243], v[216:219], a[128:131], a[240:243]\n"
"ds_read_b64_tr_b16 v[208:209], v240 offset:0\n"
"v_mfma_f32_16x16x32_bf16 a[240:243], v[220:223], a[132:135], a[240:243]\n"
"v_mfma_f32_16x16x32_bf16 a[244:247], v[216:219], a[136:139], a[244:247]\n"
"ds_read_b64_tr_b16 v[210:211], v240 offset:1536\n"
"v_mfma_f32_16x16x32_bf16 a[244:247], v[220:223], a[140:143], a[244:247]\n"
"v_mfma_f32_16x16x32_bf16 a[248:251], v[216:219], a[144:147], a[248:251]\n"
"ds_read_b64_tr_b16 v[212:213], v240 offset:12288\n"
"v_mfma_f32_16x16x32_bf16 a[248:251], v[220:223], a[148:151], a[248:251]\n"
"v_mfma_f32_16x16x32_bf16 a[252:255], v[216:219], a[152:155], a[252:255]\n"
"ds_read_b64_tr_b16 v[214:215], v240 offset:13824\n"
"v_mfma_f32_16x16x32_bf16 a[252:255], v[220:223], a[156:159], a[252:255]\n"
"buffer_load_dwordx4 a[116:119], v249, s[72:75], s93 offen offset:1024 sc1\n"
"v_mfma_f32_16x16x32_bf16 v[0:3], v[192:195], a[128:131], v[0:3]\n"
"ds_read_b64_tr_b16 v[216:217], v240 offset:8\n"
"s_waitcnt lgkmcnt(3)\n"
"v_mfma_f32_16x16x32_bf16 v[0:3], v[196:199], a[132:135], v[0:3]\n"
"v_mfma_f32_16x16x32_bf16 v[4:7], v[192:195], a[136:139], v[4:7]\n"
"ds_read_b64_tr_b16 v[218:219], v240 offset:1544\n"
"v_mfma_f32_16x16x32_bf16 v[4:7], v[196:199], a[140:143], v[4:7]\n"
"v_mfma_f32_16x16x32_bf16 v[8:11], v[192:195], a[144:147], v[8:11]\n"
"ds_read_b64_tr_b16 v[220:221], v240 offset:12296\n"
"v_mfma_f32_16x16x32_bf16 v[8:11], v[196:199], a[148:151], v[8:11]\n"
"v_mfma_f32_16x16x32_bf16 v[12:15], v[192:195], a[152:155], v[12:15]\n"
"ds_read_b64_tr_b16 v[222:223], v240 offset:13832\n"
"s_add_u32 s93, s93, 2048\n"
"v_mfma_f32_16x16x32_bf16 v[12:15], v[196:199], a[156:159], v[12:15]\n"
"buffer_load_dwordx4 a[120:123], v249, s[72:75], s94 offen offset:0 sc1\n"
"v_mfma_f32_16x16x32_bf16 v[16:19], v[200:203], a[128:131], v[16:19]\n"
"ds_read_b64_tr_b16 v[192:193], v239 offset:128\n"
"v_mfma_f32_16x16x32_bf16 v[16:19], v[204:207], a[132:135], v[16:19]\n"
"v_mfma_f32_16x16x32_bf16 v[20:23], v[200:203], a[136:139], v[20:23]\n"
"ds_read_b64_tr_b16 v[194:195], v239 offset:1664\n"
"v_mfma_f32_16x16x32_bf16 v[20:23], v[204:207], a[140:143], v[20:23]\n"
"v_mfma_f32_16x16x32_bf16 v[24:27], v[200:203], a[144:147], v[24:27]\n"
"ds_read_b64_tr_b16 v[196:197], v239 offset:12416\n"
"v_mfma_f32_16x16x32_bf16 v[24:27], v[204:207], a[148:151], v[24:27]\n"
"v_mfma_f32_16x16x32_bf16 v[28:31], v[200:203], a[152:155], v[28:31]\n"
"ds_read_b64_tr_b16 v[198:199], v239 offset:13952\n"
"v_mfma_f32_16x16x32_bf16 v[28:31], v[204:207], a[156:159], v[28:31]\n"
"buffer_load_dwordx4 a[124:127], v249, s[72:75], s94 offen offset:1024 sc1\n"
"s_add_u32 s94, s94, 2048\n"
"v_mfma_f32_16x16x32_bf16 v[32:35], v[208:211], a[128:131], v[32:35]\n"
"ds_read_b64_tr_b16 v[200:201], v239 offset:136\n"
"s_waitcnt lgkmcnt(3)\n"
"v_mfma_f32_16x16x32_bf16 v[32:35], v[212:215], a[132:135], v[32:35]\n"
"v_mfma_f32_16x16x32_bf16 v[36:39], v[208:211], a[136:139], v[36:39]\n"
"ds_read_b64_tr_b16 v[202:203], v239 offset:1672\n"
"v_mfma_f32_16x16x32_bf16 v[36:39], v[212:215], a[140:143], v[36:39]\n"
"v_mfma_f32_16x16x32_bf16 v[40:43], v[208:211], a[144:147], v[40:43]\n"
"ds_read_b64_tr_b16 v[204:205], v239 offset:12424\n"
"v_mfma_f32_16x16x32_bf16 v[40:43], v[212:215], a[148:151], v[40:43]\n"
"v_mfma_f32_16x16x32_bf16 v[44:47], v[208:211], a[152:155], v[44:47]\n"
"ds_read_b64_tr_b16 v[206:207], v239 offset:13960\n"
"s_add_u32 s88, s88, 786432\n"
"s_add_u32 m0, s86, 39936\n"
"v_mfma_f32_16x16x32_bf16 v[44:47], v[212:215], a[156:159], v[44:47]\n"
"buffer_load_dwordx4 v242, s[64:67], s89 offen offset:0 lds\n"
"v_mfma_f32_16x16x32_bf16 v[48:51], v[216:219], a[128:131], v[48:51]\n"
"ds_read_b64_tr_b16 v[208:209], v240 offset:128\n"
"v_mfma_f32_16x16x32_bf16 v[48:51], v[220:223], a[132:135], v[48:51]\n"
"v_mfma_f32_16x16x32_bf16 v[52:55], v[216:219], a[136:139], v[52:55]\n"
"ds_read_b64_tr_b16 v[210:211], v240 offset:1664\n"
"v_mfma_f32_16x16x32_bf16 v[52:55], v[220:223], a[140:143], v[52:55]\n"
"v_mfma_f32_16x16x32_bf16 v[56:59], v[216:219], a[144:147], v[56:59]\n"
"ds_read_b64_tr_b16 v[212:213], v240 offset:12416\n"
"v_mfma_f32_16x16x32_bf16 v[56:59], v[220:223], a[148:151], v[56:59]\n"
"v_mfma_f32_16x16x32_bf16 v[60:63], v[216:219], a[152:155], v[60:63]\n"
"ds_read_b64_tr_b16 v[214:215], v240 offset:13952\n"
"v_mfma_f32_16x16x32_bf16 v[60:63], v[220:223], a[156:159], v[60:63]\n"
"buffer_load_dwordx4 v243, s[64:67], s89 offen offset:1024 lds\n"
"v_mfma_f32_16x16x32_bf16 v[64:67], v[192:195], a[128:131], v[64:67]\n"
"ds_read_b64_tr_b16 v[216:217], v240 offset:136\n"
"s_waitcnt lgkmcnt(3)\n"
"v_mfma_f32_16x16x32_bf16 v[64:67], v[196:199], a[132:135], v[64:67]\n"
"v_mfma_f32_16x16x32_bf16 v[68:71], v[192:195], a[136:139], v[68:71]\n"
"ds_read_b64_tr_b16 v[218:219], v240 offset:1672\n"
"v_mfma_f32_16x16x32_bf16 v[68:71], v[196:199], a[140:143], v[68:71]\n"
"v_mfma_f32_16x16x32_bf16 v[72:75], v[192:195], a[144:147], v[72:75]\n"
"ds_read_b64_tr_b16 v[220:221], v240 offset:12424\n"
"v_mfma_f32_16x16x32_bf16 v[72:75], v[196:199], a[148:151], v[72:75]\n"
"v_mfma_f32_16x16x32_bf16 v[76:79], v[192:195], a[152:155], v[76:79]\n"
"ds_read_b64_tr_b16 v[222:223], v240 offset:13960\n"
"v_mfma_f32_16x16x32_bf16 v[76:79], v[196:199], a[156:159], v[76:79]\n"
"buffer_load_dwordx4 v244, s[64:67], s89 offen offset:2048 lds\n"
"v_mfma_f32_16x16x32_bf16 v[80:83], v[200:203], a[128:131], v[80:83]\n"
"ds_read_b64_tr_b16 v[192:193], v239 offset:256\n"
"v_mfma_f32_16x16x32_bf16 v[80:83], v[204:207], a[132:135], v[80:83]\n"
"v_mfma_f32_16x16x32_bf16 v[84:87], v[200:203], a[136:139], v[84:87]\n"
"ds_read_b64_tr_b16 v[194:195], v239 offset:1792\n"
"v_mfma_f32_16x16x32_bf16 v[84:87], v[204:207], a[140:143], v[84:87]\n"
"v_mfma_f32_16x16x32_bf16 v[88:91], v[200:203], a[144:147], v[88:91]\n"
"ds_read_b64_tr_b16 v[196:197], v239 offset:12544\n"
"v_mfma_f32_16x16x32_bf16 v[88:91], v[204:207], a[148:151], v[88:91]\n"
"v_mfma_f32_16x16x32_bf16 v[92:95], v[200:203], a[152:155], v[92:95]\n"
"ds_read_b64_tr_b16 v[198:199], v239 offset:14080\n"
"s_add_u32 s89, s89, 786432\n"
"s_add_u32 m0, s87, 36864\n"
"v_mfma_f32_16x16x32_bf16 v[92:95], v[204:207], a[156:159], v[92:95]\n"
"buffer_load_dwordx4 v245, s[68:71], s90 offen offset:0 lds\n"
"v_mfma_f32_16x16x32_bf16 v[96:99], v[208:211], a[128:131], v[96:99]\n"
"ds_read_b64_tr_b16 v[200:201], v239 offset:264\n"
"s_waitcnt lgkmcnt(3)\n"
"v_mfma_f32_16x16x32_bf16 v[96:99], v[212:215], a[132:135], v[96:99]\n"
"v_mfma_f32_16x16x32_bf16 v[100:103], v[208:211], a[136:139], v[100:103]\n"
"ds_read_b64_tr_b16 v[202:203], v239 offset:1800\n"
"v_mfma_f32_16x16x32_bf16 v[100:103], v[212:215], a[140:143], v[100:103]\n"
"v_mfma_f32_16x16x32_bf16 v[104:107], v[208:211], a[144:147], v[104:107]\n"
"ds_read_b64_tr_b16 v[204:205], v239 offset:12552\n"
"v_mfma_f32_16x16x32_bf16 v[104:107], v[212:215], a[148:151], v[104:107]\n"
"v_mfma_f32_16x16x32_bf16 v[108:111], v[208:211], a[152:155], v[108:111]\n"
"ds_read_b64_tr_b16 v[206:207], v239 offset:14088\n"
"v_mfma_f32_16x16x32_bf16 v[108:111], v[212:215], a[156:159], v[108:111]\n"
"buffer_load_dwordx4 v246, s[68:71], s90 offen offset:1024 lds\n"
"v_mfma_f32_16x16x32_bf16 v[112:115], v[216:219], a[128:131], v[112:115]\n"
"ds_read_b64_tr_b16 v[208:209], v240 offset:256\n"
"v_mfma_f32_16x16x32_bf16 v[112:115], v[220:223], a[132:135], v[112:115]\n"
"v_mfma_f32_16x16x32_bf16 v[116:119], v[216:219], a[136:139], v[116:119]\n"
"ds_read_b64_tr_b16 v[210:211], v240 offset:1792\n"
"v_mfma_f32_16x16x32_bf16 v[116:119], v[220:223], a[140:143], v[116:119]\n"
"v_mfma_f32_16x16x32_bf16 v[120:123], v[216:219], a[144:147], v[120:123]\n"
"ds_read_b64_tr_b16 v[212:213], v240 offset:12544\n"
"v_mfma_f32_16x16x32_bf16 v[120:123], v[220:223], a[148:151], v[120:123]\n"
"v_mfma_f32_16x16x32_bf16 v[124:127], v[216:219], a[152:155], v[124:127]\n"
"ds_read_b64_tr_b16 v[214:215], v240 offset:14080\n"
"v_mfma_f32_16x16x32_bf16 v[124:127], v[220:223], a[156:159], v[124:127]\n"
"buffer_load_dwordx4 v247, s[68:71], s90 offen offset:2048 lds\n"
"s_add_u32 s90, s90, 196608\n"
"v_mfma_f32_16x16x32_bf16 v[128:131], v[192:195], a[128:131], v[128:131]\n"
"ds_read_b64_tr_b16 v[216:217], v240 offset:264\n"
"s_waitcnt lgkmcnt(3)\n"
"v_mfma_f32_16x16x32_bf16 v[128:131], v[196:199], a[132:135], v[128:131]\n"
"v_mfma_f32_16x16x32_bf16 v[132:135], v[192:195], a[136:139], v[132:135]\n"
"ds_read_b64_tr_b16 v[218:219], v240 offset:1800\n"
"v_mfma_f32_16x16x32_bf16 v[132:135], v[196:199], a[140:143], v[132:135]\n"
"v_mfma_f32_16x16x32_bf16 v[136:139], v[192:195], a[144:147], v[136:139]\n"
"ds_read_b64_tr_b16 v[220:221], v240 offset:12552\n"
"v_mfma_f32_16x16x32_bf16 v[136:139], v[196:199], a[148:151], v[136:139]\n"
"v_mfma_f32_16x16x32_bf16 v[140:143], v[192:195], a[152:155], v[140:143]\n"
"ds_read_b64_tr_b16 v[222:223], v240 offset:14088\n"
"v_mfma_f32_16x16x32_bf16 v[140:143], v[196:199], a[156:159], v[140:143]\n"
"s_waitcnt vmcnt(17)\n"
"s_barrier\n"
"v_mfma_f32_16x16x32_bf16 v[144:147], v[200:203], a[128:131], v[144:147]\n"
"ds_read_b64_tr_b16 v[192:193], v238 offset:0\n"
"v_mfma_f32_16x16x32_bf16 v[144:147], v[204:207], a[132:135], v[144:147]\n"
"v_subrev_u32 v239, 73728, v239\n"
"v_mfma_f32_16x16x32_bf16 v[148:151], v[200:203], a[136:139], v[148:151]\n"
"ds_read_b64_tr_b16 v[194:195], v238 offset:768\n"
"v_mfma_f32_16x16x32_bf16 v[148:151], v[204:207], a[140:143], v[148:151]\n"
"v_subrev_u32 v240, 73728, v240\n"
"v_mfma_f32_16x16x32_bf16 v[152:155], v[200:203], a[144:147], v[152:155]\n"
"ds_read_b64_tr_b16 v[196:197], v238 offset:3072\n"
"v_mfma_f32_16x16x32_bf16 v[152:155], v[204:207], a[148:151], v[152:155]\n"
"v_subrev_u32 v241, 73728, v241\n"
"v_mfma_f32_16x16x32_bf16 v[156:159], v[200:203], a[152:155], v[156:159]\n"
"ds_read_b64_tr_b16 v[198:199], v238 offset:3840\n"
"s_add_u32 m0, s86, 73728\n"
"v_mfma_f32_16x16x32_bf16 v[156:159], v[204:207], a[156:159], v[156:159]\n"
"buffer_load_dwordx4 v242, s[64:67], s88 offen offset:0 lds\n"
"v_mfma_f32_16x16x32_bf16 v[160:163], v[208:211], a[128:131], v[160:163]\n"
"ds_read_b64_tr_b16 v[200:201], v238 offset:6144\n"
"s_waitcnt lgkmcnt(3)\n"
"v_mfma_f32_16x16x32_bf16 v[160:163], v[212:215], a[132:135], v[160:163]\n"
"v_mfma_f32_16x16x32_bf16 v[164:167], v[208:211], a[136:139], v[164:167]\n"
"ds_read_b64_tr_b16 v[202:203], v238 offset:6912\n"
"v_mfma_f32_16x16x32_bf16 v[164:167], v[212:215], a[140:143], v[164:167]\n"
"v_mfma_f32_16x16x32_bf16 v[168:171], v[208:211], a[144:147], v[168:171]\n"
"ds_read_b64_tr_b16 v[204:205], v238 offset:9216\n"
"v_mfma_f32_16x16x32_bf16 v[168:171], v[212:215], a[148:151], v[168:171]\n"
"v_mfma_f32_16x16x32_bf16 v[172:175], v[208:211], a[152:155], v[172:175]\n"
"ds_read_b64_tr_b16 v[206:207], v238 offset:9984\n"
"v_mfma_f32_16x16x32_bf16 v[172:175], v[212:215], a[156:159], v[172:175]\n"
"buffer_load_dwordx4 v243, s[64:67], s88 offen offset:1024 lds\n"
"v_mfma_f32_16x16x32_bf16 v[176:179], v[216:219], a[128:131], v[176:179]\n"
"ds_read_b64_tr_b16 v[208:209], v238 offset:32\n"
"v_mfma_f32_16x16x32_bf16 v[176:179], v[220:223], a[132:135], v[176:179]\n"
"v_mfma_f32_16x16x32_bf16 v[180:183], v[216:219], a[136:139], v[180:183]\n"
"ds_read_b64_tr_b16 v[210:211], v238 offset:800\n"
"v_mfma_f32_16x16x32_bf16 v[180:183], v[220:223], a[140:143], v[180:183]\n"
"v_mfma_f32_16x16x32_bf16 v[184:187], v[216:219], a[144:147], v[184:187]\n"
"ds_read_b64_tr_b16 v[212:213], v238 offset:3104\n"
"v_mfma_f32_16x16x32_bf16 v[184:187], v[220:223], a[148:151], v[184:187]\n"
"v_mfma_f32_16x16x32_bf16 v[188:191], v[216:219], a[152:155], v[188:191]\n"
"ds_read_b64_tr_b16 v[214:215], v238 offset:3872\n"
"v_mfma_f32_16x16x32_bf16 v[188:191], v[220:223], a[156:159], v[188:191]\n"
"buffer_load_dwordx4 v244, s[64:67], s88 offen offset:2048 lds\n"
"s_waitcnt vmcnt(16)\n"
"v_mfma_f32_16x16x32_bf16 a[160:163], v[192:195], a[96:99], a[160:163]\n"
"ds_read_b64_tr_b16 v[216:217], v238 offset:6176\n"
"s_waitcnt vmcnt(15) lgkmcnt(3)\n"
"v_mfma_f32_16x16x32_bf16 a[160:163], v[196:199], a[100:103], a[160:163]\n"
"s_waitcnt vmcnt(14)\n"
"v_mfma_f32_16x16x32_bf16 a[164:167], v[192:195], a[104:107], a[164:167]\n"
"ds_read_b64_tr_b16 v[218:219], v238 offset:6944\n"
"s_waitcnt vmcnt(13)\n"
"v_mfma_f32_16x16x32_bf16 a[164:167], v[196:199], a[108:111], a[164:167]\n"
"s_waitcnt vmcnt(12)\n"
"v_mfma_f32_16x16x32_bf16 a[168:171], v[192:195], a[112:115], a[168:171]\n"
"ds_read_b64_tr_b16 v[220:221], v238 offset:9248\n"
"s_waitcnt vmcnt(11)\n"
"v_mfma_f32_16x16x32_bf16 a[168:171], v[196:199], a[116:119], a[168:171]\n"
"s_waitcnt vmcnt(10)\n"
"v_mfma_f32_16x16x32_bf16 a[172:175], v[192:195], a[120:123], a[172:175]\n"
"ds_read_b64_tr_b16 v[222:223], v238 offset:10016\n"
"s_waitcnt vmcnt(9)\n"
"v_mfma_f32_16x16x32_bf16 a[172:175], v[196:199], a[124:127], a[172:175]\n"
"buffer_load_dwordx4 a[128:131], v249, s[72:75], s91 offen offset:0 sc1\n"
"v_mfma_f32_16x16x32_bf16 a[208:211], v[200:203], a[96:99], a[208:211]\n"
"ds_read_b64_tr_b16 v[192:193], v238 offset:64\n"
"v_mfma_f32_16x16x32_bf16 a[208:211], v[204:207], a[100:103], a[208:211]\n"
"v_mfma_f32_16x16x32_bf16 a[212:215], v[200:203], a[104:107], a[212:215]\n"
"ds_read_b64_tr_b16 v[194:195], v238 offset:832\n"
"v_mfma_f32_16x16x32_bf16 a[212:215], v[204:207], a[108:111], a[212:215]\n"
"v_mfma_f32_16x16x32_bf16 a[216:219], v[200:203], a[112:115], a[216:219]\n"
"ds_read_b64_tr_b16 v[196:197], v238 offset:3136\n"
"v_mfma_f32_16x16x32_bf16 a[216:219], v[204:207], a[116:119], a[216:219]\n"
"v_mfma_f32_16x16x32_bf16 a[220:223], v[200:203], a[120:123], a[220:223]\n"
"ds_read_b64_tr_b16 v[198:199], v238 offset:3904\n"
"v_mfma_f32_16x16x32_bf16 a[220:223], v[204:207], a[124:127], a[220:223]\n"
"buffer_load_dwordx4 a[132:135], v249, s[72:75], s91 offen offset:1024 sc1\n"
"v_mfma_f32_16x16x32_bf16 a[176:179], v[208:211], a[96:99], a[176:179]\n"
"ds_read_b64_tr_b16 v[200:201], v238 offset:6208\n"
"s_waitcnt lgkmcnt(3)\n"
"v_mfma_f32_16x16x32_bf16 a[176:179], v[212:215], a[100:103], a[176:179]\n"
"v_mfma_f32_16x16x32_bf16 a[180:183], v[208:211], a[104:107], a[180:183]\n"
"ds_read_b64_tr_b16 v[202:203], v238 offset:6976\n"
"v_mfma_f32_16x16x32_bf16 a[180:183], v[212:215], a[108:111], a[180:183]\n"
"v_mfma_f32_16x16x32_bf16 a[184:187], v[208:211], a[112:115], a[184:187]\n"
"ds_read_b64_tr_b16 v[204:205], v238 offset:9280\n"
"v_mfma_f32_16x16x32_bf16 a[184:187], v[212:215], a[116:119], a[184:187]\n"
"v_mfma_f32_16x16x32_bf16 a[188:191], v[208:211], a[120:123], a[188:191]\n"
"ds_read_b64_tr_b16 v[206:207], v238 offset:10048\n"
"s_add_u32 s91, s91, 2048\n"
"v_mfma_f32_16x16x32_bf16 a[188:191], v[212:215], a[124:127], a[188:191]\n"
"buffer_load_dwordx4 a[136:139], v249, s[72:75], s92 offen offset:0 sc1\n"
"v_mfma_f32_16x16x32_bf16 a[224:227], v[216:219], a[96:99], a[224:227]\n"
"ds_read_b64_tr_b16 v[208:209], v236 offset:0\n"
"v_mfma_f32_16x16x32_bf16 a[224:227], v[220:223], a[100:103], a[224:227]\n"
"v_mfma_f32_16x16x32_bf16 a[228:231], v[216:219], a[104:107], a[228:231]\n"
"ds_read_b64_tr_b16 v[210:211], v236 offset:1536\n"
"v_mfma_f32_16x16x32_bf16 a[228:231], v[220:223], a[108:111], a[228:231]\n"
"v_mfma_f32_16x16x32_bf16 a[232:235], v[216:219], a[112:115], a[232:235]\n"
"ds_read_b64_tr_b16 v[212:213], v236 offset:12288\n"
"v_mfma_f32_16x16x32_bf16 a[232:235], v[220:223], a[116:119], a[232:235]\n"
"v_mfma_f32_16x16x32_bf16 a[236:239], v[216:219], a[120:123], a[236:239]\n"
"ds_read_b64_tr_b16 v[214:215], v236 offset:13824\n"
"v_mfma_f32_16x16x32_bf16 a[236:239], v[220:223], a[124:127], a[236:239]\n"
"buffer_load_dwordx4 a[140:143], v249, s[72:75], s92 offen offset:1024 sc1\n"
"v_mfma_f32_16x16x32_bf16 a[192:195], v[192:195], a[96:99], a[192:195]\n"
"ds_read_b64_tr_b16 v[216:217], v236 offset:8\n"
"s_waitcnt lgkmcnt(3)\n"
"v_mfma_f32_16x16x32_bf16 a[192:195], v[196:199], a[100:103], a[192:195]\n"
"v_mfma_f32_16x16x32_bf16 a[196:199], v[192:195], a[104:107], a[196:199]\n"
"ds_read_b64_tr_b16 v[218:219], v236 offset:1544\n"
"v_mfma_f32_16x16x32_bf16 a[196:199], v[196:199], a[108:111], a[196:199]\n"
"v_mfma_f32_16x16x32_bf16 a[200:203], v[192:195], a[112:115], a[200:203]\n"
"ds_read_b64_tr_b16 v[220:221], v236 offset:12296\n"
"v_mfma_f32_16x16x32_bf16 a[200:203], v[196:199], a[116:119], a[200:203]\n"
"v_mfma_f32_16x16x32_bf16 a[204:207], v[192:195], a[120:123], a[204:207]\n"
"ds_read_b64_tr_b16 v[222:223], v236 offset:13832\n"
"s_add_u32 s92, s92, 2048\n"
"v_mfma_f32_16x16x32_bf16 a[204:207], v[196:199], a[124:127], a[204:207]\n"
"buffer_load_dwordx4 a[144:147], v249, s[72:75], s93 offen offset:0 sc1\n"
"v_mfma_f32_16x16x32_bf16 a[240:243], v[200:203], a[96:99], a[240:243]\n"
"ds_read_b64_tr_b16 v[192:193], v237 offset:0\n"
"v_mfma_f32_16x16x32_bf16 a[240:243], v[204:207], a[100:103], a[240:243]\n"
"v_mfma_f32_16x16x32_bf16 a[244:247], v[200:203], a[104:107], a[244:247]\n"
"ds_read_b64_tr_b16 v[194:195], v237 offset:1536\n"
"v_mfma_f32_16x16x32_bf16 a[244:247], v[204:207], a[108:111], a[244:247]\n"
"v_mfma_f32_16x16x32_bf16 a[248:251], v[200:203], a[112:115], a[248:251]\n"
"ds_read_b64_tr_b16 v[196:197], v237 offset:12288\n"
"v_mfma_f32_16x16x32_bf16 a[248:251], v[204:207], a[116:119], a[248:251]\n"
"v_mfma_f32_16x16x32_bf16 a[252:255], v[200:203], a[120:123], a[252:255]\n"
"ds_read_b64_tr_b16 v[198:199], v237 offset:13824\n"
"v_mfma_f32_16x16x32_bf16 a[252:255], v[204:207], a[124:127], a[252:255]\n"
"buffer_load_dwordx4 a[148:151], v249, s[72:75], s93 offen offset:1024 sc1\n"
"v_mfma_f32_16x16x32_bf16 v[0:3], v[208:211], a[96:99], v[0:3]\n"
"ds_read_b64_tr_b16 v[200:201], v237 offset:8\n"
"s_waitcnt lgkmcnt(3)\n"
"v_mfma_f32_16x16x32_bf16 v[0:3], v[212:215], a[100:103], v[0:3]\n"
"v_mfma_f32_16x16x32_bf16 v[4:7], v[208:211], a[104:107], v[4:7]\n"
"ds_read_b64_tr_b16 v[202:203], v237 offset:1544\n"
"v_mfma_f32_16x16x32_bf16 v[4:7], v[212:215], a[108:111], v[4:7]\n"
"v_mfma_f32_16x16x32_bf16 v[8:11], v[208:211], a[112:115], v[8:11]\n"
"ds_read_b64_tr_b16 v[204:205], v237 offset:12296\n"
"v_mfma_f32_16x16x32_bf16 v[8:11], v[212:215], a[116:119], v[8:11]\n"
"v_mfma_f32_16x16x32_bf16 v[12:15], v[208:211], a[120:123], v[12:15]\n"
"ds_read_b64_tr_b16 v[206:207], v237 offset:13832\n"
"s_add_u32 s93, s93, 2048\n"
"v_mfma_f32_16x16x32_bf16 v[12:15], v[212:215], a[124:127], v[12:15]\n"
"buffer_load_dwordx4 a[152:155], v249, s[72:75], s94 offen offset:0 sc1\n"
"v_mfma_f32_16x16x32_bf16 v[16:19], v[216:219], a[96:99], v[16:19]\n"
"ds_read_b64_tr_b16 v[208:209], v236 offset:128\n"
"v_mfma_f32_16x16x32_bf16 v[16:19], v[220:223], a[100:103], v[16:19]\n"
"v_mfma_f32_16x16x32_bf16 v[20:23], v[216:219], a[104:107], v[20:23]\n"
"ds_read_b64_tr_b16 v[210:211], v236 offset:1664\n"
"v_mfma_f32_16x16x32_bf16 v[20:23], v[220:223], a[108:111], v[20:23]\n"
"v_mfma_f32_16x16x32_bf16 v[24:27], v[216:219], a[112:115], v[24:27]\n"
"ds_read_b64_tr_b16 v[212:213], v236 offset:12416\n"
"v_mfma_f32_16x16x32_bf16 v[24:27], v[220:223], a[116:119], v[24:27]\n"
"v_mfma_f32_16x16x32_bf16 v[28:31], v[216:219], a[120:123], v[28:31]\n"
"ds_read_b64_tr_b16 v[214:215], v236 offset:13952\n"
"v_mfma_f32_16x16x32_bf16 v[28:31], v[220:223], a[124:127], v[28:31]\n"
"buffer_load_dwordx4 a[156:159], v249, s[72:75], s94 offen offset:1024 sc1\n"
"s_add_u32 s94, s94, 2048\n"
"v_mfma_f32_16x16x32_bf16 v[32:35], v[192:195], a[96:99], v[32:35]\n"
"ds_read_b64_tr_b16 v[216:217], v236 offset:136\n"
"s_waitcnt lgkmcnt(3)\n"
"v_mfma_f32_16x16x32_bf16 v[32:35], v[196:199], a[100:103], v[32:35]\n"
"v_mfma_f32_16x16x32_bf16 v[36:39], v[192:195], a[104:107], v[36:39]\n"
"ds_read_b64_tr_b16 v[218:219], v236 offset:1672\n"
"v_mfma_f32_16x16x32_bf16 v[36:39], v[196:199], a[108:111], v[36:39]\n"
"v_mfma_f32_16x16x32_bf16 v[40:43], v[192:195], a[112:115], v[40:43]\n"
"ds_read_b64_tr_b16 v[220:221], v236 offset:12424\n"
"v_mfma_f32_16x16x32_bf16 v[40:43], v[196:199], a[116:119], v[40:43]\n"
"v_mfma_f32_16x16x32_bf16 v[44:47], v[192:195], a[120:123], v[44:47]\n"
"ds_read_b64_tr_b16 v[222:223], v236 offset:13960\n"
"s_add_u32 s88, s88, 786432\n"
"s_add_u32 m0, s86, 76800\n"
"v_mfma_f32_16x16x32_bf16 v[44:47], v[196:199], a[124:127], v[44:47]\n"
"buffer_load_dwordx4 v242, s[64:67], s89 offen offset:0 lds\n"
"v_mfma_f32_16x16x32_bf16 v[48:51], v[200:203], a[96:99], v[48:51]\n"
"ds_read_b64_tr_b16 v[192:193], v237 offset:128\n"
"v_mfma_f32_16x16x32_bf16 v[48:51], v[204:207], a[100:103], v[48:51]\n"
"v_mfma_f32_16x16x32_bf16 v[52:55], v[200:203], a[104:107], v[52:55]\n"
"ds_read_b64_tr_b16 v[194:195], v237 offset:1664\n"
"v_mfma_f32_16x16x32_bf16 v[52:55], v[204:207], a[108:111], v[52:55]\n"
"v_mfma_f32_16x16x32_bf16 v[56:59], v[200:203], a[112:115], v[56:59]\n"
"ds_read_b64_tr_b16 v[196:197], v237 offset:12416\n"
"v_mfma_f32_16x16x32_bf16 v[56:59], v[204:207], a[116:119], v[56:59]\n"
"v_mfma_f32_16x16x32_bf16 v[60:63], v[200:203], a[120:123], v[60:63]\n"
"ds_read_b64_tr_b16 v[198:199], v237 offset:13952\n"
"v_mfma_f32_16x16x32_bf16 v[60:63], v[204:207], a[124:127], v[60:63]\n"
"buffer_load_dwordx4 v243, s[64:67], s89 offen offset:1024 lds\n"
"v_mfma_f32_16x16x32_bf16 v[64:67], v[208:211], a[96:99], v[64:67]\n"
"ds_read_b64_tr_b16 v[200:201], v237 offset:136\n"
"s_waitcnt lgkmcnt(3)\n"
"v_mfma_f32_16x16x32_bf16 v[64:67], v[212:215], a[100:103], v[64:67]\n"
"v_mfma_f32_16x16x32_bf16 v[68:71], v[208:211], a[104:107], v[68:71]\n"
"ds_read_b64_tr_b16 v[202:203], v237 offset:1672\n"
"v_mfma_f32_16x16x32_bf16 v[68:71], v[212:215], a[108:111], v[68:71]\n"
"v_mfma_f32_16x16x32_bf16 v[72:75], v[208:211], a[112:115], v[72:75]\n"
"ds_read_b64_tr_b16 v[204:205], v237 offset:12424\n"
"v_mfma_f32_16x16x32_bf16 v[72:75], v[212:215], a[116:119], v[72:75]\n"
"v_mfma_f32_16x16x32_bf16 v[76:79], v[208:211], a[120:123], v[76:79]\n"
"ds_read_b64_tr_b16 v[206:207], v237 offset:13960\n"
"v_mfma_f32_16x16x32_bf16 v[76:79], v[212:215], a[124:127], v[76:79]\n"
"buffer_load_dwordx4 v244, s[64:67], s89 offen offset:2048 lds\n"
"v_mfma_f32_16x16x32_bf16 v[80:83], v[216:219], a[96:99], v[80:83]\n"
"ds_read_b64_tr_b16 v[208:209], v236 offset:256\n"
"v_mfma_f32_16x16x32_bf16 v[80:83], v[220:223], a[100:103], v[80:83]\n"
"v_mfma_f32_16x16x32_bf16 v[84:87], v[216:219], a[104:107], v[84:87]\n"
"ds_read_b64_tr_b16 v[210:211], v236 offset:1792\n"
"v_mfma_f32_16x16x32_bf16 v[84:87], v[220:223], a[108:111], v[84:87]\n"
"v_mfma_f32_16x16x32_bf16 v[88:91], v[216:219], a[112:115], v[88:91]\n"
"ds_read_b64_tr_b16 v[212:213], v236 offset:12544\n"
"v_mfma_f32_16x16x32_bf16 v[88:91], v[220:223], a[116:119], v[88:91]\n"
"v_mfma_f32_16x16x32_bf16 v[92:95], v[216:219], a[120:123], v[92:95]\n"
"ds_read_b64_tr_b16 v[214:215], v236 offset:14080\n"
"s_add_u32 s89, s89, 786432\n"
"s_add_u32 m0, s87, 73728\n"
"v_mfma_f32_16x16x32_bf16 v[92:95], v[220:223], a[124:127], v[92:95]\n"
"buffer_load_dwordx4 v245, s[68:71], s90 offen offset:0 lds\n"
"v_mfma_f32_16x16x32_bf16 v[96:99], v[192:195], a[96:99], v[96:99]\n"
"ds_read_b64_tr_b16 v[216:217], v236 offset:264\n"
"s_waitcnt lgkmcnt(3)\n"
"v_mfma_f32_16x16x32_bf16 v[96:99], v[196:199], a[100:103], v[96:99]\n"
"v_mfma_f32_16x16x32_bf16 v[100:103], v[192:195], a[104:107], v[100:103]\n"
"ds_read_b64_tr_b16 v[218:219], v236 offset:1800\n"
"v_mfma_f32_16x16x32_bf16 v[100:103], v[196:199], a[108:111], v[100:103]\n"
"v_mfma_f32_16x16x32_bf16 v[104:107], v[192:195], a[112:115], v[104:107]\n"
"ds_read_b64_tr_b16 v[220:221], v236 offset:12552\n"
"v_mfma_f32_16x16x32_bf16 v[104:107], v[196:199], a[116:119], v[104:107]\n"
"v_mfma_f32_16x16x32_bf16 v[108:111], v[192:195], a[120:123], v[108:111]\n"
"ds_read_b64_tr_b16 v[222:223], v236 offset:14088\n"
"v_mfma_f32_16x16x32_bf16 v[108:111], v[196:199], a[124:127], v[108:111]\n"
"buffer_load_dwordx4 v246, s[68:71], s90 offen offset:1024 lds\n"
"v_mfma_f32_16x16x32_bf16 v[112:115], v[200:203], a[96:99], v[112:115]\n"
"ds_read_b64_tr_b16 v[192:193], v237 offset:256\n"
"v_mfma_f32_16x16x32_bf16 v[112:115], v[204:207], a[100:103], v[112:115]\n"
"v_mfma_f32_16x16x32_bf16 v[116:119], v[200:203], a[104:107], v[116:119]\n"
"ds_read_b64_tr_b16 v[194:195], v237 offset:1792\n"
"v_mfma_f32_16x16x32_bf16 v[116:119], v[204:207], a[108:111], v[116:119]\n"
"v_mfma_f32_16x16x32_bf16 v[120:123], v[200:203], a[112:115], v[120:123]\n"
"ds_read_b64_tr_b16 v[196:197], v237 offset:12544\n"
"v_mfma_f32_16x16x32_bf16 v[120:123], v[204:207], a[116:119], v[120:123]\n"
"v_mfma_f32_16x16x32_bf16 v[124:127], v[200:203], a[120:123], v[124:127]\n"
"ds_read_b64_tr_b16 v[198:199], v237 offset:14080\n"
"v_mfma_f32_16x16x32_bf16 v[124:127], v[204:207], a[124:127], v[124:127]\n"
"buffer_load_dwordx4 v247, s[68:71], s90 offen offset:2048 lds\n"
"s_add_u32 s90, s90, 196608\n"
"v_mfma_f32_16x16x32_bf16 v[128:131], v[208:211], a[96:99], v[128:131]\n"
"ds_read_b64_tr_b16 v[200:201], v237 offset:264\n"
"s_waitcnt lgkmcnt(3)\n"
"v_mfma_f32_16x16x32_bf16 v[128:131], v[212:215], a[100:103], v[128:131]\n"
"v_mfma_f32_16x16x32_bf16 v[132:135], v[208:211], a[104:107], v[132:135]\n"
"ds_read_b64_tr_b16 v[202:203], v237 offset:1800\n"
"v_mfma_f32_16x16x32_bf16 v[132:135], v[212:215], a[108:111], v[132:135]\n"
"v_mfma_f32_16x16x32_bf16 v[136:139], v[208:211], a[112:115], v[136:139]\n"
"ds_read_b64_tr_b16 v[204:205], v237 offset:12552\n"
"v_mfma_f32_16x16x32_bf16 v[136:139], v[212:215], a[116:119], v[136:139]\n"
"v_mfma_f32_16x16x32_bf16 v[140:143], v[208:211], a[120:123], v[140:143]\n"
"ds_read_b64_tr_b16 v[206:207], v237 offset:14088\n"
"v_mfma_f32_16x16x32_bf16 v[140:143], v[212:215], a[124:127], v[140:143]\n"
"s_waitcnt vmcnt(17)\n"
"s_barrier\n"
"v_mfma_f32_16x16x32_bf16 v[144:147], v[216:219], a[96:99], v[144:147]\n"
"ds_read_b64_tr_b16 v[208:209], v241 offset:0\n"
"v_mfma_f32_16x16x32_bf16 v[144:147], v[220:223], a[100:103], v[144:147]\n"
"v_add_u32 v236, 73728, v236\n"
"v_mfma_f32_16x16x32_bf16 v[148:151], v[216:219], a[104:107], v[148:151]\n"
"ds_read_b64_tr_b16 v[210:211], v241 offset:768\n"
"v_mfma_f32_16x16x32_bf16 v[148:151], v[220:223], a[108:111], v[148:151]\n"
"v_add_u32 v237, 73728, v237\n"
"v_mfma_f32_16x16x32_bf16 v[152:155], v[216:219], a[112:115], v[152:155]\n"
"ds_read_b64_tr_b16 v[212:213], v241 offset:3072\n"
"v_mfma_f32_16x16x32_bf16 v[152:155], v[220:223], a[116:119], v[152:155]\n"
"v_add_u32 v238, 73728, v238\n"
"v_mfma_f32_16x16x32_bf16 v[156:159], v[216:219], a[120:123], v[156:159]\n"
"ds_read_b64_tr_b16 v[214:215], v241 offset:3840\n"
"s_add_u32 m0, s86, 110592\n"
"v_mfma_f32_16x16x32_bf16 v[156:159], v[220:223], a[124:127], v[156:159]\n"
"buffer_load_dwordx4 v242, s[64:67], s88 offen offset:0 lds\n"
"v_mfma_f32_16x16x32_bf16 v[160:163], v[192:195], a[96:99], v[160:163]\n"
"ds_read_b64_tr_b16 v[216:217], v241 offset:6144\n"
"s_waitcnt lgkmcnt(3)\n"
"v_mfma_f32_16x16x32_bf16 v[160:163], v[196:199], a[100:103], v[160:163]\n"
"v_mfma_f32_16x16x32_bf16 v[164:167], v[192:195], a[104:107], v[164:167]\n"
"ds_read_b64_tr_b16 v[218:219], v241 offset:6912\n"
"v_mfma_f32_16x16x32_bf16 v[164:167], v[196:199], a[108:111], v[164:167]\n"
"v_mfma_f32_16x16x32_bf16 v[168:171], v[192:195], a[112:115], v[168:171]\n"
"ds_read_b64_tr_b16 v[220:221], v241 offset:9216\n"
"v_mfma_f32_16x16x32_bf16 v[168:171], v[196:199], a[116:119], v[168:171]\n"
"v_mfma_f32_16x16x32_bf16 v[172:175], v[192:195], a[120:123], v[172:175]\n"
"ds_read_b64_tr_b16 v[222:223], v241 offset:9984\n"
"v_mfma_f32_16x16x32_bf16 v[172:175], v[196:199], a[124:127], v[172:175]\n"
"buffer_load_dwordx4 v243, s[64:67], s88 offen offset:1024 lds\n"
"v_mfma_f32_16x16x32_bf16 v[176:179], v[200:203], a[96:99], v[176:179]\n"
"ds_read_b64_tr_b16 v[192:193], v241 offset:32\n"
"v_mfma_f32_16x16x32_bf16 v[176:179], v[204:207], a[100:103], v[176:179]\n"
"v_mfma_f32_16x16x32_bf16 v[180:183], v[200:203], a[104:107], v[180:183]\n"
"ds_read_b64_tr_b16 v[194:195], v241 offset:800\n"
"v_mfma_f32_16x16x32_bf16 v[180:183], v[204:207], a[108:111], v[180:183]\n"
"v_mfma_f32_16x16x32_bf16 v[184:187], v[200:203], a[112:115], v[184:187]\n"
"ds_read_b64_tr_b16 v[196:197], v241 offset:3104\n"
"v_mfma_f32_16x16x32_bf16 v[184:187], v[204:207], a[116:119], v[184:187]\n"
"v_mfma_f32_16x16x32_bf16 v[188:191], v[200:203], a[120:123], v[188:191]\n"
"ds_read_b64_tr_b16 v[198:199], v241 offset:3872\n"
"v_mfma_f32_16x16x32_bf16 v[188:191], v[204:207], a[124:127], v[188:191]\n"
"buffer_load_dwordx4 v244, s[64:67], s88 offen offset:2048 lds\n"
"s_waitcnt vmcnt(16)\n"
"v_mfma_f32_16x16x32_bf16 a[160:163], v[208:211], a[128:131], a[160:163]\n"
"ds_read_b64_tr_b16 v[200:201], v241 offset:6176\n"
"s_waitcnt vmcnt(15) lgkmcnt(3)\n"
"v_mfma_f32_16x16x32_bf16 a[160:163], v[212:215], a[132:135], a[160:163]\n"
"s_waitcnt vmcnt(14)\n"
"v_mfma_f32_16x16x32_bf16 a[164:167], v[208:211], a[136:139], a[164:167]\n"
"ds_read_b64_tr_b16 v[202:203], v241 offset:6944\n"
"s_waitcnt vmcnt(13)\n"
"v_mfma_f32_16x16x32_bf16 a[164:167], v[212:215], a[140:143], a[164:167]\n"
"s_waitcnt vmcnt(12)\n"
"v_mfma_f32_16x16x32_bf16 a[168:171], v[208:211], a[144:147], a[168:171]\n"
"ds_read_b64_tr_b16 v[204:205], v241 offset:9248\n"
"s_waitcnt vmcnt(11)\n"
"v_mfma_f32_16x16x32_bf16 a[168:171], v[212:215], a[148:151], a[168:171]\n"
"s_waitcnt vmcnt(10)\n"
"v_mfma_f32_16x16x32_bf16 a[172:175], v[208:211], a[152:155], a[172:175]\n"
"ds_read_b64_tr_b16 v[206:207], v241 offset:10016\n"
"s_waitcnt vmcnt(9)\n"
"v_mfma_f32_16x16x32_bf16 a[172:175], v[212:215], a[156:159], a[172:175]\n"
"buffer_load_dwordx4 a[96:99], v249, s[72:75], s91 offen offset:0 sc1\n"
"v_mfma_f32_16x16x32_bf16 a[208:211], v[216:219], a[128:131], a[208:211]\n"
"ds_read_b64_tr_b16 v[208:209], v241 offset:64\n"
"v_mfma_f32_16x16x32_bf16 a[208:211], v[220:223], a[132:135], a[208:211]\n"
"v_mfma_f32_16x16x32_bf16 a[212:215], v[216:219], a[136:139], a[212:215]\n"
"ds_read_b64_tr_b16 v[210:211], v241 offset:832\n"
"v_mfma_f32_16x16x32_bf16 a[212:215], v[220:223], a[140:143], a[212:215]\n"
"v_mfma_f32_16x16x32_bf16 a[216:219], v[216:219], a[144:147], a[216:219]\n"
"ds_read_b64_tr_b16 v[212:213], v241 offset:3136\n"
"v_mfma_f32_16x16x32_bf16 a[216:219], v[220:223], a[148:151], a[216:219]\n"
"v_mfma_f32_16x16x32_bf16 a[220:223], v[216:219], a[152:155], a[220:223]\n"
"ds_read_b64_tr_b16 v[214:215], v241 offset:3904\n"
"v_mfma_f32_16x16x32_bf16 a[220:223], v[220:223], a[156:159], a[220:223]\n"
"buffer_load_dwordx4 a[100:103], v249, s[72:75], s91 offen offset:1024 sc1\n"
"v_mfma_f32_16x16x32_bf16 a[176:179], v[192:195], a[128:131], a[176:179]\n"
"ds_read_b64_tr_b16 v[216:217], v241 offset:6208\n"
"s_waitcnt lgkmcnt(3)\n"
"v_mfma_f32_16x16x32_bf16 a[176:179], v[196:199], a[132:135], a[176:179]\n"
"v_mfma_f32_16x16x32_bf16 a[180:183], v[192:195], a[136:139], a[180:183]\n"
"ds_read_b64_tr_b16 v[218:219], v241 offset:6976\n"
"v_mfma_f32_16x16x32_bf16 a[180:183], v[196:199], a[140:143], a[180:183]\n"
"v_mfma_f32_16x16x32_bf16 a[184:187], v[192:195], a[144:147], a[184:187]\n"
"ds_read_b64_tr_b16 v[220:221], v241 offset:9280\n"
"v_mfma_f32_16x16x32_bf16 a[184:187], v[196:199], a[148:151], a[184:187]\n"
"v_mfma_f32_16x16x32_bf16 a[188:191], v[192:195], a[152:155], a[188:191]\n"
"ds_read_b64_tr_b16 v[222:223], v241 offset:10048\n"
"s_add_u32 s91, s91, 2048\n"
"v_mfma_f32_16x16x32_bf16 a[188:191], v[196:199], a[156:159], a[188:191]\n"
"buffer_load_dwordx4 a[104:107], v249, s[72:75], s92 offen offset:0 sc1\n"
"v_mfma_f32_16x16x32_bf16 a[224:227], v[200:203], a[128:131], a[224:227]\n"
"ds_read_b64_tr_b16 v[192:193], v239 offset:0\n"
"v_mfma_f32_16x16x32_bf16 a[224:227], v[204:207], a[132:135], a[224:227]\n"
"v_mfma_f32_16x16x32_bf16 a[228:231], v[200:203], a[136:139], a[228:231]\n"
"ds_read_b64_tr_b16 v[194:195], v239 offset:1536\n"
"v_mfma_f32_16x16x32_bf16 a[228:231], v[204:207], a[140:143], a[228:231]\n"
"v_mfma_f32_16x16x32_bf16 a[232:235], v[200:203], a[144:147], a[232:235]\n"
"ds_read_b64_tr_b16 v[196:197], v239 offset:12288\n"
"v_mfma_f32_16x16x32_bf16 a[232:235], v[204:207], a[148:151], a[232:235]\n"
"v_mfma_f32_16x16x32_bf16 a[236:239], v[200:203], a[152:155], a[236:239]\n"
"ds_read_b64_tr_b16 v[198:199], v239 offset:13824\n"
"v_mfma_f32_16x16x32_bf16 a[236:239], v[204:207], a[156:159], a[236:239]\n"
"buffer_load_dwordx4 a[108:111], v249, s[72:75], s92 offen offset:1024 sc1\n"
"v_mfma_f32_16x16x32_bf16 a[192:195], v[208:211], a[128:131], a[192:195]\n"
"ds_read_b64_tr_b16 v[200:201], v239 offset:8\n"
"s_waitcnt lgkmcnt(3)\n"
"v_mfma_f32_16x16x32_bf16 a[192:195], v[212:215], a[132:135], a[192:195]\n"
"v_mfma_f32_16x16x32_bf16 a[196:199], v[208:211], a[136:139], a[196:199]\n"
"ds_read_b64_tr_b16 v[202:203], v239 offset:1544\n"
"v_mfma_f32_16x16x32_bf16 a[196:199], v[212:215], a[140:143], a[196:199]\n"
"v_mfma_f32_16x16x32_bf16 a[200:203], v[208:211], a[144:147], a[200:203]\n"
"ds_read_b64_tr_b16 v[204:205], v239 offset:12296\n"
"v_mfma_f32_16x16x32_bf16 a[200:203], v[212:215], a[148:151], a[200:203]\n"
"v_mfma_f32_16x16x32_bf16 a[204:207], v[208:211], a[152:155], a[204:207]\n"
"ds_read_b64_tr_b16 v[206:207], v239 offset:13832\n"
"s_add_u32 s92, s92, 2048\n"
"v_mfma_f32_16x16x32_bf16 a[204:207], v[212:215], a[156:159], a[204:207]\n"
"buffer_load_dwordx4 a[112:115], v249, s[72:75], s93 offen offset:0 sc1\n"
"v_mfma_f32_16x16x32_bf16 a[240:243], v[216:219], a[128:131], a[240:243]\n"
"ds_read_b64_tr_b16 v[208:209], v240 offset:0\n"
"v_mfma_f32_16x16x32_bf16 a[240:243], v[220:223], a[132:135], a[240:243]\n"
"v_mfma_f32_16x16x32_bf16 a[244:247], v[216:219], a[136:139], a[244:247]\n"
"ds_read_b64_tr_b16 v[210:211], v240 offset:1536\n"
"v_mfma_f32_16x16x32_bf16 a[244:247], v[220:223], a[140:143], a[244:247]\n"
"v_mfma_f32_16x16x32_bf16 a[248:251], v[216:219], a[144:147], a[248:251]\n"
"ds_read_b64_tr_b16 v[212:213], v240 offset:12288\n"
"v_mfma_f32_16x16x32_bf16 a[248:251], v[220:223], a[148:151], a[248:251]\n"
"v_mfma_f32_16x16x32_bf16 a[252:255], v[216:219], a[152:155], a[252:255]\n"
"ds_read_b64_tr_b16 v[214:215], v240 offset:13824\n"
"v_mfma_f32_16x16x32_bf16 a[252:255], v[220:223], a[156:159], a[252:255]\n"
"buffer_load_dwordx4 a[116:119], v249, s[72:75], s93 offen offset:1024 sc1\n"
"v_mfma_f32_16x16x32_bf16 v[0:3], v[192:195], a[128:131], v[0:3]\n"
"ds_read_b64_tr_b16 v[216:217], v240 offset:8\n"
"s_waitcnt lgkmcnt(3)\n"
"v_mfma_f32_16x16x32_bf16 v[0:3], v[196:199], a[132:135], v[0:3]\n"
"v_mfma_f32_16x16x32_bf16 v[4:7], v[192:195], a[136:139], v[4:7]\n"
"ds_read_b64_tr_b16 v[218:219], v240 offset:1544\n"
"v_mfma_f32_16x16x32_bf16 v[4:7], v[196:199], a[140:143], v[4:7]\n"
"v_mfma_f32_16x16x32_bf16 v[8:11], v[192:195], a[144:147], v[8:11]\n"
"ds_read_b64_tr_b16 v[220:221], v240 offset:12296\n"
"v_mfma_f32_16x16x32_bf16 v[8:11], v[196:199], a[148:151], v[8:11]\n"
"v_mfma_f32_16x16x32_bf16 v[12:15], v[192:195], a[152:155], v[12:15]\n"
"ds_read_b64_tr_b16 v[222:223], v240 offset:13832\n"
"s_add_u32 s93, s93, 2048\n"
"v_mfma_f32_16x16x32_bf16 v[12:15], v[196:199], a[156:159], v[12:15]\n"
"buffer_load_dwordx4 a[120:123], v249, s[72:75], s94 offen offset:0 sc1\n"
"v_mfma_f32_16x16x32_bf16 v[16:19], v[200:203], a[128:131], v[16:19]\n"
"ds_read_b64_tr_b16 v[192:193], v239 offset:128\n"
"v_mfma_f32_16x16x32_bf16 v[16:19], v[204:207], a[132:135], v[16:19]\n"
"v_mfma_f32_16x16x32_bf16 v[20:23], v[200:203], a[136:139], v[20:23]\n"
"ds_read_b64_tr_b16 v[194:195], v239 offset:1664\n"
"v_mfma_f32_16x16x32_bf16 v[20:23], v[204:207], a[140:143], v[20:23]\n"
"v_mfma_f32_16x16x32_bf16 v[24:27], v[200:203], a[144:147], v[24:27]\n"
"ds_read_b64_tr_b16 v[196:197], v239 offset:12416\n"
"v_mfma_f32_16x16x32_bf16 v[24:27], v[204:207], a[148:151], v[24:27]\n"
"v_mfma_f32_16x16x32_bf16 v[28:31], v[200:203], a[152:155], v[28:31]\n"
"ds_read_b64_tr_b16 v[198:199], v239 offset:13952\n"
"v_mfma_f32_16x16x32_bf16 v[28:31], v[204:207], a[156:159], v[28:31]\n"
"buffer_load_dwordx4 a[124:127], v249, s[72:75], s94 offen offset:1024 sc1\n"
"s_add_u32 s94, s94, 2048\n"
"v_mfma_f32_16x16x32_bf16 v[32:35], v[208:211], a[128:131], v[32:35]\n"
"ds_read_b64_tr_b16 v[200:201], v239 offset:136\n"
"s_waitcnt lgkmcnt(3)\n"
"v_mfma_f32_16x16x32_bf16 v[32:35], v[212:215], a[132:135], v[32:35]\n"
"v_mfma_f32_16x16x32_bf16 v[36:39], v[208:211], a[136:139], v[36:39]\n"
"ds_read_b64_tr_b16 v[202:203], v239 offset:1672\n"
"v_mfma_f32_16x16x32_bf16 v[36:39], v[212:215], a[140:143], v[36:39]\n"
"v_mfma_f32_16x16x32_bf16 v[40:43], v[208:211], a[144:147], v[40:43]\n"
"ds_read_b64_tr_b16 v[204:205], v239 offset:12424\n"
"v_mfma_f32_16x16x32_bf16 v[40:43], v[212:215], a[148:151], v[40:43]\n"
"v_mfma_f32_16x16x32_bf16 v[44:47], v[208:211], a[152:155], v[44:47]\n"
"ds_read_b64_tr_b16 v[206:207], v239 offset:13960\n"
"s_add_u32 s88, s88, 786432\n"
"s_add_u32 m0, s86, 113664\n"
"v_mfma_f32_16x16x32_bf16 v[44:47], v[212:215], a[156:159], v[44:47]\n"
"buffer_load_dwordx4 v242, s[64:67], s89 offen offset:0 lds\n"
"v_mfma_f32_16x16x32_bf16 v[48:51], v[216:219], a[128:131], v[48:51]\n"
"ds_read_b64_tr_b16 v[208:209], v240 offset:128\n"
"v_mfma_f32_16x16x32_bf16 v[48:51], v[220:223], a[132:135], v[48:51]\n"
"v_mfma_f32_16x16x32_bf16 v[52:55], v[216:219], a[136:139], v[52:55]\n"
"ds_read_b64_tr_b16 v[210:211], v240 offset:1664\n"
"v_mfma_f32_16x16x32_bf16 v[52:55], v[220:223], a[140:143], v[52:55]\n"
"v_mfma_f32_16x16x32_bf16 v[56:59], v[216:219], a[144:147], v[56:59]\n"
"ds_read_b64_tr_b16 v[212:213], v240 offset:12416\n"
"v_mfma_f32_16x16x32_bf16 v[56:59], v[220:223], a[148:151], v[56:59]\n"
"v_mfma_f32_16x16x32_bf16 v[60:63], v[216:219], a[152:155], v[60:63]\n"
"ds_read_b64_tr_b16 v[214:215], v240 offset:13952\n"
"v_mfma_f32_16x16x32_bf16 v[60:63], v[220:223], a[156:159], v[60:63]\n"
"buffer_load_dwordx4 v243, s[64:67], s89 offen offset:1024 lds\n"
"v_mfma_f32_16x16x32_bf16 v[64:67], v[192:195], a[128:131], v[64:67]\n"
"ds_read_b64_tr_b16 v[216:217], v240 offset:136\n"
"s_waitcnt lgkmcnt(3)\n"
"v_mfma_f32_16x16x32_bf16 v[64:67], v[196:199], a[132:135], v[64:67]\n"
"v_mfma_f32_16x16x32_bf16 v[68:71], v[192:195], a[136:139], v[68:71]\n"
"ds_read_b64_tr_b16 v[218:219], v240 offset:1672\n"
"v_mfma_f32_16x16x32_bf16 v[68:71], v[196:199], a[140:143], v[68:71]\n"
"v_mfma_f32_16x16x32_bf16 v[72:75], v[192:195], a[144:147], v[72:75]\n"
"ds_read_b64_tr_b16 v[220:221], v240 offset:12424\n"
"v_mfma_f32_16x16x32_bf16 v[72:75], v[196:199], a[148:151], v[72:75]\n"
"v_mfma_f32_16x16x32_bf16 v[76:79], v[192:195], a[152:155], v[76:79]\n"
"ds_read_b64_tr_b16 v[222:223], v240 offset:13960\n"
"v_mfma_f32_16x16x32_bf16 v[76:79], v[196:199], a[156:159], v[76:79]\n"
"buffer_load_dwordx4 v244, s[64:67], s89 offen offset:2048 lds\n"
"v_mfma_f32_16x16x32_bf16 v[80:83], v[200:203], a[128:131], v[80:83]\n"
"ds_read_b64_tr_b16 v[192:193], v239 offset:256\n"
"v_mfma_f32_16x16x32_bf16 v[80:83], v[204:207], a[132:135], v[80:83]\n"
"v_mfma_f32_16x16x32_bf16 v[84:87], v[200:203], a[136:139], v[84:87]\n"
"ds_read_b64_tr_b16 v[194:195], v239 offset:1792\n"
"v_mfma_f32_16x16x32_bf16 v[84:87], v[204:207], a[140:143], v[84:87]\n"
"v_mfma_f32_16x16x32_bf16 v[88:91], v[200:203], a[144:147], v[88:91]\n"
"ds_read_b64_tr_b16 v[196:197], v239 offset:12544\n"
"v_mfma_f32_16x16x32_bf16 v[88:91], v[204:207], a[148:151], v[88:91]\n"
"v_mfma_f32_16x16x32_bf16 v[92:95], v[200:203], a[152:155], v[92:95]\n"
"ds_read_b64_tr_b16 v[198:199], v239 offset:14080\n"
"s_add_u32 s89, s89, 786432\n"
"s_add_u32 m0, s87, 110592\n"
"v_mfma_f32_16x16x32_bf16 v[92:95], v[204:207], a[156:159], v[92:95]\n"
"buffer_load_dwordx4 v245, s[68:71], s90 offen offset:0 lds\n"
"v_mfma_f32_16x16x32_bf16 v[96:99], v[208:211], a[128:131], v[96:99]\n"
"ds_read_b64_tr_b16 v[200:201], v239 offset:264\n"
"s_waitcnt lgkmcnt(3)\n"
"v_mfma_f32_16x16x32_bf16 v[96:99], v[212:215], a[132:135], v[96:99]\n"
"v_mfma_f32_16x16x32_bf16 v[100:103], v[208:211], a[136:139], v[100:103]\n"
"ds_read_b64_tr_b16 v[202:203], v239 offset:1800\n"
"v_mfma_f32_16x16x32_bf16 v[100:103], v[212:215], a[140:143], v[100:103]\n"
"v_mfma_f32_16x16x32_bf16 v[104:107], v[208:211], a[144:147], v[104:107]\n"
"ds_read_b64_tr_b16 v[204:205], v239 offset:12552\n"
"v_mfma_f32_16x16x32_bf16 v[104:107], v[212:215], a[148:151], v[104:107]\n"
"v_mfma_f32_16x16x32_bf16 v[108:111], v[208:211], a[152:155], v[108:111]\n"
"ds_read_b64_tr_b16 v[206:207], v239 offset:14088\n"
"v_mfma_f32_16x16x32_bf16 v[108:111], v[212:215], a[156:159], v[108:111]\n"
"buffer_load_dwordx4 v246, s[68:71], s90 offen offset:1024 lds\n"
"v_mfma_f32_16x16x32_bf16 v[112:115], v[216:219], a[128:131], v[112:115]\n"
"ds_read_b64_tr_b16 v[208:209], v240 offset:256\n"
"v_mfma_f32_16x16x32_bf16 v[112:115], v[220:223], a[132:135], v[112:115]\n"
"v_mfma_f32_16x16x32_bf16 v[116:119], v[216:219], a[136:139], v[116:119]\n"
"ds_read_b64_tr_b16 v[210:211], v240 offset:1792\n"
"v_mfma_f32_16x16x32_bf16 v[116:119], v[220:223], a[140:143], v[116:119]\n"
"v_mfma_f32_16x16x32_bf16 v[120:123], v[216:219], a[144:147], v[120:123]\n"
"ds_read_b64_tr_b16 v[212:213], v240 offset:12544\n"
"v_mfma_f32_16x16x32_bf16 v[120:123], v[220:223], a[148:151], v[120:123]\n"
"v_mfma_f32_16x16x32_bf16 v[124:127], v[216:219], a[152:155], v[124:127]\n"
"ds_read_b64_tr_b16 v[214:215], v240 offset:14080\n"
"v_mfma_f32_16x16x32_bf16 v[124:127], v[220:223], a[156:159], v[124:127]\n"
"buffer_load_dwordx4 v247, s[68:71], s90 offen offset:2048 lds\n"
"s_add_u32 s90, s90, 196608\n"
"v_mfma_f32_16x16x32_bf16 v[128:131], v[192:195], a[128:131], v[128:131]\n"
"ds_read_b64_tr_b16 v[216:217], v240 offset:264\n"
"s_waitcnt lgkmcnt(3)\n"
"v_mfma_f32_16x16x32_bf16 v[128:131], v[196:199], a[132:135], v[128:131]\n"
"v_mfma_f32_16x16x32_bf16 v[132:135], v[192:195], a[136:139], v[132:135]\n"
"ds_read_b64_tr_b16 v[218:219], v240 offset:1800\n"
"v_mfma_f32_16x16x32_bf16 v[132:135], v[196:199], a[140:143], v[132:135]\n"
"v_mfma_f32_16x16x32_bf16 v[136:139], v[192:195], a[144:147], v[136:139]\n"
"ds_read_b64_tr_b16 v[220:221], v240 offset:12552\n"
"v_mfma_f32_16x16x32_bf16 v[136:139], v[196:199], a[148:151], v[136:139]\n"
"v_mfma_f32_16x16x32_bf16 v[140:143], v[192:195], a[152:155], v[140:143]\n"
"ds_read_b64_tr_b16 v[222:223], v240 offset:14088\n"
"v_mfma_f32_16x16x32_bf16 v[140:143], v[196:199], a[156:159], v[140:143]\n"
"s_waitcnt vmcnt(17)\n"
"s_barrier\n"
"v_mfma_f32_16x16x32_bf16 v[144:147], v[200:203], a[128:131], v[144:147]\n"
"ds_read_b64_tr_b16 v[192:193], v238 offset:0\n"
"v_mfma_f32_16x16x32_bf16 v[144:147], v[204:207], a[132:135], v[144:147]\n"
"v_add_u32 v239, 73728, v239\n"
"v_mfma_f32_16x16x32_bf16 v[148:151], v[200:203], a[136:139], v[148:151]\n"
"ds_read_b64_tr_b16 v[194:195], v238 offset:768\n"
"v_mfma_f32_16x16x32_bf16 v[148:151], v[204:207], a[140:143], v[148:151]\n"
"v_add_u32 v240, 73728, v240\n"
"v_mfma_f32_16x16x32_bf16 v[152:155], v[200:203], a[144:147], v[152:155]\n"
"ds_read_b64_tr_b16 v[196:197], v238 offset:3072\n"
"v_mfma_f32_16x16x32_bf16 v[152:155], v[204:207], a[148:151], v[152:155]\n"
"v_add_u32 v241, 73728, v241\n"
"v_mfma_f32_16x16x32_bf16 v[156:159], v[200:203], a[152:155], v[156:159]\n"
"ds_read_b64_tr_b16 v[198:199], v238 offset:3840\n"
"s_mov_b32 m0, s76\n"
"v_mfma_f32_16x16x32_bf16 v[156:159], v[204:207], a[156:159], v[156:159]\n"
"buffer_load_dwordx4 v230, s[56:59], s84 offen offset:0 lds\n"
"v_mfma_f32_16x16x32_bf16 v[160:163], v[208:211], a[128:131], v[160:163]\n"
"ds_read_b64_tr_b16 v[200:201], v238 offset:6144\n"
"s_waitcnt lgkmcnt(3)\n"
"v_mfma_f32_16x16x32_bf16 v[160:163], v[212:215], a[132:135], v[160:163]\n"
"v_mfma_f32_16x16x32_bf16 v[164:167], v[208:211], a[136:139], v[164:167]\n"
"ds_read_b64_tr_b16 v[202:203], v238 offset:6912\n"
"v_mfma_f32_16x16x32_bf16 v[164:167], v[212:215], a[140:143], v[164:167]\n"
"v_mfma_f32_16x16x32_bf16 v[168:171], v[208:211], a[144:147], v[168:171]\n"
"ds_read_b64_tr_b16 v[204:205], v238 offset:9216\n"
"v_mfma_f32_16x16x32_bf16 v[168:171], v[212:215], a[148:151], v[168:171]\n"
"v_mfma_f32_16x16x32_bf16 v[172:175], v[208:211], a[152:155], v[172:175]\n"
"ds_read_b64_tr_b16 v[206:207], v238 offset:9984\n"
"v_mfma_f32_16x16x32_bf16 v[172:175], v[212:215], a[156:159], v[172:175]\n"
"buffer_load_dwordx4 v231, s[56:59], s84 offen offset:1024 lds\n"
"v_mfma_f32_16x16x32_bf16 v[176:179], v[216:219], a[128:131], v[176:179]\n"
"ds_read_b64_tr_b16 v[208:209], v238 offset:32\n"
"v_mfma_f32_16x16x32_bf16 v[176:179], v[220:223], a[132:135], v[176:179]\n"
"v_mfma_f32_16x16x32_bf16 v[180:183], v[216:219], a[136:139], v[180:183]\n"
"ds_read_b64_tr_b16 v[210:211], v238 offset:800\n"
"v_mfma_f32_16x16x32_bf16 v[180:183], v[220:223], a[140:143], v[180:183]\n"
"v_mfma_f32_16x16x32_bf16 v[184:187], v[216:219], a[144:147], v[184:187]\n"
"ds_read_b64_tr_b16 v[212:213], v238 offset:3104\n"
"v_mfma_f32_16x16x32_bf16 v[184:187], v[220:223], a[148:151], v[184:187]\n"
"v_mfma_f32_16x16x32_bf16 v[188:191], v[216:219], a[152:155], v[188:191]\n"
"ds_read_b64_tr_b16 v[214:215], v238 offset:3872\n"
"v_mfma_f32_16x16x32_bf16 v[188:191], v[220:223], a[156:159], v[188:191]\n"
"buffer_load_dwordx4 v232, s[56:59], s84 offen offset:2048 lds\n"
"s_waitcnt vmcnt(16)\n"
"v_mfma_f32_16x16x32_bf16 a[160:163], v[192:195], a[96:99], a[160:163]\n"
"ds_read_b64_tr_b16 v[216:217], v238 offset:6176\n"
"s_waitcnt vmcnt(15) lgkmcnt(3)\n"
"v_mfma_f32_16x16x32_bf16 a[160:163], v[196:199], a[100:103], a[160:163]\n"
"s_waitcnt vmcnt(14)\n"
"v_mfma_f32_16x16x32_bf16 a[164:167], v[192:195], a[104:107], a[164:167]\n"
"ds_read_b64_tr_b16 v[218:219], v238 offset:6944\n"
"s_waitcnt vmcnt(13)\n"
"v_mfma_f32_16x16x32_bf16 a[164:167], v[196:199], a[108:111], a[164:167]\n"
"s_waitcnt vmcnt(12)\n"
"v_mfma_f32_16x16x32_bf16 a[168:171], v[192:195], a[112:115], a[168:171]\n"
"ds_read_b64_tr_b16 v[220:221], v238 offset:9248\n"
"s_waitcnt vmcnt(11)\n"
"v_mfma_f32_16x16x32_bf16 a[168:171], v[196:199], a[116:119], a[168:171]\n"
"s_waitcnt vmcnt(10)\n"
"v_mfma_f32_16x16x32_bf16 a[172:175], v[192:195], a[120:123], a[172:175]\n"
"ds_read_b64_tr_b16 v[222:223], v238 offset:10016\n"
"s_waitcnt vmcnt(9)\n"
"v_mfma_f32_16x16x32_bf16 a[172:175], v[196:199], a[124:127], a[172:175]\n"
"buffer_load_dwordx4 a[128:131], v249, s[72:75], s91 offen offset:0 sc1\n"
"v_mfma_f32_16x16x32_bf16 a[208:211], v[200:203], a[96:99], a[208:211]\n"
"ds_read_b64_tr_b16 v[192:193], v238 offset:64\n"
"v_mfma_f32_16x16x32_bf16 a[208:211], v[204:207], a[100:103], a[208:211]\n"
"v_mfma_f32_16x16x32_bf16 a[212:215], v[200:203], a[104:107], a[212:215]\n"
"ds_read_b64_tr_b16 v[194:195], v238 offset:832\n"
"v_mfma_f32_16x16x32_bf16 a[212:215], v[204:207], a[108:111], a[212:215]\n"
"v_mfma_f32_16x16x32_bf16 a[216:219], v[200:203], a[112:115], a[216:219]\n"
"ds_read_b64_tr_b16 v[196:197], v238 offset:3136\n"
"v_mfma_f32_16x16x32_bf16 a[216:219], v[204:207], a[116:119], a[216:219]\n"
"v_mfma_f32_16x16x32_bf16 a[220:223], v[200:203], a[120:123], a[220:223]\n"
"ds_read_b64_tr_b16 v[198:199], v238 offset:3904\n"
"v_mfma_f32_16x16x32_bf16 a[220:223], v[204:207], a[124:127], a[220:223]\n"
"buffer_load_dwordx4 a[132:135], v249, s[72:75], s91 offen offset:1024 sc1\n"
"v_mfma_f32_16x16x32_bf16 a[176:179], v[208:211], a[96:99], a[176:179]\n"
"ds_read_b64_tr_b16 v[200:201], v238 offset:6208\n"
"s_waitcnt lgkmcnt(3)\n"
"v_mfma_f32_16x16x32_bf16 a[176:179], v[212:215], a[100:103], a[176:179]\n"
"v_mfma_f32_16x16x32_bf16 a[180:183], v[208:211], a[104:107], a[180:183]\n"
"ds_read_b64_tr_b16 v[202:203], v238 offset:6976\n"
"v_mfma_f32_16x16x32_bf16 a[180:183], v[212:215], a[108:111], a[180:183]\n"
"v_mfma_f32_16x16x32_bf16 a[184:187], v[208:211], a[112:115], a[184:187]\n"
"ds_read_b64_tr_b16 v[204:205], v238 offset:9280\n"
"v_mfma_f32_16x16x32_bf16 a[184:187], v[212:215], a[116:119], a[184:187]\n"
"v_mfma_f32_16x16x32_bf16 a[188:191], v[208:211], a[120:123], a[188:191]\n"
"ds_read_b64_tr_b16 v[206:207], v238 offset:10048\n"
"s_add_u32 s91, s91, 2048\n"
"v_mfma_f32_16x16x32_bf16 a[188:191], v[212:215], a[124:127], a[188:191]\n"
"buffer_load_dwordx4 a[136:139], v249, s[72:75], s92 offen offset:0 sc1\n"
"v_mfma_f32_16x16x32_bf16 a[224:227], v[216:219], a[96:99], a[224:227]\n"
"ds_read_b64_tr_b16 v[208:209], v236 offset:0\n"
"v_mfma_f32_16x16x32_bf16 a[224:227], v[220:223], a[100:103], a[224:227]\n"
"v_mfma_f32_16x16x32_bf16 a[228:231], v[216:219], a[104:107], a[228:231]\n"
"ds_read_b64_tr_b16 v[210:211], v236 offset:1536\n"
"v_mfma_f32_16x16x32_bf16 a[228:231], v[220:223], a[108:111], a[228:231]\n"
"v_mfma_f32_16x16x32_bf16 a[232:235], v[216:219], a[112:115], a[232:235]\n"
"ds_read_b64_tr_b16 v[212:213], v236 offset:12288\n"
"v_mfma_f32_16x16x32_bf16 a[232:235], v[220:223], a[116:119], a[232:235]\n"
"v_mfma_f32_16x16x32_bf16 a[236:239], v[216:219], a[120:123], a[236:239]\n"
"ds_read_b64_tr_b16 v[214:215], v236 offset:13824\n"
"v_mfma_f32_16x16x32_bf16 a[236:239], v[220:223], a[124:127], a[236:239]\n"
"buffer_load_dwordx4 a[140:143], v249, s[72:75], s92 offen offset:1024 sc1\n"
"v_mfma_f32_16x16x32_bf16 a[192:195], v[192:195], a[96:99], a[192:195]\n"
"ds_read_b64_tr_b16 v[216:217], v236 offset:8\n"
"s_waitcnt lgkmcnt(3)\n"
"v_mfma_f32_16x16x32_bf16 a[192:195], v[196:199], a[100:103], a[192:195]\n"
"v_mfma_f32_16x16x32_bf16 a[196:199], v[192:195], a[104:107], a[196:199]\n"
"ds_read_b64_tr_b16 v[218:219], v236 offset:1544\n"
"v_mfma_f32_16x16x32_bf16 a[196:199], v[196:199], a[108:111], a[196:199]\n"
"v_mfma_f32_16x16x32_bf16 a[200:203], v[192:195], a[112:115], a[200:203]\n"
"ds_read_b64_tr_b16 v[220:221], v236 offset:12296\n"
"v_mfma_f32_16x16x32_bf16 a[200:203], v[196:199], a[116:119], a[200:203]\n"
"v_mfma_f32_16x16x32_bf16 a[204:207], v[192:195], a[120:123], a[204:207]\n"
"ds_read_b64_tr_b16 v[222:223], v236 offset:13832\n"
"s_add_u32 s92, s92, 2048\n"
"v_mfma_f32_16x16x32_bf16 a[204:207], v[196:199], a[124:127], a[204:207]\n"
"buffer_load_dwordx4 a[144:147], v249, s[72:75], s93 offen offset:0 sc1\n"
"v_mfma_f32_16x16x32_bf16 a[240:243], v[200:203], a[96:99], a[240:243]\n"
"ds_read_b64_tr_b16 v[192:193], v237 offset:0\n"
"v_mfma_f32_16x16x32_bf16 a[240:243], v[204:207], a[100:103], a[240:243]\n"
"v_mfma_f32_16x16x32_bf16 a[244:247], v[200:203], a[104:107], a[244:247]\n"
"ds_read_b64_tr_b16 v[194:195], v237 offset:1536\n"
"v_mfma_f32_16x16x32_bf16 a[244:247], v[204:207], a[108:111], a[244:247]\n"
"v_mfma_f32_16x16x32_bf16 a[248:251], v[200:203], a[112:115], a[248:251]\n"
"ds_read_b64_tr_b16 v[196:197], v237 offset:12288\n"
"v_mfma_f32_16x16x32_bf16 a[248:251], v[204:207], a[116:119], a[248:251]\n"
"v_mfma_f32_16x16x32_bf16 a[252:255], v[200:203], a[120:123], a[252:255]\n"
"ds_read_b64_tr_b16 v[198:199], v237 offset:13824\n"
"v_mfma_f32_16x16x32_bf16 a[252:255], v[204:207], a[124:127], a[252:255]\n"
"buffer_load_dwordx4 a[148:151], v249, s[72:75], s93 offen offset:1024 sc1\n"
"v_mfma_f32_16x16x32_bf16 v[0:3], v[208:211], a[96:99], v[0:3]\n"
"ds_read_b64_tr_b16 v[200:201], v237 offset:8\n"
"s_waitcnt lgkmcnt(3)\n"
"v_mfma_f32_16x16x32_bf16 v[0:3], v[212:215], a[100:103], v[0:3]\n"
"v_mfma_f32_16x16x32_bf16 v[4:7], v[208:211], a[104:107], v[4:7]\n"
"ds_read_b64_tr_b16 v[202:203], v237 offset:1544\n"
"v_mfma_f32_16x16x32_bf16 v[4:7], v[212:215], a[108:111], v[4:7]\n"
"v_mfma_f32_16x16x32_bf16 v[8:11], v[208:211], a[112:115], v[8:11]\n"
"ds_read_b64_tr_b16 v[204:205], v237 offset:12296\n"
"v_mfma_f32_16x16x32_bf16 v[8:11], v[212:215], a[116:119], v[8:11]\n"
"v_mfma_f32_16x16x32_bf16 v[12:15], v[208:211], a[120:123], v[12:15]\n"
"ds_read_b64_tr_b16 v[206:207], v237 offset:13832\n"
"s_add_u32 s93, s93, 2048\n"
"v_mfma_f32_16x16x32_bf16 v[12:15], v[212:215], a[124:127], v[12:15]\n"
"buffer_load_dwordx4 a[152:155], v249, s[72:75], s94 offen offset:0 sc1\n"
"v_mfma_f32_16x16x32_bf16 v[16:19], v[216:219], a[96:99], v[16:19]\n"
"ds_read_b64_tr_b16 v[208:209], v236 offset:128\n"
"v_mfma_f32_16x16x32_bf16 v[16:19], v[220:223], a[100:103], v[16:19]\n"
"v_mfma_f32_16x16x32_bf16 v[20:23], v[216:219], a[104:107], v[20:23]\n"
"ds_read_b64_tr_b16 v[210:211], v236 offset:1664\n"
"v_mfma_f32_16x16x32_bf16 v[20:23], v[220:223], a[108:111], v[20:23]\n"
"v_mfma_f32_16x16x32_bf16 v[24:27], v[216:219], a[112:115], v[24:27]\n"
"ds_read_b64_tr_b16 v[212:213], v236 offset:12416\n"
"v_mfma_f32_16x16x32_bf16 v[24:27], v[220:223], a[116:119], v[24:27]\n"
"v_mfma_f32_16x16x32_bf16 v[28:31], v[216:219], a[120:123], v[28:31]\n"
"ds_read_b64_tr_b16 v[214:215], v236 offset:13952\n"
"v_mfma_f32_16x16x32_bf16 v[28:31], v[220:223], a[124:127], v[28:31]\n"
"buffer_load_dwordx4 a[156:159], v249, s[72:75], s94 offen offset:1024 sc1\n"
"s_add_u32 s94, s94, 2048\n"
"v_mfma_f32_16x16x32_bf16 v[32:35], v[192:195], a[96:99], v[32:35]\n"
"ds_read_b64_tr_b16 v[216:217], v236 offset:136\n"
"s_waitcnt lgkmcnt(3)\n"
"v_mfma_f32_16x16x32_bf16 v[32:35], v[196:199], a[100:103], v[32:35]\n"
"v_mfma_f32_16x16x32_bf16 v[36:39], v[192:195], a[104:107], v[36:39]\n"
"ds_read_b64_tr_b16 v[218:219], v236 offset:1672\n"
"v_mfma_f32_16x16x32_bf16 v[36:39], v[196:199], a[108:111], v[36:39]\n"
"v_mfma_f32_16x16x32_bf16 v[40:43], v[192:195], a[112:115], v[40:43]\n"
"ds_read_b64_tr_b16 v[220:221], v236 offset:12424\n"
"v_mfma_f32_16x16x32_bf16 v[40:43], v[196:199], a[116:119], v[40:43]\n"
"v_mfma_f32_16x16x32_bf16 v[44:47], v[192:195], a[120:123], v[44:47]\n"
"ds_read_b64_tr_b16 v[222:223], v236 offset:13960\n"
"s_add_u32 s84, s84, 12288\n"
"s_mov_b32 m0, s77\n"
"v_mfma_f32_16x16x32_bf16 v[44:47], v[196:199], a[124:127], v[44:47]\n"
"buffer_load_dwordx4 v230, s[56:59], s84 offen offset:0 lds\n"
"v_mfma_f32_16x16x32_bf16 v[48:51], v[200:203], a[96:99], v[48:51]\n"
"ds_read_b64_tr_b16 v[192:193], v237 offset:128\n"
"v_mfma_f32_16x16x32_bf16 v[48:51], v[204:207], a[100:103], v[48:51]\n"
"v_mfma_f32_16x16x32_bf16 v[52:55], v[200:203], a[104:107], v[52:55]\n"
"ds_read_b64_tr_b16 v[194:195], v237 offset:1664\n"
"v_mfma_f32_16x16x32_bf16 v[52:55], v[204:207], a[108:111], v[52:55]\n"
"v_mfma_f32_16x16x32_bf16 v[56:59], v[200:203], a[112:115], v[56:59]\n"
"ds_read_b64_tr_b16 v[196:197], v237 offset:12416\n"
"v_mfma_f32_16x16x32_bf16 v[56:59], v[204:207], a[116:119], v[56:59]\n"
"v_mfma_f32_16x16x32_bf16 v[60:63], v[200:203], a[120:123], v[60:63]\n"
"ds_read_b64_tr_b16 v[198:199], v237 offset:13952\n"
"v_mfma_f32_16x16x32_bf16 v[60:63], v[204:207], a[124:127], v[60:63]\n"
"buffer_load_dwordx4 v231, s[56:59], s84 offen offset:1024 lds\n"
"v_mfma_f32_16x16x32_bf16 v[64:67], v[208:211], a[96:99], v[64:67]\n"
"ds_read_b64_tr_b16 v[200:201], v237 offset:136\n"
"s_waitcnt lgkmcnt(3)\n"
"v_mfma_f32_16x16x32_bf16 v[64:67], v[212:215], a[100:103], v[64:67]\n"
"v_mfma_f32_16x16x32_bf16 v[68:71], v[208:211], a[104:107], v[68:71]\n"
"ds_read_b64_tr_b16 v[202:203], v237 offset:1672\n"
"v_mfma_f32_16x16x32_bf16 v[68:71], v[212:215], a[108:111], v[68:71]\n"
"v_mfma_f32_16x16x32_bf16 v[72:75], v[208:211], a[112:115], v[72:75]\n"
"ds_read_b64_tr_b16 v[204:205], v237 offset:12424\n"
"v_mfma_f32_16x16x32_bf16 v[72:75], v[212:215], a[116:119], v[72:75]\n"
"v_mfma_f32_16x16x32_bf16 v[76:79], v[208:211], a[120:123], v[76:79]\n"
"ds_read_b64_tr_b16 v[206:207], v237 offset:13960\n"
"v_mfma_f32_16x16x32_bf16 v[76:79], v[212:215], a[124:127], v[76:79]\n"
"buffer_load_dwordx4 v232, s[56:59], s84 offen offset:2048 lds\n"
"v_mfma_f32_16x16x32_bf16 v[80:83], v[216:219], a[96:99], v[80:83]\n"
"ds_read_b64_tr_b16 v[208:209], v236 offset:256\n"
"v_mfma_f32_16x16x32_bf16 v[80:83], v[220:223], a[100:103], v[80:83]\n"
"v_mfma_f32_16x16x32_bf16 v[84:87], v[216:219], a[104:107], v[84:87]\n"
"ds_read_b64_tr_b16 v[210:211], v236 offset:1792\n"
"v_mfma_f32_16x16x32_bf16 v[84:87], v[220:223], a[108:111], v[84:87]\n"
"v_mfma_f32_16x16x32_bf16 v[88:91], v[216:219], a[112:115], v[88:91]\n"
"ds_read_b64_tr_b16 v[212:213], v236 offset:12544\n"
"v_mfma_f32_16x16x32_bf16 v[88:91], v[220:223], a[116:119], v[88:91]\n"
"v_mfma_f32_16x16x32_bf16 v[92:95], v[216:219], a[120:123], v[92:95]\n"
"ds_read_b64_tr_b16 v[214:215], v236 offset:14080\n"
"s_add_u32 s84, s84, 12288\n"
"s_mov_b32 m0, s78\n"
"v_mfma_f32_16x16x32_bf16 v[92:95], v[220:223], a[124:127], v[92:95]\n"
"buffer_load_dwordx4 v230, s[56:59], s84 offen offset:0 lds\n"
"v_mfma_f32_16x16x32_bf16 v[96:99], v[192:195], a[96:99], v[96:99]\n"
"ds_read_b64_tr_b16 v[216:217], v236 offset:264\n"
"s_waitcnt lgkmcnt(3)\n"
"v_mfma_f32_16x16x32_bf16 v[96:99], v[196:199], a[100:103], v[96:99]\n"
"v_mfma_f32_16x16x32_bf16 v[100:103], v[192:195], a[104:107], v[100:103]\n"
"ds_read_b64_tr_b16 v[218:219], v236 offset:1800\n"
"v_mfma_f32_16x16x32_bf16 v[100:103], v[196:199], a[108:111], v[100:103]\n"
"v_mfma_f32_16x16x32_bf16 v[104:107], v[192:195], a[112:115], v[104:107]\n"
"ds_read_b64_tr_b16 v[220:221], v236 offset:12552\n"
"v_mfma_f32_16x16x32_bf16 v[104:107], v[196:199], a[116:119], v[104:107]\n"
"v_mfma_f32_16x16x32_bf16 v[108:111], v[192:195], a[120:123], v[108:111]\n"
"ds_read_b64_tr_b16 v[222:223], v236 offset:14088\n"
"v_mfma_f32_16x16x32_bf16 v[108:111], v[196:199], a[124:127], v[108:111]\n"
"buffer_load_dwordx4 v231, s[56:59], s84 offen offset:1024 lds\n"
"v_mfma_f32_16x16x32_bf16 v[112:115], v[200:203], a[96:99], v[112:115]\n"
"ds_read_b64_tr_b16 v[192:193], v237 offset:256\n"
"v_mfma_f32_16x16x32_bf16 v[112:115], v[204:207], a[100:103], v[112:115]\n"
"v_mfma_f32_16x16x32_bf16 v[116:119], v[200:203], a[104:107], v[116:119]\n"
"ds_read_b64_tr_b16 v[194:195], v237 offset:1792\n"
"v_mfma_f32_16x16x32_bf16 v[116:119], v[204:207], a[108:111], v[116:119]\n"
"v_mfma_f32_16x16x32_bf16 v[120:123], v[200:203], a[112:115], v[120:123]\n"
"ds_read_b64_tr_b16 v[196:197], v237 offset:12544\n"
"v_mfma_f32_16x16x32_bf16 v[120:123], v[204:207], a[116:119], v[120:123]\n"
"v_mfma_f32_16x16x32_bf16 v[124:127], v[200:203], a[120:123], v[124:127]\n"
"ds_read_b64_tr_b16 v[198:199], v237 offset:14080\n"
"v_mfma_f32_16x16x32_bf16 v[124:127], v[204:207], a[124:127], v[124:127]\n"
"buffer_load_dwordx4 v232, s[56:59], s84 offen offset:2048 lds\n"
"v_mfma_f32_16x16x32_bf16 v[128:131], v[208:211], a[96:99], v[128:131]\n"
"ds_read_b64_tr_b16 v[200:201], v237 offset:264\n"
"s_waitcnt lgkmcnt(3)\n"
"v_mfma_f32_16x16x32_bf16 v[128:131], v[212:215], a[100:103], v[128:131]\n"
"v_mfma_f32_16x16x32_bf16 v[132:135], v[208:211], a[104:107], v[132:135]\n"
"ds_read_b64_tr_b16 v[202:203], v237 offset:1800\n"
"v_mfma_f32_16x16x32_bf16 v[132:135], v[212:215], a[108:111], v[132:135]\n"
"v_mfma_f32_16x16x32_bf16 v[136:139], v[208:211], a[112:115], v[136:139]\n"
"ds_read_b64_tr_b16 v[204:205], v237 offset:12552\n"
"v_mfma_f32_16x16x32_bf16 v[136:139], v[212:215], a[116:119], v[136:139]\n"
"v_mfma_f32_16x16x32_bf16 v[140:143], v[208:211], a[120:123], v[140:143]\n"
"ds_read_b64_tr_b16 v[206:207], v237 offset:14088\n"
"v_mfma_f32_16x16x32_bf16 v[140:143], v[212:215], a[124:127], v[140:143]\n"
"s_waitcnt vmcnt(17)\n"
"s_barrier\n"
"v_mfma_f32_16x16x32_bf16 v[144:147], v[216:219], a[96:99], v[144:147]\n"
"ds_read_b64_tr_b16 v[208:209], v241 offset:0\n"
"s_mov_b32 m0, s80\n"
"v_mfma_f32_16x16x32_bf16 v[144:147], v[220:223], a[100:103], v[144:147]\n"
"buffer_load_dwordx4 v233, s[60:63], s85 offen offset:0 lds\n"
"v_mfma_f32_16x16x32_bf16 v[148:151], v[216:219], a[104:107], v[148:151]\n"
"ds_read_b64_tr_b16 v[210:211], v241 offset:768\n"
"v_mfma_f32_16x16x32_bf16 v[148:151], v[220:223], a[108:111], v[148:151]\n"
"v_mfma_f32_16x16x32_bf16 v[152:155], v[216:219], a[112:115], v[152:155]\n"
"ds_read_b64_tr_b16 v[212:213], v241 offset:3072\n"
"v_mfma_f32_16x16x32_bf16 v[152:155], v[220:223], a[116:119], v[152:155]\n"
"v_mfma_f32_16x16x32_bf16 v[156:159], v[216:219], a[120:123], v[156:159]\n"
"ds_read_b64_tr_b16 v[214:215], v241 offset:3840\n"
"v_mfma_f32_16x16x32_bf16 v[156:159], v[220:223], a[124:127], v[156:159]\n"
"v_mfma_f32_16x16x32_bf16 v[160:163], v[192:195], a[96:99], v[160:163]\n"
"ds_read_b64_tr_b16 v[216:217], v241 offset:6144\n"
"s_waitcnt lgkmcnt(3)\n"
"v_mfma_f32_16x16x32_bf16 v[160:163], v[196:199], a[100:103], v[160:163]\n"
"buffer_load_dwordx4 v234, s[60:63], s85 offen offset:1024 lds\n"
"v_mfma_f32_16x16x32_bf16 v[164:167], v[192:195], a[104:107], v[164:167]\n"
"ds_read_b64_tr_b16 v[218:219], v241 offset:6912\n"
"v_mfma_f32_16x16x32_bf16 v[164:167], v[196:199], a[108:111], v[164:167]\n"
"v_mfma_f32_16x16x32_bf16 v[168:171], v[192:195], a[112:115], v[168:171]\n"
"ds_read_b64_tr_b16 v[220:221], v241 offset:9216\n"
"v_mfma_f32_16x16x32_bf16 v[168:171], v[196:199], a[116:119], v[168:171]\n"
"v_mfma_f32_16x16x32_bf16 v[172:175], v[192:195], a[120:123], v[172:175]\n"
"ds_read_b64_tr_b16 v[222:223], v241 offset:9984\n"
"v_mfma_f32_16x16x32_bf16 v[172:175], v[196:199], a[124:127], v[172:175]\n"
"v_mfma_f32_16x16x32_bf16 v[176:179], v[200:203], a[96:99], v[176:179]\n"
"ds_read_b64_tr_b16 v[192:193], v241 offset:32\n"
"v_mfma_f32_16x16x32_bf16 v[176:179], v[204:207], a[100:103], v[176:179]\n"
"buffer_load_dwordx4 v235, s[60:63], s85 offen offset:2048 lds\n"
"v_mfma_f32_16x16x32_bf16 v[180:183], v[200:203], a[104:107], v[180:183]\n"
"ds_read_b64_tr_b16 v[194:195], v241 offset:800\n"
"v_mfma_f32_16x16x32_bf16 v[180:183], v[204:207], a[108:111], v[180:183]\n"
"v_mfma_f32_16x16x32_bf16 v[184:187], v[200:203], a[112:115], v[184:187]\n"
"ds_read_b64_tr_b16 v[196:197], v241 offset:3104\n"
"v_mfma_f32_16x16x32_bf16 v[184:187], v[204:207], a[116:119], v[184:187]\n"
"v_mfma_f32_16x16x32_bf16 v[188:191], v[200:203], a[120:123], v[188:191]\n"
"ds_read_b64_tr_b16 v[198:199], v241 offset:3872\n"
"v_mfma_f32_16x16x32_bf16 v[188:191], v[204:207], a[124:127], v[188:191]\n"
"s_waitcnt vmcnt(16)\n"
"v_mfma_f32_16x16x32_bf16 a[160:163], v[208:211], a[128:131], a[160:163]\n"
"ds_read_b64_tr_b16 v[200:201], v241 offset:6176\n"
"s_add_u32 s85, s85, 12288\n"
"s_mov_b32 m0, s81\n"
"s_waitcnt vmcnt(15) lgkmcnt(3)\n"
"v_mfma_f32_16x16x32_bf16 a[160:163], v[212:215], a[132:135], a[160:163]\n"
"buffer_load_dwordx4 v233, s[60:63], s85 offen offset:0 lds\n"
"s_waitcnt vmcnt(15)\n"
"v_mfma_f32_16x16x32_bf16 a[164:167], v[208:211], a[136:139], a[164:167]\n"
"ds_read_b64_tr_b16 v[202:203], v241 offset:6944\n"
"s_waitcnt vmcnt(14)\n"
"v_mfma_f32_16x16x32_bf16 a[164:167], v[212:215], a[140:143], a[164:167]\n"
"s_waitcnt vmcnt(13)\n"
"v_mfma_f32_16x16x32_bf16 a[168:171], v[208:211], a[144:147], a[168:171]\n"
"ds_read_b64_tr_b16 v[204:205], v241 offset:9248\n"
"s_waitcnt vmcnt(12)\n"
"v_mfma_f32_16x16x32_bf16 a[168:171], v[212:215], a[148:151], a[168:171]\n"
"s_waitcnt vmcnt(11)\n"
"v_mfma_f32_16x16x32_bf16 a[172:175], v[208:211], a[152:155], a[172:175]\n"
"ds_read_b64_tr_b16 v[206:207], v241 offset:10016\n"
"s_waitcnt vmcnt(10)\n"
"v_mfma_f32_16x16x32_bf16 a[172:175], v[212:215], a[156:159], a[172:175]\n"
"v_mfma_f32_16x16x32_bf16 a[208:211], v[216:219], a[128:131], a[208:211]\n"
"ds_read_b64_tr_b16 v[208:209], v241 offset:64\n"
"v_mfma_f32_16x16x32_bf16 a[208:211], v[220:223], a[132:135], a[208:211]\n"
"buffer_load_dwordx4 v234, s[60:63], s85 offen offset:1024 lds\n"
"v_mfma_f32_16x16x32_bf16 a[212:215], v[216:219], a[136:139], a[212:215]\n"
"ds_read_b64_tr_b16 v[210:211], v241 offset:832\n"
"v_mfma_f32_16x16x32_bf16 a[212:215], v[220:223], a[140:143], a[212:215]\n"
"v_mfma_f32_16x16x32_bf16 a[216:219], v[216:219], a[144:147], a[216:219]\n"
"ds_read_b64_tr_b16 v[212:213], v241 offset:3136\n"
"v_mfma_f32_16x16x32_bf16 a[216:219], v[220:223], a[148:151], a[216:219]\n"
"v_mfma_f32_16x16x32_bf16 a[220:223], v[216:219], a[152:155], a[220:223]\n"
"ds_read_b64_tr_b16 v[214:215], v241 offset:3904\n"
"v_mfma_f32_16x16x32_bf16 a[220:223], v[220:223], a[156:159], a[220:223]\n"
"v_mfma_f32_16x16x32_bf16 a[176:179], v[192:195], a[128:131], a[176:179]\n"
"ds_read_b64_tr_b16 v[216:217], v241 offset:6208\n"
"s_waitcnt lgkmcnt(3)\n"
"v_mfma_f32_16x16x32_bf16 a[176:179], v[196:199], a[132:135], a[176:179]\n"
"buffer_load_dwordx4 v235, s[60:63], s85 offen offset:2048 lds\n"
"v_mfma_f32_16x16x32_bf16 a[180:183], v[192:195], a[136:139], a[180:183]\n"
"ds_read_b64_tr_b16 v[218:219], v241 offset:6976\n"
"v_mfma_f32_16x16x32_bf16 a[180:183], v[196:199], a[140:143], a[180:183]\n"
"v_mfma_f32_16x16x32_bf16 a[184:187], v[192:195], a[144:147], a[184:187]\n"
"ds_read_b64_tr_b16 v[220:221], v241 offset:9280\n"
"v_mfma_f32_16x16x32_bf16 a[184:187], v[196:199], a[148:151], a[184:187]\n"
"v_mfma_f32_16x16x32_bf16 a[188:191], v[192:195], a[152:155], a[188:191]\n"
"ds_read_b64_tr_b16 v[222:223], v241 offset:10048\n"
"v_mfma_f32_16x16x32_bf16 a[188:191], v[196:199], a[156:159], a[188:191]\n"
"v_mfma_f32_16x16x32_bf16 a[224:227], v[200:203], a[128:131], a[224:227]\n"
"ds_read_b64_tr_b16 v[192:193], v239 offset:0\n"
"v_mfma_f32_16x16x32_bf16 a[224:227], v[204:207], a[132:135], a[224:227]\n"
"v_mfma_f32_16x16x32_bf16 a[228:231], v[200:203], a[136:139], a[228:231]\n"
"ds_read_b64_tr_b16 v[194:195], v239 offset:1536\n"
"v_mfma_f32_16x16x32_bf16 a[228:231], v[204:207], a[140:143], a[228:231]\n"
"v_mfma_f32_16x16x32_bf16 a[232:235], v[200:203], a[144:147], a[232:235]\n"
"ds_read_b64_tr_b16 v[196:197], v239 offset:12288\n"
"v_mfma_f32_16x16x32_bf16 a[232:235], v[204:207], a[148:151], a[232:235]\n"
"v_mfma_f32_16x16x32_bf16 a[236:239], v[200:203], a[152:155], a[236:239]\n"
"ds_read_b64_tr_b16 v[198:199], v239 offset:13824\n"
"v_mfma_f32_16x16x32_bf16 a[236:239], v[204:207], a[156:159], a[236:239]\n"
"v_mfma_f32_16x16x32_bf16 a[192:195], v[208:211], a[128:131], a[192:195]\n"
"ds_read_b64_tr_b16 v[200:201], v239 offset:8\n"
"s_waitcnt lgkmcnt(3)\n"
"v_mfma_f32_16x16x32_bf16 a[192:195], v[212:215], a[132:135], a[192:195]\n"
"v_mfma_f32_16x16x32_bf16 a[196:199], v[208:211], a[136:139], a[196:199]\n"
"ds_read_b64_tr_b16 v[202:203], v239 offset:1544\n"
"v_mfma_f32_16x16x32_bf16 a[196:199], v[212:215], a[140:143], a[196:199]\n"
"v_mfma_f32_16x16x32_bf16 a[200:203], v[208:211], a[144:147], a[200:203]\n"
"ds_read_b64_tr_b16 v[204:205], v239 offset:12296\n"
"v_mfma_f32_16x16x32_bf16 a[200:203], v[212:215], a[148:151], a[200:203]\n"
"v_mfma_f32_16x16x32_bf16 a[204:207], v[208:211], a[152:155], a[204:207]\n"
"ds_read_b64_tr_b16 v[206:207], v239 offset:13832\n"
"v_mfma_f32_16x16x32_bf16 a[204:207], v[212:215], a[156:159], a[204:207]\n"
"v_mfma_f32_16x16x32_bf16 a[240:243], v[216:219], a[128:131], a[240:243]\n"
"ds_read_b64_tr_b16 v[208:209], v240 offset:0\n"
"v_mfma_f32_16x16x32_bf16 a[240:243], v[220:223], a[132:135], a[240:243]\n"
"v_mfma_f32_16x16x32_bf16 a[244:247], v[216:219], a[136:139], a[244:247]\n"
"ds_read_b64_tr_b16 v[210:211], v240 offset:1536\n"
"v_mfma_f32_16x16x32_bf16 a[244:247], v[220:223], a[140:143], a[244:247]\n"
"v_mfma_f32_16x16x32_bf16 a[248:251], v[216:219], a[144:147], a[248:251]\n"
"ds_read_b64_tr_b16 v[212:213], v240 offset:12288\n"
"v_mfma_f32_16x16x32_bf16 a[248:251], v[220:223], a[148:151], a[248:251]\n"
"v_mfma_f32_16x16x32_bf16 a[252:255], v[216:219], a[152:155], a[252:255]\n"
"ds_read_b64_tr_b16 v[214:215], v240 offset:13824\n"
"v_mfma_f32_16x16x32_bf16 a[252:255], v[220:223], a[156:159], a[252:255]\n"
"v_mfma_f32_16x16x32_bf16 v[0:3], v[192:195], a[128:131], v[0:3]\n"
"ds_read_b64_tr_b16 v[216:217], v240 offset:8\n"
"s_waitcnt lgkmcnt(3)\n"
"v_mfma_f32_16x16x32_bf16 v[0:3], v[196:199], a[132:135], v[0:3]\n"
"v_mfma_f32_16x16x32_bf16 v[4:7], v[192:195], a[136:139], v[4:7]\n"
"ds_read_b64_tr_b16 v[218:219], v240 offset:1544\n"
"v_mfma_f32_16x16x32_bf16 v[4:7], v[196:199], a[140:143], v[4:7]\n"
"v_mfma_f32_16x16x32_bf16 v[8:11], v[192:195], a[144:147], v[8:11]\n"
"ds_read_b64_tr_b16 v[220:221], v240 offset:12296\n"
"v_mfma_f32_16x16x32_bf16 v[8:11], v[196:199], a[148:151], v[8:11]\n"
"v_mfma_f32_16x16x32_bf16 v[12:15], v[192:195], a[152:155], v[12:15]\n"
"ds_read_b64_tr_b16 v[222:223], v240 offset:13832\n"
"v_mfma_f32_16x16x32_bf16 v[12:15], v[196:199], a[156:159], v[12:15]\n"
"v_mfma_f32_16x16x32_bf16 v[16:19], v[200:203], a[128:131], v[16:19]\n"
"ds_read_b64_tr_b16 v[192:193], v239 offset:128\n"
"v_mfma_f32_16x16x32_bf16 v[16:19], v[204:207], a[132:135], v[16:19]\n"
"v_mfma_f32_16x16x32_bf16 v[20:23], v[200:203], a[136:139], v[20:23]\n"
"ds_read_b64_tr_b16 v[194:195], v239 offset:1664\n"
"v_mfma_f32_16x16x32_bf16 v[20:23], v[204:207], a[140:143], v[20:23]\n"
"v_mfma_f32_16x16x32_bf16 v[24:27], v[200:203], a[144:147], v[24:27]\n"
"ds_read_b64_tr_b16 v[196:197], v239 offset:12416\n"
"v_mfma_f32_16x16x32_bf16 v[24:27], v[204:207], a[148:151], v[24:27]\n"
"v_mfma_f32_16x16x32_bf16 v[28:31], v[200:203], a[152:155], v[28:31]\n"
"ds_read_b64_tr_b16 v[198:199], v239 offset:13952\n"
"v_mfma_f32_16x16x32_bf16 v[28:31], v[204:207], a[156:159], v[28:31]\n"
"v_mfma_f32_16x16x32_bf16 v[32:35], v[208:211], a[128:131], v[32:35]\n"
"ds_read_b64_tr_b16 v[200:201], v239 offset:136\n"
"s_waitcnt lgkmcnt(3)\n"
"v_mfma_f32_16x16x32_bf16 v[32:35], v[212:215], a[132:135], v[32:35]\n"
"v_mfma_f32_16x16x32_bf16 v[36:39], v[208:211], a[136:139], v[36:39]\n"
"ds_read_b64_tr_b16 v[202:203], v239 offset:1672\n"
"v_mfma_f32_16x16x32_bf16 v[36:39], v[212:215], a[140:143], v[36:39]\n"
"v_mfma_f32_16x16x32_bf16 v[40:43], v[208:211], a[144:147], v[40:43]\n"
"ds_read_b64_tr_b16 v[204:205], v239 offset:12424\n"
"v_mfma_f32_16x16x32_bf16 v[40:43], v[212:215], a[148:151], v[40:43]\n"
"v_mfma_f32_16x16x32_bf16 v[44:47], v[208:211], a[152:155], v[44:47]\n"
"ds_read_b64_tr_b16 v[206:207], v239 offset:13960\n"
"v_mfma_f32_16x16x32_bf16 v[44:47], v[212:215], a[156:159], v[44:47]\n"
"v_mfma_f32_16x16x32_bf16 v[48:51], v[216:219], a[128:131], v[48:51]\n"
"ds_read_b64_tr_b16 v[208:209], v240 offset:128\n"
"v_mfma_f32_16x16x32_bf16 v[48:51], v[220:223], a[132:135], v[48:51]\n"
"v_mfma_f32_16x16x32_bf16 v[52:55], v[216:219], a[136:139], v[52:55]\n"
"ds_read_b64_tr_b16 v[210:211], v240 offset:1664\n"
"v_mfma_f32_16x16x32_bf16 v[52:55], v[220:223], a[140:143], v[52:55]\n"
"v_mfma_f32_16x16x32_bf16 v[56:59], v[216:219], a[144:147], v[56:59]\n"
"ds_read_b64_tr_b16 v[212:213], v240 offset:12416\n"
"v_mfma_f32_16x16x32_bf16 v[56:59], v[220:223], a[148:151], v[56:59]\n"
"v_mfma_f32_16x16x32_bf16 v[60:63], v[216:219], a[152:155], v[60:63]\n"
"ds_read_b64_tr_b16 v[214:215], v240 offset:13952\n"
"v_mfma_f32_16x16x32_bf16 v[60:63], v[220:223], a[156:159], v[60:63]\n"
"v_mfma_f32_16x16x32_bf16 v[64:67], v[192:195], a[128:131], v[64:67]\n"
"ds_read_b64_tr_b16 v[216:217], v240 offset:136\n"
"s_waitcnt lgkmcnt(3)\n"
"v_mfma_f32_16x16x32_bf16 v[64:67], v[196:199], a[132:135], v[64:67]\n"
"v_mfma_f32_16x16x32_bf16 v[68:71], v[192:195], a[136:139], v[68:71]\n"
"ds_read_b64_tr_b16 v[218:219], v240 offset:1672\n"
"v_mfma_f32_16x16x32_bf16 v[68:71], v[196:199], a[140:143], v[68:71]\n"
"v_mfma_f32_16x16x32_bf16 v[72:75], v[192:195], a[144:147], v[72:75]\n"
"ds_read_b64_tr_b16 v[220:221], v240 offset:12424\n"
"v_mfma_f32_16x16x32_bf16 v[72:75], v[196:199], a[148:151], v[72:75]\n"
"v_mfma_f32_16x16x32_bf16 v[76:79], v[192:195], a[152:155], v[76:79]\n"
"ds_read_b64_tr_b16 v[222:223], v240 offset:13960\n"
"v_mfma_f32_16x16x32_bf16 v[76:79], v[196:199], a[156:159], v[76:79]\n"
"v_mfma_f32_16x16x32_bf16 v[80:83], v[200:203], a[128:131], v[80:83]\n"
"ds_read_b64_tr_b16 v[192:193], v239 offset:256\n"
"v_mfma_f32_16x16x32_bf16 v[80:83], v[204:207], a[132:135], v[80:83]\n"
"v_mfma_f32_16x16x32_bf16 v[84:87], v[200:203], a[136:139], v[84:87]\n"
"ds_read_b64_tr_b16 v[194:195], v239 offset:1792\n"
"v_mfma_f32_16x16x32_bf16 v[84:87], v[204:207], a[140:143], v[84:87]\n"
"v_mfma_f32_16x16x32_bf16 v[88:91], v[200:203], a[144:147], v[88:91]\n"
"ds_read_b64_tr_b16 v[196:197], v239 offset:12544\n"
"v_mfma_f32_16x16x32_bf16 v[88:91], v[204:207], a[148:151], v[88:91]\n"
"v_mfma_f32_16x16x32_bf16 v[92:95], v[200:203], a[152:155], v[92:95]\n"
"ds_read_b64_tr_b16 v[198:199], v239 offset:14080\n"
"v_mfma_f32_16x16x32_bf16 v[92:95], v[204:207], a[156:159], v[92:95]\n"
"v_mfma_f32_16x16x32_bf16 v[96:99], v[208:211], a[128:131], v[96:99]\n"
"ds_read_b64_tr_b16 v[200:201], v239 offset:264\n"
"s_waitcnt lgkmcnt(3)\n"
"v_mfma_f32_16x16x32_bf16 v[96:99], v[212:215], a[132:135], v[96:99]\n"
"v_mfma_f32_16x16x32_bf16 v[100:103], v[208:211], a[136:139], v[100:103]\n"
"ds_read_b64_tr_b16 v[202:203], v239 offset:1800\n"
"v_mfma_f32_16x16x32_bf16 v[100:103], v[212:215], a[140:143], v[100:103]\n"
"v_mfma_f32_16x16x32_bf16 v[104:107], v[208:211], a[144:147], v[104:107]\n"
"ds_read_b64_tr_b16 v[204:205], v239 offset:12552\n"
"v_mfma_f32_16x16x32_bf16 v[104:107], v[212:215], a[148:151], v[104:107]\n"
"v_mfma_f32_16x16x32_bf16 v[108:111], v[208:211], a[152:155], v[108:111]\n"
"ds_read_b64_tr_b16 v[206:207], v239 offset:14088\n"
"v_mfma_f32_16x16x32_bf16 v[108:111], v[212:215], a[156:159], v[108:111]\n"
"v_mfma_f32_16x16x32_bf16 v[112:115], v[216:219], a[128:131], v[112:115]\n"
"ds_read_b64_tr_b16 v[208:209], v240 offset:256\n"
"v_mfma_f32_16x16x32_bf16 v[112:115], v[220:223], a[132:135], v[112:115]\n"
"v_mfma_f32_16x16x32_bf16 v[116:119], v[216:219], a[136:139], v[116:119]\n"
"ds_read_b64_tr_b16 v[210:211], v240 offset:1792\n"
"v_mfma_f32_16x16x32_bf16 v[116:119], v[220:223], a[140:143], v[116:119]\n"
"v_mfma_f32_16x16x32_bf16 v[120:123], v[216:219], a[144:147], v[120:123]\n"
"ds_read_b64_tr_b16 v[212:213], v240 offset:12544\n"
"v_mfma_f32_16x16x32_bf16 v[120:123], v[220:223], a[148:151], v[120:123]\n"
"v_mfma_f32_16x16x32_bf16 v[124:127], v[216:219], a[152:155], v[124:127]\n"
"ds_read_b64_tr_b16 v[214:215], v240 offset:14080\n"
"v_mfma_f32_16x16x32_bf16 v[124:127], v[220:223], a[156:159], v[124:127]\n"
"v_mfma_f32_16x16x32_bf16 v[128:131], v[192:195], a[128:131], v[128:131]\n"
"ds_read_b64_tr_b16 v[216:217], v240 offset:264\n"
"s_waitcnt lgkmcnt(3)\n"
"v_mfma_f32_16x16x32_bf16 v[128:131], v[196:199], a[132:135], v[128:131]\n"
"v_mfma_f32_16x16x32_bf16 v[132:135], v[192:195], a[136:139], v[132:135]\n"
"ds_read_b64_tr_b16 v[218:219], v240 offset:1800\n"
"v_mfma_f32_16x16x32_bf16 v[132:135], v[196:199], a[140:143], v[132:135]\n"
"v_mfma_f32_16x16x32_bf16 v[136:139], v[192:195], a[144:147], v[136:139]\n"
"ds_read_b64_tr_b16 v[220:221], v240 offset:12552\n"
"v_mfma_f32_16x16x32_bf16 v[136:139], v[196:199], a[148:151], v[136:139]\n"
"v_mfma_f32_16x16x32_bf16 v[140:143], v[192:195], a[152:155], v[140:143]\n"
"ds_read_b64_tr_b16 v[222:223], v240 offset:14088\n"
"v_mfma_f32_16x16x32_bf16 v[140:143], v[196:199], a[156:159], v[140:143]\n"
"v_mfma_f32_16x16x32_bf16 v[144:147], v[200:203], a[128:131], v[144:147]\n"
"v_mfma_f32_16x16x32_bf16 v[144:147], v[204:207], a[132:135], v[144:147]\n"
"v_mfma_f32_16x16x32_bf16 v[148:151], v[200:203], a[136:139], v[148:151]\n"
"v_mfma_f32_16x16x32_bf16 v[148:151], v[204:207], a[140:143], v[148:151]\n"
"v_mfma_f32_16x16x32_bf16 v[152:155], v[200:203], a[144:147], v[152:155]\n"
"v_mfma_f32_16x16x32_bf16 v[152:155], v[204:207], a[148:151], v[152:155]\n"
"v_mfma_f32_16x16x32_bf16 v[156:159], v[200:203], a[152:155], v[156:159]\n"
"v_mfma_f32_16x16x32_bf16 v[156:159], v[204:207], a[156:159], v[156:159]\n"
"v_mfma_f32_16x16x32_bf16 v[160:163], v[208:211], a[128:131], v[160:163]\n"
"s_waitcnt lgkmcnt(0)\n"
"v_mfma_f32_16x16x32_bf16 v[160:163], v[212:215], a[132:135], v[160:163]\n"
"v_mfma_f32_16x16x32_bf16 v[164:167], v[208:211], a[136:139], v[164:167]\n"
"v_mfma_f32_16x16x32_bf16 v[164:167], v[212:215], a[140:143], v[164:167]\n"
"v_mfma_f32_16x16x32_bf16 v[168:171], v[208:211], a[144:147], v[168:171]\n"
"v_mfma_f32_16x16x32_bf16 v[168:171], v[212:215], a[148:151], v[168:171]\n"
"v_mfma_f32_16x16x32_bf16 v[172:175], v[208:211], a[152:155], v[172:175]\n"
"v_mfma_f32_16x16x32_bf16 v[172:175], v[212:215], a[156:159], v[172:175]\n"
"v_mfma_f32_16x16x32_bf16 v[176:179], v[216:219], a[128:131], v[176:179]\n"
"v_mfma_f32_16x16x32_bf16 v[176:179], v[220:223], a[132:135], v[176:179]\n"
"v_mfma_f32_16x16x32_bf16 v[180:183], v[216:219], a[136:139], v[180:183]\n"
"v_mfma_f32_16x16x32_bf16 v[180:183], v[220:223], a[140:143], v[180:183]\n"
"v_mfma_f32_16x16x32_bf16 v[184:187], v[216:219], a[144:147], v[184:187]\n"
"v_mfma_f32_16x16x32_bf16 v[184:187], v[220:223], a[148:151], v[184:187]\n"
"v_mfma_f32_16x16x32_bf16 v[188:191], v[216:219], a[152:155], v[188:191]\n"
"v_mfma_f32_16x16x32_bf16 v[188:191], v[220:223], a[156:159], v[188:191]\n"
"s_barrier\n"
"v_add_u32 v192, 0, v255\n"
"v_mul_u32_u24 v193, 3277, v192\n"
"v_lshrrev_b32 v193, 16, v193\n"
"v_mul_u32_u24 v194, 20, v193\n"
"v_sub_u32 v194, v192, v194\n"
"v_bfe_u32 v195, v193, 2, 1\n"
"v_lshlrev_b32 v195, 1, v195\n"
"v_xor_b32 v194, v194, v195\n"
"v_mul_u32_u24 v193, 1280, v193\n"
"v_lshl_add_u32 v193, v194, 4, v193\n"
"v_mov_b32 v244, v193\n"
"v_add_u32 v192, 64, v255\n"
"v_mul_u32_u24 v193, 3277, v192\n"
"v_lshrrev_b32 v193, 16, v193\n"
"v_mul_u32_u24 v194, 20, v193\n"
"v_sub_u32 v194, v192, v194\n"
"v_bfe_u32 v195, v193, 2, 1\n"
"v_lshlrev_b32 v195, 1, v195\n"
"v_xor_b32 v194, v194, v195\n"
"v_mul_u32_u24 v193, 1280, v193\n"
"v_lshl_add_u32 v193, v194, 4, v193\n"
"v_subrev_u32 v245, 1024, v193\n"
"v_add_u32 v192, 128, v255\n"
"v_mul_u32_u24 v193, 3277, v192\n"
"v_lshrrev_b32 v193, 16, v193\n"
"v_mul_u32_u24 v194, 20, v193\n"
"v_sub_u32 v194, v192, v194\n"
"v_bfe_u32 v195, v193, 2, 1\n"
"v_lshlrev_b32 v195, 1, v195\n"
"v_xor_b32 v194, v194, v195\n"
"v_mul_u32_u24 v193, 1280, v193\n"
"v_lshl_add_u32 v193, v194, 4, v193\n"
"v_subrev_u32 v246, 2048, v193\n"
"v_add_u32 v192, 192, v255\n"
"v_mul_u32_u24 v193, 3277, v192\n"
"v_lshrrev_b32 v193, 16, v193\n"
"v_mul_u32_u24 v194, 20, v193\n"
"v_sub_u32 v194, v192, v194\n"
"v_bfe_u32 v195, v193, 2, 1\n"
"v_lshlrev_b32 v195, 1, v195\n"
"v_xor_b32 v194, v194, v195\n"
"v_mul_u32_u24 v193, 1280, v193\n"
"v_lshl_add_u32 v193, v194, 4, v193\n"
"v_subrev_u32 v247, 3072, v193\n"
"v_add_u32 v192, 256, v255\n"
"v_mul_u32_u24 v193, 3277, v192\n"
"v_lshrrev_b32 v193, 16, v193\n"
"v_mul_u32_u24 v194, 20, v193\n"
"v_sub_u32 v194, v192, v194\n"
"v_bfe_u32 v195, v193, 2, 1\n"
"v_lshlrev_b32 v195, 1, v195\n"
"v_xor_b32 v194, v194, v195\n"
"v_mul_u32_u24 v193, 1280, v193\n"
"v_lshl_add_u32 v193, v194, 4, v193\n"
"v_mov_b32 v248, v193\n"
"s_mov_b32 s64, s44\n"
"s_mov_b32 s65, s45\n"
"s_and_b32 s65, s65, 0xffff\n"
"s_mov_b32 s66, 7864320\n"
"s_mov_b32 s67, 0x20000\n"
"s_mov_b32 s68, s50\n"
"s_mov_b32 s69, s51\n"
"s_and_b32 s69, s69, 0xffff\n"
"s_mov_b32 s70, 1966080\n"
"s_mov_b32 s71, 0x20000\n"
"s_mul_i32 s86, s22, 5120\n"
"s_lshr_b32 s25, s23, 1\n"
"s_mul_i32 s25, s25, 384\n"
"s_lshl_b32 s26, s22, 4\n"
"s_add_u32 s25, s25, s26\n"
"s_mul_i32 s25, s25, 1280\n"
"s_mul_i32 s27, s24, 320\n"
"s_add_u32 s88, s25, s27\n"
"s_nop 7\n"
"s_mov_b32 s90, 0xbfb8aa3b\n"
"s_mov_b32 s91, 0xbfb8aa3b\n"
"s_mov_b32 s92, 1.0\n"
"s_mov_b32 s93, 1.0\n"
"s_lshl_b32 s25, s21, 4\n"
"s_lshl_b32 s26, s22, 2\n"
"s_add_u32 s25, s25, s26\n"
"s_mul_i32 s25, s25, 49152\n"
"s_add_u32 s25, s25, 2686976\n"
"s_mul_i32 s26, s20, 3\n"
"s_lshr_b32 s28, s26, 1\n"
"s_lshl_b32 s28, s28, 10\n"
"s_add_u32 s94, s28, s25\n"
"s_and_b32 s95, s20, 1\n"
"v_lshrrev_b32 v250, 1, v249\n"
"v_accvgpr_read_b32 v192, a160\n"
"v_accvgpr_read_b32 v193, a161\n"
"v_accvgpr_read_b32 v194, a162\n"
"v_accvgpr_read_b32 v195, a163\n"
"v_accvgpr_read_b32 v200, a208\n"
"v_accvgpr_read_b32 v201, a209\n"
"v_accvgpr_read_b32 v202, a210\n"
"v_accvgpr_read_b32 v203, a211\n"
"v_pk_mul_f32 v[196:197], v[192:193], s[90:91]\n"
"v_pk_mul_f32 v[198:199], v[194:195], s[90:91]\n"
"v_exp_f32 v196, v196\n"
"v_exp_f32 v197, v197\n"
"v_exp_f32 v198, v198\n"
"v_exp_f32 v199, v199\n"
"v_pk_mul_f32 v[192:193], v[192:193], v[200:201]\n"
"v_pk_mul_f32 v[194:195], v[194:195], v[202:203]\n"
"v_pk_add_f32 v[196:197], v[196:197], s[92:93]\n"
"v_pk_add_f32 v[198:199], v[198:199], s[92:93]\n"
"v_rcp_f32 v196, v196\n"
"v_rcp_f32 v197, v197\n"
"v_rcp_f32 v198, v198\n"
"v_rcp_f32 v199, v199\n"
"s_nop 0\n"
"v_pk_mul_f32 v[192:193], v[192:193], v[196:197]\n"
"v_pk_mul_f32 v[194:195], v[194:195], v[198:199]\n"
"v_cvt_pk_bf16_f32 v236, v192, v193\n"
"v_cvt_pk_bf16_f32 v237, v194, v195\n"
"s_add_u32 m0, s86, 98304\n"
"s_nop 0\n"
"buffer_load_dwordx4 v244, s[68:71], s88 offen offset:0 lds\n"
"v_accvgpr_read_b32 v208, a176\n"
"v_accvgpr_read_b32 v209, a177\n"
"v_accvgpr_read_b32 v210, a178\n"
"v_accvgpr_read_b32 v211, a179\n"
"v_accvgpr_read_b32 v216, a224\n"
"v_accvgpr_read_b32 v217, a225\n"
"v_accvgpr_read_b32 v218, a226\n"
"v_accvgpr_read_b32 v219, a227\n"
"v_pk_mul_f32 v[212:213], v[208:209], s[90:91]\n"
"v_pk_mul_f32 v[214:215], v[210:211], s[90:91]\n"
"v_exp_f32 v212, v212\n"
"v_exp_f32 v213, v213\n"
"v_exp_f32 v214, v214\n"
"v_exp_f32 v215, v215\n"
"v_pk_mul_f32 v[208:209], v[208:209], v[216:217]\n"
"v_pk_mul_f32 v[210:211], v[210:211], v[218:219]\n"
"v_pk_add_f32 v[212:213], v[212:213], s[92:93]\n"
"v_pk_add_f32 v[214:215], v[214:215], s[92:93]\n"
"v_rcp_f32 v212, v212\n"
"v_rcp_f32 v213, v213\n"
"v_rcp_f32 v214, v214\n"
"v_rcp_f32 v215, v215\n"
"s_nop 0\n"
"v_pk_mul_f32 v[208:209], v[208:209], v[212:213]\n"
"v_pk_mul_f32 v[210:211], v[210:211], v[214:215]\n"
"v_cvt_pk_bf16_f32 v238, v208, v209\n"
"v_cvt_pk_bf16_f32 v239, v210, v211\n"
"buffer_load_dwordx4 v245, s[68:71], s88 offen offset:1024 lds\n"
"v_accvgpr_read_b32 v192, a192\n"
"v_accvgpr_read_b32 v193, a193\n"
"v_accvgpr_read_b32 v194, a194\n"
"v_accvgpr_read_b32 v195, a195\n"
"v_accvgpr_read_b32 v200, a240\n"
"v_accvgpr_read_b32 v201, a241\n"
"v_accvgpr_read_b32 v202, a242\n"
"v_accvgpr_read_b32 v203, a243\n"
"v_pk_mul_f32 v[196:197], v[192:193], s[90:91]\n"
"v_pk_mul_f32 v[198:199], v[194:195], s[90:91]\n"
"v_exp_f32 v196, v196\n"
"v_exp_f32 v197, v197\n"
"v_exp_f32 v198, v198\n"
"v_exp_f32 v199, v199\n"
"v_pk_mul_f32 v[192:193], v[192:193], v[200:201]\n"
"v_pk_mul_f32 v[194:195], v[194:195], v[202:203]\n"
"v_pk_add_f32 v[196:197], v[196:197], s[92:93]\n"
"v_pk_add_f32 v[198:199], v[198:199], s[92:93]\n"
"v_rcp_f32 v196, v196\n"
"v_rcp_f32 v197, v197\n"
"v_rcp_f32 v198, v198\n"
"v_rcp_f32 v199, v199\n"
"s_nop 0\n"
"v_pk_mul_f32 v[192:193], v[192:193], v[196:197]\n"
"v_pk_mul_f32 v[194:195], v[194:195], v[198:199]\n"
"v_cvt_pk_bf16_f32 v240, v192, v193\n"
"v_cvt_pk_bf16_f32 v241, v194, v195\n"
"buffer_load_dwordx4 v246, s[68:71], s88 offen offset:2048 lds\n"
"s_cmp_lg_u32 s95, 0\n"
"s_cbranch_scc1 HODD0_%=\n"
"buffer_store_dwordx4 v[236:239], v249, s[72:75], s94 offen sc1\n"
"buffer_store_dwordx2 v[240:241], v250, s[72:75], s94 offen offset:1024 sc1\n"
"s_branch HDONE0_%=\n"
"HODD0_%=:\n"
"buffer_store_dwordx2 v[236:237], v250, s[72:75], s94 offen offset:512 sc1\n"
"buffer_store_dwordx4 v[238:241], v249, s[72:75], s94 offen offset:1024 sc1\n"
"s_nop 1\n"
"HDONE0_%=:\n"
"s_add_u32 s94, s94, 49152\n"
"v_accvgpr_read_b32 v208, a164\n"
"v_accvgpr_read_b32 v209, a165\n"
"v_accvgpr_read_b32 v210, a166\n"
"v_accvgpr_read_b32 v211, a167\n"
"v_accvgpr_read_b32 v216, a212\n"
"v_accvgpr_read_b32 v217, a213\n"
"v_accvgpr_read_b32 v218, a214\n"
"v_accvgpr_read_b32 v219, a215\n"
"v_pk_mul_f32 v[212:213], v[208:209], s[90:91]\n"
"v_pk_mul_f32 v[214:215], v[210:211], s[90:91]\n"
"v_exp_f32 v212, v212\n"
"v_exp_f32 v213, v213\n"
"v_exp_f32 v214, v214\n"
"v_exp_f32 v215, v215\n"
"v_pk_mul_f32 v[208:209], v[208:209], v[216:217]\n"
"v_pk_mul_f32 v[210:211], v[210:211], v[218:219]\n"
"v_pk_add_f32 v[212:213], v[212:213], s[92:93]\n"
"v_pk_add_f32 v[214:215], v[214:215], s[92:93]\n"
"v_rcp_f32 v212, v212\n"
"v_rcp_f32 v213, v213\n"
"v_rcp_f32 v214, v214\n"
"v_rcp_f32 v215, v215\n"
"s_nop 0\n"
"v_pk_mul_f32 v[208:209], v[208:209], v[212:213]\n"
"v_pk_mul_f32 v[210:211], v[210:211], v[214:215]\n"
"v_cvt_pk_bf16_f32 v236, v208, v209\n"
"v_cvt_pk_bf16_f32 v237, v210, v211\n"
"buffer_load_dwordx4 v247, s[68:71], s88 offen offset:3072 lds\n"
"v_accvgpr_read_b32 v192, a180\n"
"v_accvgpr_read_b32 v193, a181\n"
"v_accvgpr_read_b32 v194, a182\n"
"v_accvgpr_read_b32 v195, a183\n"
"v_accvgpr_read_b32 v200, a228\n"
"v_accvgpr_read_b32 v201, a229\n"
"v_accvgpr_read_b32 v202, a230\n"
"v_accvgpr_read_b32 v203, a231\n"
"v_pk_mul_f32 v[196:197], v[192:193], s[90:91]\n"
"v_pk_mul_f32 v[198:199], v[194:195], s[90:91]\n"
"v_exp_f32 v196, v196\n"
"v_exp_f32 v197, v197\n"
"v_exp_f32 v198, v198\n"
"v_exp_f32 v199, v199\n"
"v_pk_mul_f32 v[192:193], v[192:193], v[200:201]\n"
"v_pk_mul_f32 v[194:195], v[194:195], v[202:203]\n"
"v_pk_add_f32 v[196:197], v[196:197], s[92:93]\n"
"v_pk_add_f32 v[198:199], v[198:199], s[92:93]\n"
"v_rcp_f32 v196, v196\n"
"v_rcp_f32 v197, v197\n"
"v_rcp_f32 v198, v198\n"
"v_rcp_f32 v199, v199\n"
"s_nop 0\n"
"v_pk_mul_f32 v[192:193], v[192:193], v[196:197]\n"
"v_pk_mul_f32 v[194:195], v[194:195], v[198:199]\n"
"v_cvt_pk_bf16_f32 v238, v192, v193\n"
"v_cvt_pk_bf16_f32 v239, v194, v195\n"
"s_add_u32 m0, s86, 102400\n"
"s_nop 0\n"
"buffer_load_dwordx4 v248, s[68:71], s88 offen lds\n"
"v_accvgpr_read_b32 v208, a196\n"
"v_accvgpr_read_b32 v209, a197\n"
"v_accvgpr_read_b32 v210, a198\n"
"v_accvgpr_read_b32 v211, a199\n"
"v_accvgpr_read_b32 v216, a244\n"
"v_accvgpr_read_b32 v217, a245\n"
"v_accvgpr_read_b32 v218, a246\n"
"v_accvgpr_read_b32 v219, a247\n"
"v_pk_mul_f32 v[212:213], v[208:209], s[90:91]\n"
"v_pk_mul_f32 v[214:215], v[210:211], s[90:91]\n"
"v_exp_f32 v212, v212\n"
"v_exp_f32 v213, v213\n"
"v_exp_f32 v214, v214\n"
"v_exp_f32 v215, v215\n"
"v_pk_mul_f32 v[208:209], v[208:209], v[216:217]\n"
"v_pk_mul_f32 v[210:211], v[210:211], v[218:219]\n"
"v_pk_add_f32 v[212:213], v[212:213], s[92:93]\n"
"v_pk_add_f32 v[214:215], v[214:215], s[92:93]\n"
"v_rcp_f32 v212, v212\n"
"v_rcp_f32 v213, v213\n"
"v_rcp_f32 v214, v214\n"
"v_rcp_f32 v215, v215\n"
"s_nop 0\n"
"v_pk_mul_f32 v[208:209], v[208:209], v[212:213]\n"
"v_pk_mul_f32 v[210:211], v[210:211], v[214:215]\n"
"v_cvt_pk_bf16_f32 v240, v208, v209\n"
"v_cvt_pk_bf16_f32 v241, v210, v211\n"
"s_add_u32 s88, s88, 81920\n"
"s_add_u32 m0, s86, 118784\n"
"s_nop 0\n"
"buffer_load_dwordx4 v244, s[68:71], s88 offen offset:0 lds\n"
"s_cmp_lg_u32 s95, 0\n"
"s_cbranch_scc1 HODD1_%=\n"
"buffer_store_dwordx4 v[236:239], v249, s[72:75], s94 offen sc1\n"
"buffer_store_dwordx2 v[240:241], v250, s[72:75], s94 offen offset:1024 sc1\n"
"s_branch HDONE1_%=\n"
"HODD1_%=:\n"
"buffer_store_dwordx2 v[236:237], v250, s[72:75], s94 offen offset:512 sc1\n"
"buffer_store_dwordx4 v[238:241], v249, s[72:75], s94 offen offset:1024 sc1\n"
"s_nop 1\n"
"HDONE1_%=:\n"
"s_add_u32 s94, s94, 49152\n"
"v_accvgpr_read_b32 v192, a168\n"
"v_accvgpr_read_b32 v193, a169\n"
"v_accvgpr_read_b32 v194, a170\n"
"v_accvgpr_read_b32 v195, a171\n"
"v_accvgpr_read_b32 v200, a216\n"
"v_accvgpr_read_b32 v201, a217\n"
"v_accvgpr_read_b32 v202, a218\n"
"v_accvgpr_read_b32 v203, a219\n"
"v_pk_mul_f32 v[196:197], v[192:193], s[90:91]\n"
"v_pk_mul_f32 v[198:199], v[194:195], s[90:91]\n"
"v_exp_f32 v196, v196\n"
"v_exp_f32 v197, v197\n"
"v_exp_f32 v198, v198\n"
"v_exp_f32 v199, v199\n"
"v_pk_mul_f32 v[192:193], v[192:193], v[200:201]\n"
"v_pk_mul_f32 v[194:195], v[194:195], v[202:203]\n"
"v_pk_add_f32 v[196:197], v[196:197], s[92:93]\n"
"v_pk_add_f32 v[198:199], v[198:199], s[92:93]\n"
"v_rcp_f32 v196, v196\n"
"v_rcp_f32 v197, v197\n"
"v_rcp_f32 v198, v198\n"
"v_rcp_f32 v199, v199\n"
"s_nop 0\n"
"v_pk_mul_f32 v[192:193], v[192:193], v[196:197]\n"
"v_pk_mul_f32 v[194:195], v[194:195], v[198:199]\n"
"v_cvt_pk_bf16_f32 v236, v192, v193\n"
"v_cvt_pk_bf16_f32 v237, v194, v195\n"
"buffer_load_dwordx4 v245, s[68:71], s88 offen offset:1024 lds\n"
"v_accvgpr_read_b32 v208, a184\n"
"v_accvgpr_read_b32 v209, a185\n"
"v_accvgpr_read_b32 v210, a186\n"
"v_accvgpr_read_b32 v211, a187\n"
"v_accvgpr_read_b32 v216, a232\n"
"v_accvgpr_read_b32 v217, a233\n"
"v_accvgpr_read_b32 v218, a234\n"
"v_accvgpr_read_b32 v219, a235\n"
"v_pk_mul_f32 v[212:213], v[208:209], s[90:91]\n"
"v_pk_mul_f32 v[214:215], v[210:211], s[90:91]\n"
"v_exp_f32 v212, v212\n"
"v_exp_f32 v213, v213\n"
"v_exp_f32 v214, v214\n"
"v_exp_f32 v215, v215\n"
"v_pk_mul_f32 v[208:209], v[208:209], v[216:217]\n"
"v_pk_mul_f32 v[210:211], v[210:211], v[218:219]\n"
"v_pk_add_f32 v[212:213], v[212:213], s[92:93]\n"
"v_pk_add_f32 v[214:215], v[214:215], s[92:93]\n"
"v_rcp_f32 v212, v212\n"
"v_rcp_f32 v213, v213\n"
"v_rcp_f32 v214, v214\n"
"v_rcp_f32 v215, v215\n"
"s_nop 0\n"
"v_pk_mul_f32 v[208:209], v[208:209], v[212:213]\n"
"v_pk_mul_f32 v[210:211], v[210:211], v[214:215]\n"
"v_cvt_pk_bf16_f32 v238, v208, v209\n"
"v_cvt_pk_bf16_f32 v239, v210, v211\n"
"buffer_load_dwordx4 v246, s[68:71], s88 offen offset:2048 lds\n"
"v_accvgpr_read_b32 v192, a200\n"
"v_accvgpr_read_b32 v193, a201\n"
"v_accvgpr_read_b32 v194, a202\n"
"v_accvgpr_read_b32 v195, a203\n"
"v_accvgpr_read_b32 v200, a248\n"
"v_accvgpr_read_b32 v201, a249\n"
"v_accvgpr_read_b32 v202, a250\n"
"v_accvgpr_read_b32 v203, a251\n"
"v_pk_mul_f32 v[196:197], v[192:193], s[90:91]\n"
"v_pk_mul_f32 v[198:199], v[194:195], s[90:91]\n"
"v_exp_f32 v196, v196\n"
"v_exp_f32 v197, v197\n"
"v_exp_f32 v198, v198\n"
"v_exp_f32 v199, v199\n"
"v_pk_mul_f32 v[192:193], v[192:193], v[200:201]\n"
"v_pk_mul_f32 v[194:195], v[194:195], v[202:203]\n"
"v_pk_add_f32 v[196:197], v[196:197], s[92:93]\n"
"v_pk_add_f32 v[198:199], v[198:199], s[92:93]\n"
"v_rcp_f32 v196, v196\n"
"v_rcp_f32 v197, v197\n"
"v_rcp_f32 v198, v198\n"
"v_rcp_f32 v199, v199\n"
"s_nop 0\n"
"v_pk_mul_f32 v[192:193], v[192:193], v[196:197]\n"
"v_pk_mul_f32 v[194:195], v[194:195], v[198:199]\n"
"v_cvt_pk_bf16_f32 v240, v192, v193\n"
"v_cvt_pk_bf16_f32 v241, v194, v195\n"
"buffer_load_dwordx4 v247, s[68:71], s88 offen offset:3072 lds\n"
"s_cmp_lg_u32 s95, 0\n"
"s_cbranch_scc1 HODD2_%=\n"
"buffer_store_dwordx4 v[236:239], v249, s[72:75], s94 offen sc1\n"
"buffer_store_dwordx2 v[240:241], v250, s[72:75], s94 offen offset:1024 sc1\n"
"s_branch HDONE2_%=\n"
"HODD2_%=:\n"
"buffer_store_dwordx2 v[236:237], v250, s[72:75], s94 offen offset:512 sc1\n"
"buffer_store_dwordx4 v[238:241], v249, s[72:75], s94 offen offset:1024 sc1\n"
"s_nop 1\n"
"HDONE2_%=:\n"
"s_add_u32 s94, s94, 49152\n"
"v_accvgpr_read_b32 v208, a172\n"
"v_accvgpr_read_b32 v209, a173\n"
"v_accvgpr_read_b32 v210, a174\n"
"v_accvgpr_read_b32 v211, a175\n"
"v_accvgpr_read_b32 v216, a220\n"
"v_accvgpr_read_b32 v217, a221\n"
"v_accvgpr_read_b32 v218, a222\n"
"v_accvgpr_read_b32 v219, a223\n"
"v_pk_mul_f32 v[212:213], v[208:209], s[90:91]\n"
"v_pk_mul_f32 v[214:215], v[210:211], s[90:91]\n"
"v_exp_f32 v212, v212\n"
"v_exp_f32 v213, v213\n"
"v_exp_f32 v214, v214\n"
"v_exp_f32 v215, v215\n"
"v_pk_mul_f32 v[208:209], v[208:209], v[216:217]\n"
"v_pk_mul_f32 v[210:211], v[210:211], v[218:219]\n"
"v_pk_add_f32 v[212:213], v[212:213], s[92:93]\n"
"v_pk_add_f32 v[214:215], v[214:215], s[92:93]\n"
"v_rcp_f32 v212, v212\n"
"v_rcp_f32 v213, v213\n"
"v_rcp_f32 v214, v214\n"
"v_rcp_f32 v215, v215\n"
"s_nop 0\n"
"v_pk_mul_f32 v[208:209], v[208:209], v[212:213]\n"
"v_pk_mul_f32 v[210:211], v[210:211], v[214:215]\n"
"v_cvt_pk_bf16_f32 v236, v208, v209\n"
"v_cvt_pk_bf16_f32 v237, v210, v211\n"
"s_add_u32 m0, s86, 122880\n"
"s_nop 0\n"
"buffer_load_dwordx4 v248, s[68:71], s88 offen lds\n"
"v_accvgpr_read_b32 v192, a188\n"
"v_accvgpr_read_b32 v193, a189\n"
"v_accvgpr_read_b32 v194, a190\n"
"v_accvgpr_read_b32 v195, a191\n"
"v_accvgpr_read_b32 v200, a236\n"
"v_accvgpr_read_b32 v201, a237\n"
"v_accvgpr_read_b32 v202, a238\n"
"v_accvgpr_read_b32 v203, a239\n"
"v_pk_mul_f32 v[196:197], v[192:193], s[90:91]\n"
"v_pk_mul_f32 v[198:199], v[194:195], s[90:91]\n"
"v_exp_f32 v196, v196\n"
"v_exp_f32 v197, v197\n"
"v_exp_f32 v198, v198\n"
"v_exp_f32 v199, v199\n"
"v_pk_mul_f32 v[192:193], v[192:193], v[200:201]\n"
"v_pk_mul_f32 v[194:195], v[194:195], v[202:203]\n"
"v_pk_add_f32 v[196:197], v[196:197], s[92:93]\n"
"v_pk_add_f32 v[198:199], v[198:199], s[92:93]\n"
"v_rcp_f32 v196, v196\n"
"v_rcp_f32 v197, v197\n"
"v_rcp_f32 v198, v198\n"
"v_rcp_f32 v199, v199\n"
"s_nop 0\n"
"v_pk_mul_f32 v[192:193], v[192:193], v[196:197]\n"
"v_pk_mul_f32 v[194:195], v[194:195], v[198:199]\n"
"v_cvt_pk_bf16_f32 v238, v192, v193\n"
"v_cvt_pk_bf16_f32 v239, v194, v195\n"
"s_add_u32 s88, s88, 81920\n"
"s_add_u32 m0, s86, 139264\n"
"s_nop 0\n"
"buffer_load_dwordx4 v244, s[68:71], s88 offen offset:0 lds\n"
"v_accvgpr_read_b32 v208, a204\n"
"v_accvgpr_read_b32 v209, a205\n"
"v_accvgpr_read_b32 v210, a206\n"
"v_accvgpr_read_b32 v211, a207\n"
"v_accvgpr_read_b32 v216, a252\n"
"v_accvgpr_read_b32 v217, a253\n"
"v_accvgpr_read_b32 v218, a254\n"
"v_accvgpr_read_b32 v219, a255\n"
"v_pk_mul_f32 v[212:213], v[208:209], s[90:91]\n"
"v_pk_mul_f32 v[214:215], v[210:211], s[90:91]\n"
"v_exp_f32 v212, v212\n"
"v_exp_f32 v213, v213\n"
"v_exp_f32 v214, v214\n"
"v_exp_f32 v215, v215\n"
"v_pk_mul_f32 v[208:209], v[208:209], v[216:217]\n"
"v_pk_mul_f32 v[210:211], v[210:211], v[218:219]\n"
"v_pk_add_f32 v[212:213], v[212:213], s[92:93]\n"
"v_pk_add_f32 v[214:215], v[214:215], s[92:93]\n"
"v_rcp_f32 v212, v212\n"
"v_rcp_f32 v213, v213\n"
"v_rcp_f32 v214, v214\n"
"v_rcp_f32 v215, v215\n"
"s_nop 0\n"
"v_pk_mul_f32 v[208:209], v[208:209], v[212:213]\n"
"v_pk_mul_f32 v[210:211], v[210:211], v[214:215]\n"
"v_cvt_pk_bf16_f32 v240, v208, v209\n"
"v_cvt_pk_bf16_f32 v241, v210, v211\n"
"buffer_load_dwordx4 v245, s[68:71], s88 offen offset:1024 lds\n"
"s_cmp_lg_u32 s95, 0\n"
"s_cbranch_scc1 HODD3_%=\n"
"buffer_store_dwordx4 v[236:239], v249, s[72:75], s94 offen sc1\n"
"buffer_store_dwordx2 v[240:241], v250, s[72:75], s94 offen offset:1024 sc1\n"
"s_branch HDONE3_%=\n"
"HODD3_%=:\n"
"buffer_store_dwordx2 v[236:237], v250, s[72:75], s94 offen offset:512 sc1\n"
"buffer_store_dwordx4 v[238:241], v249, s[72:75], s94 offen offset:1024 sc1\n"
"s_nop 1\n"
"HDONE3_%=:\n"
"s_add_u32 s94, s94, 49152\n"
"buffer_load_dwordx4 v246, s[68:71], s88 offen offset:2048 lds\n"
"buffer_load_dwordx4 v247, s[68:71], s88 offen offset:3072 lds\n"
"s_add_u32 m0, s86, 143360\n"
"s_nop 0\n"
"buffer_load_dwordx4 v248, s[68:71], s88 offen lds\n"
"s_mov_b32 s90, 0x3dd53b95\n"
"s_mov_b32 s91, 0x3dd53b95\n"
"v_pk_mul_f32 v[0:1], v[0:1], s[90:91]\n"
"v_pk_mul_f32 v[2:3], v[2:3], s[90:91]\n"
"v_pk_mul_f32 v[4:5], v[4:5], s[90:91]\n"
"v_pk_mul_f32 v[6:7], v[6:7], s[90:91]\n"
"v_pk_mul_f32 v[8:9], v[8:9], s[90:91]\n"
"v_pk_mul_f32 v[10:11], v[10:11], s[90:91]\n"
"v_pk_mul_f32 v[12:13], v[12:13], s[90:91]\n"
"v_pk_mul_f32 v[14:15], v[14:15], s[90:91]\n"
"v_pk_mul_f32 v[16:17], v[16:17], s[90:91]\n"
"v_pk_mul_f32 v[18:19], v[18:19], s[90:91]\n"
"v_pk_mul_f32 v[20:21], v[20:21], s[90:91]\n"
"v_pk_mul_f32 v[22:23], v[22:23], s[90:91]\n"
"v_pk_mul_f32 v[24:25], v[24:25], s[90:91]\n"
"v_pk_mul_f32 v[26:27], v[26:27], s[90:91]\n"
"v_pk_mul_f32 v[28:29], v[28:29], s[90:91]\n"
"v_pk_mul_f32 v[30:31], v[30:31], s[90:91]\n"
"v_cvt_pk_bf16_f32 v0, v0, v1\n"
"v_cvt_pk_bf16_f32 v1, v2, v3\n"
"v_cvt_pk_bf16_f32 v2, v16, v17\n"
"v_cvt_pk_bf16_f32 v3, v18, v19\n"
"v_cvt_pk_bf16_f32 v4, v4, v5\n"
"v_cvt_pk_bf16_f32 v5, v6, v7\n"
"v_cvt_pk_bf16_f32 v6, v20, v21\n"
"v_cvt_pk_bf16_f32 v7, v22, v23\n"
"v_cvt_pk_bf16_f32 v8, v8, v9\n"
"v_cvt_pk_bf16_f32 v9, v10, v11\n"
"v_cvt_pk_bf16_f32 v10, v24, v25\n"
"v_cvt_pk_bf16_f32 v11, v26, v27\n"
"v_cvt_pk_bf16_f32 v12, v12, v13\n"
"v_cvt_pk_bf16_f32 v13, v14, v15\n"
"v_cvt_pk_bf16_f32 v14, v28, v29\n"
"v_cvt_pk_bf16_f32 v15, v30, v31\n"
"v_pk_mul_f32 v[32:33], v[32:33], s[90:91]\n"
"v_pk_mul_f32 v[34:35], v[34:35], s[90:91]\n"
"v_pk_mul_f32 v[36:37], v[36:37], s[90:91]\n"
"v_pk_mul_f32 v[38:39], v[38:39], s[90:91]\n"
"v_pk_mul_f32 v[40:41], v[40:41], s[90:91]\n"
"v_pk_mul_f32 v[42:43], v[42:43], s[90:91]\n"
"v_pk_mul_f32 v[44:45], v[44:45], s[90:91]\n"
"v_pk_mul_f32 v[46:47], v[46:47], s[90:91]\n"
"v_pk_mul_f32 v[48:49], v[48:49], s[90:91]\n"
"v_pk_mul_f32 v[50:51], v[50:51], s[90:91]\n"
"v_pk_mul_f32 v[52:53], v[52:53], s[90:91]\n"
"v_pk_mul_f32 v[54:55], v[54:55], s[90:91]\n"
"v_pk_mul_f32 v[56:57], v[56:57], s[90:91]\n"
"v_pk_mul_f32 v[58:59], v[58:59], s[90:91]\n"
"v_pk_mul_f32 v[60:61], v[60:61], s[90:91]\n"
"v_pk_mul_f32 v[62:63], v[62:63], s[90:91]\n"
"v_cvt_pk_bf16_f32 v16, v32, v33\n"
"v_cvt_pk_bf16_f32 v17, v34, v35\n"
"v_cvt_pk_bf16_f32 v18, v48, v49\n"
"v_cvt_pk_bf16_f32 v19, v50, v51\n"
"v_cvt_pk_bf16_f32 v20, v36, v37\n"
"v_cvt_pk_bf16_f32 v21, v38, v39\n"
"v_cvt_pk_bf16_f32 v22, v52, v53\n"
"v_cvt_pk_bf16_f32 v23, v54, v55\n"
"v_cvt_pk_bf16_f32 v24, v40, v41\n"
"v_cvt_pk_bf16_f32 v25, v42, v43\n"
"v_cvt_pk_bf16_f32 v26, v56, v57\n"
"v_cvt_pk_bf16_f32 v27, v58, v59\n"
"v_cvt_pk_bf16_f32 v28, v44, v45\n"
"v_cvt_pk_bf16_f32 v29, v46, v47\n"
"v_cvt_pk_bf16_f32 v30, v60, v61\n"
"v_cvt_pk_bf16_f32 v31, v62, v63\n"
"v_pk_mul_f32 v[64:65], v[64:65], s[90:91]\n"
"v_pk_mul_f32 v[66:67], v[66:67], s[90:91]\n"
"v_pk_mul_f32 v[68:69], v[68:69], s[90:91]\n"
"v_pk_mul_f32 v[70:71], v[70:71], s[90:91]\n"
"v_pk_mul_f32 v[72:73], v[72:73], s[90:91]\n"
"v_pk_mul_f32 v[74:75], v[74:75], s[90:91]\n"
"v_pk_mul_f32 v[76:77], v[76:77], s[90:91]\n"
"v_pk_mul_f32 v[78:79], v[78:79], s[90:91]\n"
"v_pk_mul_f32 v[80:81], v[80:81], s[90:91]\n"
"v_pk_mul_f32 v[82:83], v[82:83], s[90:91]\n"
"v_pk_mul_f32 v[84:85], v[84:85], s[90:91]\n"
"v_pk_mul_f32 v[86:87], v[86:87], s[90:91]\n"
"v_pk_mul_f32 v[88:89], v[88:89], s[90:91]\n"
"v_pk_mul_f32 v[90:91], v[90:91], s[90:91]\n"
"v_pk_mul_f32 v[92:93], v[92:93], s[90:91]\n"
"v_pk_mul_f32 v[94:95], v[94:95], s[90:91]\n"
"v_cvt_pk_bf16_f32 v32, v64, v65\n"
"v_cvt_pk_bf16_f32 v33, v66, v67\n"
"v_cvt_pk_bf16_f32 v34, v80, v81\n"
"v_cvt_pk_bf16_f32 v35, v82, v83\n"
"v_cvt_pk_bf16_f32 v36, v68, v69\n"
"v_cvt_pk_bf16_f32 v37, v70, v71\n"
"v_cvt_pk_bf16_f32 v38, v84, v85\n"
"v_cvt_pk_bf16_f32 v39, v86, v87\n"
"v_cvt_pk_bf16_f32 v40, v72, v73\n"
"v_cvt_pk_bf16_f32 v41, v74, v75\n"
"v_cvt_pk_bf16_f32 v42, v88, v89\n"
"v_cvt_pk_bf16_f32 v43, v90, v91\n"
"v_cvt_pk_bf16_f32 v44, v76, v77\n"
"v_cvt_pk_bf16_f32 v45, v78, v79\n"
"v_cvt_pk_bf16_f32 v46, v92, v93\n"
"v_cvt_pk_bf16_f32 v47, v94, v95\n"
"v_pk_mul_f32 v[96:97], v[96:97], s[90:91]\n"
"v_pk_mul_f32 v[98:99], v[98:99], s[90:91]\n"
"v_pk_mul_f32 v[100:101], v[100:101], s[90:91]\n"
"v_pk_mul_f32 v[102:103], v[102:103], s[90:91]\n"
"v_pk_mul_f32 v[104:105], v[104:105], s[90:91]\n"
"v_pk_mul_f32 v[106:107], v[106:107], s[90:91]\n"
"v_pk_mul_f32 v[108:109], v[108:109], s[90:91]\n"
"v_pk_mul_f32 v[110:111], v[110:111], s[90:91]\n"
"v_pk_mul_f32 v[112:113], v[112:113], s[90:91]\n"
"v_pk_mul_f32 v[114:115], v[114:115], s[90:91]\n"
"v_pk_mul_f32 v[116:117], v[116:117], s[90:91]\n"
"v_pk_mul_f32 v[118:119], v[118:119], s[90:91]\n"
"v_pk_mul_f32 v[120:121], v[120:121], s[90:91]\n"
"v_pk_mul_f32 v[122:123], v[122:123], s[90:91]\n"
"v_pk_mul_f32 v[124:125], v[124:125], s[90:91]\n"
"v_pk_mul_f32 v[126:127], v[126:127], s[90:91]\n"
"v_cvt_pk_bf16_f32 v48, v96, v97\n"
"v_cvt_pk_bf16_f32 v49, v98, v99\n"
"v_cvt_pk_bf16_f32 v50, v112, v113\n"
"v_cvt_pk_bf16_f32 v51, v114, v115\n"
"v_cvt_pk_bf16_f32 v52, v100, v101\n"
"v_cvt_pk_bf16_f32 v53, v102, v103\n"
"v_cvt_pk_bf16_f32 v54, v116, v117\n"
"v_cvt_pk_bf16_f32 v55, v118, v119\n"
"v_cvt_pk_bf16_f32 v56, v104, v105\n"
"v_cvt_pk_bf16_f32 v57, v106, v107\n"
"v_cvt_pk_bf16_f32 v58, v120, v121\n"
"v_cvt_pk_bf16_f32 v59, v122, v123\n"
"v_cvt_pk_bf16_f32 v60, v108, v109\n"
"v_cvt_pk_bf16_f32 v61, v110, v111\n"
"v_cvt_pk_bf16_f32 v62, v124, v125\n"
"v_cvt_pk_bf16_f32 v63, v126, v127\n"
"v_pk_mul_f32 v[128:129], v[128:129], s[90:91]\n"
"v_pk_mul_f32 v[130:131], v[130:131], s[90:91]\n"
"v_pk_mul_f32 v[132:133], v[132:133], s[90:91]\n"
"v_pk_mul_f32 v[134:135], v[134:135], s[90:91]\n"
"v_pk_mul_f32 v[136:137], v[136:137], s[90:91]\n"
"v_pk_mul_f32 v[138:139], v[138:139], s[90:91]\n"
"v_pk_mul_f32 v[140:141], v[140:141], s[90:91]\n"
"v_pk_mul_f32 v[142:143], v[142:143], s[90:91]\n"
"v_pk_mul_f32 v[144:145], v[144:145], s[90:91]\n"
"v_pk_mul_f32 v[146:147], v[146:147], s[90:91]\n"
"v_pk_mul_f32 v[148:149], v[148:149], s[90:91]\n"
"v_pk_mul_f32 v[150:151], v[150:151], s[90:91]\n"
"v_pk_mul_f32 v[152:153], v[152:153], s[90:91]\n"
"v_pk_mul_f32 v[154:155], v[154:155], s[90:91]\n"
"v_pk_mul_f32 v[156:157], v[156:157], s[90:91]\n"
"v_pk_mul_f32 v[158:159], v[158:159], s[90:91]\n"
"v_cvt_pk_bf16_f32 v64, v128, v129\n"
"v_cvt_pk_bf16_f32 v65, v130, v131\n"
"v_cvt_pk_bf16_f32 v66, v144, v145\n"
"v_cvt_pk_bf16_f32 v67, v146, v147\n"
"v_cvt_pk_bf16_f32 v68, v132, v133\n"
"v_cvt_pk_bf16_f32 v69, v134, v135\n"
"v_cvt_pk_bf16_f32 v70, v148, v149\n"
"v_cvt_pk_bf16_f32 v71, v150, v151\n"
"v_cvt_pk_bf16_f32 v72, v136, v137\n"
"v_cvt_pk_bf16_f32 v73, v138, v139\n"
"v_cvt_pk_bf16_f32 v74, v152, v153\n"
"v_cvt_pk_bf16_f32 v75, v154, v155\n"
"v_cvt_pk_bf16_f32 v76, v140, v141\n"
"v_cvt_pk_bf16_f32 v77, v142, v143\n"
"v_cvt_pk_bf16_f32 v78, v156, v157\n"
"v_cvt_pk_bf16_f32 v79, v158, v159\n"
"v_pk_mul_f32 v[160:161], v[160:161], s[90:91]\n"
"v_pk_mul_f32 v[162:163], v[162:163], s[90:91]\n"
"v_pk_mul_f32 v[164:165], v[164:165], s[90:91]\n"
"v_pk_mul_f32 v[166:167], v[166:167], s[90:91]\n"
"v_pk_mul_f32 v[168:169], v[168:169], s[90:91]\n"
"v_pk_mul_f32 v[170:171], v[170:171], s[90:91]\n"
"v_pk_mul_f32 v[172:173], v[172:173], s[90:91]\n"
"v_pk_mul_f32 v[174:175], v[174:175], s[90:91]\n"
"v_pk_mul_f32 v[176:177], v[176:177], s[90:91]\n"
"v_pk_mul_f32 v[178:179], v[178:179], s[90:91]\n"
"v_pk_mul_f32 v[180:181], v[180:181], s[90:91]\n"
"v_pk_mul_f32 v[182:183], v[182:183], s[90:91]\n"
"v_pk_mul_f32 v[184:185], v[184:185], s[90:91]\n"
"v_pk_mul_f32 v[186:187], v[186:187], s[90:91]\n"
"v_pk_mul_f32 v[188:189], v[188:189], s[90:91]\n"
"v_pk_mul_f32 v[190:191], v[190:191], s[90:91]\n"
"v_cvt_pk_bf16_f32 v80, v160, v161\n"
"v_cvt_pk_bf16_f32 v81, v162, v163\n"
"v_cvt_pk_bf16_f32 v82, v176, v177\n"
"v_cvt_pk_bf16_f32 v83, v178, v179\n"
"v_cvt_pk_bf16_f32 v84, v164, v165\n"
"v_cvt_pk_bf16_f32 v85, v166, v167\n"
"v_cvt_pk_bf16_f32 v86, v180, v181\n"
"v_cvt_pk_bf16_f32 v87, v182, v183\n"
"v_cvt_pk_bf16_f32 v88, v168, v169\n"
"v_cvt_pk_bf16_f32 v89, v170, v171\n"
"v_cvt_pk_bf16_f32 v90, v184, v185\n"
"v_cvt_pk_bf16_f32 v91, v186, v187\n"
"v_cvt_pk_bf16_f32 v92, v172, v173\n"
"v_cvt_pk_bf16_f32 v93, v174, v175\n"
"v_cvt_pk_bf16_f32 v94, v188, v189\n"
"v_cvt_pk_bf16_f32 v95, v190, v191\n"
"s_add_u32 s88, s88, 81920\n"
"v_accvgpr_write_b32 a192, v80\n"
"v_accvgpr_write_b32 a193, v81\n"
"v_accvgpr_write_b32 a194, v82\n"
"v_accvgpr_write_b32 a195, v83\n"
"v_accvgpr_write_b32 a196, v84\n"
"v_accvgpr_write_b32 a197, v85\n"
"v_accvgpr_write_b32 a198, v86\n"
"v_accvgpr_write_b32 a199, v87\n"
"v_accvgpr_write_b32 a200, v88\n"
"v_accvgpr_write_b32 a201, v89\n"
"v_accvgpr_write_b32 a202, v90\n"
"v_accvgpr_write_b32 a203, v91\n"
"v_accvgpr_write_b32 a204, v92\n"
"v_accvgpr_write_b32 a205, v93\n"
"v_accvgpr_write_b32 a206, v94\n"
"v_accvgpr_write_b32 a207, v95\n"
"v_mov_b32 v240, 0xff800000\n"
"v_mov_b32 v241, 0xff800000\n"
"v_mov_b32 v242, 0xff800000\n"
"v_mov_b32 v243, 0xff800000\n"
"RESTART_%=:\n"
"v_mbcnt_lo_u32_b32 v253, -1, 0\n"
"v_mbcnt_hi_u32_b32 v253, -1, v253\n"
"v_lshlrev_b32 v249, 4, v253\n"
"v_lshlrev_b32 v182, 3, v253\n"
"v_lshrrev_b32 v176, 4, v253\n"
"v_bfe_u32 v177, v253, 2, 2\n"
"v_lshl_add_u32 v178, v176, 2, v177\n"
"v_mul_u32_u24 v178, 320, v178\n"
"v_and_b32 v177, 3, v253\n"
"v_lshl_add_u32 v178, v177, 3, v178\n"
"v_and_b32 v176, 1, v176\n"
"v_xor_b32 v177, 0, v176\n"
"v_lshl_add_u32 v250, v177, 5, v178\n"
"v_add_u32 v250, 98304, v250\n"
"v_xor_b32 v177, 1, v176\n"
"v_lshl_add_u32 v251, v177, 5, v178\n"
"v_add_u32 v251, 98304, v251\n"
"v_mov_b32 v254, 1\n"
"v_mov_b32 v253, v182\n"
"s_lshl_b32 s26, s22, 4\n"
"s_mul_i32 s27, s24, 320\n"
"s_mul_i32 s25, s23, 768\n"
"s_add_u32 s25, s25, s26\n"
"s_mul_i32 s25, s25, 1280\n"
"s_add_u32 s94, s25, s27\n"
"s_lshl_b32 s25, s21, 4\n"
"s_lshl_b32 s26, s22, 1\n"
"s_add_u32 s25, s25, s26\n"
"s_and_b32 s26, s23, 1\n"
"s_lshl_b32 s26, s26, 3\n"
"s_add_u32 s25, s25, s26\n"
"s_mul_i32 s25, s25, 49152\n"
"s_lshr_b32 s26, s23, 1\n"
"s_mul_i32 s26, s26, 12288\n"
"s_add_u32 s25, s25, s26\n"
"s_add_u32 s90, s25, 2686976\n"
"s_add_u32 s91, s25, 2736128\n"
"v_mov_b32 v236, 0\n"
"v_mov_b32 v237, 0\n"
"v_mov_b32 v238, 0\n"
"v_mov_b32 v239, 0\n"
"s_waitcnt vmcnt(23)\n"
"s_barrier\n"
"ds_read_b128 v[176:179], v224 offset:0\n"
"ds_read_b128 v[180:183], v225 offset:0\n"
"ds_read_b128 v[184:187], v224 offset:128\n"
"ds_read_b128 v[188:191], v225 offset:128\n"
"ds_read_b128 v[192:195], v224 offset:256\n"
"ds_read_b128 v[196:199], v225 offset:256\n"
"s_waitcnt lgkmcnt(0)\n"
"v_mfma_f32_16x16x32_bf16 v[96:99], v[176:179], v[0:3], 0\n"
"v_mfma_f32_16x16x32_bf16 v[96:99], v[180:183], v[16:19], v[96:99]\n"
"ds_read_b128 v[200:203], v224 offset:6144\n"
"v_mfma_f32_16x16x32_bf16 v[96:99], v[184:187], v[32:35], v[96:99]\n"
"v_mfma_f32_16x16x32_bf16 v[96:99], v[188:191], v[48:51], v[96:99]\n"
"ds_read_b128 v[204:207], v225 offset:6144\n"
"v_mfma_f32_16x16x32_bf16 v[96:99], v[192:195], v[64:67], v[96:99]\n"
"ds_read_b128 v[208:211], v224 offset:6272\n"
"v_mfma_f32_16x16x32_bf16 v[96:99], v[196:199], a[192:195], v[96:99]\n"
"v_mfma_f32_16x16x32_bf16 v[100:103], v[176:179], v[4:7], 0\n"
"ds_read_b128 v[212:215], v225 offset:6272\n"
"v_mfma_f32_16x16x32_bf16 v[100:103], v[180:183], v[20:23], v[100:103]\n"
"v_mfma_f32_16x16x32_bf16 v[100:103], v[184:187], v[36:39], v[100:103]\n"
"ds_read_b128 v[216:219], v224 offset:6400\n"
"v_mfma_f32_16x16x32_bf16 v[100:103], v[188:191], v[52:55], v[100:103]\n"
"ds_read_b128 v[220:223], v225 offset:6400\n"
"v_mfma_f32_16x16x32_bf16 v[100:103], v[192:195], v[68:71], v[100:103]\n"
"v_mfma_f32_16x16x32_bf16 v[100:103], v[196:199], a[196:199], v[100:103]\n"
"v_mfma_f32_16x16x32_bf16 v[104:107], v[176:179], v[8:11], 0\n"
"v_mfma_f32_16x16x32_bf16 v[104:107], v[180:183], v[24:27], v[104:107]\n"
"v_mfma_f32_16x16x32_bf16 v[104:107], v[184:187], v[40:43], v[104:107]\n"
"v_mfma_f32_16x16x32_bf16 v[104:107], v[188:191], v[56:59], v[104:107]\n"
"v_mfma_f32_16x16x32_bf16 v[104:107], v[192:195], v[72:75], v[104:107]\n"
"v_mfma_f32_16x16x32_bf16 v[104:107], v[196:199], a[200:203], v[104:107]\n"
"s_barrier\n"
"v_mfma_f32_16x16x32_bf16 v[108:111], v[176:179], v[12:15], 0\n"
"v_mfma_f32_16x16x32_bf16 v[108:111], v[180:183], v[28:31], v[108:111]\n"
"v_mfma_f32_16x16x32_bf16 v[108:111], v[184:187], v[44:47], v[108:111]\n"
"v_mfma_f32_16x16x32_bf16 v[108:111], v[188:191], v[60:63], v[108:111]\n"
"v_mfma_f32_16x16x32_bf16 v[108:111], v[192:195], v[76:79], v[108:111]\n"
"v_mfma_f32_16x16x32_bf16 v[108:111], v[196:199], a[204:207], v[108:111]\n"
"s_waitcnt lgkmcnt(0)\n"
"v_mfma_f32_16x16x32_bf16 v[112:115], v[200:203], v[0:3], 0\n"
"v_mfma_f32_16x16x32_bf16 v[112:115], v[204:207], v[16:19], v[112:115]\n"
"v_mfma_f32_16x16x32_bf16 v[112:115], v[208:211], v[32:35], v[112:115]\n"
"v_mfma_f32_16x16x32_bf16 v[112:115], v[212:215], v[48:51], v[112:115]\n"
"v_mfma_f32_16x16x32_bf16 v[112:115], v[216:219], v[64:67], v[112:115]\n"
"v_mfma_f32_16x16x32_bf16 v[112:115], v[220:223], a[192:195], v[112:115]\n"
"v_mfma_f32_16x16x32_bf16 v[116:119], v[200:203], v[4:7], 0\n"
"v_mfma_f32_16x16x32_bf16 v[116:119], v[204:207], v[20:23], v[116:119]\n"
"v_mfma_f32_16x16x32_bf16 v[116:119], v[208:211], v[36:39], v[116:119]\n"
"v_mfma_f32_16x16x32_bf16 v[116:119], v[212:215], v[52:55], v[116:119]\n"
"v_mfma_f32_16x16x32_bf16 v[116:119], v[216:219], v[68:71], v[116:119]\n"
"v_mfma_f32_16x16x32_bf16 v[116:119], v[220:223], a[196:199], v[116:119]\n"
"v_mfma_f32_16x16x32_bf16 v[120:123], v[200:203], v[8:11], 0\n"
"v_mfma_f32_16x16x32_bf16 v[120:123], v[204:207], v[24:27], v[120:123]\n"
"v_mfma_f32_16x16x32_bf16 v[120:123], v[208:211], v[40:43], v[120:123]\n"
"v_mfma_f32_16x16x32_bf16 v[120:123], v[212:215], v[56:59], v[120:123]\n"
"v_mfma_f32_16x16x32_bf16 v[120:123], v[216:219], v[72:75], v[120:123]\n"
"v_mfma_f32_16x16x32_bf16 v[120:123], v[220:223], a[200:203], v[120:123]\n"
"v_mfma_f32_16x16x32_bf16 v[124:127], v[200:203], v[12:15], 0\n"
"v_mfma_f32_16x16x32_bf16 v[124:127], v[204:207], v[28:31], v[124:127]\n"
"v_mfma_f32_16x16x32_bf16 v[124:127], v[208:211], v[44:47], v[124:127]\n"
"v_mfma_f32_16x16x32_bf16 v[124:127], v[212:215], v[60:63], v[124:127]\n"
"v_mfma_f32_16x16x32_bf16 v[124:127], v[216:219], v[76:79], v[124:127]\n"
"v_mfma_f32_16x16x32_bf16 v[124:127], v[220:223], a[204:207], v[124:127]\n"
"s_nop 7\n"
"v_max3_f32 v144, v96, v97, v98\n"
"v_max3_f32 v144, v144, v99, v112\n"
"v_max3_f32 v144, v144, v113, v114\n"
"v_max3_f32 v144, v144, v115, v240\n"
"v_mov_b32 v145, v144\n"
"s_nop 1\n"
"v_permlane16_swap_b32 v144, v145\n"
"s_nop 1\n"
"v_max_f32 v144, v144, v145\n"
"v_mov_b32 v145, v144\n"
"s_nop 1\n"
"v_permlane32_swap_b32 v144, v145\n"
"s_nop 1\n"
"v_max_f32 v144, v144, v145\n"
"v_sub_f32 v96, v96, v144\n"
"v_sub_f32 v97, v97, v144\n"
"v_sub_f32 v98, v98, v144\n"
"v_sub_f32 v99, v99, v144\n"
"v_sub_f32 v112, v112, v144\n"
"v_sub_f32 v113, v113, v144\n"
"v_sub_f32 v114, v114, v144\n"
"v_sub_f32 v115, v115, v144\n"
"v_sub_f32 v160, 0, v144\n"
"v_sub_f32 v161, 0, v144\n"
"v_sub_f32 v162, 0, v144\n"
"v_sub_f32 v163, 0, v144\n"
"v_max3_f32 v144, v100, v101, v102\n"
"v_max3_f32 v144, v144, v103, v116\n"
"v_max3_f32 v144, v144, v117, v118\n"
"v_max3_f32 v144, v144, v119, v241\n"
"v_mov_b32 v145, v144\n"
"s_nop 1\n"
"v_permlane16_swap_b32 v144, v145\n"
"s_nop 1\n"
"v_max_f32 v144, v144, v145\n"
"v_mov_b32 v145, v144\n"
"s_nop 1\n"
"v_permlane32_swap_b32 v144, v145\n"
"s_nop 1\n"
"v_max_f32 v144, v144, v145\n"
"v_sub_f32 v100, v100, v144\n"
"v_sub_f32 v101, v101, v144\n"
"v_sub_f32 v102, v102, v144\n"
"v_sub_f32 v103, v103, v144\n"
"v_sub_f32 v116, v116, v144\n"
"v_sub_f32 v117, v117, v144\n"
"v_sub_f32 v118, v118, v144\n"
"v_sub_f32 v119, v119, v144\n"
"v_sub_f32 v164, 0, v144\n"
"v_sub_f32 v165, 0, v144\n"
"v_sub_f32 v166, 0, v144\n"
"v_sub_f32 v167, 0, v144\n"
"v_max3_f32 v144, v104, v105, v106\n"
"v_max3_f32 v144, v144, v107, v120\n"
"v_max3_f32 v144, v144, v121, v122\n"
"v_max3_f32 v144, v144, v123, v242\n"
"v_mov_b32 v145, v144\n"
"s_nop 1\n"
"v_permlane16_swap_b32 v144, v145\n"
"s_nop 1\n"
"v_max_f32 v144, v144, v145\n"
"v_mov_b32 v145, v144\n"
"s_nop 1\n"
"v_permlane32_swap_b32 v144, v145\n"
"s_nop 1\n"
"v_max_f32 v144, v144, v145\n"
"v_sub_f32 v104, v104, v144\n"
"v_sub_f32 v105, v105, v144\n"
"v_sub_f32 v106, v106, v144\n"
"v_sub_f32 v107, v107, v144\n"
"v_sub_f32 v120, v120, v144\n"
"v_sub_f32 v121, v121, v144\n"
"v_sub_f32 v122, v122, v144\n"
"v_sub_f32 v123, v123, v144\n"
"v_sub_f32 v168, 0, v144\n"
"v_sub_f32 v169, 0, v144\n"
"v_sub_f32 v170, 0, v144\n"
"v_sub_f32 v171, 0, v144\n"
"v_max3_f32 v144, v108, v109, v110\n"
"v_max3_f32 v144, v144, v111, v124\n"
"v_max3_f32 v144, v144, v125, v126\n"
"v_max3_f32 v144, v144, v127, v243\n"
"v_mov_b32 v145, v144\n"
"s_nop 1\n"
"v_permlane16_swap_b32 v144, v145\n"
"s_nop 1\n"
"v_max_f32 v144, v144, v145\n"
"v_mov_b32 v145, v144\n"
"s_nop 1\n"
"v_permlane32_swap_b32 v144, v145\n"
"s_nop 1\n"
"v_max_f32 v144, v144, v145\n"
"v_sub_f32 v108, v108, v144\n"
"v_sub_f32 v109, v109, v144\n"
"v_sub_f32 v110, v110, v144\n"
"v_sub_f32 v111, v111, v144\n"
"v_sub_f32 v124, v124, v144\n"
"v_sub_f32 v125, v125, v144\n"
"v_sub_f32 v126, v126, v144\n"
"v_sub_f32 v127, v127, v144\n"
"v_sub_f32 v172, 0, v144\n"
"v_sub_f32 v173, 0, v144\n"
"v_sub_f32 v174, 0, v144\n"
"v_sub_f32 v175, 0, v144\n"
"v_exp_f32 v96, v96\n"
"s_nop 0\n"
"v_add_f32 v236, v236, v96\n"
"v_exp_f32 v97, v97\n"
"s_nop 0\n"
"v_cvt_pk_bf16_f32 v128, v96, v97\n"
"v_exp_f32 v98, v98\n"
"v_add_f32 v236, v236, v97\n"
"v_add_f32 v236, v236, v98\n"
"v_exp_f32 v99, v99\n"
"s_nop 0\n"
"v_cvt_pk_bf16_f32 v129, v98, v99\n"
"v_add_f32 v236, v236, v99\n"
"v_exp_f32 v100, v100\n"
"s_nop 0\n"
"v_add_f32 v237, v237, v100\n"
"v_exp_f32 v101, v101\n"
"s_nop 0\n"
"v_cvt_pk_bf16_f32 v132, v100, v101\n"
"v_add_f32 v237, v237, v101\n"
"v_exp_f32 v102, v102\n"
"s_nop 0\n"
"v_add_f32 v237, v237, v102\n"
"v_exp_f32 v103, v103\n"
"s_nop 0\n"
"v_cvt_pk_bf16_f32 v133, v102, v103\n"
"v_exp_f32 v104, v104\n"
"v_add_f32 v237, v237, v103\n"
"v_exp_f32 v105, v105\n"
"s_nop 0\n"
"v_cvt_pk_bf16_f32 v136, v104, v105\n"
"v_exp_f32 v106, v106\n"
"v_add_f32 v238, v238, v104\n"
"v_add_f32 v238, v238, v105\n"
"v_add_f32 v238, v238, v106\n"
"v_exp_f32 v107, v107\n"
"s_nop 0\n"
"v_cvt_pk_bf16_f32 v137, v106, v107\n"
"v_add_f32 v238, v238, v107\n"
"v_exp_f32 v108, v108\n"
"s_nop 0\n"
"v_add_f32 v239, v239, v108\n"
"v_exp_f32 v109, v109\n"
"s_nop 0\n"
"v_cvt_pk_bf16_f32 v140, v108, v109\n"
"v_add_f32 v239, v239, v109\n"
"v_exp_f32 v110, v110\n"
"s_nop 0\n"
"v_add_f32 v239, v239, v110\n"
"v_exp_f32 v111, v111\n"
"s_nop 0\n"
"v_cvt_pk_bf16_f32 v141, v110, v111\n"
"v_add_f32 v239, v239, v111\n"
"v_exp_f32 v112, v112\n"
"s_nop 0\n"
"v_add_f32 v236, v236, v112\n"
"v_exp_f32 v113, v113\n"
"s_nop 0\n"
"v_cvt_pk_bf16_f32 v130, v112, v113\n"
"v_add_f32 v236, v236, v113\n"
"s_barrier\n"
"v_exp_f32 v114, v114\n"
"s_nop 0\n"
"v_add_f32 v236, v236, v114\n"
"v_exp_f32 v115, v115\n"
"s_nop 0\n"
"v_cvt_pk_bf16_f32 v131, v114, v115\n"
"v_add_f32 v236, v236, v115\n"
"s_mov_b32 m0, s79\n"
"s_add_u32 s84, s84, 12288\n"
"v_exp_f32 v116, v116\n"
"buffer_load_dwordx4 v230, s[56:59], s84 offen offset:0 sc0 sc1 lds\n"
"v_add_f32 v237, v237, v116\n"
"v_exp_f32 v117, v117\n"
"s_nop 0\n"
"v_cvt_pk_bf16_f32 v134, v116, v117\n"
"v_add_f32 v237, v237, v117\n"
"v_exp_f32 v118, v118\n"
"buffer_load_dwordx4 v231, s[56:59], s84 offen offset:1024 sc0 sc1 lds\n"
"v_add_f32 v237, v237, v118\n"
"v_exp_f32 v119, v119\n"
"s_nop 0\n"
"v_cvt_pk_bf16_f32 v135, v118, v119\n"
"v_add_f32 v237, v237, v119\n"
"v_exp_f32 v120, v120\n"
"s_nop 0\n"
"v_add_f32 v238, v238, v120\n"
"ds_read_b128 v[176:179], v224 offset:12288\n"
"v_exp_f32 v121, v121\n"
"buffer_load_dwordx4 v232, s[56:59], s84 offen offset:2048 sc0 sc1 lds\n"
"ds_read_b128 v[180:183], v225 offset:12288\n"
"v_cvt_pk_bf16_f32 v138, v120, v121\n"
"v_exp_f32 v122, v122\n"
"ds_read_b128 v[184:187], v224 offset:12416\n"
"v_add_f32 v238, v238, v121\n"
"ds_read_b128 v[188:191], v225 offset:12416\n"
"s_add_u32 s84, s84, 12288\n"
"v_exp_f32 v123, v123\n"
"ds_read_b128 v[192:195], v224 offset:12544\n"
"s_mov_b32 m0, s76\n"
"s_nop 0\n"
"buffer_load_dwordx4 v230, s[56:59], s84 offen offset:0 sc0 sc1 lds\n"
"v_exp_f32 v124, v124\n"
"ds_read_b128 v[196:199], v225 offset:12544\n"
"v_cvt_pk_bf16_f32 v139, v122, v123\n"
"v_add_f32 v238, v238, v122\n"
"v_add_f32 v238, v238, v123\n"
"v_exp_f32 v125, v125\n"
"s_nop 0\n"
"v_cvt_pk_bf16_f32 v142, v124, v125\n"
"v_add_f32 v239, v239, v124\n"
"v_add_f32 v239, v239, v125\n"
"v_exp_f32 v126, v126\n"
"buffer_load_dwordx4 v231, s[56:59], s84 offen offset:1024 sc0 sc1 lds\n"
"v_add_f32 v239, v239, v126\n"
"v_exp_f32 v127, v127\n"
"s_nop 0\n"
"v_cvt_pk_bf16_f32 v143, v126, v127\n"
"v_add_f32 v239, v239, v127\n"
"s_waitcnt lgkmcnt(0)\n"
"v_mfma_f32_16x16x32_bf16 v[96:99], v[176:179], v[0:3], v[160:163]\n"
"v_mfma_f32_16x16x32_bf16 v[96:99], v[180:183], v[16:19], v[96:99]\n"
"ds_read_b128 v[200:203], v224 offset:18432\n"
"v_mfma_f32_16x16x32_bf16 v[96:99], v[184:187], v[32:35], v[96:99]\n"
"buffer_load_dwordx4 v232, s[56:59], s84 offen offset:2048 sc0 sc1 lds\n"
"v_mfma_f32_16x16x32_bf16 v[96:99], v[188:191], v[48:51], v[96:99]\n"
"ds_read_b128 v[204:207], v225 offset:18432\n"
"v_mfma_f32_16x16x32_bf16 v[96:99], v[192:195], v[64:67], v[96:99]\n"
"ds_read_b128 v[208:211], v224 offset:18560\n"
"v_mfma_f32_16x16x32_bf16 v[96:99], v[196:199], a[192:195], v[96:99]\n"
"ds_read_b128 v[212:215], v225 offset:18560\n"
"v_mfma_f32_16x16x32_bf16 v[100:103], v[176:179], v[4:7], v[164:167]\n"
"ds_read_b128 v[216:219], v224 offset:18688\n"
"v_mfma_f32_16x16x32_bf16 v[100:103], v[180:183], v[20:23], v[100:103]\n"
"ds_read_b128 v[220:223], v225 offset:18688\n"
"v_mfma_f32_16x16x32_bf16 v[100:103], v[184:187], v[36:39], v[100:103]\n"
"s_nop 1\n"
"v_exp_f32 v96, v96\n"
"v_mfma_f32_16x16x32_bf16 v[100:103], v[188:191], v[52:55], v[100:103]\n"
"v_add_f32 v236, v236, v96\n"
"v_mfma_f32_16x16x32_bf16 v[100:103], v[192:195], v[68:71], v[100:103]\n"
"v_exp_f32 v97, v97\n"
"v_mfma_f32_16x16x32_bf16 v[100:103], v[196:199], a[196:199], v[100:103]\n"
"v_cvt_pk_bf16_f32 v80, v96, v97\n"
"v_add_f32 v236, v236, v97\n"
"v_mfma_f32_16x16x32_bf16 v[104:107], v[176:179], v[8:11], v[168:171]\n"
"v_mfma_f32_16x16x32_bf16 v[104:107], v[180:183], v[24:27], v[104:107]\n"
"v_exp_f32 v98, v98\n"
"v_mfma_f32_16x16x32_bf16 v[104:107], v[184:187], v[40:43], v[104:107]\n"
"v_add_f32 v236, v236, v98\n"
"v_mfma_f32_16x16x32_bf16 v[104:107], v[188:191], v[56:59], v[104:107]\n"
"v_exp_f32 v99, v99\n"
"v_mfma_f32_16x16x32_bf16 v[104:107], v[192:195], v[72:75], v[104:107]\n"
"v_cvt_pk_bf16_f32 v81, v98, v99\n"
"v_add_f32 v236, v236, v99\n"
"v_mfma_f32_16x16x32_bf16 v[104:107], v[196:199], a[200:203], v[104:107]\n"
"v_mfma_f32_16x16x32_bf16 v[108:111], v[176:179], v[12:15], v[172:175]\n"
"v_exp_f32 v100, v100\n"
"v_mfma_f32_16x16x32_bf16 v[108:111], v[180:183], v[28:31], v[108:111]\n"
"v_add_f32 v237, v237, v100\n"
"v_mfma_f32_16x16x32_bf16 v[108:111], v[184:187], v[44:47], v[108:111]\n"
"v_exp_f32 v101, v101\n"
"v_mfma_f32_16x16x32_bf16 v[108:111], v[188:191], v[60:63], v[108:111]\n"
"v_cvt_pk_bf16_f32 v84, v100, v101\n"
"v_add_f32 v237, v237, v101\n"
"v_mfma_f32_16x16x32_bf16 v[108:111], v[192:195], v[76:79], v[108:111]\n"
"v_mfma_f32_16x16x32_bf16 v[108:111], v[196:199], a[204:207], v[108:111]\n"
"v_exp_f32 v102, v102\n"
"s_waitcnt lgkmcnt(0)\n"
"v_mfma_f32_16x16x32_bf16 v[112:115], v[200:203], v[0:3], v[160:163]\n"
"v_add_f32 v237, v237, v102\n"
"v_mfma_f32_16x16x32_bf16 v[112:115], v[204:207], v[16:19], v[112:115]\n"
"v_exp_f32 v103, v103\n"
"v_mfma_f32_16x16x32_bf16 v[112:115], v[208:211], v[32:35], v[112:115]\n"
"ds_read_b128 v[176:179], v224 offset:24576\n"
"v_cvt_pk_bf16_f32 v85, v102, v103\n"
"v_mfma_f32_16x16x32_bf16 v[112:115], v[212:215], v[48:51], v[112:115]\n"
"ds_read_b128 v[180:183], v225 offset:24576\n"
"v_add_f32 v237, v237, v103\n"
"v_mfma_f32_16x16x32_bf16 v[112:115], v[216:219], v[64:67], v[112:115]\n"
"v_exp_f32 v104, v104\n"
"v_mfma_f32_16x16x32_bf16 v[112:115], v[220:223], a[192:195], v[112:115]\n"
"ds_read_b128 v[184:187], v224 offset:24704\n"
"v_add_f32 v238, v238, v104\n"
"v_mfma_f32_16x16x32_bf16 v[116:119], v[200:203], v[4:7], v[164:167]\n"
"v_exp_f32 v105, v105\n"
"v_mfma_f32_16x16x32_bf16 v[116:119], v[204:207], v[20:23], v[116:119]\n"
"ds_read_b128 v[188:191], v225 offset:24704\n"
"v_cvt_pk_bf16_f32 v88, v104, v105\n"
"v_mfma_f32_16x16x32_bf16 v[116:119], v[208:211], v[36:39], v[116:119]\n"
"ds_read_b128 v[192:195], v224 offset:24832\n"
"v_add_f32 v238, v238, v105\n"
"v_mfma_f32_16x16x32_bf16 v[116:119], v[212:215], v[52:55], v[116:119]\n"
"v_exp_f32 v106, v106\n"
"v_mfma_f32_16x16x32_bf16 v[116:119], v[216:219], v[68:71], v[116:119]\n"
"ds_read_b128 v[196:199], v225 offset:24832\n"
"v_add_f32 v238, v238, v106\n"
"v_mfma_f32_16x16x32_bf16 v[116:119], v[220:223], a[196:199], v[116:119]\n"
"v_exp_f32 v107, v107\n"
"v_mfma_f32_16x16x32_bf16 v[120:123], v[200:203], v[8:11], v[168:171]\n"
"v_cvt_pk_bf16_f32 v89, v106, v107\n"
"v_add_f32 v238, v238, v107\n"
"v_mfma_f32_16x16x32_bf16 v[120:123], v[204:207], v[24:27], v[120:123]\n"
"v_mfma_f32_16x16x32_bf16 v[120:123], v[208:211], v[40:43], v[120:123]\n"
"v_exp_f32 v108, v108\n"
"v_mfma_f32_16x16x32_bf16 v[120:123], v[212:215], v[56:59], v[120:123]\n"
"v_add_f32 v239, v239, v108\n"
"v_mfma_f32_16x16x32_bf16 v[120:123], v[216:219], v[72:75], v[120:123]\n"
"v_exp_f32 v109, v109\n"
"v_mfma_f32_16x16x32_bf16 v[120:123], v[220:223], a[200:203], v[120:123]\n"
"v_cvt_pk_bf16_f32 v92, v108, v109\n"
"v_add_f32 v239, v239, v109\n"
"v_mfma_f32_16x16x32_bf16 v[124:127], v[200:203], v[12:15], v[172:175]\n"
"v_mfma_f32_16x16x32_bf16 v[124:127], v[204:207], v[28:31], v[124:127]\n"
"v_exp_f32 v110, v110\n"
"v_mfma_f32_16x16x32_bf16 v[124:127], v[208:211], v[44:47], v[124:127]\n"
"v_add_f32 v239, v239, v110\n"
"v_mfma_f32_16x16x32_bf16 v[124:127], v[212:215], v[60:63], v[124:127]\n"
"v_exp_f32 v111, v111\n"
"v_mfma_f32_16x16x32_bf16 v[124:127], v[216:219], v[76:79], v[124:127]\n"
"v_cvt_pk_bf16_f32 v93, v110, v111\n"
"v_add_f32 v239, v239, v111\n"
"v_mfma_f32_16x16x32_bf16 v[124:127], v[220:223], a[204:207], v[124:127]\n"
"s_waitcnt lgkmcnt(0)\n"
"v_mfma_f32_16x16x32_bf16 v[96:99], v[176:179], v[0:3], v[160:163]\n"
"v_exp_f32 v112, v112\n"
"v_mfma_f32_16x16x32_bf16 v[96:99], v[180:183], v[16:19], v[96:99]\n"
"ds_read_b128 v[200:203], v224 offset:30720\n"
"v_add_f32 v236, v236, v112\n"
"v_mfma_f32_16x16x32_bf16 v[96:99], v[184:187], v[32:35], v[96:99]\n"
"v_exp_f32 v113, v113\n"
"v_mfma_f32_16x16x32_bf16 v[96:99], v[188:191], v[48:51], v[96:99]\n"
"ds_read_b128 v[204:207], v225 offset:30720\n"
"v_cvt_pk_bf16_f32 v82, v112, v113\n"
"v_mfma_f32_16x16x32_bf16 v[96:99], v[192:195], v[64:67], v[96:99]\n"
"ds_read_b128 v[208:211], v224 offset:30848\n"
"v_add_f32 v236, v236, v113\n"
"v_mfma_f32_16x16x32_bf16 v[96:99], v[196:199], a[192:195], v[96:99]\n"
"v_exp_f32 v114, v114\n"
"v_mfma_f32_16x16x32_bf16 v[100:103], v[176:179], v[4:7], v[164:167]\n"
"ds_read_b128 v[212:215], v225 offset:30848\n"
"v_add_f32 v236, v236, v114\n"
"v_mfma_f32_16x16x32_bf16 v[100:103], v[180:183], v[20:23], v[100:103]\n"
"v_exp_f32 v115, v115\n"
"v_mfma_f32_16x16x32_bf16 v[100:103], v[184:187], v[36:39], v[100:103]\n"
"ds_read_b128 v[216:219], v224 offset:30976\n"
"v_cvt_pk_bf16_f32 v83, v114, v115\n"
"v_mfma_f32_16x16x32_bf16 v[100:103], v[188:191], v[52:55], v[100:103]\n"
"ds_read_b128 v[220:223], v225 offset:30976\n"
"v_add_f32 v236, v236, v115\n"
"v_mfma_f32_16x16x32_bf16 v[100:103], v[192:195], v[68:71], v[100:103]\n"
"v_exp_f32 v116, v116\n"
"v_mfma_f32_16x16x32_bf16 v[100:103], v[196:199], a[196:199], v[100:103]\n"
"v_add_f32 v237, v237, v116\n"
"v_mfma_f32_16x16x32_bf16 v[104:107], v[176:179], v[8:11], v[168:171]\n"
"v_exp_f32 v117, v117\n"
"v_mfma_f32_16x16x32_bf16 v[104:107], v[180:183], v[24:27], v[104:107]\n"
"v_cvt_pk_bf16_f32 v86, v116, v117\n"
"v_add_f32 v237, v237, v117\n"
"v_mfma_f32_16x16x32_bf16 v[104:107], v[184:187], v[40:43], v[104:107]\n"
"v_mfma_f32_16x16x32_bf16 v[104:107], v[188:191], v[56:59], v[104:107]\n"
"v_exp_f32 v118, v118\n"
"v_mfma_f32_16x16x32_bf16 v[104:107], v[192:195], v[72:75], v[104:107]\n"
"v_add_f32 v237, v237, v118\n"
"v_mfma_f32_16x16x32_bf16 v[104:107], v[196:199], a[200:203], v[104:107]\n"
"s_waitcnt vmcnt(9)\n"
"s_barrier\n"
"s_add_u32 s28, s22, s31\n"
"s_cmp_lg_u32 s28, 0\n"
"s_cbranch_scc1 SIGSKIP1_%=\n"
"s_lshl_b32 s28, s21, 3\n"
"s_add_u32 s28, s28, s23\n"
"s_andn2_b32 s28, s28, 1\n"
"s_add_u32 s28, s28, 0\n"
"s_lshl_b32 s28, s28, 8\n"
"v_mov_b32 v255, s28\n"
"s_mov_b64 exec, 1\n"
"global_atomic_add v255, v254, s[54:55]\n"
"s_mov_b64 exec, -1\n"
"SIGSKIP1_%=:\n"
"v_exp_f32 v119, v119\n"
"v_mfma_f32_16x16x32_bf16 v[108:111], v[176:179], v[12:15], v[172:175]\n"
"v_cvt_pk_bf16_f32 v87, v118, v119\n"
"v_add_f32 v237, v237, v119\n"
"v_mfma_f32_16x16x32_bf16 v[108:111], v[180:183], v[28:31], v[108:111]\n"
"ds_read_b64_tr_b16 v[176:177], v226 offset:0\n"
"v_mfma_f32_16x16x32_bf16 v[108:111], v[184:187], v[44:47], v[108:111]\n"
"v_exp_f32 v120, v120\n"
"v_mfma_f32_16x16x32_bf16 v[108:111], v[188:191], v[60:63], v[108:111]\n"
"ds_read_b64_tr_b16 v[178:179], v226 offset:6144\n"
"v_add_f32 v238, v238, v120\n"
"v_mfma_f32_16x16x32_bf16 v[108:111], v[192:195], v[76:79], v[108:111]\n"
"v_exp_f32 v121, v121\n"
"v_mbcnt_lo_u32_b32 v243, -1, 0\n"
"v_mbcnt_hi_u32_b32 v243, -1, v243\n"
"v_mfma_f32_16x16x32_bf16 v[108:111], v[196:199], a[204:207], v[108:111]\n"
"ds_read_b64_tr_b16 v[180:181], v226 offset:12288\n"
"v_cvt_pk_bf16_f32 v90, v120, v121\n"
"s_waitcnt lgkmcnt(3)\n"
"v_mfma_f32_16x16x32_bf16 v[112:115], v[200:203], v[0:3], v[160:163]\n"
"ds_read_b64_tr_b16 v[182:183], v226 offset:18432\n"
"v_add_f32 v238, v238, v121\n"
"v_mfma_f32_16x16x32_bf16 v[112:115], v[204:207], v[16:19], v[112:115]\n"
"v_exp_f32 v122, v122\n"
"v_cmp_lt_u32 vcc, 2, v243\n"
"v_mov_b32 v242, 0\n"
"v_mov_b32 v241, 896\n"
"v_mfma_f32_16x16x32_bf16 v[112:115], v[208:211], v[32:35], v[112:115]\n"
"ds_read_b64_tr_b16 v[190:191], v227 offset:18432\n"
"v_add_f32 v238, v238, v122\n"
"v_mfma_f32_16x16x32_bf16 v[112:115], v[212:215], v[48:51], v[112:115]\n"
"v_exp_f32 v123, v123\n"
"v_mfma_f32_16x16x32_bf16 v[112:115], v[216:219], v[64:67], v[112:115]\n"
"ds_read_b64_tr_b16 v[184:185], v227 offset:0\n"
"v_cvt_pk_bf16_f32 v91, v122, v123\n"
"v_cndmask_b32 v242, v242, v241, vcc\n"
"v_lshl_add_u32 v242, v243, 7, v242\n"
"v_mfma_f32_16x16x32_bf16 v[112:115], v[220:223], a[192:195], v[112:115]\n"
"ds_read_b64_tr_b16 v[186:187], v227 offset:6144\n"
"v_add_f32 v238, v238, v123\n"
"v_mfma_f32_16x16x32_bf16 v[116:119], v[200:203], v[4:7], v[164:167]\n"
"v_exp_f32 v124, v124\n"
"v_mfma_f32_16x16x32_bf16 v[116:119], v[204:207], v[20:23], v[116:119]\n"
"ds_read_b64_tr_b16 v[188:189], v227 offset:12288\n"
"v_add_f32 v239, v239, v124\n"
"v_cmp_lt_u32 vcc, 5, v243\n"
"v_mov_b32 v241, 0x7fff0000\n"
"s_nop 0\n"
"v_mfma_f32_16x16x32_bf16 v[116:119], v[208:211], v[36:39], v[116:119]\n"
"v_exp_f32 v125, v125\n"
"v_mfma_f32_16x16x32_bf16 v[116:119], v[212:215], v[52:55], v[116:119]\n"
"v_cvt_pk_bf16_f32 v94, v124, v125\n"
"v_add_f32 v239, v239, v125\n"
"v_mfma_f32_16x16x32_bf16 v[116:119], v[216:219], v[68:71], v[116:119]\n"
"ds_read_b64_tr_b16 v[198:199], v228 offset:18432\n"
"v_cndmask_b32 v243, v242, v241, vcc\n"
"v_mfma_f32_16x16x32_bf16 v[116:119], v[220:223], a[196:199], v[116:119]\n"
"v_exp_f32 v126, v126\n"
"v_mfma_f32_16x16x32_bf16 v[120:123], v[200:203], v[8:11], v[168:171]\n"
"ds_read_b64_tr_b16 v[192:193], v228 offset:0\n"
"v_add_f32 v239, v239, v126\n"
"v_mfma_f32_16x16x32_bf16 v[120:123], v[204:207], v[24:27], v[120:123]\n"
"ds_read_b64_tr_b16 v[194:195], v228 offset:6144\n"
"s_mul_i32 s87, s23, 768\n"
"s_mul_i32 s28, s21, 96\n"
"s_add_u32 s87, s87, s28\n"
"s_mul_i32 s28, s22, 24\n"
"s_add_u32 s87, s87, s28\n"
"s_mul_i32 s87, s87, 1280\n"
"s_mul_i32 s28, s24, 320\n"
"s_add_u32 s87, s87, s28\n"
"v_mfma_f32_16x16x32_bf16 v[120:123], v[208:211], v[40:43], v[120:123]\n"
"v_exp_f32 v127, v127\n"
"v_mfma_f32_16x16x32_bf16 v[120:123], v[212:215], v[56:59], v[120:123]\n"
"ds_read_b64_tr_b16 v[196:197], v228 offset:12288\n"
"v_cvt_pk_bf16_f32 v95, v126, v127\n"
"v_mfma_f32_16x16x32_bf16 v[120:123], v[216:219], v[72:75], v[120:123]\n"
"v_add_f32 v239, v239, v127\n"
"v_mfma_f32_16x16x32_bf16 v[120:123], v[220:223], a[200:203], v[120:123]\n"
"v_mfma_f32_16x16x32_bf16 v[124:127], v[200:203], v[12:15], v[172:175]\n"
"v_mfma_f32_16x16x32_bf16 v[124:127], v[204:207], v[28:31], v[124:127]\n"
"ds_read_b64_tr_b16 v[202:203], v229 offset:6144\n"
"v_mfma_f32_16x16x32_bf16 v[124:127], v[208:211], v[44:47], v[124:127]\n"
"ds_read_b64_tr_b16 v[200:201], v229 offset:0\n"
"v_mfma_f32_16x16x32_bf16 v[124:127], v[212:215], v[60:63], v[124:127]\n"
"v_exp_f32 v96, v96\n"
"v_mfma_f32_16x16x32_bf16 v[124:127], v[216:219], v[76:79], v[124:127]\n"
"ds_read_b64_tr_b16 v[204:205], v229 offset:12288\n"
"v_add_f32 v236, v236, v96\n"
"v_mfma_f32_16x16x32_bf16 v[124:127], v[220:223], a[204:207], v[124:127]\n"
"ds_read_b64_tr_b16 v[206:207], v229 offset:18432\n"
"s_add_u32 s85, s85, 12288\n"
"s_waitcnt lgkmcnt(4)\n"
"v_mfma_f32_16x16x32_bf16 a[0:3], v[176:179], v[128:131], 0\n"
"v_exp_f32 v97, v97\n"
"v_mfma_f32_16x16x32_bf16 a[0:3], v[180:183], v[80:83], a[0:3]\n"
"v_cvt_pk_bf16_f32 v144, v96, v97\n"
"s_mov_b32 m0, s82\n"
"v_mfma_f32_16x16x32_bf16 a[4:7], v[176:179], v[132:135], 0\n"
"buffer_load_dwordx4 v233, s[60:63], s85 offen offset:0 sc0 sc1 lds\n"
"v_mfma_f32_16x16x32_bf16 a[4:7], v[180:183], v[84:87], a[4:7]\n"
"v_exp_f32 v98, v98\n"
"v_mfma_f32_16x16x32_bf16 a[8:11], v[176:179], v[136:139], 0\n"
"ds_read_b64_tr_b16 v[208:209], v226 offset:128\n"
"v_add_f32 v236, v236, v97\n"
"v_mfma_f32_16x16x32_bf16 a[8:11], v[180:183], v[88:91], a[8:11]\n"
"ds_read_b64_tr_b16 v[210:211], v226 offset:6272\n"
"v_add_f32 v236, v236, v98\n"
"v_mfma_f32_16x16x32_bf16 a[12:15], v[176:179], v[140:143], 0\n"
"v_exp_f32 v99, v99\n"
"v_mfma_f32_16x16x32_bf16 a[12:15], v[180:183], v[92:95], a[12:15]\n"
"ds_read_b64_tr_b16 v[212:213], v226 offset:12416\n"
"v_cvt_pk_bf16_f32 v145, v98, v99\n"
"v_mfma_f32_16x16x32_bf16 a[28:31], v[184:187], v[140:143], 0\n"
"ds_read_b64_tr_b16 v[214:215], v226 offset:18560\n"
"v_add_f32 v236, v236, v99\n"
"v_mfma_f32_16x16x32_bf16 a[28:31], v[188:191], v[92:95], a[28:31]\n"
"v_exp_f32 v100, v100\n"
"v_mfma_f32_16x16x32_bf16 a[24:27], v[184:187], v[136:139], 0\n"
"buffer_load_dwordx4 v234, s[60:63], s85 offen offset:1024 sc0 sc1 lds\n"
"v_mfma_f32_16x16x32_bf16 a[24:27], v[188:191], v[88:91], a[24:27]\n"
"ds_read_b64_tr_b16 v[216:217], v227 offset:128\n"
"v_add_f32 v237, v237, v100\n"
"v_mfma_f32_16x16x32_bf16 a[20:23], v[184:187], v[132:135], 0\n"
"v_exp_f32 v101, v101\n"
"v_mfma_f32_16x16x32_bf16 a[20:23], v[188:191], v[84:87], a[20:23]\n"
"ds_read_b64_tr_b16 v[218:219], v227 offset:6272\n"
"v_cvt_pk_bf16_f32 v148, v100, v101\n"
"v_mfma_f32_16x16x32_bf16 a[16:19], v[184:187], v[128:131], 0\n"
"ds_read_b64_tr_b16 v[220:221], v227 offset:12416\n"
"v_add_f32 v237, v237, v101\n"
"v_mfma_f32_16x16x32_bf16 a[16:19], v[188:191], v[80:83], a[16:19]\n"
"v_exp_f32 v102, v102\n"
"v_mfma_f32_16x16x32_bf16 a[32:35], v[192:195], v[128:131], 0\n"
"ds_read_b64_tr_b16 v[222:223], v227 offset:18560\n"
"v_add_f32 v237, v237, v102\n"
"v_mfma_f32_16x16x32_bf16 a[32:35], v[196:199], v[80:83], a[32:35]\n"
"ds_read_b64_tr_b16 v[182:183], v228 offset:18560\n"
"v_mfma_f32_16x16x32_bf16 a[36:39], v[192:195], v[132:135], 0\n"
"v_exp_f32 v103, v103\n"
"v_mfma_f32_16x16x32_bf16 a[36:39], v[196:199], v[84:87], a[36:39]\n"
"buffer_load_dwordx4 v235, s[60:63], s85 offen offset:2048 sc0 sc1 lds\n"
"v_mfma_f32_16x16x32_bf16 a[40:43], v[192:195], v[136:139], 0\n"
"ds_read_b64_tr_b16 v[176:177], v228 offset:128\n"
"v_cvt_pk_bf16_f32 v149, v102, v103\n"
"v_mfma_f32_16x16x32_bf16 a[40:43], v[196:199], v[88:91], a[40:43]\n"
"v_exp_f32 v104, v104\n"
"v_mfma_f32_16x16x32_bf16 a[44:47], v[192:195], v[140:143], 0\n"
"ds_read_b64_tr_b16 v[178:179], v228 offset:6272\n"
"v_add_f32 v237, v237, v103\n"
"v_mfma_f32_16x16x32_bf16 a[44:47], v[196:199], v[92:95], a[44:47]\n"
"ds_read_b64_tr_b16 v[180:181], v228 offset:12416\n"
"s_add_u32 s85, s85, 12288\n"
"s_waitcnt lgkmcnt(4)\n"
"v_mfma_f32_16x16x32_bf16 a[60:63], v[200:203], v[140:143], 0\n"
"v_exp_f32 v105, v105\n"
"v_mfma_f32_16x16x32_bf16 a[60:63], v[204:207], v[92:95], a[60:63]\n"
"v_cvt_pk_bf16_f32 v152, v104, v105\n"
"s_mov_b32 m0, s83\n"
"v_mfma_f32_16x16x32_bf16 a[56:59], v[200:203], v[136:139], 0\n"
"buffer_load_dwordx4 v233, s[60:63], s85 offen offset:0 sc0 sc1 lds\n"
"v_mfma_f32_16x16x32_bf16 a[56:59], v[204:207], v[88:91], a[56:59]\n"
"v_exp_f32 v106, v106\n"
"v_mfma_f32_16x16x32_bf16 a[52:55], v[200:203], v[132:135], 0\n"
"ds_read_b64_tr_b16 v[184:185], v229 offset:128\n"
"v_add_f32 v238, v238, v104\n"
"v_mfma_f32_16x16x32_bf16 a[52:55], v[204:207], v[84:87], a[52:55]\n"
"ds_read_b64_tr_b16 v[186:187], v229 offset:6272\n"
"v_add_f32 v238, v238, v105\n"
"v_mfma_f32_16x16x32_bf16 a[48:51], v[200:203], v[128:131], 0\n"
"ds_read_b64_tr_b16 v[188:189], v229 offset:12416\n"
"v_add_f32 v238, v238, v106\n"
"v_mfma_f32_16x16x32_bf16 a[48:51], v[204:207], v[80:83], a[48:51]\n"
"v_exp_f32 v107, v107\n"
"v_mfma_f32_16x16x32_bf16 a[64:67], v[208:211], v[128:131], 0\n"
"ds_read_b64_tr_b16 v[190:191], v229 offset:18560\n"
"v_cvt_pk_bf16_f32 v153, v106, v107\n"
"v_mfma_f32_16x16x32_bf16 a[64:67], v[212:215], v[80:83], a[64:67]\n"
"ds_read_b64_tr_b16 v[198:199], v226 offset:18688\n"
"v_add_f32 v238, v238, v107\n"
"v_mfma_f32_16x16x32_bf16 a[68:71], v[208:211], v[132:135], 0\n"
"v_exp_f32 v108, v108\n"
"v_mfma_f32_16x16x32_bf16 a[68:71], v[212:215], v[84:87], a[68:71]\n"
"buffer_load_dwordx4 v234, s[60:63], s85 offen offset:1024 sc0 sc1 lds\n"
"v_mfma_f32_16x16x32_bf16 a[72:75], v[208:211], v[136:139], 0\n"
"ds_read_b64_tr_b16 v[192:193], v226 offset:256\n"
"v_add_f32 v239, v239, v108\n"
"v_mfma_f32_16x16x32_bf16 a[72:75], v[212:215], v[88:91], a[72:75]\n"
"v_exp_f32 v109, v109\n"
"v_mfma_f32_16x16x32_bf16 a[76:79], v[208:211], v[140:143], 0\n"
"ds_read_b64_tr_b16 v[194:195], v226 offset:6400\n"
"v_cvt_pk_bf16_f32 v156, v108, v109\n"
"v_mfma_f32_16x16x32_bf16 a[76:79], v[212:215], v[92:95], a[76:79]\n"
"ds_read_b64_tr_b16 v[196:197], v226 offset:12544\n"
"v_add_f32 v239, v239, v109\n"
"v_mfma_f32_16x16x32_bf16 a[92:95], v[216:219], v[140:143], 0\n"
"v_exp_f32 v110, v110\n"
"v_mfma_f32_16x16x32_bf16 a[92:95], v[220:223], v[92:95], a[92:95]\n"
"v_add_f32 v239, v239, v110\n"
"v_mfma_f32_16x16x32_bf16 a[88:91], v[216:219], v[136:139], 0\n"
"buffer_load_dwordx4 v235, s[60:63], s85 offen offset:2048 sc0 sc1 lds\n"
"v_mfma_f32_16x16x32_bf16 a[88:91], v[220:223], v[88:91], a[88:91]\n"
"v_exp_f32 v111, v111\n"
"v_mfma_f32_16x16x32_bf16 a[84:87], v[216:219], v[132:135], 0\n"
"ds_read_b64_tr_b16 v[200:201], v227 offset:256\n"
"v_cvt_pk_bf16_f32 v157, v110, v111\n"
"v_mfma_f32_16x16x32_bf16 a[84:87], v[220:223], v[84:87], a[84:87]\n"
"ds_read_b64_tr_b16 v[202:203], v227 offset:6400\n"
"v_add_f32 v239, v239, v111\n"
"v_mfma_f32_16x16x32_bf16 a[80:83], v[216:219], v[128:131], 0\n"
"v_exp_f32 v112, v112\n"
"v_mfma_f32_16x16x32_bf16 a[80:83], v[220:223], v[80:83], a[80:83]\n"
"ds_read_b64_tr_b16 v[204:205], v227 offset:12544\n"
"v_add_f32 v236, v236, v112\n"
"s_waitcnt lgkmcnt(3)\n"
"v_mfma_f32_16x16x32_bf16 a[96:99], v[176:179], v[128:131], 0\n"
"ds_read_b64_tr_b16 v[206:207], v227 offset:18688\n"
"v_mfma_f32_16x16x32_bf16 a[96:99], v[180:183], v[80:83], a[96:99]\n"
"v_exp_f32 v113, v113\n"
"v_mfma_f32_16x16x32_bf16 a[100:103], v[176:179], v[132:135], 0\n"
"ds_read_b64_tr_b16 v[214:215], v228 offset:18688\n"
"v_cvt_pk_bf16_f32 v146, v112, v113\n"
"v_mfma_f32_16x16x32_bf16 a[100:103], v[180:183], v[84:87], a[100:103]\n"
"ds_read_b64_tr_b16 v[208:209], v228 offset:256\n"
"v_add_f32 v236, v236, v113\n"
"v_mfma_f32_16x16x32_bf16 a[104:107], v[176:179], v[136:139], 0\n"
"s_waitcnt vmcnt(6)\n"
"s_barrier\n"
"v_exp_f32 v114, v114\n"
"v_mfma_f32_16x16x32_bf16 a[104:107], v[180:183], v[88:91], a[104:107]\n"
"ds_read_b64_tr_b16 v[210:211], v228 offset:6400\n"
"v_add_f32 v236, v236, v114\n"
"v_mfma_f32_16x16x32_bf16 a[108:111], v[176:179], v[140:143], 0\n"
"ds_read_b64_tr_b16 v[212:213], v228 offset:12544\n"
"v_mfma_f32_16x16x32_bf16 a[108:111], v[180:183], v[92:95], a[108:111]\n"
"v_exp_f32 v115, v115\n"
"v_mfma_f32_16x16x32_bf16 a[124:127], v[184:187], v[140:143], 0\n"
"v_cvt_pk_bf16_f32 v147, v114, v115\n"
"v_add_f32 v236, v236, v115\n"
"v_mfma_f32_16x16x32_bf16 a[124:127], v[188:191], v[92:95], a[124:127]\n"
"s_mov_b32 m0, s77\n"
"s_add_u32 s84, s84, 12288\n"
"v_mfma_f32_16x16x32_bf16 a[120:123], v[184:187], v[136:139], 0\n"
"v_exp_f32 v116, v116\n"
"v_mfma_f32_16x16x32_bf16 a[120:123], v[188:191], v[88:91], a[120:123]\n"
"buffer_load_dwordx4 v230, s[56:59], s84 offen offset:0 sc0 sc1 lds\n"
"v_mfma_f32_16x16x32_bf16 a[116:119], v[184:187], v[132:135], 0\n"
"ds_read_b64_tr_b16 v[216:217], v229 offset:256\n"
"v_add_f32 v237, v237, v116\n"
"v_mfma_f32_16x16x32_bf16 a[116:119], v[188:191], v[84:87], a[116:119]\n"
"ds_read_b64_tr_b16 v[218:219], v229 offset:6400\n"
"v_mfma_f32_16x16x32_bf16 a[112:115], v[184:187], v[128:131], 0\n"
"v_exp_f32 v117, v117\n"
"v_mfma_f32_16x16x32_bf16 a[112:115], v[188:191], v[80:83], a[112:115]\n"
"ds_read_b64_tr_b16 v[220:221], v229 offset:12544\n"
"v_cvt_pk_bf16_f32 v150, v116, v117\n"
"v_mfma_f32_16x16x32_bf16 a[128:131], v[192:195], v[128:131], 0\n"
"ds_read_b64_tr_b16 v[222:223], v229 offset:18688\n"
"v_add_f32 v237, v237, v117\n"
"v_mfma_f32_16x16x32_bf16 a[128:131], v[196:199], v[80:83], a[128:131]\n"
"v_exp_f32 v118, v118\n"
"v_mfma_f32_16x16x32_bf16 a[132:135], v[192:195], v[132:135], 0\n"
"buffer_load_dwordx4 v231, s[56:59], s84 offen offset:1024 sc0 sc1 lds\n"
"v_mfma_f32_16x16x32_bf16 a[132:135], v[196:199], v[84:87], a[132:135]\n"
"v_add_f32 v237, v237, v118\n"
"v_mfma_f32_16x16x32_bf16 a[136:139], v[192:195], v[136:139], 0\n"
"v_exp_f32 v119, v119\n"
"v_mfma_f32_16x16x32_bf16 a[136:139], v[196:199], v[88:91], a[136:139]\n"
"v_cvt_pk_bf16_f32 v151, v118, v119\n"
"v_add_f32 v237, v237, v119\n"
"v_mfma_f32_16x16x32_bf16 a[140:143], v[192:195], v[140:143], 0\n"
"v_mfma_f32_16x16x32_bf16 a[140:143], v[196:199], v[92:95], a[140:143]\n"
"v_exp_f32 v120, v120\n"
"s_waitcnt lgkmcnt(0)\n"
"v_mfma_f32_16x16x32_bf16 a[156:159], v[200:203], v[140:143], 0\n"
"v_add_f32 v238, v238, v120\n"
"v_mfma_f32_16x16x32_bf16 a[156:159], v[204:207], v[92:95], a[156:159]\n"
"ds_read_b128 v[176:179], v224 offset:36864\n"
"v_mfma_f32_16x16x32_bf16 a[152:155], v[200:203], v[136:139], 0\n"
"v_exp_f32 v121, v121\n"
"v_mfma_f32_16x16x32_bf16 a[152:155], v[204:207], v[88:91], a[152:155]\n"
"buffer_load_dwordx4 v232, s[56:59], s84 offen offset:2048 sc0 sc1 lds\n"
"v_mfma_f32_16x16x32_bf16 a[148:151], v[200:203], v[132:135], 0\n"
"ds_read_b128 v[180:183], v225 offset:36864\n"
"v_cvt_pk_bf16_f32 v154, v120, v121\n"
"v_mfma_f32_16x16x32_bf16 a[148:151], v[204:207], v[84:87], a[148:151]\n"
"v_exp_f32 v122, v122\n"
"v_mfma_f32_16x16x32_bf16 a[144:147], v[200:203], v[128:131], 0\n"
"ds_read_b128 v[184:187], v224 offset:36992\n"
"v_add_f32 v238, v238, v121\n"
"v_mfma_f32_16x16x32_bf16 a[144:147], v[204:207], v[80:83], a[144:147]\n"
"ds_read_b128 v[188:191], v225 offset:36992\n"
"s_add_u32 s84, s84, 12288\n"
"v_mfma_f32_16x16x32_bf16 a[160:163], v[208:211], v[128:131], 0\n"
"v_exp_f32 v123, v123\n"
"v_mfma_f32_16x16x32_bf16 a[160:163], v[212:215], v[80:83], a[160:163]\n"
"ds_read_b128 v[192:195], v224 offset:37120\n"
"s_mov_b32 m0, s78\n"
"v_mfma_f32_16x16x32_bf16 a[164:167], v[208:211], v[132:135], 0\n"
"buffer_load_dwordx4 v230, s[56:59], s84 offen offset:0 sc0 sc1 lds\n"
"v_mfma_f32_16x16x32_bf16 a[164:167], v[212:215], v[84:87], a[164:167]\n"
"v_exp_f32 v124, v124\n"
"v_mfma_f32_16x16x32_bf16 a[168:171], v[208:211], v[136:139], 0\n"
"ds_read_b128 v[196:199], v225 offset:37120\n"
"v_cvt_pk_bf16_f32 v155, v122, v123\n"
"v_mfma_f32_16x16x32_bf16 a[168:171], v[212:215], v[88:91], a[168:171]\n"
"v_add_f32 v238, v238, v122\n"
"v_add_f32 v238, v238, v123\n"
"v_mfma_f32_16x16x32_bf16 a[172:175], v[208:211], v[140:143], 0\n"
"v_exp_f32 v125, v125\n"
"v_mfma_f32_16x16x32_bf16 a[172:175], v[212:215], v[92:95], a[172:175]\n"
"v_cvt_pk_bf16_f32 v158, v124, v125\n"
"v_add_f32 v239, v239, v124\n"
"v_mfma_f32_16x16x32_bf16 a[188:191], v[216:219], v[140:143], 0\n"
"v_add_f32 v239, v239, v125\n"
"v_mfma_f32_16x16x32_bf16 a[188:191], v[220:223], v[92:95], a[188:191]\n"
"v_exp_f32 v126, v126\n"
"v_mfma_f32_16x16x32_bf16 a[184:187], v[216:219], v[136:139], 0\n"
"buffer_load_dwordx4 v231, s[56:59], s84 offen offset:1024 sc0 sc1 lds\n"
"v_mfma_f32_16x16x32_bf16 a[184:187], v[220:223], v[88:91], a[184:187]\n"
"v_add_f32 v239, v239, v126\n"
"v_mfma_f32_16x16x32_bf16 a[180:183], v[216:219], v[132:135], 0\n"
"v_mfma_f32_16x16x32_bf16 a[180:183], v[220:223], v[84:87], a[180:183]\n"
"v_exp_f32 v127, v127\n"
"v_mfma_f32_16x16x32_bf16 a[176:179], v[216:219], v[128:131], 0\n"
"v_cvt_pk_bf16_f32 v159, v126, v127\n"
"v_add_f32 v239, v239, v127\n"
"v_mfma_f32_16x16x32_bf16 a[176:179], v[220:223], v[80:83], a[176:179]\n"
"s_waitcnt lgkmcnt(0)\n"
"v_mfma_f32_16x16x32_bf16 v[96:99], v[176:179], v[0:3], v[160:163]\n"
"v_mfma_f32_16x16x32_bf16 v[96:99], v[180:183], v[16:19], v[96:99]\n"
"ds_read_b128 v[200:203], v224 offset:43008\n"
"v_mfma_f32_16x16x32_bf16 v[96:99], v[184:187], v[32:35], v[96:99]\n"
"buffer_load_dwordx4 v232, s[56:59], s84 offen offset:2048 sc0 sc1 lds\n"
"v_mfma_f32_16x16x32_bf16 v[96:99], v[188:191], v[48:51], v[96:99]\n"
"ds_read_b128 v[204:207], v225 offset:43008\n"
"v_mfma_f32_16x16x32_bf16 v[96:99], v[192:195], v[64:67], v[96:99]\n"
"ds_read_b128 v[208:211], v224 offset:43136\n"
"v_mfma_f32_16x16x32_bf16 v[96:99], v[196:199], a[192:195], v[96:99]\n"
"ds_read_b128 v[212:215], v225 offset:43136\n"
"v_mfma_f32_16x16x32_bf16 v[100:103], v[176:179], v[4:7], v[164:167]\n"
"ds_read_b128 v[216:219], v224 offset:43264\n"
"v_mfma_f32_16x16x32_bf16 v[100:103], v[180:183], v[20:23], v[100:103]\n"
"ds_read_b128 v[220:223], v225 offset:43264\n"
"v_mfma_f32_16x16x32_bf16 v[100:103], v[184:187], v[36:39], v[100:103]\n"
"s_nop 1\n"
"v_exp_f32 v96, v96\n"
"v_mfma_f32_16x16x32_bf16 v[100:103], v[188:191], v[52:55], v[100:103]\n"
"v_add_f32 v236, v236, v96\n"
"v_mfma_f32_16x16x32_bf16 v[100:103], v[192:195], v[68:71], v[100:103]\n"
"v_exp_f32 v97, v97\n"
"v_mfma_f32_16x16x32_bf16 v[100:103], v[196:199], a[196:199], v[100:103]\n"
"v_cvt_pk_bf16_f32 v80, v96, v97\n"
"v_add_f32 v236, v236, v97\n"
"v_mfma_f32_16x16x32_bf16 v[104:107], v[176:179], v[8:11], v[168:171]\n"
"v_mfma_f32_16x16x32_bf16 v[104:107], v[180:183], v[24:27], v[104:107]\n"
"v_exp_f32 v98, v98\n"
"v_mfma_f32_16x16x32_bf16 v[104:107], v[184:187], v[40:43], v[104:107]\n"
"v_add_f32 v236, v236, v98\n"
"v_mfma_f32_16x16x32_bf16 v[104:107], v[188:191], v[56:59], v[104:107]\n"
"v_exp_f32 v99, v99\n"
"v_mfma_f32_16x16x32_bf16 v[104:107], v[192:195], v[72:75], v[104:107]\n"
"v_cvt_pk_bf16_f32 v81, v98, v99\n"
"v_add_f32 v236, v236, v99\n"
"v_mfma_f32_16x16x32_bf16 v[104:107], v[196:199], a[200:203], v[104:107]\n"
"v_mfma_f32_16x16x32_bf16 v[108:111], v[176:179], v[12:15], v[172:175]\n"
"v_exp_f32 v100, v100\n"
"v_mfma_f32_16x16x32_bf16 v[108:111], v[180:183], v[28:31], v[108:111]\n"
"v_add_f32 v237, v237, v100\n"
"v_mfma_f32_16x16x32_bf16 v[108:111], v[184:187], v[44:47], v[108:111]\n"
"v_exp_f32 v101, v101\n"
"v_mfma_f32_16x16x32_bf16 v[108:111], v[188:191], v[60:63], v[108:111]\n"
"v_cvt_pk_bf16_f32 v84, v100, v101\n"
"v_add_f32 v237, v237, v101\n"
"v_mfma_f32_16x16x32_bf16 v[108:111], v[192:195], v[76:79], v[108:111]\n"
"v_mfma_f32_16x16x32_bf16 v[108:111], v[196:199], a[204:207], v[108:111]\n"
"v_exp_f32 v102, v102\n"
"s_waitcnt lgkmcnt(0)\n"
"v_mfma_f32_16x16x32_bf16 v[112:115], v[200:203], v[0:3], v[160:163]\n"
"v_add_f32 v237, v237, v102\n"
"v_mfma_f32_16x16x32_bf16 v[112:115], v[204:207], v[16:19], v[112:115]\n"
"v_exp_f32 v103, v103\n"
"v_mfma_f32_16x16x32_bf16 v[112:115], v[208:211], v[32:35], v[112:115]\n"
"ds_read_b128 v[176:179], v224 offset:0\n"
"v_cvt_pk_bf16_f32 v85, v102, v103\n"
"v_mfma_f32_16x16x32_bf16 v[112:115], v[212:215], v[48:51], v[112:115]\n"
"ds_read_b128 v[180:183], v225 offset:0\n"
"v_add_f32 v237, v237, v103\n"
"v_mfma_f32_16x16x32_bf16 v[112:115], v[216:219], v[64:67], v[112:115]\n"
"v_exp_f32 v104, v104\n"
"v_mfma_f32_16x16x32_bf16 v[112:115], v[220:223], a[192:195], v[112:115]\n"
"ds_read_b128 v[184:187], v224 offset:128\n"
"v_add_f32 v238, v238, v104\n"
"v_mfma_f32_16x16x32_bf16 v[116:119], v[200:203], v[4:7], v[164:167]\n"
"v_exp_f32 v105, v105\n"
"v_mfma_f32_16x16x32_bf16 v[116:119], v[204:207], v[20:23], v[116:119]\n"
"ds_read_b128 v[188:191], v225 offset:128\n"
"v_cvt_pk_bf16_f32 v88, v104, v105\n"
"v_mfma_f32_16x16x32_bf16 v[116:119], v[208:211], v[36:39], v[116:119]\n"
"ds_read_b128 v[192:195], v224 offset:256\n"
"v_add_f32 v238, v238, v105\n"
"v_mfma_f32_16x16x32_bf16 v[116:119], v[212:215], v[52:55], v[116:119]\n"
"v_exp_f32 v106, v106\n"
"v_mfma_f32_16x16x32_bf16 v[116:119], v[216:219], v[68:71], v[116:119]\n"
"ds_read_b128 v[196:199], v225 offset:256\n"
"v_add_f32 v238, v238, v106\n"
"v_mfma_f32_16x16x32_bf16 v[116:119], v[220:223], a[196:199], v[116:119]\n"
"v_exp_f32 v107, v107\n"
"v_mfma_f32_16x16x32_bf16 v[120:123], v[200:203], v[8:11], v[168:171]\n"
"v_cvt_pk_bf16_f32 v89, v106, v107\n"
"v_add_f32 v238, v238, v107\n"
"v_mfma_f32_16x16x32_bf16 v[120:123], v[204:207], v[24:27], v[120:123]\n"
"v_mfma_f32_16x16x32_bf16 v[120:123], v[208:211], v[40:43], v[120:123]\n"
"v_exp_f32 v108, v108\n"
"v_mfma_f32_16x16x32_bf16 v[120:123], v[212:215], v[56:59], v[120:123]\n"
"v_add_f32 v239, v239, v108\n"
"v_mfma_f32_16x16x32_bf16 v[120:123], v[216:219], v[72:75], v[120:123]\n"
"v_exp_f32 v109, v109\n"
"v_mfma_f32_16x16x32_bf16 v[120:123], v[220:223], a[200:203], v[120:123]\n"
"v_cvt_pk_bf16_f32 v92, v108, v109\n"
"v_add_f32 v239, v239, v109\n"
"v_mfma_f32_16x16x32_bf16 v[124:127], v[200:203], v[12:15], v[172:175]\n"
"v_mfma_f32_16x16x32_bf16 v[124:127], v[204:207], v[28:31], v[124:127]\n"
"v_exp_f32 v110, v110\n"
"v_mfma_f32_16x16x32_bf16 v[124:127], v[208:211], v[44:47], v[124:127]\n"
"v_add_f32 v239, v239, v110\n"
"v_mfma_f32_16x16x32_bf16 v[124:127], v[212:215], v[60:63], v[124:127]\n"
"v_exp_f32 v111, v111\n"
"v_mfma_f32_16x16x32_bf16 v[124:127], v[216:219], v[76:79], v[124:127]\n"
"v_cvt_pk_bf16_f32 v93, v110, v111\n"
"v_add_f32 v239, v239, v111\n"
"v_mfma_f32_16x16x32_bf16 v[124:127], v[220:223], a[204:207], v[124:127]\n"
"s_mov_b32 s30, 14\n"
"ATTLOOP_%=:\n"
"s_waitcnt lgkmcnt(0)\n"
"v_mfma_f32_16x16x32_bf16 v[96:99], v[176:179], v[0:3], v[160:163]\n"
"v_exp_f32 v112, v112\n"
"v_mfma_f32_16x16x32_bf16 v[96:99], v[180:183], v[16:19], v[96:99]\n"
"ds_read_b128 v[200:203], v224 offset:6144\n"
"v_add_f32 v236, v236, v112\n"
"v_mfma_f32_16x16x32_bf16 v[96:99], v[184:187], v[32:35], v[96:99]\n"
"v_exp_f32 v113, v113\n"
"v_mfma_f32_16x16x32_bf16 v[96:99], v[188:191], v[48:51], v[96:99]\n"
"ds_read_b128 v[204:207], v225 offset:6144\n"
"v_cvt_pk_bf16_f32 v82, v112, v113\n"
"v_mfma_f32_16x16x32_bf16 v[96:99], v[192:195], v[64:67], v[96:99]\n"
"ds_read_b128 v[208:211], v224 offset:6272\n"
"v_add_f32 v236, v236, v113\n"
"v_mfma_f32_16x16x32_bf16 v[96:99], v[196:199], a[192:195], v[96:99]\n"
"v_exp_f32 v114, v114\n"
"v_mfma_f32_16x16x32_bf16 v[100:103], v[176:179], v[4:7], v[164:167]\n"
"ds_read_b128 v[212:215], v225 offset:6272\n"
"v_add_f32 v236, v236, v114\n"
"v_mfma_f32_16x16x32_bf16 v[100:103], v[180:183], v[20:23], v[100:103]\n"
"v_exp_f32 v115, v115\n"
"v_mfma_f32_16x16x32_bf16 v[100:103], v[184:187], v[36:39], v[100:103]\n"
"ds_read_b128 v[216:219], v224 offset:6400\n"
"v_cvt_pk_bf16_f32 v83, v114, v115\n"
"v_mfma_f32_16x16x32_bf16 v[100:103], v[188:191], v[52:55], v[100:103]\n"
"ds_read_b128 v[220:223], v225 offset:6400\n"
"v_add_f32 v236, v236, v115\n"
"v_mfma_f32_16x16x32_bf16 v[100:103], v[192:195], v[68:71], v[100:103]\n"
"v_exp_f32 v116, v116\n"
"v_mfma_f32_16x16x32_bf16 v[100:103], v[196:199], a[196:199], v[100:103]\n"
"v_add_f32 v237, v237, v116\n"
"v_mfma_f32_16x16x32_bf16 v[104:107], v[176:179], v[8:11], v[168:171]\n"
"v_exp_f32 v117, v117\n"
"v_mfma_f32_16x16x32_bf16 v[104:107], v[180:183], v[24:27], v[104:107]\n"
"v_cvt_pk_bf16_f32 v86, v116, v117\n"
"v_add_f32 v237, v237, v117\n"
"v_mfma_f32_16x16x32_bf16 v[104:107], v[184:187], v[40:43], v[104:107]\n"
"v_mfma_f32_16x16x32_bf16 v[104:107], v[188:191], v[56:59], v[104:107]\n"
"v_exp_f32 v118, v118\n"
"v_mfma_f32_16x16x32_bf16 v[104:107], v[192:195], v[72:75], v[104:107]\n"
"v_add_f32 v237, v237, v118\n"
"v_mfma_f32_16x16x32_bf16 v[104:107], v[196:199], a[200:203], v[104:107]\n"
"s_waitcnt vmcnt(6)\n"
"s_barrier\n"
"v_exp_f32 v119, v119\n"
"v_mfma_f32_16x16x32_bf16 v[108:111], v[176:179], v[12:15], v[172:175]\n"
"v_cvt_pk_bf16_f32 v87, v118, v119\n"
"v_add_f32 v237, v237, v119\n"
"v_mfma_f32_16x16x32_bf16 v[108:111], v[180:183], v[28:31], v[108:111]\n"
"ds_read_b64_tr_b16 v[176:177], v226 offset:24576\n"
"v_mfma_f32_16x16x32_bf16 v[108:111], v[184:187], v[44:47], v[108:111]\n"
"v_exp_f32 v120, v120\n"
"v_mfma_f32_16x16x32_bf16 v[108:111], v[188:191], v[60:63], v[108:111]\n"
"ds_read_b64_tr_b16 v[178:179], v226 offset:30720\n"
"v_add_f32 v238, v238, v120\n"
"v_mfma_f32_16x16x32_bf16 v[108:111], v[192:195], v[76:79], v[108:111]\n"
"v_exp_f32 v121, v121\n"
"v_mfma_f32_16x16x32_bf16 v[108:111], v[196:199], a[204:207], v[108:111]\n"
"ds_read_b64_tr_b16 v[180:181], v226 offset:36864\n"
"v_cvt_pk_bf16_f32 v90, v120, v121\n"
"s_waitcnt lgkmcnt(3)\n"
"v_mfma_f32_16x16x32_bf16 v[112:115], v[200:203], v[0:3], v[160:163]\n"
"ds_read_b64_tr_b16 v[182:183], v226 offset:43008\n"
"v_add_f32 v238, v238, v121\n"
"v_mfma_f32_16x16x32_bf16 v[112:115], v[204:207], v[16:19], v[112:115]\n"
"v_exp_f32 v122, v122\n"
"v_mfma_f32_16x16x32_bf16 v[112:115], v[208:211], v[32:35], v[112:115]\n"
"ds_read_b64_tr_b16 v[190:191], v227 offset:43008\n"
"v_add_f32 v238, v238, v122\n"
"v_mfma_f32_16x16x32_bf16 v[112:115], v[212:215], v[48:51], v[112:115]\n"
"v_exp_f32 v123, v123\n"
"v_mfma_f32_16x16x32_bf16 v[112:115], v[216:219], v[64:67], v[112:115]\n"
"ds_read_b64_tr_b16 v[184:185], v227 offset:24576\n"
"v_cvt_pk_bf16_f32 v91, v122, v123\n"
"v_mfma_f32_16x16x32_bf16 v[112:115], v[220:223], a[192:195], v[112:115]\n"
"ds_read_b64_tr_b16 v[186:187], v227 offset:30720\n"
"v_add_f32 v238, v238, v123\n"
"v_mfma_f32_16x16x32_bf16 v[116:119], v[200:203], v[4:7], v[164:167]\n"
"v_exp_f32 v124, v124\n"
"v_mfma_f32_16x16x32_bf16 v[116:119], v[204:207], v[20:23], v[116:119]\n"
"ds_read_b64_tr_b16 v[188:189], v227 offset:36864\n"
"v_add_f32 v239, v239, v124\n"
"v_mfma_f32_16x16x32_bf16 v[116:119], v[208:211], v[36:39], v[116:119]\n"
"v_exp_f32 v125, v125\n"
"v_mfma_f32_16x16x32_bf16 v[116:119], v[212:215], v[52:55], v[116:119]\n"
"v_cvt_pk_bf16_f32 v94, v124, v125\n"
"v_add_f32 v239, v239, v125\n"
"v_mfma_f32_16x16x32_bf16 v[116:119], v[216:219], v[68:71], v[116:119]\n"
"ds_read_b64_tr_b16 v[198:199], v228 offset:43008\n"
"v_mfma_f32_16x16x32_bf16 v[116:119], v[220:223], a[196:199], v[116:119]\n"
"v_exp_f32 v126, v126\n"
"v_mfma_f32_16x16x32_bf16 v[120:123], v[200:203], v[8:11], v[168:171]\n"
"ds_read_b64_tr_b16 v[192:193], v228 offset:24576\n"
"v_add_f32 v239, v239, v126\n"
"v_mfma_f32_16x16x32_bf16 v[120:123], v[204:207], v[24:27], v[120:123]\n"
"ds_read_b64_tr_b16 v[194:195], v228 offset:30720\n"
"v_mfma_f32_16x16x32_bf16 v[120:123], v[208:211], v[40:43], v[120:123]\n"
"v_exp_f32 v127, v127\n"
"v_mfma_f32_16x16x32_bf16 v[120:123], v[212:215], v[56:59], v[120:123]\n"
"ds_read_b64_tr_b16 v[196:197], v228 offset:36864\n"
"v_cvt_pk_bf16_f32 v95, v126, v127\n"
"v_mfma_f32_16x16x32_bf16 v[120:123], v[216:219], v[72:75], v[120:123]\n"
"v_add_f32 v239, v239, v127\n"
"v_mfma_f32_16x16x32_bf16 v[120:123], v[220:223], a[200:203], v[120:123]\n"
"v_mfma_f32_16x16x32_bf16 v[124:127], v[200:203], v[12:15], v[172:175]\n"
"v_mfma_f32_16x16x32_bf16 v[124:127], v[204:207], v[28:31], v[124:127]\n"
"ds_read_b64_tr_b16 v[202:203], v229 offset:30720\n"
"v_mfma_f32_16x16x32_bf16 v[124:127], v[208:211], v[44:47], v[124:127]\n"
"ds_read_b64_tr_b16 v[200:201], v229 offset:24576\n"
"v_mfma_f32_16x16x32_bf16 v[124:127], v[212:215], v[60:63], v[124:127]\n"
"v_exp_f32 v96, v96\n"
"v_mfma_f32_16x16x32_bf16 v[124:127], v[216:219], v[76:79], v[124:127]\n"
"ds_read_b64_tr_b16 v[204:205], v229 offset:36864\n"
"v_add_f32 v236, v236, v96\n"
"v_mfma_f32_16x16x32_bf16 v[124:127], v[220:223], a[204:207], v[124:127]\n"
"ds_read_b64_tr_b16 v[206:207], v229 offset:43008\n"
"s_add_u32 s85, s85, 12288\n"
"s_waitcnt lgkmcnt(4)\n"
"v_mfma_f32_16x16x32_bf16 a[0:3], v[176:179], v[144:147], a[0:3]\n"
"v_exp_f32 v97, v97\n"
"v_mfma_f32_16x16x32_bf16 a[0:3], v[180:183], v[80:83], a[0:3]\n"
"v_cvt_pk_bf16_f32 v128, v96, v97\n"
"s_mov_b32 m0, s80\n"
"v_mfma_f32_16x16x32_bf16 a[4:7], v[176:179], v[148:151], a[4:7]\n"
"buffer_load_dwordx4 v233, s[60:63], s85 offen offset:0 sc0 sc1 lds\n"
"v_mfma_f32_16x16x32_bf16 a[4:7], v[180:183], v[84:87], a[4:7]\n"
"v_exp_f32 v98, v98\n"
"v_mfma_f32_16x16x32_bf16 a[8:11], v[176:179], v[152:155], a[8:11]\n"
"ds_read_b64_tr_b16 v[208:209], v226 offset:24704\n"
"v_add_f32 v236, v236, v97\n"
"v_mfma_f32_16x16x32_bf16 a[8:11], v[180:183], v[88:91], a[8:11]\n"
"ds_read_b64_tr_b16 v[210:211], v226 offset:30848\n"
"v_add_f32 v236, v236, v98\n"
"v_mfma_f32_16x16x32_bf16 a[12:15], v[176:179], v[156:159], a[12:15]\n"
"v_exp_f32 v99, v99\n"
"v_mfma_f32_16x16x32_bf16 a[12:15], v[180:183], v[92:95], a[12:15]\n"
"ds_read_b64_tr_b16 v[212:213], v226 offset:36992\n"
"v_cvt_pk_bf16_f32 v129, v98, v99\n"
"v_mfma_f32_16x16x32_bf16 a[28:31], v[184:187], v[156:159], a[28:31]\n"
"ds_read_b64_tr_b16 v[214:215], v226 offset:43136\n"
"v_add_f32 v236, v236, v99\n"
"v_mfma_f32_16x16x32_bf16 a[28:31], v[188:191], v[92:95], a[28:31]\n"
"v_exp_f32 v100, v100\n"
"v_mfma_f32_16x16x32_bf16 a[24:27], v[184:187], v[152:155], a[24:27]\n"
"buffer_load_dwordx4 v234, s[60:63], s85 offen offset:1024 sc0 sc1 lds\n"
"v_mfma_f32_16x16x32_bf16 a[24:27], v[188:191], v[88:91], a[24:27]\n"
"ds_read_b64_tr_b16 v[216:217], v227 offset:24704\n"
"v_add_f32 v237, v237, v100\n"
"v_mfma_f32_16x16x32_bf16 a[20:23], v[184:187], v[148:151], a[20:23]\n"
"v_exp_f32 v101, v101\n"
"v_mfma_f32_16x16x32_bf16 a[20:23], v[188:191], v[84:87], a[20:23]\n"
"ds_read_b64_tr_b16 v[218:219], v227 offset:30848\n"
"v_cvt_pk_bf16_f32 v132, v100, v101\n"
"v_mfma_f32_16x16x32_bf16 a[16:19], v[184:187], v[144:147], a[16:19]\n"
"ds_read_b64_tr_b16 v[220:221], v227 offset:36992\n"
"v_add_f32 v237, v237, v101\n"
"v_mfma_f32_16x16x32_bf16 a[16:19], v[188:191], v[80:83], a[16:19]\n"
"v_exp_f32 v102, v102\n"
"v_mfma_f32_16x16x32_bf16 a[32:35], v[192:195], v[144:147], a[32:35]\n"
"ds_read_b64_tr_b16 v[222:223], v227 offset:43136\n"
"v_add_f32 v237, v237, v102\n"
"v_mfma_f32_16x16x32_bf16 a[32:35], v[196:199], v[80:83], a[32:35]\n"
"ds_read_b64_tr_b16 v[182:183], v228 offset:43136\n"
"v_mfma_f32_16x16x32_bf16 a[36:39], v[192:195], v[148:151], a[36:39]\n"
"v_exp_f32 v103, v103\n"
"v_mfma_f32_16x16x32_bf16 a[36:39], v[196:199], v[84:87], a[36:39]\n"
"buffer_load_dwordx4 v235, s[60:63], s85 offen offset:2048 sc0 sc1 lds\n"
"v_mfma_f32_16x16x32_bf16 a[40:43], v[192:195], v[152:155], a[40:43]\n"
"ds_read_b64_tr_b16 v[176:177], v228 offset:24704\n"
"v_cvt_pk_bf16_f32 v133, v102, v103\n"
"v_mfma_f32_16x16x32_bf16 a[40:43], v[196:199], v[88:91], a[40:43]\n"
"v_exp_f32 v104, v104\n"
"v_mfma_f32_16x16x32_bf16 a[44:47], v[192:195], v[156:159], a[44:47]\n"
"ds_read_b64_tr_b16 v[178:179], v228 offset:30848\n"
"v_add_f32 v237, v237, v103\n"
"v_mfma_f32_16x16x32_bf16 a[44:47], v[196:199], v[92:95], a[44:47]\n"
"ds_read_b64_tr_b16 v[180:181], v228 offset:36992\n"
"s_add_u32 s85, s85, 12288\n"
"s_waitcnt lgkmcnt(4)\n"
"v_mfma_f32_16x16x32_bf16 a[60:63], v[200:203], v[156:159], a[60:63]\n"
"v_exp_f32 v105, v105\n"
"v_mfma_f32_16x16x32_bf16 a[60:63], v[204:207], v[92:95], a[60:63]\n"
"v_cvt_pk_bf16_f32 v136, v104, v105\n"
"s_mov_b32 m0, s81\n"
"v_mfma_f32_16x16x32_bf16 a[56:59], v[200:203], v[152:155], a[56:59]\n"
"buffer_load_dwordx4 v233, s[60:63], s85 offen offset:0 sc0 sc1 lds\n"
"v_mfma_f32_16x16x32_bf16 a[56:59], v[204:207], v[88:91], a[56:59]\n"
"v_exp_f32 v106, v106\n"
"v_mfma_f32_16x16x32_bf16 a[52:55], v[200:203], v[148:151], a[52:55]\n"
"ds_read_b64_tr_b16 v[184:185], v229 offset:24704\n"
"v_add_f32 v238, v238, v104\n"
"v_mfma_f32_16x16x32_bf16 a[52:55], v[204:207], v[84:87], a[52:55]\n"
"ds_read_b64_tr_b16 v[186:187], v229 offset:30848\n"
"v_add_f32 v238, v238, v105\n"
"v_mfma_f32_16x16x32_bf16 a[48:51], v[200:203], v[144:147], a[48:51]\n"
"ds_read_b64_tr_b16 v[188:189], v229 offset:36992\n"
"v_add_f32 v238, v238, v106\n"
"v_mfma_f32_16x16x32_bf16 a[48:51], v[204:207], v[80:83], a[48:51]\n"
"v_exp_f32 v107, v107\n"
"v_mfma_f32_16x16x32_bf16 a[64:67], v[208:211], v[144:147], a[64:67]\n"
"ds_read_b64_tr_b16 v[190:191], v229 offset:43136\n"
"v_cvt_pk_bf16_f32 v137, v106, v107\n"
"v_mfma_f32_16x16x32_bf16 a[64:67], v[212:215], v[80:83], a[64:67]\n"
"ds_read_b64_tr_b16 v[198:199], v226 offset:43264\n"
"v_add_f32 v238, v238, v107\n"
"v_mfma_f32_16x16x32_bf16 a[68:71], v[208:211], v[148:151], a[68:71]\n"
"v_exp_f32 v108, v108\n"
"v_mfma_f32_16x16x32_bf16 a[68:71], v[212:215], v[84:87], a[68:71]\n"
"buffer_load_dwordx4 v234, s[60:63], s85 offen offset:1024 sc0 sc1 lds\n"
"v_mfma_f32_16x16x32_bf16 a[72:75], v[208:211], v[152:155], a[72:75]\n"
"ds_read_b64_tr_b16 v[192:193], v226 offset:24832\n"
"v_add_f32 v239, v239, v108\n"
"v_mfma_f32_16x16x32_bf16 a[72:75], v[212:215], v[88:91], a[72:75]\n"
"v_exp_f32 v109, v109\n"
"v_mfma_f32_16x16x32_bf16 a[76:79], v[208:211], v[156:159], a[76:79]\n"
"ds_read_b64_tr_b16 v[194:195], v226 offset:30976\n"
"v_cvt_pk_bf16_f32 v140, v108, v109\n"
"v_mfma_f32_16x16x32_bf16 a[76:79], v[212:215], v[92:95], a[76:79]\n"
"ds_read_b64_tr_b16 v[196:197], v226 offset:37120\n"
"v_add_f32 v239, v239, v109\n"
"v_mfma_f32_16x16x32_bf16 a[92:95], v[216:219], v[156:159], a[92:95]\n"
"v_exp_f32 v110, v110\n"
"v_mfma_f32_16x16x32_bf16 a[92:95], v[220:223], v[92:95], a[92:95]\n"
"v_add_f32 v239, v239, v110\n"
"v_mfma_f32_16x16x32_bf16 a[88:91], v[216:219], v[152:155], a[88:91]\n"
"buffer_load_dwordx4 v235, s[60:63], s85 offen offset:2048 sc0 sc1 lds\n"
"v_mfma_f32_16x16x32_bf16 a[88:91], v[220:223], v[88:91], a[88:91]\n"
"v_exp_f32 v111, v111\n"
"v_mfma_f32_16x16x32_bf16 a[84:87], v[216:219], v[148:151], a[84:87]\n"
"ds_read_b64_tr_b16 v[200:201], v227 offset:24832\n"
"v_cvt_pk_bf16_f32 v141, v110, v111\n"
"v_mfma_f32_16x16x32_bf16 a[84:87], v[220:223], v[84:87], a[84:87]\n"
"ds_read_b64_tr_b16 v[202:203], v227 offset:30976\n"
"v_add_f32 v239, v239, v111\n"
"v_mfma_f32_16x16x32_bf16 a[80:83], v[216:219], v[144:147], a[80:83]\n"
"v_exp_f32 v112, v112\n"
"v_mfma_f32_16x16x32_bf16 a[80:83], v[220:223], v[80:83], a[80:83]\n"
"ds_read_b64_tr_b16 v[204:205], v227 offset:37120\n"
"v_add_f32 v236, v236, v112\n"
"s_waitcnt lgkmcnt(3)\n"
"v_mfma_f32_16x16x32_bf16 a[96:99], v[176:179], v[144:147], a[96:99]\n"
"ds_read_b64_tr_b16 v[206:207], v227 offset:43264\n"
"v_mfma_f32_16x16x32_bf16 a[96:99], v[180:183], v[80:83], a[96:99]\n"
"v_exp_f32 v113, v113\n"
"v_mfma_f32_16x16x32_bf16 a[100:103], v[176:179], v[148:151], a[100:103]\n"
"ds_read_b64_tr_b16 v[214:215], v228 offset:43264\n"
"v_cvt_pk_bf16_f32 v130, v112, v113\n"
"v_mfma_f32_16x16x32_bf16 a[100:103], v[180:183], v[84:87], a[100:103]\n"
"ds_read_b64_tr_b16 v[208:209], v228 offset:24832\n"
"v_add_f32 v236, v236, v113\n"
"v_mfma_f32_16x16x32_bf16 a[104:107], v[176:179], v[152:155], a[104:107]\n"
"s_waitcnt vmcnt(6)\n"
"s_barrier\n"
"v_exp_f32 v114, v114\n"
"v_mfma_f32_16x16x32_bf16 a[104:107], v[180:183], v[88:91], a[104:107]\n"
"ds_read_b64_tr_b16 v[210:211], v228 offset:30976\n"
"v_add_f32 v236, v236, v114\n"
"v_mfma_f32_16x16x32_bf16 a[108:111], v[176:179], v[156:159], a[108:111]\n"
"ds_read_b64_tr_b16 v[212:213], v228 offset:37120\n"
"v_mfma_f32_16x16x32_bf16 a[108:111], v[180:183], v[92:95], a[108:111]\n"
"v_exp_f32 v115, v115\n"
"v_mfma_f32_16x16x32_bf16 a[124:127], v[184:187], v[156:159], a[124:127]\n"
"v_cvt_pk_bf16_f32 v131, v114, v115\n"
"v_add_f32 v236, v236, v115\n"
"v_mfma_f32_16x16x32_bf16 a[124:127], v[188:191], v[92:95], a[124:127]\n"
"s_mov_b32 m0, s79\n"
"s_add_u32 s84, s84, 12288\n"
"v_mfma_f32_16x16x32_bf16 a[120:123], v[184:187], v[152:155], a[120:123]\n"
"v_exp_f32 v116, v116\n"
"v_mfma_f32_16x16x32_bf16 a[120:123], v[188:191], v[88:91], a[120:123]\n"
"buffer_load_dwordx4 v230, s[56:59], s84 offen offset:0 sc0 sc1 lds\n"
"v_mfma_f32_16x16x32_bf16 a[116:119], v[184:187], v[148:151], a[116:119]\n"
"ds_read_b64_tr_b16 v[216:217], v229 offset:24832\n"
"v_add_f32 v237, v237, v116\n"
"v_mfma_f32_16x16x32_bf16 a[116:119], v[188:191], v[84:87], a[116:119]\n"
"ds_read_b64_tr_b16 v[218:219], v229 offset:30976\n"
"v_mfma_f32_16x16x32_bf16 a[112:115], v[184:187], v[144:147], a[112:115]\n"
"v_exp_f32 v117, v117\n"
"v_mfma_f32_16x16x32_bf16 a[112:115], v[188:191], v[80:83], a[112:115]\n"
"ds_read_b64_tr_b16 v[220:221], v229 offset:37120\n"
"v_cvt_pk_bf16_f32 v134, v116, v117\n"
"v_mfma_f32_16x16x32_bf16 a[128:131], v[192:195], v[144:147], a[128:131]\n"
"ds_read_b64_tr_b16 v[222:223], v229 offset:43264\n"
"v_add_f32 v237, v237, v117\n"
"v_mfma_f32_16x16x32_bf16 a[128:131], v[196:199], v[80:83], a[128:131]\n"
"v_exp_f32 v118, v118\n"
"v_mfma_f32_16x16x32_bf16 a[132:135], v[192:195], v[148:151], a[132:135]\n"
"buffer_load_dwordx4 v231, s[56:59], s84 offen offset:1024 sc0 sc1 lds\n"
"v_mfma_f32_16x16x32_bf16 a[132:135], v[196:199], v[84:87], a[132:135]\n"
"v_add_f32 v237, v237, v118\n"
"v_mfma_f32_16x16x32_bf16 a[136:139], v[192:195], v[152:155], a[136:139]\n"
"v_exp_f32 v119, v119\n"
"v_mfma_f32_16x16x32_bf16 a[136:139], v[196:199], v[88:91], a[136:139]\n"
"v_cvt_pk_bf16_f32 v135, v118, v119\n"
"v_add_f32 v237, v237, v119\n"
"v_mfma_f32_16x16x32_bf16 a[140:143], v[192:195], v[156:159], a[140:143]\n"
"v_mfma_f32_16x16x32_bf16 a[140:143], v[196:199], v[92:95], a[140:143]\n"
"v_exp_f32 v120, v120\n"
"s_waitcnt lgkmcnt(0)\n"
"v_mfma_f32_16x16x32_bf16 a[156:159], v[200:203], v[156:159], a[156:159]\n"
"v_add_f32 v238, v238, v120\n"
"v_mfma_f32_16x16x32_bf16 a[156:159], v[204:207], v[92:95], a[156:159]\n"
"ds_read_b128 v[176:179], v224 offset:12288\n"
"v_mfma_f32_16x16x32_bf16 a[152:155], v[200:203], v[152:155], a[152:155]\n"
"v_exp_f32 v121, v121\n"
"v_mfma_f32_16x16x32_bf16 a[152:155], v[204:207], v[88:91], a[152:155]\n"
"buffer_load_dwordx4 v232, s[56:59], s84 offen offset:2048 sc0 sc1 lds\n"
"v_mfma_f32_16x16x32_bf16 a[148:151], v[200:203], v[148:151], a[148:151]\n"
"ds_read_b128 v[180:183], v225 offset:12288\n"
"v_cvt_pk_bf16_f32 v138, v120, v121\n"
"v_mfma_f32_16x16x32_bf16 a[148:151], v[204:207], v[84:87], a[148:151]\n"
"v_exp_f32 v122, v122\n"
"v_mfma_f32_16x16x32_bf16 a[144:147], v[200:203], v[144:147], a[144:147]\n"
"ds_read_b128 v[184:187], v224 offset:12416\n"
"v_add_f32 v238, v238, v121\n"
"v_mfma_f32_16x16x32_bf16 a[144:147], v[204:207], v[80:83], a[144:147]\n"
"ds_read_b128 v[188:191], v225 offset:12416\n"
"s_add_u32 s84, s84, 12288\n"
"v_mfma_f32_16x16x32_bf16 a[160:163], v[208:211], v[144:147], a[160:163]\n"
"v_exp_f32 v123, v123\n"
"v_mfma_f32_16x16x32_bf16 a[160:163], v[212:215], v[80:83], a[160:163]\n"
"ds_read_b128 v[192:195], v224 offset:12544\n"
"s_mov_b32 m0, s76\n"
"v_mfma_f32_16x16x32_bf16 a[164:167], v[208:211], v[148:151], a[164:167]\n"
"buffer_load_dwordx4 v230, s[56:59], s84 offen offset:0 sc0 sc1 lds\n"
"v_mfma_f32_16x16x32_bf16 a[164:167], v[212:215], v[84:87], a[164:167]\n"
"v_exp_f32 v124, v124\n"
"v_mfma_f32_16x16x32_bf16 a[168:171], v[208:211], v[152:155], a[168:171]\n"
"ds_read_b128 v[196:199], v225 offset:12544\n"
"v_cvt_pk_bf16_f32 v139, v122, v123\n"
"v_mfma_f32_16x16x32_bf16 a[168:171], v[212:215], v[88:91], a[168:171]\n"
"v_add_f32 v238, v238, v122\n"
"v_add_f32 v238, v238, v123\n"
"v_mfma_f32_16x16x32_bf16 a[172:175], v[208:211], v[156:159], a[172:175]\n"
"v_exp_f32 v125, v125\n"
"v_mfma_f32_16x16x32_bf16 a[172:175], v[212:215], v[92:95], a[172:175]\n"
"v_cvt_pk_bf16_f32 v142, v124, v125\n"
"v_add_f32 v239, v239, v124\n"
"v_mfma_f32_16x16x32_bf16 a[188:191], v[216:219], v[156:159], a[188:191]\n"
"v_add_f32 v239, v239, v125\n"
"v_mfma_f32_16x16x32_bf16 a[188:191], v[220:223], v[92:95], a[188:191]\n"
"v_exp_f32 v126, v126\n"
"v_mfma_f32_16x16x32_bf16 a[184:187], v[216:219], v[152:155], a[184:187]\n"
"buffer_load_dwordx4 v231, s[56:59], s84 offen offset:1024 sc0 sc1 lds\n"
"v_mfma_f32_16x16x32_bf16 a[184:187], v[220:223], v[88:91], a[184:187]\n"
"v_add_f32 v239, v239, v126\n"
"v_mfma_f32_16x16x32_bf16 a[180:183], v[216:219], v[148:151], a[180:183]\n"
"v_mfma_f32_16x16x32_bf16 a[180:183], v[220:223], v[84:87], a[180:183]\n"
"v_exp_f32 v127, v127\n"
"v_mfma_f32_16x16x32_bf16 a[176:179], v[216:219], v[144:147], a[176:179]\n"
"v_cvt_pk_bf16_f32 v143, v126, v127\n"
"v_add_f32 v239, v239, v127\n"
"v_mfma_f32_16x16x32_bf16 a[176:179], v[220:223], v[80:83], a[176:179]\n"
"s_waitcnt lgkmcnt(0)\n"
"v_mfma_f32_16x16x32_bf16 v[96:99], v[176:179], v[0:3], v[160:163]\n"
"v_mfma_f32_16x16x32_bf16 v[96:99], v[180:183], v[16:19], v[96:99]\n"
"ds_read_b128 v[200:203], v224 offset:18432\n"
"v_mfma_f32_16x16x32_bf16 v[96:99], v[184:187], v[32:35], v[96:99]\n"
"buffer_load_dwordx4 v232, s[56:59], s84 offen offset:2048 sc0 sc1 lds\n"
"v_mfma_f32_16x16x32_bf16 v[96:99], v[188:191], v[48:51], v[96:99]\n"
"ds_read_b128 v[204:207], v225 offset:18432\n"
"v_mfma_f32_16x16x32_bf16 v[96:99], v[192:195], v[64:67], v[96:99]\n"
"ds_read_b128 v[208:211], v224 offset:18560\n"
"v_mfma_f32_16x16x32_bf16 v[96:99], v[196:199], a[192:195], v[96:99]\n"
"ds_read_b128 v[212:215], v225 offset:18560\n"
"v_mfma_f32_16x16x32_bf16 v[100:103], v[176:179], v[4:7], v[164:167]\n"
"ds_read_b128 v[216:219], v224 offset:18688\n"
"v_mfma_f32_16x16x32_bf16 v[100:103], v[180:183], v[20:23], v[100:103]\n"
"ds_read_b128 v[220:223], v225 offset:18688\n"
"v_mfma_f32_16x16x32_bf16 v[100:103], v[184:187], v[36:39], v[100:103]\n"
"s_nop 1\n"
"v_exp_f32 v96, v96\n"
"v_mfma_f32_16x16x32_bf16 v[100:103], v[188:191], v[52:55], v[100:103]\n"
"v_add_f32 v236, v236, v96\n"
"v_mfma_f32_16x16x32_bf16 v[100:103], v[192:195], v[68:71], v[100:103]\n"
"v_exp_f32 v97, v97\n"
"v_mfma_f32_16x16x32_bf16 v[100:103], v[196:199], a[196:199], v[100:103]\n"
"v_cvt_pk_bf16_f32 v80, v96, v97\n"
"v_add_f32 v236, v236, v97\n"
"v_mfma_f32_16x16x32_bf16 v[104:107], v[176:179], v[8:11], v[168:171]\n"
"v_mfma_f32_16x16x32_bf16 v[104:107], v[180:183], v[24:27], v[104:107]\n"
"v_exp_f32 v98, v98\n"
"v_mfma_f32_16x16x32_bf16 v[104:107], v[184:187], v[40:43], v[104:107]\n"
"v_add_f32 v236, v236, v98\n"
"v_mfma_f32_16x16x32_bf16 v[104:107], v[188:191], v[56:59], v[104:107]\n"
"v_exp_f32 v99, v99\n"
"v_mfma_f32_16x16x32_bf16 v[104:107], v[192:195], v[72:75], v[104:107]\n"
"v_cvt_pk_bf16_f32 v81, v98, v99\n"
"v_add_f32 v236, v236, v99\n"
"v_mfma_f32_16x16x32_bf16 v[104:107], v[196:199], a[200:203], v[104:107]\n"
"v_mfma_f32_16x16x32_bf16 v[108:111], v[176:179], v[12:15], v[172:175]\n"
"v_exp_f32 v100, v100\n"
"v_mfma_f32_16x16x32_bf16 v[108:111], v[180:183], v[28:31], v[108:111]\n"
"v_add_f32 v237, v237, v100\n"
"v_mfma_f32_16x16x32_bf16 v[108:111], v[184:187], v[44:47], v[108:111]\n"
"v_exp_f32 v101, v101\n"
"v_mfma_f32_16x16x32_bf16 v[108:111], v[188:191], v[60:63], v[108:111]\n"
"v_cvt_pk_bf16_f32 v84, v100, v101\n"
"v_add_f32 v237, v237, v101\n"
"v_mfma_f32_16x16x32_bf16 v[108:111], v[192:195], v[76:79], v[108:111]\n"
"v_mfma_f32_16x16x32_bf16 v[108:111], v[196:199], a[204:207], v[108:111]\n"
"v_exp_f32 v102, v102\n"
"s_waitcnt lgkmcnt(0)\n"
"v_mfma_f32_16x16x32_bf16 v[112:115], v[200:203], v[0:3], v[160:163]\n"
"v_add_f32 v237, v237, v102\n"
"v_mfma_f32_16x16x32_bf16 v[112:115], v[204:207], v[16:19], v[112:115]\n"
"v_exp_f32 v103, v103\n"
"v_mfma_f32_16x16x32_bf16 v[112:115], v[208:211], v[32:35], v[112:115]\n"
"ds_read_b128 v[176:179], v224 offset:24576\n"
"v_cvt_pk_bf16_f32 v85, v102, v103\n"
"v_mfma_f32_16x16x32_bf16 v[112:115], v[212:215], v[48:51], v[112:115]\n"
"ds_read_b128 v[180:183], v225 offset:24576\n"
"v_add_f32 v237, v237, v103\n"
"v_mfma_f32_16x16x32_bf16 v[112:115], v[216:219], v[64:67], v[112:115]\n"
"v_exp_f32 v104, v104\n"
"v_mfma_f32_16x16x32_bf16 v[112:115], v[220:223], a[192:195], v[112:115]\n"
"ds_read_b128 v[184:187], v224 offset:24704\n"
"v_add_f32 v238, v238, v104\n"
"v_mfma_f32_16x16x32_bf16 v[116:119], v[200:203], v[4:7], v[164:167]\n"
"v_exp_f32 v105, v105\n"
"v_mfma_f32_16x16x32_bf16 v[116:119], v[204:207], v[20:23], v[116:119]\n"
"ds_read_b128 v[188:191], v225 offset:24704\n"
"v_cvt_pk_bf16_f32 v88, v104, v105\n"
"v_mfma_f32_16x16x32_bf16 v[116:119], v[208:211], v[36:39], v[116:119]\n"
"ds_read_b128 v[192:195], v224 offset:24832\n"
"v_add_f32 v238, v238, v105\n"
"v_mfma_f32_16x16x32_bf16 v[116:119], v[212:215], v[52:55], v[116:119]\n"
"v_exp_f32 v106, v106\n"
"v_mfma_f32_16x16x32_bf16 v[116:119], v[216:219], v[68:71], v[116:119]\n"
"ds_read_b128 v[196:199], v225 offset:24832\n"
"v_add_f32 v238, v238, v106\n"
"v_mfma_f32_16x16x32_bf16 v[116:119], v[220:223], a[196:199], v[116:119]\n"
"v_exp_f32 v107, v107\n"
"v_mfma_f32_16x16x32_bf16 v[120:123], v[200:203], v[8:11], v[168:171]\n"
"v_cvt_pk_bf16_f32 v89, v106, v107\n"
"v_add_f32 v238, v238, v107\n"
"v_mfma_f32_16x16x32_bf16 v[120:123], v[204:207], v[24:27], v[120:123]\n"
"v_mfma_f32_16x16x32_bf16 v[120:123], v[208:211], v[40:43], v[120:123]\n"
"v_exp_f32 v108, v108\n"
"v_mfma_f32_16x16x32_bf16 v[120:123], v[212:215], v[56:59], v[120:123]\n"
"v_add_f32 v239, v239, v108\n"
"v_mfma_f32_16x16x32_bf16 v[120:123], v[216:219], v[72:75], v[120:123]\n"
"v_exp_f32 v109, v109\n"
"v_mfma_f32_16x16x32_bf16 v[120:123], v[220:223], a[200:203], v[120:123]\n"
"v_cvt_pk_bf16_f32 v92, v108, v109\n"
"v_add_f32 v239, v239, v109\n"
"v_mfma_f32_16x16x32_bf16 v[124:127], v[200:203], v[12:15], v[172:175]\n"
"v_mfma_f32_16x16x32_bf16 v[124:127], v[204:207], v[28:31], v[124:127]\n"
"v_exp_f32 v110, v110\n"
"v_mfma_f32_16x16x32_bf16 v[124:127], v[208:211], v[44:47], v[124:127]\n"
"v_add_f32 v239, v239, v110\n"
"v_mfma_f32_16x16x32_bf16 v[124:127], v[212:215], v[60:63], v[124:127]\n"
"v_exp_f32 v111, v111\n"
"v_mfma_f32_16x16x32_bf16 v[124:127], v[216:219], v[76:79], v[124:127]\n"
"v_cvt_pk_bf16_f32 v93, v110, v111\n"
"v_add_f32 v239, v239, v111\n"
"v_mfma_f32_16x16x32_bf16 v[124:127], v[220:223], a[204:207], v[124:127]\n"
"s_waitcnt lgkmcnt(0)\n"
"v_mfma_f32_16x16x32_bf16 v[96:99], v[176:179], v[0:3], v[160:163]\n"
"v_exp_f32 v112, v112\n"
"v_mfma_f32_16x16x32_bf16 v[96:99], v[180:183], v[16:19], v[96:99]\n"
"ds_read_b128 v[200:203], v224 offset:30720\n"
"v_add_f32 v236, v236, v112\n"
"v_mfma_f32_16x16x32_bf16 v[96:99], v[184:187], v[32:35], v[96:99]\n"
"v_exp_f32 v113, v113\n"
"v_mfma_f32_16x16x32_bf16 v[96:99], v[188:191], v[48:51], v[96:99]\n"
"ds_read_b128 v[204:207], v225 offset:30720\n"
"v_cvt_pk_bf16_f32 v82, v112, v113\n"
"v_mfma_f32_16x16x32_bf16 v[96:99], v[192:195], v[64:67], v[96:99]\n"
"ds_read_b128 v[208:211], v224 offset:30848\n"
"v_add_f32 v236, v236, v113\n"
"v_mfma_f32_16x16x32_bf16 v[96:99], v[196:199], a[192:195], v[96:99]\n"
"v_exp_f32 v114, v114\n"
"v_mfma_f32_16x16x32_bf16 v[100:103], v[176:179], v[4:7], v[164:167]\n"
"ds_read_b128 v[212:215], v225 offset:30848\n"
"v_add_f32 v236, v236, v114\n"
"v_mfma_f32_16x16x32_bf16 v[100:103], v[180:183], v[20:23], v[100:103]\n"
"v_exp_f32 v115, v115\n"
"v_mfma_f32_16x16x32_bf16 v[100:103], v[184:187], v[36:39], v[100:103]\n"
"ds_read_b128 v[216:219], v224 offset:30976\n"
"v_cvt_pk_bf16_f32 v83, v114, v115\n"
"v_mfma_f32_16x16x32_bf16 v[100:103], v[188:191], v[52:55], v[100:103]\n"
"ds_read_b128 v[220:223], v225 offset:30976\n"
"v_add_f32 v236, v236, v115\n"
"v_mfma_f32_16x16x32_bf16 v[100:103], v[192:195], v[68:71], v[100:103]\n"
"v_exp_f32 v116, v116\n"
"v_mfma_f32_16x16x32_bf16 v[100:103], v[196:199], a[196:199], v[100:103]\n"
"v_add_f32 v237, v237, v116\n"
"v_mfma_f32_16x16x32_bf16 v[104:107], v[176:179], v[8:11], v[168:171]\n"
"v_exp_f32 v117, v117\n"
"v_mfma_f32_16x16x32_bf16 v[104:107], v[180:183], v[24:27], v[104:107]\n"
"v_cvt_pk_bf16_f32 v86, v116, v117\n"
"v_add_f32 v237, v237, v117\n"
"v_mfma_f32_16x16x32_bf16 v[104:107], v[184:187], v[40:43], v[104:107]\n"
"v_mfma_f32_16x16x32_bf16 v[104:107], v[188:191], v[56:59], v[104:107]\n"
"v_exp_f32 v118, v118\n"
"v_mfma_f32_16x16x32_bf16 v[104:107], v[192:195], v[72:75], v[104:107]\n"
"v_add_f32 v237, v237, v118\n"
"v_mfma_f32_16x16x32_bf16 v[104:107], v[196:199], a[200:203], v[104:107]\n"
"s_waitcnt vmcnt(6)\n"
"s_barrier\n"
"v_exp_f32 v119, v119\n"
"v_mfma_f32_16x16x32_bf16 v[108:111], v[176:179], v[12:15], v[172:175]\n"
"v_cvt_pk_bf16_f32 v87, v118, v119\n"
"v_add_f32 v237, v237, v119\n"
"v_mfma_f32_16x16x32_bf16 v[108:111], v[180:183], v[28:31], v[108:111]\n"
"ds_read_b64_tr_b16 v[176:177], v226 offset:0\n"
"v_mfma_f32_16x16x32_bf16 v[108:111], v[184:187], v[44:47], v[108:111]\n"
"v_exp_f32 v120, v120\n"
"v_mfma_f32_16x16x32_bf16 v[108:111], v[188:191], v[60:63], v[108:111]\n"
"ds_read_b64_tr_b16 v[178:179], v226 offset:6144\n"
"v_add_f32 v238, v238, v120\n"
"v_mfma_f32_16x16x32_bf16 v[108:111], v[192:195], v[76:79], v[108:111]\n"
"v_exp_f32 v121, v121\n"
"v_mfma_f32_16x16x32_bf16 v[108:111], v[196:199], a[204:207], v[108:111]\n"
"ds_read_b64_tr_b16 v[180:181], v226 offset:12288\n"
"v_cvt_pk_bf16_f32 v90, v120, v121\n"
"s_waitcnt lgkmcnt(3)\n"
"v_mfma_f32_16x16x32_bf16 v[112:115], v[200:203], v[0:3], v[160:163]\n"
"ds_read_b64_tr_b16 v[182:183], v226 offset:18432\n"
"v_add_f32 v238, v238, v121\n"
"v_mfma_f32_16x16x32_bf16 v[112:115], v[204:207], v[16:19], v[112:115]\n"
"v_exp_f32 v122, v122\n"
"v_mfma_f32_16x16x32_bf16 v[112:115], v[208:211], v[32:35], v[112:115]\n"
"ds_read_b64_tr_b16 v[190:191], v227 offset:18432\n"
"v_add_f32 v238, v238, v122\n"
"v_mfma_f32_16x16x32_bf16 v[112:115], v[212:215], v[48:51], v[112:115]\n"
"v_exp_f32 v123, v123\n"
"v_mfma_f32_16x16x32_bf16 v[112:115], v[216:219], v[64:67], v[112:115]\n"
"ds_read_b64_tr_b16 v[184:185], v227 offset:0\n"
"v_cvt_pk_bf16_f32 v91, v122, v123\n"
"v_mfma_f32_16x16x32_bf16 v[112:115], v[220:223], a[192:195], v[112:115]\n"
"ds_read_b64_tr_b16 v[186:187], v227 offset:6144\n"
"v_add_f32 v238, v238, v123\n"
"v_mfma_f32_16x16x32_bf16 v[116:119], v[200:203], v[4:7], v[164:167]\n"
"v_exp_f32 v124, v124\n"
"v_mfma_f32_16x16x32_bf16 v[116:119], v[204:207], v[20:23], v[116:119]\n"
"ds_read_b64_tr_b16 v[188:189], v227 offset:12288\n"
"v_add_f32 v239, v239, v124\n"
"v_mfma_f32_16x16x32_bf16 v[116:119], v[208:211], v[36:39], v[116:119]\n"
"v_exp_f32 v125, v125\n"
"v_mfma_f32_16x16x32_bf16 v[116:119], v[212:215], v[52:55], v[116:119]\n"
"v_cvt_pk_bf16_f32 v94, v124, v125\n"
"v_add_f32 v239, v239, v125\n"
"v_mfma_f32_16x16x32_bf16 v[116:119], v[216:219], v[68:71], v[116:119]\n"
"ds_read_b64_tr_b16 v[198:199], v228 offset:18432\n"
"v_mfma_f32_16x16x32_bf16 v[116:119], v[220:223], a[196:199], v[116:119]\n"
"v_exp_f32 v126, v126\n"
"v_mfma_f32_16x16x32_bf16 v[120:123], v[200:203], v[8:11], v[168:171]\n"
"ds_read_b64_tr_b16 v[192:193], v228 offset:0\n"
"v_add_f32 v239, v239, v126\n"
"v_mfma_f32_16x16x32_bf16 v[120:123], v[204:207], v[24:27], v[120:123]\n"
"ds_read_b64_tr_b16 v[194:195], v228 offset:6144\n"
"v_mfma_f32_16x16x32_bf16 v[120:123], v[208:211], v[40:43], v[120:123]\n"
"v_exp_f32 v127, v127\n"
"v_mfma_f32_16x16x32_bf16 v[120:123], v[212:215], v[56:59], v[120:123]\n"
"ds_read_b64_tr_b16 v[196:197], v228 offset:12288\n"
"v_cvt_pk_bf16_f32 v95, v126, v127\n"
"v_mfma_f32_16x16x32_bf16 v[120:123], v[216:219], v[72:75], v[120:123]\n"
"v_add_f32 v239, v239, v127\n"
"v_mfma_f32_16x16x32_bf16 v[120:123], v[220:223], a[200:203], v[120:123]\n"
"v_mfma_f32_16x16x32_bf16 v[124:127], v[200:203], v[12:15], v[172:175]\n"
"v_mfma_f32_16x16x32_bf16 v[124:127], v[204:207], v[28:31], v[124:127]\n"
"ds_read_b64_tr_b16 v[202:203], v229 offset:6144\n"
"v_mfma_f32_16x16x32_bf16 v[124:127], v[208:211], v[44:47], v[124:127]\n"
"ds_read_b64_tr_b16 v[200:201], v229 offset:0\n"
"v_mfma_f32_16x16x32_bf16 v[124:127], v[212:215], v[60:63], v[124:127]\n"
"v_exp_f32 v96, v96\n"
"v_mfma_f32_16x16x32_bf16 v[124:127], v[216:219], v[76:79], v[124:127]\n"
"ds_read_b64_tr_b16 v[204:205], v229 offset:12288\n"
"v_add_f32 v236, v236, v96\n"
"v_mfma_f32_16x16x32_bf16 v[124:127], v[220:223], a[204:207], v[124:127]\n"
"ds_read_b64_tr_b16 v[206:207], v229 offset:18432\n"
"s_add_u32 s85, s85, 12288\n"
"s_waitcnt lgkmcnt(4)\n"
"v_mfma_f32_16x16x32_bf16 a[0:3], v[176:179], v[128:131], a[0:3]\n"
"v_exp_f32 v97, v97\n"
"v_mfma_f32_16x16x32_bf16 a[0:3], v[180:183], v[80:83], a[0:3]\n"
"v_cvt_pk_bf16_f32 v144, v96, v97\n"
"s_mov_b32 m0, s82\n"
"v_mfma_f32_16x16x32_bf16 a[4:7], v[176:179], v[132:135], a[4:7]\n"
"buffer_load_dwordx4 v233, s[60:63], s85 offen offset:0 sc0 sc1 lds\n"
"v_mfma_f32_16x16x32_bf16 a[4:7], v[180:183], v[84:87], a[4:7]\n"
"v_exp_f32 v98, v98\n"
"v_mfma_f32_16x16x32_bf16 a[8:11], v[176:179], v[136:139], a[8:11]\n"
"ds_read_b64_tr_b16 v[208:209], v226 offset:128\n"
"v_add_f32 v236, v236, v97\n"
"v_mfma_f32_16x16x32_bf16 a[8:11], v[180:183], v[88:91], a[8:11]\n"
"ds_read_b64_tr_b16 v[210:211], v226 offset:6272\n"
"v_add_f32 v236, v236, v98\n"
"v_mfma_f32_16x16x32_bf16 a[12:15], v[176:179], v[140:143], a[12:15]\n"
"v_exp_f32 v99, v99\n"
"v_mfma_f32_16x16x32_bf16 a[12:15], v[180:183], v[92:95], a[12:15]\n"
"ds_read_b64_tr_b16 v[212:213], v226 offset:12416\n"
"v_cvt_pk_bf16_f32 v145, v98, v99\n"
"v_mfma_f32_16x16x32_bf16 a[28:31], v[184:187], v[140:143], a[28:31]\n"
"ds_read_b64_tr_b16 v[214:215], v226 offset:18560\n"
"v_add_f32 v236, v236, v99\n"
"v_mfma_f32_16x16x32_bf16 a[28:31], v[188:191], v[92:95], a[28:31]\n"
"v_exp_f32 v100, v100\n"
"v_mfma_f32_16x16x32_bf16 a[24:27], v[184:187], v[136:139], a[24:27]\n"
"buffer_load_dwordx4 v234, s[60:63], s85 offen offset:1024 sc0 sc1 lds\n"
"v_mfma_f32_16x16x32_bf16 a[24:27], v[188:191], v[88:91], a[24:27]\n"
"ds_read_b64_tr_b16 v[216:217], v227 offset:128\n"
"v_add_f32 v237, v237, v100\n"
"v_mfma_f32_16x16x32_bf16 a[20:23], v[184:187], v[132:135], a[20:23]\n"
"v_exp_f32 v101, v101\n"
"v_mfma_f32_16x16x32_bf16 a[20:23], v[188:191], v[84:87], a[20:23]\n"
"ds_read_b64_tr_b16 v[218:219], v227 offset:6272\n"
"v_cvt_pk_bf16_f32 v148, v100, v101\n"
"v_mfma_f32_16x16x32_bf16 a[16:19], v[184:187], v[128:131], a[16:19]\n"
"ds_read_b64_tr_b16 v[220:221], v227 offset:12416\n"
"v_add_f32 v237, v237, v101\n"
"v_mfma_f32_16x16x32_bf16 a[16:19], v[188:191], v[80:83], a[16:19]\n"
"v_exp_f32 v102, v102\n"
"v_mfma_f32_16x16x32_bf16 a[32:35], v[192:195], v[128:131], a[32:35]\n"
"ds_read_b64_tr_b16 v[222:223], v227 offset:18560\n"
"v_add_f32 v237, v237, v102\n"
"v_mfma_f32_16x16x32_bf16 a[32:35], v[196:199], v[80:83], a[32:35]\n"
"ds_read_b64_tr_b16 v[182:183], v228 offset:18560\n"
"v_mfma_f32_16x16x32_bf16 a[36:39], v[192:195], v[132:135], a[36:39]\n"
"v_exp_f32 v103, v103\n"
"v_mfma_f32_16x16x32_bf16 a[36:39], v[196:199], v[84:87], a[36:39]\n"
"buffer_load_dwordx4 v235, s[60:63], s85 offen offset:2048 sc0 sc1 lds\n"
"v_mfma_f32_16x16x32_bf16 a[40:43], v[192:195], v[136:139], a[40:43]\n"
"ds_read_b64_tr_b16 v[176:177], v228 offset:128\n"
"v_cvt_pk_bf16_f32 v149, v102, v103\n"
"v_mfma_f32_16x16x32_bf16 a[40:43], v[196:199], v[88:91], a[40:43]\n"
"v_exp_f32 v104, v104\n"
"v_mfma_f32_16x16x32_bf16 a[44:47], v[192:195], v[140:143], a[44:47]\n"
"ds_read_b64_tr_b16 v[178:179], v228 offset:6272\n"
"v_add_f32 v237, v237, v103\n"
"v_mfma_f32_16x16x32_bf16 a[44:47], v[196:199], v[92:95], a[44:47]\n"
"ds_read_b64_tr_b16 v[180:181], v228 offset:12416\n"
"s_add_u32 s85, s85, 12288\n"
"s_waitcnt lgkmcnt(4)\n"
"v_mfma_f32_16x16x32_bf16 a[60:63], v[200:203], v[140:143], a[60:63]\n"
"v_exp_f32 v105, v105\n"
"v_mfma_f32_16x16x32_bf16 a[60:63], v[204:207], v[92:95], a[60:63]\n"
"v_cvt_pk_bf16_f32 v152, v104, v105\n"
"s_mov_b32 m0, s83\n"
"v_mfma_f32_16x16x32_bf16 a[56:59], v[200:203], v[136:139], a[56:59]\n"
"buffer_load_dwordx4 v233, s[60:63], s85 offen offset:0 sc0 sc1 lds\n"
"v_mfma_f32_16x16x32_bf16 a[56:59], v[204:207], v[88:91], a[56:59]\n"
"v_exp_f32 v106, v106\n"
"v_mfma_f32_16x16x32_bf16 a[52:55], v[200:203], v[132:135], a[52:55]\n"
"ds_read_b64_tr_b16 v[184:185], v229 offset:128\n"
"v_add_f32 v238, v238, v104\n"
"v_mfma_f32_16x16x32_bf16 a[52:55], v[204:207], v[84:87], a[52:55]\n"
"ds_read_b64_tr_b16 v[186:187], v229 offset:6272\n"
"v_add_f32 v238, v238, v105\n"
"v_mfma_f32_16x16x32_bf16 a[48:51], v[200:203], v[128:131], a[48:51]\n"
"ds_read_b64_tr_b16 v[188:189], v229 offset:12416\n"
"v_add_f32 v238, v238, v106\n"
"v_mfma_f32_16x16x32_bf16 a[48:51], v[204:207], v[80:83], a[48:51]\n"
"v_exp_f32 v107, v107\n"
"v_mfma_f32_16x16x32_bf16 a[64:67], v[208:211], v[128:131], a[64:67]\n"
"ds_read_b64_tr_b16 v[190:191], v229 offset:18560\n"
"v_cvt_pk_bf16_f32 v153, v106, v107\n"
"v_mfma_f32_16x16x32_bf16 a[64:67], v[212:215], v[80:83], a[64:67]\n"
"ds_read_b64_tr_b16 v[198:199], v226 offset:18688\n"
"v_add_f32 v238, v238, v107\n"
"v_mfma_f32_16x16x32_bf16 a[68:71], v[208:211], v[132:135], a[68:71]\n"
"v_exp_f32 v108, v108\n"
"v_mfma_f32_16x16x32_bf16 a[68:71], v[212:215], v[84:87], a[68:71]\n"
"buffer_load_dwordx4 v234, s[60:63], s85 offen offset:1024 sc0 sc1 lds\n"
"v_mfma_f32_16x16x32_bf16 a[72:75], v[208:211], v[136:139], a[72:75]\n"
"ds_read_b64_tr_b16 v[192:193], v226 offset:256\n"
"v_add_f32 v239, v239, v108\n"
"v_mfma_f32_16x16x32_bf16 a[72:75], v[212:215], v[88:91], a[72:75]\n"
"v_exp_f32 v109, v109\n"
"v_mfma_f32_16x16x32_bf16 a[76:79], v[208:211], v[140:143], a[76:79]\n"
"ds_read_b64_tr_b16 v[194:195], v226 offset:6400\n"
"v_cvt_pk_bf16_f32 v156, v108, v109\n"
"buffer_load_dword a255, v243, s[64:67], s87 offen\n"
"v_mfma_f32_16x16x32_bf16 a[76:79], v[212:215], v[92:95], a[76:79]\n"
"ds_read_b64_tr_b16 v[196:197], v226 offset:12544\n"
"v_add_f32 v239, v239, v109\n"
"v_mfma_f32_16x16x32_bf16 a[92:95], v[216:219], v[140:143], a[92:95]\n"
"v_exp_f32 v110, v110\n"
"s_add_u32 s87, s87, 2560\n"
"v_mfma_f32_16x16x32_bf16 a[92:95], v[220:223], v[92:95], a[92:95]\n"
"v_add_f32 v239, v239, v110\n"
"v_mfma_f32_16x16x32_bf16 a[88:91], v[216:219], v[136:139], a[88:91]\n"
"buffer_load_dwordx4 v235, s[60:63], s85 offen offset:2048 sc0 sc1 lds\n"
"v_mfma_f32_16x16x32_bf16 a[88:91], v[220:223], v[88:91], a[88:91]\n"
"v_exp_f32 v111, v111\n"
"v_mfma_f32_16x16x32_bf16 a[84:87], v[216:219], v[132:135], a[84:87]\n"
"ds_read_b64_tr_b16 v[200:201], v227 offset:256\n"
"v_cvt_pk_bf16_f32 v157, v110, v111\n"
"v_mfma_f32_16x16x32_bf16 a[84:87], v[220:223], v[84:87], a[84:87]\n"
"ds_read_b64_tr_b16 v[202:203], v227 offset:6400\n"
"v_add_f32 v239, v239, v111\n"
"v_mfma_f32_16x16x32_bf16 a[80:83], v[216:219], v[128:131], a[80:83]\n"
"v_exp_f32 v112, v112\n"
"v_mfma_f32_16x16x32_bf16 a[80:83], v[220:223], v[80:83], a[80:83]\n"
"ds_read_b64_tr_b16 v[204:205], v227 offset:12544\n"
"v_add_f32 v236, v236, v112\n"
"s_waitcnt lgkmcnt(3)\n"
"v_mfma_f32_16x16x32_bf16 a[96:99], v[176:179], v[128:131], a[96:99]\n"
"ds_read_b64_tr_b16 v[206:207], v227 offset:18688\n"
"v_mfma_f32_16x16x32_bf16 a[96:99], v[180:183], v[80:83], a[96:99]\n"
"v_exp_f32 v113, v113\n"
"v_mfma_f32_16x16x32_bf16 a[100:103], v[176:179], v[132:135], a[100:103]\n"
"ds_read_b64_tr_b16 v[214:215], v228 offset:18688\n"
"v_cvt_pk_bf16_f32 v146, v112, v113\n"
"v_mfma_f32_16x16x32_bf16 a[100:103], v[180:183], v[84:87], a[100:103]\n"
"ds_read_b64_tr_b16 v[208:209], v228 offset:256\n"
"v_add_f32 v236, v236, v113\n"
"v_mfma_f32_16x16x32_bf16 a[104:107], v[176:179], v[136:139], a[104:107]\n"
"s_waitcnt vmcnt(6)\n"
"s_barrier\n"
"v_exp_f32 v114, v114\n"
"v_mfma_f32_16x16x32_bf16 a[104:107], v[180:183], v[88:91], a[104:107]\n"
"ds_read_b64_tr_b16 v[210:211], v228 offset:6400\n"
"v_add_f32 v236, v236, v114\n"
"v_mfma_f32_16x16x32_bf16 a[108:111], v[176:179], v[140:143], a[108:111]\n"
"ds_read_b64_tr_b16 v[212:213], v228 offset:12544\n"
"v_mfma_f32_16x16x32_bf16 a[108:111], v[180:183], v[92:95], a[108:111]\n"
"v_exp_f32 v115, v115\n"
"v_mfma_f32_16x16x32_bf16 a[124:127], v[184:187], v[140:143], a[124:127]\n"
"v_cvt_pk_bf16_f32 v147, v114, v115\n"
"v_add_f32 v236, v236, v115\n"
"v_mfma_f32_16x16x32_bf16 a[124:127], v[188:191], v[92:95], a[124:127]\n"
"s_mov_b32 m0, s77\n"
"s_add_u32 s84, s84, 12288\n"
"v_mfma_f32_16x16x32_bf16 a[120:123], v[184:187], v[136:139], a[120:123]\n"
"v_exp_f32 v116, v116\n"
"v_mfma_f32_16x16x32_bf16 a[120:123], v[188:191], v[88:91], a[120:123]\n"
"buffer_load_dwordx4 v230, s[56:59], s84 offen offset:0 sc0 sc1 lds\n"
"v_mfma_f32_16x16x32_bf16 a[116:119], v[184:187], v[132:135], a[116:119]\n"
"ds_read_b64_tr_b16 v[216:217], v229 offset:256\n"
"v_add_f32 v237, v237, v116\n"
"v_mfma_f32_16x16x32_bf16 a[116:119], v[188:191], v[84:87], a[116:119]\n"
"ds_read_b64_tr_b16 v[218:219], v229 offset:6400\n"
"v_mfma_f32_16x16x32_bf16 a[112:115], v[184:187], v[128:131], a[112:115]\n"
"v_exp_f32 v117, v117\n"
"v_mfma_f32_16x16x32_bf16 a[112:115], v[188:191], v[80:83], a[112:115]\n"
"ds_read_b64_tr_b16 v[220:221], v229 offset:12544\n"
"v_cvt_pk_bf16_f32 v150, v116, v117\n"
"v_mfma_f32_16x16x32_bf16 a[128:131], v[192:195], v[128:131], a[128:131]\n"
"ds_read_b64_tr_b16 v[222:223], v229 offset:18688\n"
"v_add_f32 v237, v237, v117\n"
"v_mfma_f32_16x16x32_bf16 a[128:131], v[196:199], v[80:83], a[128:131]\n"
"v_exp_f32 v118, v118\n"
"v_mfma_f32_16x16x32_bf16 a[132:135], v[192:195], v[132:135], a[132:135]\n"
"buffer_load_dwordx4 v231, s[56:59], s84 offen offset:1024 sc0 sc1 lds\n"
"v_mfma_f32_16x16x32_bf16 a[132:135], v[196:199], v[84:87], a[132:135]\n"
"v_add_f32 v237, v237, v118\n"
"v_mfma_f32_16x16x32_bf16 a[136:139], v[192:195], v[136:139], a[136:139]\n"
"v_exp_f32 v119, v119\n"
"v_mfma_f32_16x16x32_bf16 a[136:139], v[196:199], v[88:91], a[136:139]\n"
"v_cvt_pk_bf16_f32 v151, v118, v119\n"
"v_add_f32 v237, v237, v119\n"
"v_mfma_f32_16x16x32_bf16 a[140:143], v[192:195], v[140:143], a[140:143]\n"
"v_mfma_f32_16x16x32_bf16 a[140:143], v[196:199], v[92:95], a[140:143]\n"
"v_exp_f32 v120, v120\n"
"s_waitcnt lgkmcnt(0)\n"
"v_mfma_f32_16x16x32_bf16 a[156:159], v[200:203], v[140:143], a[156:159]\n"
"v_add_f32 v238, v238, v120\n"
"v_mfma_f32_16x16x32_bf16 a[156:159], v[204:207], v[92:95], a[156:159]\n"
"ds_read_b128 v[176:179], v224 offset:36864\n"
"v_mfma_f32_16x16x32_bf16 a[152:155], v[200:203], v[136:139], a[152:155]\n"
"v_exp_f32 v121, v121\n"
"v_mfma_f32_16x16x32_bf16 a[152:155], v[204:207], v[88:91], a[152:155]\n"
"buffer_load_dwordx4 v232, s[56:59], s84 offen offset:2048 sc0 sc1 lds\n"
"v_mfma_f32_16x16x32_bf16 a[148:151], v[200:203], v[132:135], a[148:151]\n"
"ds_read_b128 v[180:183], v225 offset:36864\n"
"v_cvt_pk_bf16_f32 v154, v120, v121\n"
"v_mfma_f32_16x16x32_bf16 a[148:151], v[204:207], v[84:87], a[148:151]\n"
"v_exp_f32 v122, v122\n"
"v_mfma_f32_16x16x32_bf16 a[144:147], v[200:203], v[128:131], a[144:147]\n"
"ds_read_b128 v[184:187], v224 offset:36992\n"
"v_add_f32 v238, v238, v121\n"
"v_mfma_f32_16x16x32_bf16 a[144:147], v[204:207], v[80:83], a[144:147]\n"
"ds_read_b128 v[188:191], v225 offset:36992\n"
"s_add_u32 s84, s84, 12288\n"
"v_mfma_f32_16x16x32_bf16 a[160:163], v[208:211], v[128:131], a[160:163]\n"
"v_exp_f32 v123, v123\n"
"v_mfma_f32_16x16x32_bf16 a[160:163], v[212:215], v[80:83], a[160:163]\n"
"ds_read_b128 v[192:195], v224 offset:37120\n"
"s_mov_b32 m0, s78\n"
"v_mfma_f32_16x16x32_bf16 a[164:167], v[208:211], v[132:135], a[164:167]\n"
"buffer_load_dwordx4 v230, s[56:59], s84 offen offset:0 sc0 sc1 lds\n"
"v_mfma_f32_16x16x32_bf16 a[164:167], v[212:215], v[84:87], a[164:167]\n"
"v_exp_f32 v124, v124\n"
"v_mfma_f32_16x16x32_bf16 a[168:171], v[208:211], v[136:139], a[168:171]\n"
"ds_read_b128 v[196:199], v225 offset:37120\n"
"v_cvt_pk_bf16_f32 v155, v122, v123\n"
"v_mfma_f32_16x16x32_bf16 a[168:171], v[212:215], v[88:91], a[168:171]\n"
"v_add_f32 v238, v238, v122\n"
"v_add_f32 v238, v238, v123\n"
"v_mfma_f32_16x16x32_bf16 a[172:175], v[208:211], v[140:143], a[172:175]\n"
"v_exp_f32 v125, v125\n"
"v_mfma_f32_16x16x32_bf16 a[172:175], v[212:215], v[92:95], a[172:175]\n"
"v_cvt_pk_bf16_f32 v158, v124, v125\n"
"v_add_f32 v239, v239, v124\n"
"v_mfma_f32_16x16x32_bf16 a[188:191], v[216:219], v[140:143], a[188:191]\n"
"v_add_f32 v239, v239, v125\n"
"v_mfma_f32_16x16x32_bf16 a[188:191], v[220:223], v[92:95], a[188:191]\n"
"v_exp_f32 v126, v126\n"
"v_mfma_f32_16x16x32_bf16 a[184:187], v[216:219], v[136:139], a[184:187]\n"
"buffer_load_dwordx4 v231, s[56:59], s84 offen offset:1024 sc0 sc1 lds\n"
"v_mfma_f32_16x16x32_bf16 a[184:187], v[220:223], v[88:91], a[184:187]\n"
"v_add_f32 v239, v239, v126\n"
"v_mfma_f32_16x16x32_bf16 a[180:183], v[216:219], v[132:135], a[180:183]\n"
"v_mfma_f32_16x16x32_bf16 a[180:183], v[220:223], v[84:87], a[180:183]\n"
"v_exp_f32 v127, v127\n"
"v_mfma_f32_16x16x32_bf16 a[176:179], v[216:219], v[128:131], a[176:179]\n"
"v_cvt_pk_bf16_f32 v159, v126, v127\n"
"v_add_f32 v239, v239, v127\n"
"v_mfma_f32_16x16x32_bf16 a[176:179], v[220:223], v[80:83], a[176:179]\n"
"s_waitcnt lgkmcnt(0)\n"
"v_mfma_f32_16x16x32_bf16 v[96:99], v[176:179], v[0:3], v[160:163]\n"
"v_mfma_f32_16x16x32_bf16 v[96:99], v[180:183], v[16:19], v[96:99]\n"
"ds_read_b128 v[200:203], v224 offset:43008\n"
"v_mfma_f32_16x16x32_bf16 v[96:99], v[184:187], v[32:35], v[96:99]\n"
"buffer_load_dwordx4 v232, s[56:59], s84 offen offset:2048 sc0 sc1 lds\n"
"v_mfma_f32_16x16x32_bf16 v[96:99], v[188:191], v[48:51], v[96:99]\n"
"ds_read_b128 v[204:207], v225 offset:43008\n"
"v_mfma_f32_16x16x32_bf16 v[96:99], v[192:195], v[64:67], v[96:99]\n"
"ds_read_b128 v[208:211], v224 offset:43136\n"
"v_mfma_f32_16x16x32_bf16 v[96:99], v[196:199], a[192:195], v[96:99]\n"
"ds_read_b128 v[212:215], v225 offset:43136\n"
"v_mfma_f32_16x16x32_bf16 v[100:103], v[176:179], v[4:7], v[164:167]\n"
"ds_read_b128 v[216:219], v224 offset:43264\n"
"v_mfma_f32_16x16x32_bf16 v[100:103], v[180:183], v[20:23], v[100:103]\n"
"ds_read_b128 v[220:223], v225 offset:43264\n"
"v_mfma_f32_16x16x32_bf16 v[100:103], v[184:187], v[36:39], v[100:103]\n"
"s_nop 1\n"
"v_exp_f32 v96, v96\n"
"v_mfma_f32_16x16x32_bf16 v[100:103], v[188:191], v[52:55], v[100:103]\n"
"v_add_f32 v236, v236, v96\n"
"v_mfma_f32_16x16x32_bf16 v[100:103], v[192:195], v[68:71], v[100:103]\n"
"v_exp_f32 v97, v97\n"
"v_mfma_f32_16x16x32_bf16 v[100:103], v[196:199], a[196:199], v[100:103]\n"
"v_cvt_pk_bf16_f32 v80, v96, v97\n"
"v_add_f32 v236, v236, v97\n"
"v_mfma_f32_16x16x32_bf16 v[104:107], v[176:179], v[8:11], v[168:171]\n"
"v_mfma_f32_16x16x32_bf16 v[104:107], v[180:183], v[24:27], v[104:107]\n"
"v_exp_f32 v98, v98\n"
"v_mfma_f32_16x16x32_bf16 v[104:107], v[184:187], v[40:43], v[104:107]\n"
"v_add_f32 v236, v236, v98\n"
"v_mfma_f32_16x16x32_bf16 v[104:107], v[188:191], v[56:59], v[104:107]\n"
"v_exp_f32 v99, v99\n"
"v_mfma_f32_16x16x32_bf16 v[104:107], v[192:195], v[72:75], v[104:107]\n"
"v_cvt_pk_bf16_f32 v81, v98, v99\n"
"v_add_f32 v236, v236, v99\n"
"v_mfma_f32_16x16x32_bf16 v[104:107], v[196:199], a[200:203], v[104:107]\n"
"v_mfma_f32_16x16x32_bf16 v[108:111], v[176:179], v[12:15], v[172:175]\n"
"v_exp_f32 v100, v100\n"
"v_mfma_f32_16x16x32_bf16 v[108:111], v[180:183], v[28:31], v[108:111]\n"
"v_add_f32 v237, v237, v100\n"
"v_mfma_f32_16x16x32_bf16 v[108:111], v[184:187], v[44:47], v[108:111]\n"
"v_exp_f32 v101, v101\n"
"v_mfma_f32_16x16x32_bf16 v[108:111], v[188:191], v[60:63], v[108:111]\n"
"v_cvt_pk_bf16_f32 v84, v100, v101\n"
"v_add_f32 v237, v237, v101\n"
"v_mfma_f32_16x16x32_bf16 v[108:111], v[192:195], v[76:79], v[108:111]\n"
"v_mfma_f32_16x16x32_bf16 v[108:111], v[196:199], a[204:207], v[108:111]\n"
"v_exp_f32 v102, v102\n"
"s_waitcnt lgkmcnt(0)\n"
"v_mfma_f32_16x16x32_bf16 v[112:115], v[200:203], v[0:3], v[160:163]\n"
"v_add_f32 v237, v237, v102\n"
"v_mfma_f32_16x16x32_bf16 v[112:115], v[204:207], v[16:19], v[112:115]\n"
"v_exp_f32 v103, v103\n"
"v_mfma_f32_16x16x32_bf16 v[112:115], v[208:211], v[32:35], v[112:115]\n"
"ds_read_b128 v[176:179], v224 offset:0\n"
"v_cvt_pk_bf16_f32 v85, v102, v103\n"
"v_mfma_f32_16x16x32_bf16 v[112:115], v[212:215], v[48:51], v[112:115]\n"
"ds_read_b128 v[180:183], v225 offset:0\n"
"v_add_f32 v237, v237, v103\n"
"v_mfma_f32_16x16x32_bf16 v[112:115], v[216:219], v[64:67], v[112:115]\n"
"v_exp_f32 v104, v104\n"
"v_mfma_f32_16x16x32_bf16 v[112:115], v[220:223], a[192:195], v[112:115]\n"
"ds_read_b128 v[184:187], v224 offset:128\n"
"v_add_f32 v238, v238, v104\n"
"v_mfma_f32_16x16x32_bf16 v[116:119], v[200:203], v[4:7], v[164:167]\n"
"v_exp_f32 v105, v105\n"
"v_mfma_f32_16x16x32_bf16 v[116:119], v[204:207], v[20:23], v[116:119]\n"
"ds_read_b128 v[188:191], v225 offset:128\n"
"v_cvt_pk_bf16_f32 v88, v104, v105\n"
"v_mfma_f32_16x16x32_bf16 v[116:119], v[208:211], v[36:39], v[116:119]\n"
"ds_read_b128 v[192:195], v224 offset:256\n"
"v_add_f32 v238, v238, v105\n"
"v_mfma_f32_16x16x32_bf16 v[116:119], v[212:215], v[52:55], v[116:119]\n"
"v_exp_f32 v106, v106\n"
"v_mfma_f32_16x16x32_bf16 v[116:119], v[216:219], v[68:71], v[116:119]\n"
"ds_read_b128 v[196:199], v225 offset:256\n"
"v_add_f32 v238, v238, v106\n"
"v_mfma_f32_16x16x32_bf16 v[116:119], v[220:223], a[196:199], v[116:119]\n"
"v_exp_f32 v107, v107\n"
"v_mfma_f32_16x16x32_bf16 v[120:123], v[200:203], v[8:11], v[168:171]\n"
"v_cvt_pk_bf16_f32 v89, v106, v107\n"
"v_add_f32 v238, v238, v107\n"
"v_mfma_f32_16x16x32_bf16 v[120:123], v[204:207], v[24:27], v[120:123]\n"
"v_mfma_f32_16x16x32_bf16 v[120:123], v[208:211], v[40:43], v[120:123]\n"
"v_exp_f32 v108, v108\n"
"v_mfma_f32_16x16x32_bf16 v[120:123], v[212:215], v[56:59], v[120:123]\n"
"v_add_f32 v239, v239, v108\n"
"v_mfma_f32_16x16x32_bf16 v[120:123], v[216:219], v[72:75], v[120:123]\n"
"v_exp_f32 v109, v109\n"
"v_mfma_f32_16x16x32_bf16 v[120:123], v[220:223], a[200:203], v[120:123]\n"
"v_cvt_pk_bf16_f32 v92, v108, v109\n"
"v_add_f32 v239, v239, v109\n"
"v_mfma_f32_16x16x32_bf16 v[124:127], v[200:203], v[12:15], v[172:175]\n"
"v_mfma_f32_16x16x32_bf16 v[124:127], v[204:207], v[28:31], v[124:127]\n"
"v_exp_f32 v110, v110\n"
"v_mfma_f32_16x16x32_bf16 v[124:127], v[208:211], v[44:47], v[124:127]\n"
"v_add_f32 v239, v239, v110\n"
"v_mfma_f32_16x16x32_bf16 v[124:127], v[212:215], v[60:63], v[124:127]\n"
"v_exp_f32 v111, v111\n"
"v_mfma_f32_16x16x32_bf16 v[124:127], v[216:219], v[76:79], v[124:127]\n"
"v_cvt_pk_bf16_f32 v93, v110, v111\n"
"v_add_f32 v239, v239, v111\n"
"v_mfma_f32_16x16x32_bf16 v[124:127], v[220:223], a[204:207], v[124:127]\n"
"s_sub_u32 s30, s30, 1\n"
"s_cmp_lg_u32 s30, 0\n"
"s_cbranch_scc1 ATTLOOP_%=\n"
"s_waitcnt lgkmcnt(0)\n"
"v_mfma_f32_16x16x32_bf16 v[96:99], v[176:179], v[0:3], v[160:163]\n"
"v_exp_f32 v112, v112\n"
"v_mfma_f32_16x16x32_bf16 v[96:99], v[180:183], v[16:19], v[96:99]\n"
"ds_read_b128 v[200:203], v224 offset:6144\n"
"v_add_f32 v236, v236, v112\n"
"v_mfma_f32_16x16x32_bf16 v[96:99], v[184:187], v[32:35], v[96:99]\n"
"v_exp_f32 v113, v113\n"
"v_mfma_f32_16x16x32_bf16 v[96:99], v[188:191], v[48:51], v[96:99]\n"
"ds_read_b128 v[204:207], v225 offset:6144\n"
"v_cvt_pk_bf16_f32 v82, v112, v113\n"
"v_mfma_f32_16x16x32_bf16 v[96:99], v[192:195], v[64:67], v[96:99]\n"
"ds_read_b128 v[208:211], v224 offset:6272\n"
"v_add_f32 v236, v236, v113\n"
"v_mfma_f32_16x16x32_bf16 v[96:99], v[196:199], a[192:195], v[96:99]\n"
"v_exp_f32 v114, v114\n"
"v_mfma_f32_16x16x32_bf16 v[100:103], v[176:179], v[4:7], v[164:167]\n"
"ds_read_b128 v[212:215], v225 offset:6272\n"
"v_add_f32 v236, v236, v114\n"
"v_mfma_f32_16x16x32_bf16 v[100:103], v[180:183], v[20:23], v[100:103]\n"
"v_exp_f32 v115, v115\n"
"v_mfma_f32_16x16x32_bf16 v[100:103], v[184:187], v[36:39], v[100:103]\n"
"ds_read_b128 v[216:219], v224 offset:6400\n"
"v_cvt_pk_bf16_f32 v83, v114, v115\n"
"v_mfma_f32_16x16x32_bf16 v[100:103], v[188:191], v[52:55], v[100:103]\n"
"ds_read_b128 v[220:223], v225 offset:6400\n"
"v_add_f32 v236, v236, v115\n"
"v_mfma_f32_16x16x32_bf16 v[100:103], v[192:195], v[68:71], v[100:103]\n"
"v_exp_f32 v116, v116\n"
"v_mfma_f32_16x16x32_bf16 v[100:103], v[196:199], a[196:199], v[100:103]\n"
"v_add_f32 v237, v237, v116\n"
"v_mfma_f32_16x16x32_bf16 v[104:107], v[176:179], v[8:11], v[168:171]\n"
"v_exp_f32 v117, v117\n"
"v_mfma_f32_16x16x32_bf16 v[104:107], v[180:183], v[24:27], v[104:107]\n"
"v_cvt_pk_bf16_f32 v86, v116, v117\n"
"v_add_f32 v237, v237, v117\n"
"v_mfma_f32_16x16x32_bf16 v[104:107], v[184:187], v[40:43], v[104:107]\n"
"v_mfma_f32_16x16x32_bf16 v[104:107], v[188:191], v[56:59], v[104:107]\n"
"v_exp_f32 v118, v118\n"
"v_mfma_f32_16x16x32_bf16 v[104:107], v[192:195], v[72:75], v[104:107]\n"
"v_add_f32 v237, v237, v118\n"
"v_mfma_f32_16x16x32_bf16 v[104:107], v[196:199], a[200:203], v[104:107]\n"
"s_waitcnt vmcnt(6)\n"
"s_barrier\n"
"v_exp_f32 v119, v119\n"
"v_mfma_f32_16x16x32_bf16 v[108:111], v[176:179], v[12:15], v[172:175]\n"
"v_cvt_pk_bf16_f32 v87, v118, v119\n"
"v_add_f32 v237, v237, v119\n"
"v_mfma_f32_16x16x32_bf16 v[108:111], v[180:183], v[28:31], v[108:111]\n"
"ds_read_b64_tr_b16 v[176:177], v226 offset:24576\n"
"v_mfma_f32_16x16x32_bf16 v[108:111], v[184:187], v[44:47], v[108:111]\n"
"v_exp_f32 v120, v120\n"
"v_mfma_f32_16x16x32_bf16 v[108:111], v[188:191], v[60:63], v[108:111]\n"
"ds_read_b64_tr_b16 v[178:179], v226 offset:30720\n"
"v_add_f32 v238, v238, v120\n"
"v_mfma_f32_16x16x32_bf16 v[108:111], v[192:195], v[76:79], v[108:111]\n"
"v_exp_f32 v121, v121\n"
"v_mfma_f32_16x16x32_bf16 v[108:111], v[196:199], a[204:207], v[108:111]\n"
"ds_read_b64_tr_b16 v[180:181], v226 offset:36864\n"
"v_cvt_pk_bf16_f32 v90, v120, v121\n"
"s_waitcnt lgkmcnt(3)\n"
"v_mfma_f32_16x16x32_bf16 v[112:115], v[200:203], v[0:3], v[160:163]\n"
"ds_read_b64_tr_b16 v[182:183], v226 offset:43008\n"
"v_add_f32 v238, v238, v121\n"
"s_lshl_b32 s28, s21, 3\n"
"s_add_u32 s28, s28, s23\n"
"s_andn2_b32 s28, s28, 1\n"
"s_add_u32 s28, s28, 0\n"
"s_lshl_b32 s28, s28, 8\n"
"v_mov_b32 v255, s28\n"
"global_load_dword v252, v255, s[54:55] sc1\n"
"v_mfma_f32_16x16x32_bf16 v[112:115], v[204:207], v[16:19], v[112:115]\n"
"v_exp_f32 v122, v122\n"
"v_mfma_f32_16x16x32_bf16 v[112:115], v[208:211], v[32:35], v[112:115]\n"
"ds_read_b64_tr_b16 v[190:191], v227 offset:43008\n"
"v_add_f32 v238, v238, v122\n"
"v_mfma_f32_16x16x32_bf16 v[112:115], v[212:215], v[48:51], v[112:115]\n"
"v_exp_f32 v123, v123\n"
"v_mfma_f32_16x16x32_bf16 v[112:115], v[216:219], v[64:67], v[112:115]\n"
"ds_read_b64_tr_b16 v[184:185], v227 offset:24576\n"
"v_cvt_pk_bf16_f32 v91, v122, v123\n"
"v_mfma_f32_16x16x32_bf16 v[112:115], v[220:223], a[192:195], v[112:115]\n"
"ds_read_b64_tr_b16 v[186:187], v227 offset:30720\n"
"v_add_f32 v238, v238, v123\n"
"v_mfma_f32_16x16x32_bf16 v[116:119], v[200:203], v[4:7], v[164:167]\n"
"v_exp_f32 v124, v124\n"
"v_mfma_f32_16x16x32_bf16 v[116:119], v[204:207], v[20:23], v[116:119]\n"
"ds_read_b64_tr_b16 v[188:189], v227 offset:36864\n"
"v_add_f32 v239, v239, v124\n"
"v_mfma_f32_16x16x32_bf16 v[116:119], v[208:211], v[36:39], v[116:119]\n"
"v_exp_f32 v125, v125\n"
"v_mfma_f32_16x16x32_bf16 v[116:119], v[212:215], v[52:55], v[116:119]\n"
"v_cvt_pk_bf16_f32 v94, v124, v125\n"
"v_add_f32 v239, v239, v125\n"
"v_mfma_f32_16x16x32_bf16 v[116:119], v[216:219], v[68:71], v[116:119]\n"
"ds_read_b64_tr_b16 v[198:199], v228 offset:43008\n"
"v_mfma_f32_16x16x32_bf16 v[116:119], v[220:223], a[196:199], v[116:119]\n"
"v_exp_f32 v126, v126\n"
"v_mfma_f32_16x16x32_bf16 v[120:123], v[200:203], v[8:11], v[168:171]\n"
"ds_read_b64_tr_b16 v[192:193], v228 offset:24576\n"
"v_add_f32 v239, v239, v126\n"
"v_mfma_f32_16x16x32_bf16 v[120:123], v[204:207], v[24:27], v[120:123]\n"
"ds_read_b64_tr_b16 v[194:195], v228 offset:30720\n"
"v_mfma_f32_16x16x32_bf16 v[120:123], v[208:211], v[40:43], v[120:123]\n"
"v_exp_f32 v127, v127\n"
"v_mfma_f32_16x16x32_bf16 v[120:123], v[212:215], v[56:59], v[120:123]\n"
"ds_read_b64_tr_b16 v[196:197], v228 offset:36864\n"
"v_cvt_pk_bf16_f32 v95, v126, v127\n"
"v_mfma_f32_16x16x32_bf16 v[120:123], v[216:219], v[72:75], v[120:123]\n"
"v_add_f32 v239, v239, v127\n"
"v_mfma_f32_16x16x32_bf16 v[120:123], v[220:223], a[200:203], v[120:123]\n"
"v_mfma_f32_16x16x32_bf16 v[124:127], v[200:203], v[12:15], v[172:175]\n"
"v_mfma_f32_16x16x32_bf16 v[124:127], v[204:207], v[28:31], v[124:127]\n"
"ds_read_b64_tr_b16 v[202:203], v229 offset:30720\n"
"v_mfma_f32_16x16x32_bf16 v[124:127], v[208:211], v[44:47], v[124:127]\n"
"ds_read_b64_tr_b16 v[200:201], v229 offset:24576\n"
"v_mfma_f32_16x16x32_bf16 v[124:127], v[212:215], v[60:63], v[124:127]\n"
"v_exp_f32 v96, v96\n"
"v_mfma_f32_16x16x32_bf16 v[124:127], v[216:219], v[76:79], v[124:127]\n"
"ds_read_b64_tr_b16 v[204:205], v229 offset:36864\n"
"v_add_f32 v236, v236, v96\n"
"v_mfma_f32_16x16x32_bf16 v[124:127], v[220:223], a[204:207], v[124:127]\n"
"ds_read_b64_tr_b16 v[206:207], v229 offset:43008\n"
"s_add_u32 s85, s85, 12288\n"
"s_waitcnt lgkmcnt(4)\n"
"v_mfma_f32_16x16x32_bf16 a[0:3], v[176:179], v[144:147], a[0:3]\n"
"v_exp_f32 v97, v97\n"
"v_mfma_f32_16x16x32_bf16 a[0:3], v[180:183], v[80:83], a[0:3]\n"
"v_cvt_pk_bf16_f32 v128, v96, v97\n"
"s_mov_b32 m0, s80\n"
"v_mfma_f32_16x16x32_bf16 a[4:7], v[176:179], v[148:151], a[4:7]\n"
"buffer_load_dwordx4 v233, s[60:63], s85 offen offset:0 sc0 sc1 lds\n"
"v_mfma_f32_16x16x32_bf16 a[4:7], v[180:183], v[84:87], a[4:7]\n"
"v_exp_f32 v98, v98\n"
"v_mfma_f32_16x16x32_bf16 a[8:11], v[176:179], v[152:155], a[8:11]\n"
"ds_read_b64_tr_b16 v[208:209], v226 offset:24704\n"
"v_add_f32 v236, v236, v97\n"
"v_mfma_f32_16x16x32_bf16 a[8:11], v[180:183], v[88:91], a[8:11]\n"
"ds_read_b64_tr_b16 v[210:211], v226 offset:30848\n"
"v_add_f32 v236, v236, v98\n"
"v_mfma_f32_16x16x32_bf16 a[12:15], v[176:179], v[156:159], a[12:15]\n"
"v_exp_f32 v99, v99\n"
"v_mfma_f32_16x16x32_bf16 a[12:15], v[180:183], v[92:95], a[12:15]\n"
"ds_read_b64_tr_b16 v[212:213], v226 offset:36992\n"
"v_cvt_pk_bf16_f32 v129, v98, v99\n"
"v_mfma_f32_16x16x32_bf16 a[28:31], v[184:187], v[156:159], a[28:31]\n"
"ds_read_b64_tr_b16 v[214:215], v226 offset:43136\n"
"v_add_f32 v236, v236, v99\n"
"v_mfma_f32_16x16x32_bf16 a[28:31], v[188:191], v[92:95], a[28:31]\n"
"v_exp_f32 v100, v100\n"
"v_mfma_f32_16x16x32_bf16 a[24:27], v[184:187], v[152:155], a[24:27]\n"
"buffer_load_dwordx4 v234, s[60:63], s85 offen offset:1024 sc0 sc1 lds\n"
"v_mfma_f32_16x16x32_bf16 a[24:27], v[188:191], v[88:91], a[24:27]\n"
"ds_read_b64_tr_b16 v[216:217], v227 offset:24704\n"
"v_add_f32 v237, v237, v100\n"
"v_mfma_f32_16x16x32_bf16 a[20:23], v[184:187], v[148:151], a[20:23]\n"
"v_exp_f32 v101, v101\n"
"v_mfma_f32_16x16x32_bf16 a[20:23], v[188:191], v[84:87], a[20:23]\n"
"ds_read_b64_tr_b16 v[218:219], v227 offset:30848\n"
"v_cvt_pk_bf16_f32 v132, v100, v101\n"
"v_mfma_f32_16x16x32_bf16 a[16:19], v[184:187], v[144:147], a[16:19]\n"
"ds_read_b64_tr_b16 v[220:221], v227 offset:36992\n"
"v_add_f32 v237, v237, v101\n"
"v_mfma_f32_16x16x32_bf16 a[16:19], v[188:191], v[80:83], a[16:19]\n"
"v_exp_f32 v102, v102\n"
"v_mfma_f32_16x16x32_bf16 a[32:35], v[192:195], v[144:147], a[32:35]\n"
"ds_read_b64_tr_b16 v[222:223], v227 offset:43136\n"
"v_add_f32 v237, v237, v102\n"
"v_mfma_f32_16x16x32_bf16 a[32:35], v[196:199], v[80:83], a[32:35]\n"
"ds_read_b64_tr_b16 v[182:183], v228 offset:43136\n"
"v_mfma_f32_16x16x32_bf16 a[36:39], v[192:195], v[148:151], a[36:39]\n"
"v_exp_f32 v103, v103\n"
"v_mfma_f32_16x16x32_bf16 a[36:39], v[196:199], v[84:87], a[36:39]\n"
"buffer_load_dwordx4 v235, s[60:63], s85 offen offset:2048 sc0 sc1 lds\n"
"v_mfma_f32_16x16x32_bf16 a[40:43], v[192:195], v[152:155], a[40:43]\n"
"ds_read_b64_tr_b16 v[176:177], v228 offset:24704\n"
"v_cvt_pk_bf16_f32 v133, v102, v103\n"
"v_mfma_f32_16x16x32_bf16 a[40:43], v[196:199], v[88:91], a[40:43]\n"
"v_exp_f32 v104, v104\n"
"v_mfma_f32_16x16x32_bf16 a[44:47], v[192:195], v[156:159], a[44:47]\n"
"ds_read_b64_tr_b16 v[178:179], v228 offset:30848\n"
"v_add_f32 v237, v237, v103\n"
"v_mfma_f32_16x16x32_bf16 a[44:47], v[196:199], v[92:95], a[44:47]\n"
"ds_read_b64_tr_b16 v[180:181], v228 offset:36992\n"
"s_add_u32 s85, s85, 12288\n"
"s_waitcnt lgkmcnt(4)\n"
"v_mfma_f32_16x16x32_bf16 a[60:63], v[200:203], v[156:159], a[60:63]\n"
"v_exp_f32 v105, v105\n"
"v_mfma_f32_16x16x32_bf16 a[60:63], v[204:207], v[92:95], a[60:63]\n"
"v_cvt_pk_bf16_f32 v136, v104, v105\n"
"s_mov_b32 m0, s81\n"
"v_mfma_f32_16x16x32_bf16 a[56:59], v[200:203], v[152:155], a[56:59]\n"
"buffer_load_dwordx4 v233, s[60:63], s85 offen offset:0 sc0 sc1 lds\n"
"v_mfma_f32_16x16x32_bf16 a[56:59], v[204:207], v[88:91], a[56:59]\n"
"v_exp_f32 v106, v106\n"
"v_mfma_f32_16x16x32_bf16 a[52:55], v[200:203], v[148:151], a[52:55]\n"
"ds_read_b64_tr_b16 v[184:185], v229 offset:24704\n"
"v_add_f32 v238, v238, v104\n"
"v_mfma_f32_16x16x32_bf16 a[52:55], v[204:207], v[84:87], a[52:55]\n"
"ds_read_b64_tr_b16 v[186:187], v229 offset:30848\n"
"v_add_f32 v238, v238, v105\n"
"v_mfma_f32_16x16x32_bf16 a[48:51], v[200:203], v[144:147], a[48:51]\n"
"ds_read_b64_tr_b16 v[188:189], v229 offset:36992\n"
"v_add_f32 v238, v238, v106\n"
"v_mfma_f32_16x16x32_bf16 a[48:51], v[204:207], v[80:83], a[48:51]\n"
"v_exp_f32 v107, v107\n"
"v_mfma_f32_16x16x32_bf16 a[64:67], v[208:211], v[144:147], a[64:67]\n"
"ds_read_b64_tr_b16 v[190:191], v229 offset:43136\n"
"v_cvt_pk_bf16_f32 v137, v106, v107\n"
"v_mfma_f32_16x16x32_bf16 a[64:67], v[212:215], v[80:83], a[64:67]\n"
"ds_read_b64_tr_b16 v[198:199], v226 offset:43264\n"
"v_add_f32 v238, v238, v107\n"
"v_mfma_f32_16x16x32_bf16 a[68:71], v[208:211], v[148:151], a[68:71]\n"
"v_exp_f32 v108, v108\n"
"v_mfma_f32_16x16x32_bf16 a[68:71], v[212:215], v[84:87], a[68:71]\n"
"buffer_load_dwordx4 v234, s[60:63], s85 offen offset:1024 sc0 sc1 lds\n"
"v_mfma_f32_16x16x32_bf16 a[72:75], v[208:211], v[152:155], a[72:75]\n"
"ds_read_b64_tr_b16 v[192:193], v226 offset:24832\n"
"v_add_f32 v239, v239, v108\n"
"v_mfma_f32_16x16x32_bf16 a[72:75], v[212:215], v[88:91], a[72:75]\n"
"v_exp_f32 v109, v109\n"
"v_mfma_f32_16x16x32_bf16 a[76:79], v[208:211], v[156:159], a[76:79]\n"
"ds_read_b64_tr_b16 v[194:195], v226 offset:30976\n"
"v_cvt_pk_bf16_f32 v140, v108, v109\n"
"v_mfma_f32_16x16x32_bf16 a[76:79], v[212:215], v[92:95], a[76:79]\n"
"ds_read_b64_tr_b16 v[196:197], v226 offset:37120\n"
"v_add_f32 v239, v239, v109\n"
"v_mfma_f32_16x16x32_bf16 a[92:95], v[216:219], v[156:159], a[92:95]\n"
"v_exp_f32 v110, v110\n"
"v_mfma_f32_16x16x32_bf16 a[92:95], v[220:223], v[92:95], a[92:95]\n"
"v_add_f32 v239, v239, v110\n"
"v_mfma_f32_16x16x32_bf16 a[88:91], v[216:219], v[152:155], a[88:91]\n"
"buffer_load_dwordx4 v235, s[60:63], s85 offen offset:2048 sc0 sc1 lds\n"
"v_mfma_f32_16x16x32_bf16 a[88:91], v[220:223], v[88:91], a[88:91]\n"
"v_exp_f32 v111, v111\n"
"v_mfma_f32_16x16x32_bf16 a[84:87], v[216:219], v[148:151], a[84:87]\n"
"ds_read_b64_tr_b16 v[200:201], v227 offset:24832\n"
"v_cvt_pk_bf16_f32 v141, v110, v111\n"
"v_mfma_f32_16x16x32_bf16 a[84:87], v[220:223], v[84:87], a[84:87]\n"
"ds_read_b64_tr_b16 v[202:203], v227 offset:30976\n"
"v_add_f32 v239, v239, v111\n"
"v_mfma_f32_16x16x32_bf16 a[80:83], v[216:219], v[144:147], a[80:83]\n"
"v_exp_f32 v112, v112\n"
"v_mfma_f32_16x16x32_bf16 a[80:83], v[220:223], v[80:83], a[80:83]\n"
"ds_read_b64_tr_b16 v[204:205], v227 offset:37120\n"
"v_add_f32 v236, v236, v112\n"
"s_waitcnt lgkmcnt(3)\n"
"v_mfma_f32_16x16x32_bf16 a[96:99], v[176:179], v[144:147], a[96:99]\n"
"ds_read_b64_tr_b16 v[206:207], v227 offset:43264\n"
"v_mfma_f32_16x16x32_bf16 a[96:99], v[180:183], v[80:83], a[96:99]\n"
"v_exp_f32 v113, v113\n"
"v_mfma_f32_16x16x32_bf16 a[100:103], v[176:179], v[148:151], a[100:103]\n"
"ds_read_b64_tr_b16 v[214:215], v228 offset:43264\n"
"v_cvt_pk_bf16_f32 v130, v112, v113\n"
"v_mfma_f32_16x16x32_bf16 a[100:103], v[180:183], v[84:87], a[100:103]\n"
"ds_read_b64_tr_b16 v[208:209], v228 offset:24832\n"
"v_add_f32 v236, v236, v113\n"
"v_mfma_f32_16x16x32_bf16 a[104:107], v[176:179], v[152:155], a[104:107]\n"
"s_waitcnt vmcnt(7)\n"
"s_barrier\n"
"v_exp_f32 v114, v114\n"
"v_mfma_f32_16x16x32_bf16 a[104:107], v[180:183], v[88:91], a[104:107]\n"
"ds_read_b64_tr_b16 v[210:211], v228 offset:30976\n"
"v_add_f32 v236, v236, v114\n"
"v_mfma_f32_16x16x32_bf16 a[108:111], v[176:179], v[156:159], a[108:111]\n"
"ds_read_b64_tr_b16 v[212:213], v228 offset:37120\n"
"v_mfma_f32_16x16x32_bf16 a[108:111], v[180:183], v[92:95], a[108:111]\n"
"v_exp_f32 v115, v115\n"
"v_mfma_f32_16x16x32_bf16 a[124:127], v[184:187], v[156:159], a[124:127]\n"
"v_cvt_pk_bf16_f32 v131, v114, v115\n"
"v_add_f32 v236, v236, v115\n"
"v_mfma_f32_16x16x32_bf16 a[124:127], v[188:191], v[92:95], a[124:127]\n"
"s_mov_b32 m0, s79\n"
"s_add_u32 s84, s84, 12288\n"
"v_mfma_f32_16x16x32_bf16 a[120:123], v[184:187], v[152:155], a[120:123]\n"
"v_exp_f32 v116, v116\n"
"v_mfma_f32_16x16x32_bf16 a[120:123], v[188:191], v[88:91], a[120:123]\n"
"buffer_load_dwordx4 v230, s[56:59], s84 offen offset:0 sc0 sc1 lds\n"
"v_mfma_f32_16x16x32_bf16 a[116:119], v[184:187], v[148:151], a[116:119]\n"
"ds_read_b64_tr_b16 v[216:217], v229 offset:24832\n"
"v_add_f32 v237, v237, v116\n"
"v_mfma_f32_16x16x32_bf16 a[116:119], v[188:191], v[84:87], a[116:119]\n"
"ds_read_b64_tr_b16 v[218:219], v229 offset:30976\n"
"v_mfma_f32_16x16x32_bf16 a[112:115], v[184:187], v[144:147], a[112:115]\n"
"v_exp_f32 v117, v117\n"
"v_mfma_f32_16x16x32_bf16 a[112:115], v[188:191], v[80:83], a[112:115]\n"
"ds_read_b64_tr_b16 v[220:221], v229 offset:37120\n"
"v_cvt_pk_bf16_f32 v134, v116, v117\n"
"v_mfma_f32_16x16x32_bf16 a[128:131], v[192:195], v[144:147], a[128:131]\n"
"ds_read_b64_tr_b16 v[222:223], v229 offset:43264\n"
"v_add_f32 v237, v237, v117\n"
"v_mfma_f32_16x16x32_bf16 a[128:131], v[196:199], v[80:83], a[128:131]\n"
"v_exp_f32 v118, v118\n"
"v_mfma_f32_16x16x32_bf16 a[132:135], v[192:195], v[148:151], a[132:135]\n"
"buffer_load_dwordx4 v231, s[56:59], s84 offen offset:1024 sc0 sc1 lds\n"
"v_mfma_f32_16x16x32_bf16 a[132:135], v[196:199], v[84:87], a[132:135]\n"
"v_add_f32 v237, v237, v118\n"
"v_mfma_f32_16x16x32_bf16 a[136:139], v[192:195], v[152:155], a[136:139]\n"
"v_exp_f32 v119, v119\n"
"v_mfma_f32_16x16x32_bf16 a[136:139], v[196:199], v[88:91], a[136:139]\n"
"v_cvt_pk_bf16_f32 v135, v118, v119\n"
"v_add_f32 v237, v237, v119\n"
"v_mfma_f32_16x16x32_bf16 a[140:143], v[192:195], v[156:159], a[140:143]\n"
"v_mfma_f32_16x16x32_bf16 a[140:143], v[196:199], v[92:95], a[140:143]\n"
"v_exp_f32 v120, v120\n"
"s_waitcnt lgkmcnt(0)\n"
"v_mfma_f32_16x16x32_bf16 a[156:159], v[200:203], v[156:159], a[156:159]\n"
"v_add_f32 v238, v238, v120\n"
"v_mfma_f32_16x16x32_bf16 a[156:159], v[204:207], v[92:95], a[156:159]\n"
"ds_read_b128 v[176:179], v224 offset:12288\n"
"v_mfma_f32_16x16x32_bf16 a[152:155], v[200:203], v[152:155], a[152:155]\n"
"v_exp_f32 v121, v121\n"
"v_mfma_f32_16x16x32_bf16 a[152:155], v[204:207], v[88:91], a[152:155]\n"
"buffer_load_dwordx4 v232, s[56:59], s84 offen offset:2048 sc0 sc1 lds\n"
"v_mfma_f32_16x16x32_bf16 a[148:151], v[200:203], v[148:151], a[148:151]\n"
"ds_read_b128 v[180:183], v225 offset:12288\n"
"v_cvt_pk_bf16_f32 v138, v120, v121\n"
"v_mfma_f32_16x16x32_bf16 a[148:151], v[204:207], v[84:87], a[148:151]\n"
"v_exp_f32 v122, v122\n"
"v_mfma_f32_16x16x32_bf16 a[144:147], v[200:203], v[144:147], a[144:147]\n"
"ds_read_b128 v[184:187], v224 offset:12416\n"
"v_add_f32 v238, v238, v121\n"
"v_mfma_f32_16x16x32_bf16 a[144:147], v[204:207], v[80:83], a[144:147]\n"
"ds_read_b128 v[188:191], v225 offset:12416\n"
"v_mfma_f32_16x16x32_bf16 a[160:163], v[208:211], v[144:147], a[160:163]\n"
"v_exp_f32 v123, v123\n"
"v_mfma_f32_16x16x32_bf16 a[160:163], v[212:215], v[80:83], a[160:163]\n"
"ds_read_b128 v[192:195], v224 offset:12544\n"
"v_mfma_f32_16x16x32_bf16 a[164:167], v[208:211], v[148:151], a[164:167]\n"
"v_mfma_f32_16x16x32_bf16 a[164:167], v[212:215], v[84:87], a[164:167]\n"
"v_exp_f32 v124, v124\n"
"v_mfma_f32_16x16x32_bf16 a[168:171], v[208:211], v[152:155], a[168:171]\n"
"ds_read_b128 v[196:199], v225 offset:12544\n"
"v_cvt_pk_bf16_f32 v139, v122, v123\n"
"v_mfma_f32_16x16x32_bf16 a[168:171], v[212:215], v[88:91], a[168:171]\n"
"v_add_f32 v238, v238, v122\n"
"v_add_f32 v238, v238, v123\n"
"v_mfma_f32_16x16x32_bf16 a[172:175], v[208:211], v[156:159], a[172:175]\n"
"v_exp_f32 v125, v125\n"
"v_mfma_f32_16x16x32_bf16 a[172:175], v[212:215], v[92:95], a[172:175]\n"
"v_cvt_pk_bf16_f32 v142, v124, v125\n"
"v_add_f32 v239, v239, v124\n"
"v_mfma_f32_16x16x32_bf16 a[188:191], v[216:219], v[156:159], a[188:191]\n"
"v_add_f32 v239, v239, v125\n"
"v_mfma_f32_16x16x32_bf16 a[188:191], v[220:223], v[92:95], a[188:191]\n"
"v_exp_f32 v126, v126\n"
"v_mfma_f32_16x16x32_bf16 a[184:187], v[216:219], v[152:155], a[184:187]\n"
"v_mfma_f32_16x16x32_bf16 a[184:187], v[220:223], v[88:91], a[184:187]\n"
"v_add_f32 v239, v239, v126\n"
"v_mfma_f32_16x16x32_bf16 a[180:183], v[216:219], v[148:151], a[180:183]\n"
"v_mfma_f32_16x16x32_bf16 a[180:183], v[220:223], v[84:87], a[180:183]\n"
"v_exp_f32 v127, v127\n"
"v_mfma_f32_16x16x32_bf16 a[176:179], v[216:219], v[144:147], a[176:179]\n"
"v_cvt_pk_bf16_f32 v143, v126, v127\n"
"v_add_f32 v239, v239, v127\n"
"v_mfma_f32_16x16x32_bf16 a[176:179], v[220:223], v[80:83], a[176:179]\n"
"s_waitcnt lgkmcnt(0)\n"
"v_mfma_f32_16x16x32_bf16 v[96:99], v[176:179], v[0:3], v[160:163]\n"
"v_mfma_f32_16x16x32_bf16 v[96:99], v[180:183], v[16:19], v[96:99]\n"
"ds_read_b128 v[200:203], v224 offset:18432\n"
"v_mfma_f32_16x16x32_bf16 v[96:99], v[184:187], v[32:35], v[96:99]\n"
"v_mfma_f32_16x16x32_bf16 v[96:99], v[188:191], v[48:51], v[96:99]\n"
"ds_read_b128 v[204:207], v225 offset:18432\n"
"v_mfma_f32_16x16x32_bf16 v[96:99], v[192:195], v[64:67], v[96:99]\n"
"ds_read_b128 v[208:211], v224 offset:18560\n"
"v_mfma_f32_16x16x32_bf16 v[96:99], v[196:199], a[192:195], v[96:99]\n"
"ds_read_b128 v[212:215], v225 offset:18560\n"
"v_mfma_f32_16x16x32_bf16 v[100:103], v[176:179], v[4:7], v[164:167]\n"
"ds_read_b128 v[216:219], v224 offset:18688\n"
"v_mfma_f32_16x16x32_bf16 v[100:103], v[180:183], v[20:23], v[100:103]\n"
"ds_read_b128 v[220:223], v225 offset:18688\n"
"v_mfma_f32_16x16x32_bf16 v[100:103], v[184:187], v[36:39], v[100:103]\n"
"s_nop 1\n"
"v_exp_f32 v96, v96\n"
"v_mfma_f32_16x16x32_bf16 v[100:103], v[188:191], v[52:55], v[100:103]\n"
"v_add_f32 v236, v236, v96\n"
"v_mfma_f32_16x16x32_bf16 v[100:103], v[192:195], v[68:71], v[100:103]\n"
"v_exp_f32 v97, v97\n"
"v_mfma_f32_16x16x32_bf16 v[100:103], v[196:199], a[196:199], v[100:103]\n"
"v_cvt_pk_bf16_f32 v80, v96, v97\n"
"v_add_f32 v236, v236, v97\n"
"v_mfma_f32_16x16x32_bf16 v[104:107], v[176:179], v[8:11], v[168:171]\n"
"v_mfma_f32_16x16x32_bf16 v[104:107], v[180:183], v[24:27], v[104:107]\n"
"v_exp_f32 v98, v98\n"
"v_mfma_f32_16x16x32_bf16 v[104:107], v[184:187], v[40:43], v[104:107]\n"
"v_add_f32 v236, v236, v98\n"
"v_mfma_f32_16x16x32_bf16 v[104:107], v[188:191], v[56:59], v[104:107]\n"
"v_exp_f32 v99, v99\n"
"v_mfma_f32_16x16x32_bf16 v[104:107], v[192:195], v[72:75], v[104:107]\n"
"v_cvt_pk_bf16_f32 v81, v98, v99\n"
"v_add_f32 v236, v236, v99\n"
"v_mfma_f32_16x16x32_bf16 v[104:107], v[196:199], a[200:203], v[104:107]\n"
"v_mfma_f32_16x16x32_bf16 v[108:111], v[176:179], v[12:15], v[172:175]\n"
"v_exp_f32 v100, v100\n"
"v_mfma_f32_16x16x32_bf16 v[108:111], v[180:183], v[28:31], v[108:111]\n"
"v_add_f32 v237, v237, v100\n"
"v_mfma_f32_16x16x32_bf16 v[108:111], v[184:187], v[44:47], v[108:111]\n"
"v_exp_f32 v101, v101\n"
"v_mfma_f32_16x16x32_bf16 v[108:111], v[188:191], v[60:63], v[108:111]\n"
"v_cvt_pk_bf16_f32 v84, v100, v101\n"
"v_add_f32 v237, v237, v101\n"
"v_mfma_f32_16x16x32_bf16 v[108:111], v[192:195], v[76:79], v[108:111]\n"
"v_mfma_f32_16x16x32_bf16 v[108:111], v[196:199], a[204:207], v[108:111]\n"
"v_exp_f32 v102, v102\n"
"s_waitcnt lgkmcnt(0)\n"
"v_mfma_f32_16x16x32_bf16 v[112:115], v[200:203], v[0:3], v[160:163]\n"
"v_add_f32 v237, v237, v102\n"
"v_mfma_f32_16x16x32_bf16 v[112:115], v[204:207], v[16:19], v[112:115]\n"
"v_exp_f32 v103, v103\n"
"v_mfma_f32_16x16x32_bf16 v[112:115], v[208:211], v[32:35], v[112:115]\n"
"ds_read_b128 v[176:179], v224 offset:24576\n"
"v_cvt_pk_bf16_f32 v85, v102, v103\n"
"v_mfma_f32_16x16x32_bf16 v[112:115], v[212:215], v[48:51], v[112:115]\n"
"ds_read_b128 v[180:183], v225 offset:24576\n"
"v_add_f32 v237, v237, v103\n"
"v_mfma_f32_16x16x32_bf16 v[112:115], v[216:219], v[64:67], v[112:115]\n"
"v_exp_f32 v104, v104\n"
"v_mfma_f32_16x16x32_bf16 v[112:115], v[220:223], a[192:195], v[112:115]\n"
"ds_read_b128 v[184:187], v224 offset:24704\n"
"v_add_f32 v238, v238, v104\n"
"v_mfma_f32_16x16x32_bf16 v[116:119], v[200:203], v[4:7], v[164:167]\n"
"v_exp_f32 v105, v105\n"
"v_mfma_f32_16x16x32_bf16 v[116:119], v[204:207], v[20:23], v[116:119]\n"
"ds_read_b128 v[188:191], v225 offset:24704\n"
"v_cvt_pk_bf16_f32 v88, v104, v105\n"
"v_mfma_f32_16x16x32_bf16 v[116:119], v[208:211], v[36:39], v[116:119]\n"
"ds_read_b128 v[192:195], v224 offset:24832\n"
"v_add_f32 v238, v238, v105\n"
"v_mfma_f32_16x16x32_bf16 v[116:119], v[212:215], v[52:55], v[116:119]\n"
"v_exp_f32 v106, v106\n"
"v_mfma_f32_16x16x32_bf16 v[116:119], v[216:219], v[68:71], v[116:119]\n"
"ds_read_b128 v[196:199], v225 offset:24832\n"
"v_add_f32 v238, v238, v106\n"
"v_mfma_f32_16x16x32_bf16 v[116:119], v[220:223], a[196:199], v[116:119]\n"
"v_exp_f32 v107, v107\n"
"v_mfma_f32_16x16x32_bf16 v[120:123], v[200:203], v[8:11], v[168:171]\n"
"v_cvt_pk_bf16_f32 v89, v106, v107\n"
"v_add_f32 v238, v238, v107\n"
"v_mfma_f32_16x16x32_bf16 v[120:123], v[204:207], v[24:27], v[120:123]\n"
"v_mfma_f32_16x16x32_bf16 v[120:123], v[208:211], v[40:43], v[120:123]\n"
"v_exp_f32 v108, v108\n"
"v_mfma_f32_16x16x32_bf16 v[120:123], v[212:215], v[56:59], v[120:123]\n"
"v_add_f32 v239, v239, v108\n"
"v_mfma_f32_16x16x32_bf16 v[120:123], v[216:219], v[72:75], v[120:123]\n"
"v_exp_f32 v109, v109\n"
"v_mfma_f32_16x16x32_bf16 v[120:123], v[220:223], a[200:203], v[120:123]\n"
"v_cvt_pk_bf16_f32 v92, v108, v109\n"
"v_add_f32 v239, v239, v109\n"
"v_mfma_f32_16x16x32_bf16 v[124:127], v[200:203], v[12:15], v[172:175]\n"
"v_mfma_f32_16x16x32_bf16 v[124:127], v[204:207], v[28:31], v[124:127]\n"
"v_exp_f32 v110, v110\n"
"v_mfma_f32_16x16x32_bf16 v[124:127], v[208:211], v[44:47], v[124:127]\n"
"v_add_f32 v239, v239, v110\n"
"v_mfma_f32_16x16x32_bf16 v[124:127], v[212:215], v[60:63], v[124:127]\n"
"v_exp_f32 v111, v111\n"
"v_mfma_f32_16x16x32_bf16 v[124:127], v[216:219], v[76:79], v[124:127]\n"
"v_cvt_pk_bf16_f32 v93, v110, v111\n"
"v_add_f32 v239, v239, v111\n"
"v_mfma_f32_16x16x32_bf16 v[124:127], v[220:223], a[204:207], v[124:127]\n"
"s_waitcnt lgkmcnt(0)\n"
"v_mfma_f32_16x16x32_bf16 v[96:99], v[176:179], v[0:3], v[160:163]\n"
"v_exp_f32 v112, v112\n"
"v_mfma_f32_16x16x32_bf16 v[96:99], v[180:183], v[16:19], v[96:99]\n"
"ds_read_b128 v[200:203], v224 offset:30720\n"
"v_add_f32 v236, v236, v112\n"
"v_mfma_f32_16x16x32_bf16 v[96:99], v[184:187], v[32:35], v[96:99]\n"
"v_exp_f32 v113, v113\n"
"v_mfma_f32_16x16x32_bf16 v[96:99], v[188:191], v[48:51], v[96:99]\n"
"ds_read_b128 v[204:207], v225 offset:30720\n"
"v_cvt_pk_bf16_f32 v82, v112, v113\n"
"v_mfma_f32_16x16x32_bf16 v[96:99], v[192:195], v[64:67], v[96:99]\n"
"ds_read_b128 v[208:211], v224 offset:30848\n"
"v_add_f32 v236, v236, v113\n"
"v_mfma_f32_16x16x32_bf16 v[96:99], v[196:199], a[192:195], v[96:99]\n"
"v_exp_f32 v114, v114\n"
"v_mfma_f32_16x16x32_bf16 v[100:103], v[176:179], v[4:7], v[164:167]\n"
"ds_read_b128 v[212:215], v225 offset:30848\n"
"v_add_f32 v236, v236, v114\n"
"v_mfma_f32_16x16x32_bf16 v[100:103], v[180:183], v[20:23], v[100:103]\n"
"v_exp_f32 v115, v115\n"
"v_mfma_f32_16x16x32_bf16 v[100:103], v[184:187], v[36:39], v[100:103]\n"
"ds_read_b128 v[216:219], v224 offset:30976\n"
"v_cvt_pk_bf16_f32 v83, v114, v115\n"
"v_mfma_f32_16x16x32_bf16 v[100:103], v[188:191], v[52:55], v[100:103]\n"
"ds_read_b128 v[220:223], v225 offset:30976\n"
"v_add_f32 v236, v236, v115\n"
"v_mfma_f32_16x16x32_bf16 v[100:103], v[192:195], v[68:71], v[100:103]\n"
"v_exp_f32 v116, v116\n"
"v_mfma_f32_16x16x32_bf16 v[100:103], v[196:199], a[196:199], v[100:103]\n"
"v_add_f32 v237, v237, v116\n"
"v_mfma_f32_16x16x32_bf16 v[104:107], v[176:179], v[8:11], v[168:171]\n"
"v_exp_f32 v117, v117\n"
"v_mfma_f32_16x16x32_bf16 v[104:107], v[180:183], v[24:27], v[104:107]\n"
"v_cvt_pk_bf16_f32 v86, v116, v117\n"
"v_add_f32 v237, v237, v117\n"
"v_mfma_f32_16x16x32_bf16 v[104:107], v[184:187], v[40:43], v[104:107]\n"
"v_mfma_f32_16x16x32_bf16 v[104:107], v[188:191], v[56:59], v[104:107]\n"
"v_exp_f32 v118, v118\n"
"v_mfma_f32_16x16x32_bf16 v[104:107], v[192:195], v[72:75], v[104:107]\n"
"v_add_f32 v237, v237, v118\n"
"v_mfma_f32_16x16x32_bf16 v[104:107], v[196:199], a[200:203], v[104:107]\n"
"s_waitcnt vmcnt(3)\n"
"s_barrier\n"
"v_exp_f32 v119, v119\n"
"v_mfma_f32_16x16x32_bf16 v[108:111], v[176:179], v[12:15], v[172:175]\n"
"v_cvt_pk_bf16_f32 v87, v118, v119\n"
"v_add_f32 v237, v237, v119\n"
"v_mfma_f32_16x16x32_bf16 v[108:111], v[180:183], v[28:31], v[108:111]\n"
"ds_read_b64_tr_b16 v[176:177], v226 offset:0\n"
"v_mfma_f32_16x16x32_bf16 v[108:111], v[184:187], v[44:47], v[108:111]\n"
"v_exp_f32 v120, v120\n"
"v_readfirstlane_b32 s28, v252\n"
"s_nop 1\n"
"s_cmp_ge_u32 s28, 8\n"
"s_cbranch_scc1 POLLOK2_%=\n"
"s_lshl_b32 s28, s21, 3\n"
"s_add_u32 s28, s28, s23\n"
"s_andn2_b32 s28, s28, 1\n"
"s_add_u32 s28, s28, 0\n"
"s_lshl_b32 s28, s28, 8\n"
"v_mov_b32 v255, s28\n"
"s_mov_b32 s27, 8000\n"
"SPIN2_%=:\n"
"global_load_dword v252, v255, s[54:55] sc1\n"
"s_waitcnt vmcnt(0)\n"
"v_readfirstlane_b32 s28, v252\n"
"s_nop 1\n"
"s_cmp_ge_u32 s28, 8\n"
"s_cbranch_scc1 POLLOK2_%=\n"
"s_sleep 1\n"
"s_sub_u32 s27, s27, 1\n"
"s_cmp_lg_u32 s27, 0\n"
"s_cbranch_scc1 SPIN2_%=\n"
"POLLOK2_%=:\n"
"v_mfma_f32_16x16x32_bf16 v[108:111], v[188:191], v[60:63], v[108:111]\n"
"ds_read_b64_tr_b16 v[178:179], v226 offset:6144\n"
"v_add_f32 v238, v238, v120\n"
"v_mfma_f32_16x16x32_bf16 v[108:111], v[192:195], v[76:79], v[108:111]\n"
"v_exp_f32 v121, v121\n"
"s_add_u32 s92, s90, 0\n"
"v_mfma_f32_16x16x32_bf16 v[108:111], v[196:199], a[204:207], v[108:111]\n"
"ds_read_b64_tr_b16 v[180:181], v226 offset:12288\n"
"v_cvt_pk_bf16_f32 v90, v120, v121\n"
"s_waitcnt lgkmcnt(3)\n"
"v_mfma_f32_16x16x32_bf16 v[112:115], v[200:203], v[0:3], v[160:163]\n"
"ds_read_b64_tr_b16 v[182:183], v226 offset:18432\n"
"v_add_f32 v238, v238, v121\n"
"v_mfma_f32_16x16x32_bf16 v[112:115], v[204:207], v[16:19], v[112:115]\n"
"v_exp_f32 v122, v122\n"
"v_mfma_f32_16x16x32_bf16 v[112:115], v[208:211], v[32:35], v[112:115]\n"
"ds_read_b64_tr_b16 v[190:191], v227 offset:18432\n"
"v_add_f32 v238, v238, v122\n"
"v_mfma_f32_16x16x32_bf16 v[112:115], v[212:215], v[48:51], v[112:115]\n"
"v_exp_f32 v123, v123\n"
"buffer_load_dwordx4 a[208:211], v249, s[72:75], s92 offen offset:2048 sc1\n"
"v_mfma_f32_16x16x32_bf16 v[112:115], v[216:219], v[64:67], v[112:115]\n"
"ds_read_b64_tr_b16 v[184:185], v227 offset:0\n"
"v_cvt_pk_bf16_f32 v91, v122, v123\n"
"v_mfma_f32_16x16x32_bf16 v[112:115], v[220:223], a[192:195], v[112:115]\n"
"ds_read_b64_tr_b16 v[186:187], v227 offset:6144\n"
"v_add_f32 v238, v238, v123\n"
"v_mfma_f32_16x16x32_bf16 v[116:119], v[200:203], v[4:7], v[164:167]\n"
"v_exp_f32 v124, v124\n"
"v_mfma_f32_16x16x32_bf16 v[116:119], v[204:207], v[20:23], v[116:119]\n"
"ds_read_b64_tr_b16 v[188:189], v227 offset:12288\n"
"v_add_f32 v239, v239, v124\n"
"v_mfma_f32_16x16x32_bf16 v[116:119], v[208:211], v[36:39], v[116:119]\n"
"v_exp_f32 v125, v125\n"
"s_add_u32 s93, s91, 0\n"
"v_mfma_f32_16x16x32_bf16 v[116:119], v[212:215], v[52:55], v[116:119]\n"
"v_cvt_pk_bf16_f32 v94, v124, v125\n"
"v_add_f32 v239, v239, v125\n"
"v_mfma_f32_16x16x32_bf16 v[116:119], v[216:219], v[68:71], v[116:119]\n"
"ds_read_b64_tr_b16 v[198:199], v228 offset:18432\n"
"v_mfma_f32_16x16x32_bf16 v[116:119], v[220:223], a[196:199], v[116:119]\n"
"v_exp_f32 v126, v126\n"
"v_mfma_f32_16x16x32_bf16 v[120:123], v[200:203], v[8:11], v[168:171]\n"
"ds_read_b64_tr_b16 v[192:193], v228 offset:0\n"
"v_add_f32 v239, v239, v126\n"
"v_mfma_f32_16x16x32_bf16 v[120:123], v[204:207], v[24:27], v[120:123]\n"
"ds_read_b64_tr_b16 v[194:195], v228 offset:6144\n"
"buffer_load_dwordx4 a[212:215], v249, s[72:75], s93 offen offset:2048 sc1\n"
"v_mfma_f32_16x16x32_bf16 v[120:123], v[208:211], v[40:43], v[120:123]\n"
"v_exp_f32 v127, v127\n"
"v_mfma_f32_16x16x32_bf16 v[120:123], v[212:215], v[56:59], v[120:123]\n"
"ds_read_b64_tr_b16 v[196:197], v228 offset:12288\n"
"v_cvt_pk_bf16_f32 v95, v126, v127\n"
"v_mfma_f32_16x16x32_bf16 v[120:123], v[216:219], v[72:75], v[120:123]\n"
"v_add_f32 v239, v239, v127\n"
"v_mfma_f32_16x16x32_bf16 v[120:123], v[220:223], a[200:203], v[120:123]\n"
"v_mfma_f32_16x16x32_bf16 v[124:127], v[200:203], v[12:15], v[172:175]\n"
"s_add_u32 s92, s90, 0\n"
"v_mfma_f32_16x16x32_bf16 v[124:127], v[204:207], v[28:31], v[124:127]\n"
"ds_read_b64_tr_b16 v[202:203], v229 offset:6144\n"
"v_mfma_f32_16x16x32_bf16 v[124:127], v[208:211], v[44:47], v[124:127]\n"
"ds_read_b64_tr_b16 v[200:201], v229 offset:0\n"
"v_mfma_f32_16x16x32_bf16 v[124:127], v[212:215], v[60:63], v[124:127]\n"
"v_exp_f32 v96, v96\n"
"v_mfma_f32_16x16x32_bf16 v[124:127], v[216:219], v[76:79], v[124:127]\n"
"ds_read_b64_tr_b16 v[204:205], v229 offset:12288\n"
"v_add_f32 v236, v236, v96\n"
"v_mfma_f32_16x16x32_bf16 v[124:127], v[220:223], a[204:207], v[124:127]\n"
"ds_read_b64_tr_b16 v[206:207], v229 offset:18432\n"
"s_add_u32 s85, s85, 12288\n"
"buffer_load_dwordx4 a[216:219], v249, s[72:75], s92 offen offset:3072 sc1\n"
"s_waitcnt lgkmcnt(4)\n"
"v_mfma_f32_16x16x32_bf16 a[0:3], v[176:179], v[128:131], a[0:3]\n"
"v_exp_f32 v97, v97\n"
"v_mfma_f32_16x16x32_bf16 a[0:3], v[180:183], v[80:83], a[0:3]\n"
"v_cvt_pk_bf16_f32 v144, v96, v97\n"
"s_mov_b32 m0, s82\n"
"v_mfma_f32_16x16x32_bf16 a[4:7], v[176:179], v[132:135], a[4:7]\n"
"buffer_load_dwordx4 v233, s[60:63], s85 offen offset:0 sc0 sc1 lds\n"
"v_mfma_f32_16x16x32_bf16 a[4:7], v[180:183], v[84:87], a[4:7]\n"
"v_exp_f32 v98, v98\n"
"v_mfma_f32_16x16x32_bf16 a[8:11], v[176:179], v[136:139], a[8:11]\n"
"ds_read_b64_tr_b16 v[208:209], v226 offset:128\n"
"v_add_f32 v236, v236, v97\n"
"s_add_u32 s93, s91, 0\n"
"v_mfma_f32_16x16x32_bf16 a[8:11], v[180:183], v[88:91], a[8:11]\n"
"ds_read_b64_tr_b16 v[210:211], v226 offset:6272\n"
"v_add_f32 v236, v236, v98\n"
"v_mfma_f32_16x16x32_bf16 a[12:15], v[176:179], v[140:143], a[12:15]\n"
"v_exp_f32 v99, v99\n"
"v_mfma_f32_16x16x32_bf16 a[12:15], v[180:183], v[92:95], a[12:15]\n"
"ds_read_b64_tr_b16 v[212:213], v226 offset:12416\n"
"v_cvt_pk_bf16_f32 v145, v98, v99\n"
"v_mfma_f32_16x16x32_bf16 a[28:31], v[184:187], v[140:143], a[28:31]\n"
"ds_read_b64_tr_b16 v[214:215], v226 offset:18560\n"
"v_add_f32 v236, v236, v99\n"
"v_mfma_f32_16x16x32_bf16 a[28:31], v[188:191], v[92:95], a[28:31]\n"
"v_exp_f32 v100, v100\n"
"buffer_load_dwordx4 a[220:223], v249, s[72:75], s93 offen offset:3072 sc1\n"
"v_mfma_f32_16x16x32_bf16 a[24:27], v[184:187], v[136:139], a[24:27]\n"
"buffer_load_dwordx4 v234, s[60:63], s85 offen offset:1024 sc0 sc1 lds\n"
"v_mfma_f32_16x16x32_bf16 a[24:27], v[188:191], v[88:91], a[24:27]\n"
"ds_read_b64_tr_b16 v[216:217], v227 offset:128\n"
"v_add_f32 v237, v237, v100\n"
"v_mfma_f32_16x16x32_bf16 a[20:23], v[184:187], v[132:135], a[20:23]\n"
"v_exp_f32 v101, v101\n"
"v_mfma_f32_16x16x32_bf16 a[20:23], v[188:191], v[84:87], a[20:23]\n"
"ds_read_b64_tr_b16 v[218:219], v227 offset:6272\n"
"v_cvt_pk_bf16_f32 v148, v100, v101\n"
"v_mfma_f32_16x16x32_bf16 a[16:19], v[184:187], v[128:131], a[16:19]\n"
"ds_read_b64_tr_b16 v[220:221], v227 offset:12416\n"
"v_add_f32 v237, v237, v101\n"
"s_add_u32 s92, s90, 4096\n"
"v_mfma_f32_16x16x32_bf16 a[16:19], v[188:191], v[80:83], a[16:19]\n"
"v_exp_f32 v102, v102\n"
"v_mfma_f32_16x16x32_bf16 a[32:35], v[192:195], v[128:131], a[32:35]\n"
"ds_read_b64_tr_b16 v[222:223], v227 offset:18560\n"
"v_add_f32 v237, v237, v102\n"
"v_mfma_f32_16x16x32_bf16 a[32:35], v[196:199], v[80:83], a[32:35]\n"
"ds_read_b64_tr_b16 v[182:183], v228 offset:18560\n"
"v_mfma_f32_16x16x32_bf16 a[36:39], v[192:195], v[132:135], a[36:39]\n"
"v_exp_f32 v103, v103\n"
"v_mfma_f32_16x16x32_bf16 a[36:39], v[196:199], v[84:87], a[36:39]\n"
"buffer_load_dwordx4 v235, s[60:63], s85 offen offset:2048 sc0 sc1 lds\n"
"buffer_load_dwordx2 a[224:225], v253, s[72:75], s92 offen offset:0 sc1\n"
"v_mfma_f32_16x16x32_bf16 a[40:43], v[192:195], v[136:139], a[40:43]\n"
"ds_read_b64_tr_b16 v[176:177], v228 offset:128\n"
"v_cvt_pk_bf16_f32 v149, v102, v103\n"
"v_mfma_f32_16x16x32_bf16 a[40:43], v[196:199], v[88:91], a[40:43]\n"
"v_exp_f32 v104, v104\n"
"v_mfma_f32_16x16x32_bf16 a[44:47], v[192:195], v[140:143], a[44:47]\n"
"ds_read_b64_tr_b16 v[178:179], v228 offset:6272\n"
"v_add_f32 v237, v237, v103\n"
"v_mfma_f32_16x16x32_bf16 a[44:47], v[196:199], v[92:95], a[44:47]\n"
"ds_read_b64_tr_b16 v[180:181], v228 offset:12416\n"
"s_add_u32 s85, s85, 12288\n"
"s_waitcnt lgkmcnt(4)\n"
"v_mfma_f32_16x16x32_bf16 a[60:63], v[200:203], v[140:143], a[60:63]\n"
"v_exp_f32 v105, v105\n"
"buffer_load_dwordx2 a[226:227], v253, s[72:75], s92 offen offset:512 sc1\n"
"v_mfma_f32_16x16x32_bf16 a[60:63], v[204:207], v[92:95], a[60:63]\n"
"v_cvt_pk_bf16_f32 v152, v104, v105\n"
"s_mov_b32 m0, s83\n"
"v_mfma_f32_16x16x32_bf16 a[56:59], v[200:203], v[136:139], a[56:59]\n"
"buffer_load_dwordx4 v233, s[60:63], s85 offen offset:0 sc0 sc1 lds\n"
"v_mfma_f32_16x16x32_bf16 a[56:59], v[204:207], v[88:91], a[56:59]\n"
"v_exp_f32 v106, v106\n"
"v_mfma_f32_16x16x32_bf16 a[52:55], v[200:203], v[132:135], a[52:55]\n"
"ds_read_b64_tr_b16 v[184:185], v229 offset:128\n"
"v_add_f32 v238, v238, v104\n"
"v_mfma_f32_16x16x32_bf16 a[52:55], v[204:207], v[84:87], a[52:55]\n"
"ds_read_b64_tr_b16 v[186:187], v229 offset:6272\n"
"v_add_f32 v238, v238, v105\n"
"s_add_u32 s93, s91, 4096\n"
"v_mfma_f32_16x16x32_bf16 a[48:51], v[200:203], v[128:131], a[48:51]\n"
"ds_read_b64_tr_b16 v[188:189], v229 offset:12416\n"
"v_add_f32 v238, v238, v106\n"
"v_mfma_f32_16x16x32_bf16 a[48:51], v[204:207], v[80:83], a[48:51]\n"
"v_exp_f32 v107, v107\n"
"v_mfma_f32_16x16x32_bf16 a[64:67], v[208:211], v[128:131], a[64:67]\n"
"ds_read_b64_tr_b16 v[190:191], v229 offset:18560\n"
"v_cvt_pk_bf16_f32 v153, v106, v107\n"
"v_mfma_f32_16x16x32_bf16 a[64:67], v[212:215], v[80:83], a[64:67]\n"
"ds_read_b64_tr_b16 v[198:199], v226 offset:18688\n"
"v_add_f32 v238, v238, v107\n"
"v_mfma_f32_16x16x32_bf16 a[68:71], v[208:211], v[132:135], a[68:71]\n"
"v_exp_f32 v108, v108\n"
"buffer_load_dwordx2 a[228:229], v253, s[72:75], s93 offen offset:0 sc1\n"
"v_mfma_f32_16x16x32_bf16 a[68:71], v[212:215], v[84:87], a[68:71]\n"
"buffer_load_dwordx4 v234, s[60:63], s85 offen offset:1024 sc0 sc1 lds\n"
"v_mfma_f32_16x16x32_bf16 a[72:75], v[208:211], v[136:139], a[72:75]\n"
"ds_read_b64_tr_b16 v[192:193], v226 offset:256\n"
"v_add_f32 v239, v239, v108\n"
"v_mfma_f32_16x16x32_bf16 a[72:75], v[212:215], v[88:91], a[72:75]\n"
"v_exp_f32 v109, v109\n"
"v_mfma_f32_16x16x32_bf16 a[76:79], v[208:211], v[140:143], a[76:79]\n"
"ds_read_b64_tr_b16 v[194:195], v226 offset:6400\n"
"v_cvt_pk_bf16_f32 v156, v108, v109\n"
"v_mfma_f32_16x16x32_bf16 a[76:79], v[212:215], v[92:95], a[76:79]\n"
"ds_read_b64_tr_b16 v[196:197], v226 offset:12544\n"
"v_add_f32 v239, v239, v109\n"
"buffer_load_dwordx2 a[230:231], v253, s[72:75], s93 offen offset:512 sc1\n"
"v_mfma_f32_16x16x32_bf16 a[92:95], v[216:219], v[140:143], a[92:95]\n"
"v_exp_f32 v110, v110\n"
"v_mfma_f32_16x16x32_bf16 a[92:95], v[220:223], v[92:95], a[92:95]\n"
"v_add_f32 v239, v239, v110\n"
"v_mfma_f32_16x16x32_bf16 a[88:91], v[216:219], v[136:139], a[88:91]\n"
"buffer_load_dwordx4 v235, s[60:63], s85 offen offset:2048 sc0 sc1 lds\n"
"v_mfma_f32_16x16x32_bf16 a[88:91], v[220:223], v[88:91], a[88:91]\n"
"v_exp_f32 v111, v111\n"
"v_mfma_f32_16x16x32_bf16 a[84:87], v[216:219], v[132:135], a[84:87]\n"
"ds_read_b64_tr_b16 v[200:201], v227 offset:256\n"
"v_cvt_pk_bf16_f32 v157, v110, v111\n"
"s_add_u32 s92, s90, 4096\n"
"v_mfma_f32_16x16x32_bf16 a[84:87], v[220:223], v[84:87], a[84:87]\n"
"ds_read_b64_tr_b16 v[202:203], v227 offset:6400\n"
"v_add_f32 v239, v239, v111\n"
"v_mfma_f32_16x16x32_bf16 a[80:83], v[216:219], v[128:131], a[80:83]\n"
"v_exp_f32 v112, v112\n"
"v_mfma_f32_16x16x32_bf16 a[80:83], v[220:223], v[80:83], a[80:83]\n"
"ds_read_b64_tr_b16 v[204:205], v227 offset:12544\n"
"v_add_f32 v236, v236, v112\n"
"s_waitcnt lgkmcnt(3)\n"
"v_mfma_f32_16x16x32_bf16 a[96:99], v[176:179], v[128:131], a[96:99]\n"
"ds_read_b64_tr_b16 v[206:207], v227 offset:18688\n"
"v_mfma_f32_16x16x32_bf16 a[96:99], v[180:183], v[80:83], a[96:99]\n"
"v_exp_f32 v113, v113\n"
"buffer_load_dwordx4 a[232:235], v249, s[72:75], s92 offen offset:1024 sc1\n"
"v_mfma_f32_16x16x32_bf16 a[100:103], v[176:179], v[132:135], a[100:103]\n"
"ds_read_b64_tr_b16 v[214:215], v228 offset:18688\n"
"v_cvt_pk_bf16_f32 v146, v112, v113\n"
"v_mfma_f32_16x16x32_bf16 a[100:103], v[180:183], v[84:87], a[100:103]\n"
"ds_read_b64_tr_b16 v[208:209], v228 offset:256\n"
"v_add_f32 v236, v236, v113\n"
"v_mfma_f32_16x16x32_bf16 a[104:107], v[176:179], v[136:139], a[104:107]\n"
"s_waitcnt vmcnt(15)\n"
"s_barrier\n"
"v_exp_f32 v114, v114\n"
"v_mfma_f32_16x16x32_bf16 a[104:107], v[180:183], v[88:91], a[104:107]\n"
"ds_read_b64_tr_b16 v[210:211], v228 offset:6400\n"
"v_add_f32 v236, v236, v114\n"
"v_mfma_f32_16x16x32_bf16 a[108:111], v[176:179], v[140:143], a[108:111]\n"
"ds_read_b64_tr_b16 v[212:213], v228 offset:12544\n"
"s_add_u32 s93, s91, 4096\n"
"v_mfma_f32_16x16x32_bf16 a[108:111], v[180:183], v[92:95], a[108:111]\n"
"v_exp_f32 v115, v115\n"
"v_mfma_f32_16x16x32_bf16 a[124:127], v[184:187], v[140:143], a[124:127]\n"
"v_cvt_pk_bf16_f32 v147, v114, v115\n"
"v_add_f32 v236, v236, v115\n"
"v_mfma_f32_16x16x32_bf16 a[124:127], v[188:191], v[92:95], a[124:127]\n"
"v_mfma_f32_16x16x32_bf16 a[120:123], v[184:187], v[136:139], a[120:123]\n"
"v_exp_f32 v116, v116\n"
"v_mfma_f32_16x16x32_bf16 a[120:123], v[188:191], v[88:91], a[120:123]\n"
"buffer_load_dwordx4 a[236:239], v249, s[72:75], s93 offen offset:1024 sc1\n"
"v_mfma_f32_16x16x32_bf16 a[116:119], v[184:187], v[132:135], a[116:119]\n"
"ds_read_b64_tr_b16 v[216:217], v229 offset:256\n"
"v_add_f32 v237, v237, v116\n"
"v_mfma_f32_16x16x32_bf16 a[116:119], v[188:191], v[84:87], a[116:119]\n"
"ds_read_b64_tr_b16 v[218:219], v229 offset:6400\n"
"v_mfma_f32_16x16x32_bf16 a[112:115], v[184:187], v[128:131], a[112:115]\n"
"v_exp_f32 v117, v117\n"
"v_mfma_f32_16x16x32_bf16 a[112:115], v[188:191], v[80:83], a[112:115]\n"
"ds_read_b64_tr_b16 v[220:221], v229 offset:12544\n"
"v_cvt_pk_bf16_f32 v150, v116, v117\n"
"v_mfma_f32_16x16x32_bf16 a[128:131], v[192:195], v[128:131], a[128:131]\n"
"ds_read_b64_tr_b16 v[222:223], v229 offset:18688\n"
"v_add_f32 v237, v237, v117\n"
"s_add_u32 s92, s90, 4096\n"
"v_mfma_f32_16x16x32_bf16 a[128:131], v[196:199], v[80:83], a[128:131]\n"
"v_exp_f32 v118, v118\n"
"v_mfma_f32_16x16x32_bf16 a[132:135], v[192:195], v[132:135], a[132:135]\n"
"v_mfma_f32_16x16x32_bf16 a[132:135], v[196:199], v[84:87], a[132:135]\n"
"v_add_f32 v237, v237, v118\n"
"v_mfma_f32_16x16x32_bf16 a[136:139], v[192:195], v[136:139], a[136:139]\n"
"v_exp_f32 v119, v119\n"
"v_mfma_f32_16x16x32_bf16 a[136:139], v[196:199], v[88:91], a[136:139]\n"
"v_cvt_pk_bf16_f32 v151, v118, v119\n"
"v_add_f32 v237, v237, v119\n"
"buffer_load_dwordx4 a[240:243], v249, s[72:75], s92 offen offset:2048 sc1\n"
"v_mfma_f32_16x16x32_bf16 a[140:143], v[192:195], v[140:143], a[140:143]\n"
"v_mfma_f32_16x16x32_bf16 a[140:143], v[196:199], v[92:95], a[140:143]\n"
"v_exp_f32 v120, v120\n"
"s_waitcnt lgkmcnt(0)\n"
"v_mfma_f32_16x16x32_bf16 a[156:159], v[200:203], v[140:143], a[156:159]\n"
"v_add_f32 v238, v238, v120\n"
"v_mfma_f32_16x16x32_bf16 a[156:159], v[204:207], v[92:95], a[156:159]\n"
"ds_read_b128 v[176:179], v224 offset:36864\n"
"v_mfma_f32_16x16x32_bf16 a[152:155], v[200:203], v[136:139], a[152:155]\n"
"v_exp_f32 v121, v121\n"
"s_add_u32 s93, s91, 4096\n"
"v_mfma_f32_16x16x32_bf16 a[152:155], v[204:207], v[88:91], a[152:155]\n"
"v_mfma_f32_16x16x32_bf16 a[148:151], v[200:203], v[132:135], a[148:151]\n"
"ds_read_b128 v[180:183], v225 offset:36864\n"
"v_cvt_pk_bf16_f32 v154, v120, v121\n"
"v_mfma_f32_16x16x32_bf16 a[148:151], v[204:207], v[84:87], a[148:151]\n"
"v_exp_f32 v122, v122\n"
"v_mfma_f32_16x16x32_bf16 a[144:147], v[200:203], v[128:131], a[144:147]\n"
"ds_read_b128 v[184:187], v224 offset:36992\n"
"v_add_f32 v238, v238, v121\n"
"v_mfma_f32_16x16x32_bf16 a[144:147], v[204:207], v[80:83], a[144:147]\n"
"ds_read_b128 v[188:191], v225 offset:36992\n"
"buffer_load_dwordx4 a[244:247], v249, s[72:75], s93 offen offset:2048 sc1\n"
"v_mfma_f32_16x16x32_bf16 a[160:163], v[208:211], v[128:131], a[160:163]\n"
"v_exp_f32 v123, v123\n"
"v_mfma_f32_16x16x32_bf16 a[160:163], v[212:215], v[80:83], a[160:163]\n"
"ds_read_b128 v[192:195], v224 offset:37120\n"
"v_mfma_f32_16x16x32_bf16 a[164:167], v[208:211], v[132:135], a[164:167]\n"
"v_mfma_f32_16x16x32_bf16 a[164:167], v[212:215], v[84:87], a[164:167]\n"
"v_exp_f32 v124, v124\n"
"v_mfma_f32_16x16x32_bf16 a[168:171], v[208:211], v[136:139], a[168:171]\n"
"ds_read_b128 v[196:199], v225 offset:37120\n"
"v_cvt_pk_bf16_f32 v155, v122, v123\n"
"s_add_u32 s92, s90, 4096\n"
"v_mfma_f32_16x16x32_bf16 a[168:171], v[212:215], v[88:91], a[168:171]\n"
"v_add_f32 v238, v238, v122\n"
"v_add_f32 v238, v238, v123\n"
"v_mfma_f32_16x16x32_bf16 a[172:175], v[208:211], v[140:143], a[172:175]\n"
"v_exp_f32 v125, v125\n"
"v_mfma_f32_16x16x32_bf16 a[172:175], v[212:215], v[92:95], a[172:175]\n"
"v_cvt_pk_bf16_f32 v158, v124, v125\n"
"v_add_f32 v239, v239, v124\n"
"v_mfma_f32_16x16x32_bf16 a[188:191], v[216:219], v[140:143], a[188:191]\n"
"v_add_f32 v239, v239, v125\n"
"v_mfma_f32_16x16x32_bf16 a[188:191], v[220:223], v[92:95], a[188:191]\n"
"v_exp_f32 v126, v126\n"
"buffer_load_dwordx2 a[248:249], v253, s[72:75], s92 offen offset:3072 sc1\n"
"v_mfma_f32_16x16x32_bf16 a[184:187], v[216:219], v[136:139], a[184:187]\n"
"v_mfma_f32_16x16x32_bf16 a[184:187], v[220:223], v[88:91], a[184:187]\n"
"v_add_f32 v239, v239, v126\n"
"v_mfma_f32_16x16x32_bf16 a[180:183], v[216:219], v[132:135], a[180:183]\n"
"v_mfma_f32_16x16x32_bf16 a[180:183], v[220:223], v[84:87], a[180:183]\n"
"v_exp_f32 v127, v127\n"
"v_mfma_f32_16x16x32_bf16 a[176:179], v[216:219], v[128:131], a[176:179]\n"
"v_cvt_pk_bf16_f32 v159, v126, v127\n"
"v_add_f32 v239, v239, v127\n"
"buffer_load_dwordx2 a[250:251], v253, s[72:75], s92 offen offset:3584 sc1\n"
"v_mfma_f32_16x16x32_bf16 a[176:179], v[220:223], v[80:83], a[176:179]\n"
"s_waitcnt lgkmcnt(0)\n"
"v_mfma_f32_16x16x32_bf16 v[96:99], v[176:179], v[0:3], v[160:163]\n"
"v_mfma_f32_16x16x32_bf16 v[96:99], v[180:183], v[16:19], v[96:99]\n"
"ds_read_b128 v[200:203], v224 offset:43008\n"
"v_mfma_f32_16x16x32_bf16 v[96:99], v[184:187], v[32:35], v[96:99]\n"
"v_mfma_f32_16x16x32_bf16 v[96:99], v[188:191], v[48:51], v[96:99]\n"
"ds_read_b128 v[204:207], v225 offset:43008\n"
"s_add_u32 s93, s91, 4096\n"
"v_mfma_f32_16x16x32_bf16 v[96:99], v[192:195], v[64:67], v[96:99]\n"
"ds_read_b128 v[208:211], v224 offset:43136\n"
"v_mfma_f32_16x16x32_bf16 v[96:99], v[196:199], a[192:195], v[96:99]\n"
"ds_read_b128 v[212:215], v225 offset:43136\n"
"v_mfma_f32_16x16x32_bf16 v[100:103], v[176:179], v[4:7], v[164:167]\n"
"ds_read_b128 v[216:219], v224 offset:43264\n"
"v_mfma_f32_16x16x32_bf16 v[100:103], v[180:183], v[20:23], v[100:103]\n"
"ds_read_b128 v[220:223], v225 offset:43264\n"
"v_mfma_f32_16x16x32_bf16 v[100:103], v[184:187], v[36:39], v[100:103]\n"
"s_nop 1\n"
"v_exp_f32 v96, v96\n"
"buffer_load_dwordx2 a[252:253], v253, s[72:75], s93 offen offset:3072 sc1\n"
"v_mfma_f32_16x16x32_bf16 v[100:103], v[188:191], v[52:55], v[100:103]\n"
"v_add_f32 v236, v236, v96\n"
"v_mfma_f32_16x16x32_bf16 v[100:103], v[192:195], v[68:71], v[100:103]\n"
"v_exp_f32 v97, v97\n"
"v_mfma_f32_16x16x32_bf16 v[100:103], v[196:199], a[196:199], v[100:103]\n"
"v_cvt_pk_bf16_f32 v80, v96, v97\n"
"v_add_f32 v236, v236, v97\n"
"v_mfma_f32_16x16x32_bf16 v[104:107], v[176:179], v[8:11], v[168:171]\n"
"v_mfma_f32_16x16x32_bf16 v[104:107], v[180:183], v[24:27], v[104:107]\n"
"v_exp_f32 v98, v98\n"
"buffer_load_dwordx2 a[254:255], v253, s[72:75], s93 offen offset:3584 sc1\n"
"v_mfma_f32_16x16x32_bf16 v[104:107], v[184:187], v[40:43], v[104:107]\n"
"v_add_f32 v236, v236, v98\n"
"v_mfma_f32_16x16x32_bf16 v[104:107], v[188:191], v[56:59], v[104:107]\n"
"v_exp_f32 v99, v99\n"
"v_mfma_f32_16x16x32_bf16 v[104:107], v[192:195], v[72:75], v[104:107]\n"
"v_cvt_pk_bf16_f32 v81, v98, v99\n"
"v_add_f32 v236, v236, v99\n"
"v_mfma_f32_16x16x32_bf16 v[104:107], v[196:199], a[200:203], v[104:107]\n"
"v_mfma_f32_16x16x32_bf16 v[108:111], v[176:179], v[12:15], v[172:175]\n"
"v_exp_f32 v100, v100\n"
"v_mfma_f32_16x16x32_bf16 v[108:111], v[180:183], v[28:31], v[108:111]\n"
"v_add_f32 v237, v237, v100\n"
"v_mfma_f32_16x16x32_bf16 v[108:111], v[184:187], v[44:47], v[108:111]\n"
"v_exp_f32 v101, v101\n"
"v_mfma_f32_16x16x32_bf16 v[108:111], v[188:191], v[60:63], v[108:111]\n"
"v_cvt_pk_bf16_f32 v84, v100, v101\n"
"v_add_f32 v237, v237, v101\n"
"v_mfma_f32_16x16x32_bf16 v[108:111], v[192:195], v[76:79], v[108:111]\n"
"v_mfma_f32_16x16x32_bf16 v[108:111], v[196:199], a[204:207], v[108:111]\n"
"v_exp_f32 v102, v102\n"
"s_waitcnt lgkmcnt(0)\n"
"v_mfma_f32_16x16x32_bf16 v[112:115], v[200:203], v[0:3], v[160:163]\n"
"v_add_f32 v237, v237, v102\n"
"v_mfma_f32_16x16x32_bf16 v[112:115], v[204:207], v[16:19], v[112:115]\n"
"v_exp_f32 v103, v103\n"
"v_mfma_f32_16x16x32_bf16 v[112:115], v[208:211], v[32:35], v[112:115]\n"
"v_cvt_pk_bf16_f32 v85, v102, v103\n"
"v_mfma_f32_16x16x32_bf16 v[112:115], v[212:215], v[48:51], v[112:115]\n"
"v_add_f32 v237, v237, v103\n"
"v_mfma_f32_16x16x32_bf16 v[112:115], v[216:219], v[64:67], v[112:115]\n"
"v_exp_f32 v104, v104\n"
"v_mfma_f32_16x16x32_bf16 v[112:115], v[220:223], a[192:195], v[112:115]\n"
"v_add_f32 v238, v238, v104\n"
"v_mfma_f32_16x16x32_bf16 v[116:119], v[200:203], v[4:7], v[164:167]\n"
"v_exp_f32 v105, v105\n"
"v_mfma_f32_16x16x32_bf16 v[116:119], v[204:207], v[20:23], v[116:119]\n"
"v_cvt_pk_bf16_f32 v88, v104, v105\n"
"v_mfma_f32_16x16x32_bf16 v[116:119], v[208:211], v[36:39], v[116:119]\n"
"v_add_f32 v238, v238, v105\n"
"v_mfma_f32_16x16x32_bf16 v[116:119], v[212:215], v[52:55], v[116:119]\n"
"v_exp_f32 v106, v106\n"
"v_mfma_f32_16x16x32_bf16 v[116:119], v[216:219], v[68:71], v[116:119]\n"
"v_add_f32 v238, v238, v106\n"
"v_mfma_f32_16x16x32_bf16 v[116:119], v[220:223], a[196:199], v[116:119]\n"
"v_exp_f32 v107, v107\n"
"v_mfma_f32_16x16x32_bf16 v[120:123], v[200:203], v[8:11], v[168:171]\n"
"v_cvt_pk_bf16_f32 v89, v106, v107\n"
"v_add_f32 v238, v238, v107\n"
"v_mfma_f32_16x16x32_bf16 v[120:123], v[204:207], v[24:27], v[120:123]\n"
"v_mfma_f32_16x16x32_bf16 v[120:123], v[208:211], v[40:43], v[120:123]\n"
"v_exp_f32 v108, v108\n"
"v_mfma_f32_16x16x32_bf16 v[120:123], v[212:215], v[56:59], v[120:123]\n"
"v_add_f32 v239, v239, v108\n"
"v_mfma_f32_16x16x32_bf16 v[120:123], v[216:219], v[72:75], v[120:123]\n"
"v_exp_f32 v109, v109\n"
"v_mfma_f32_16x16x32_bf16 v[120:123], v[220:223], a[200:203], v[120:123]\n"
"v_cvt_pk_bf16_f32 v92, v108, v109\n"
"v_add_f32 v239, v239, v109\n"
"v_mfma_f32_16x16x32_bf16 v[124:127], v[200:203], v[12:15], v[172:175]\n"
"v_mfma_f32_16x16x32_bf16 v[124:127], v[204:207], v[28:31], v[124:127]\n"
"v_exp_f32 v110, v110\n"
"v_mfma_f32_16x16x32_bf16 v[124:127], v[208:211], v[44:47], v[124:127]\n"
"v_add_f32 v239, v239, v110\n"
"v_mfma_f32_16x16x32_bf16 v[124:127], v[212:215], v[60:63], v[124:127]\n"
"v_exp_f32 v111, v111\n"
"v_mfma_f32_16x16x32_bf16 v[124:127], v[216:219], v[76:79], v[124:127]\n"
"v_cvt_pk_bf16_f32 v93, v110, v111\n"
"v_add_f32 v239, v239, v111\n"
"v_mfma_f32_16x16x32_bf16 v[124:127], v[220:223], a[204:207], v[124:127]\n"
"v_exp_f32 v112, v112\n"
"s_nop 0\n"
"v_add_f32 v236, v236, v112\n"
"v_exp_f32 v113, v113\n"
"s_nop 0\n"
"v_cvt_pk_bf16_f32 v82, v112, v113\n"
"v_add_f32 v236, v236, v113\n"
"v_exp_f32 v114, v114\n"
"s_nop 0\n"
"v_add_f32 v236, v236, v114\n"
"v_exp_f32 v115, v115\n"
"s_nop 0\n"
"v_cvt_pk_bf16_f32 v83, v114, v115\n"
"v_add_f32 v236, v236, v115\n"
"v_exp_f32 v116, v116\n"
"s_nop 0\n"
"v_add_f32 v237, v237, v116\n"
"v_exp_f32 v117, v117\n"
"s_nop 0\n"
"v_cvt_pk_bf16_f32 v86, v116, v117\n"
"v_add_f32 v237, v237, v117\n"
"v_exp_f32 v118, v118\n"
"s_nop 0\n"
"v_add_f32 v237, v237, v118\n"
"s_waitcnt vmcnt(8)\n"
"s_barrier\n"
"v_exp_f32 v119, v119\n"
"s_nop 0\n"
"v_cvt_pk_bf16_f32 v87, v118, v119\n"
"v_add_f32 v237, v237, v119\n"
"ds_read_b64_tr_b16 v[176:177], v226 offset:24576\n"
"s_add_u32 m0, s86, 0\n"
"v_exp_f32 v120, v120\n"
"ds_read_b64_tr_b16 v[178:179], v226 offset:30720\n"
"v_add_f32 v238, v238, v120\n"
"v_exp_f32 v121, v121\n"
"ds_read_b64_tr_b16 v[180:181], v226 offset:36864\n"
"v_cvt_pk_bf16_f32 v90, v120, v121\n"
"buffer_load_dwordx4 v244, s[68:71], s88 offen offset:0 lds\n"
"ds_read_b64_tr_b16 v[182:183], v226 offset:43008\n"
"v_add_f32 v238, v238, v121\n"
"v_exp_f32 v122, v122\n"
"ds_read_b64_tr_b16 v[190:191], v227 offset:43008\n"
"v_add_f32 v238, v238, v122\n"
"v_exp_f32 v123, v123\n"
"buffer_load_dwordx4 v245, s[68:71], s88 offen offset:1024 lds\n"
"ds_read_b64_tr_b16 v[184:185], v227 offset:24576\n"
"v_cvt_pk_bf16_f32 v91, v122, v123\n"
"ds_read_b64_tr_b16 v[186:187], v227 offset:30720\n"
"v_add_f32 v238, v238, v123\n"
"v_exp_f32 v124, v124\n"
"ds_read_b64_tr_b16 v[188:189], v227 offset:36864\n"
"v_add_f32 v239, v239, v124\n"
"buffer_load_dwordx4 v246, s[68:71], s88 offen offset:2048 lds\n"
"v_exp_f32 v125, v125\n"
"s_nop 0\n"
"v_cvt_pk_bf16_f32 v94, v124, v125\n"
"v_add_f32 v239, v239, v125\n"
"ds_read_b64_tr_b16 v[198:199], v228 offset:43008\n"
"v_exp_f32 v126, v126\n"
"buffer_load_dwordx4 v247, s[68:71], s88 offen offset:3072 lds\n"
"ds_read_b64_tr_b16 v[192:193], v228 offset:24576\n"
"v_add_f32 v239, v239, v126\n"
"ds_read_b64_tr_b16 v[194:195], v228 offset:30720\n"
"v_exp_f32 v127, v127\n"
"ds_read_b64_tr_b16 v[196:197], v228 offset:36864\n"
"v_cvt_pk_bf16_f32 v95, v126, v127\n"
"s_add_u32 m0, s86, 4096\n"
"v_add_f32 v239, v239, v127\n"
"ds_read_b64_tr_b16 v[202:203], v229 offset:30720\n"
"buffer_load_dwordx4 v248, s[68:71], s88 offen lds\n"
"ds_read_b64_tr_b16 v[200:201], v229 offset:24576\n"
"ds_read_b64_tr_b16 v[204:205], v229 offset:36864\n"
"ds_read_b64_tr_b16 v[206:207], v229 offset:43008\n"
"s_add_u32 s88, s88, 81920\n"
"s_waitcnt lgkmcnt(4)\n"
"v_mfma_f32_16x16x32_bf16 a[0:3], v[176:179], v[144:147], a[0:3]\n"
"v_mfma_f32_16x16x32_bf16 a[0:3], v[180:183], v[80:83], a[0:3]\n"
"v_mov_b32 v160, v236\n"
"v_mfma_f32_16x16x32_bf16 a[4:7], v[176:179], v[148:151], a[4:7]\n"
"v_mov_b32 v96, v160\n"
"s_nop 1\n"
"v_permlane16_swap_b32 v160, v96\n"
"s_nop 1\n"
"v_add_f32 v160, v160, v96\n"
"v_mfma_f32_16x16x32_bf16 a[4:7], v[180:183], v[84:87], a[4:7]\n"
"s_add_u32 m0, s86, 20480\n"
"v_mov_b32 v96, v160\n"
"s_nop 1\n"
"v_permlane32_swap_b32 v160, v96\n"
"s_nop 1\n"
"v_add_f32 v160, v160, v96\n"
"v_mfma_f32_16x16x32_bf16 a[8:11], v[176:179], v[152:155], a[8:11]\n"
"ds_read_b64_tr_b16 v[208:209], v226 offset:24704\n"
"v_mov_b32 v162, v237\n"
"v_mfma_f32_16x16x32_bf16 a[8:11], v[180:183], v[88:91], a[8:11]\n"
"ds_read_b64_tr_b16 v[210:211], v226 offset:30848\n"
"v_mov_b32 v96, v162\n"
"s_nop 1\n"
"v_permlane16_swap_b32 v162, v96\n"
"s_nop 1\n"
"v_add_f32 v162, v162, v96\n"
"v_mfma_f32_16x16x32_bf16 a[12:15], v[176:179], v[156:159], a[12:15]\n"
"v_mov_b32 v96, v162\n"
"s_nop 1\n"
"v_permlane32_swap_b32 v162, v96\n"
"s_nop 1\n"
"v_add_f32 v162, v162, v96\n"
"v_mfma_f32_16x16x32_bf16 a[12:15], v[180:183], v[92:95], a[12:15]\n"
"ds_read_b64_tr_b16 v[212:213], v226 offset:36992\n"
"buffer_load_dwordx4 v244, s[68:71], s88 offen offset:0 lds\n"
"v_mov_b32 v164, v238\n"
"v_mfma_f32_16x16x32_bf16 a[28:31], v[184:187], v[156:159], a[28:31]\n"
"ds_read_b64_tr_b16 v[214:215], v226 offset:43136\n"
"v_mov_b32 v96, v164\n"
"s_nop 1\n"
"v_permlane16_swap_b32 v164, v96\n"
"s_nop 1\n"
"v_add_f32 v164, v164, v96\n"
"v_mfma_f32_16x16x32_bf16 a[28:31], v[188:191], v[92:95], a[28:31]\n"
"v_mov_b32 v96, v164\n"
"s_nop 1\n"
"v_permlane32_swap_b32 v164, v96\n"
"s_nop 1\n"
"v_add_f32 v164, v164, v96\n"
"v_mfma_f32_16x16x32_bf16 a[24:27], v[184:187], v[152:155], a[24:27]\n"
"v_mov_b32 v166, v239\n"
"v_mfma_f32_16x16x32_bf16 a[24:27], v[188:191], v[88:91], a[24:27]\n"
"ds_read_b64_tr_b16 v[216:217], v227 offset:24704\n"
"buffer_load_dwordx4 v245, s[68:71], s88 offen offset:1024 lds\n"
"v_mov_b32 v96, v166\n"
"s_nop 1\n"
"v_permlane16_swap_b32 v166, v96\n"
"s_nop 1\n"
"v_add_f32 v166, v166, v96\n"
"v_mfma_f32_16x16x32_bf16 a[20:23], v[184:187], v[148:151], a[20:23]\n"
"v_mov_b32 v96, v166\n"
"s_nop 1\n"
"v_permlane32_swap_b32 v166, v96\n"
"s_nop 1\n"
"v_add_f32 v166, v166, v96\n"
"v_mfma_f32_16x16x32_bf16 a[20:23], v[188:191], v[84:87], a[20:23]\n"
"ds_read_b64_tr_b16 v[218:219], v227 offset:30848\n"
"v_add_f32 v98, v160, v162\n"
"v_add_f32 v99, v164, v166\n"
"v_mfma_f32_16x16x32_bf16 a[16:19], v[184:187], v[144:147], a[16:19]\n"
"ds_read_b64_tr_b16 v[220:221], v227 offset:36992\n"
"v_add_f32 v98, v98, v99\n"
"s_mov_b32 s25, 0x53800000\n"
"v_mfma_f32_16x16x32_bf16 a[16:19], v[188:191], v[80:83], a[16:19]\n"
"buffer_load_dwordx4 v246, s[68:71], s88 offen offset:2048 lds\n"
"v_cmp_nle_f32 vcc, v98, s25\n"
"s_lshl_b32 s26, s22, 2\n"
"s_add_u32 s26, s26, 162816\n"
"v_mfma_f32_16x16x32_bf16 a[32:35], v[192:195], v[144:147], a[32:35]\n"
"ds_read_b64_tr_b16 v[222:223], v227 offset:43136\n"
"s_nop 3\n"
"s_cmp_lg_u64 vcc, 0\n"
"s_cselect_b32 s25, 1, 0\n"
"v_mfma_f32_16x16x32_bf16 a[32:35], v[196:199], v[80:83], a[32:35]\n"
"ds_read_b64_tr_b16 v[182:183], v228 offset:43136\n"
"v_mov_b32 v96, s25\n"
"v_mov_b32 v97, s26\n"
"v_mfma_f32_16x16x32_bf16 a[36:39], v[192:195], v[148:151], a[36:39]\n"
"ds_write_b32 v97, v96\n"
"v_mfma_f32_16x16x32_bf16 a[36:39], v[196:199], v[84:87], a[36:39]\n"
"buffer_load_dwordx4 v247, s[68:71], s88 offen offset:3072 lds\n"
"v_rcp_f32 v160, v160\n"
"v_mfma_f32_16x16x32_bf16 a[40:43], v[192:195], v[152:155], a[40:43]\n"
"ds_read_b64_tr_b16 v[176:177], v228 offset:24704\n"
"v_rcp_f32 v162, v162\n"
"v_mfma_f32_16x16x32_bf16 a[40:43], v[196:199], v[88:91], a[40:43]\n"
"v_rcp_f32 v164, v164\n"
"v_mfma_f32_16x16x32_bf16 a[44:47], v[192:195], v[156:159], a[44:47]\n"
"ds_read_b64_tr_b16 v[178:179], v228 offset:30848\n"
"v_rcp_f32 v166, v166\n"
"v_mfma_f32_16x16x32_bf16 a[44:47], v[196:199], v[92:95], a[44:47]\n"
"ds_read_b64_tr_b16 v[180:181], v228 offset:36992\n"
"s_add_u32 m0, s86, 24576\n"
"v_mov_b32 v97, 162816\n"
"s_waitcnt lgkmcnt(4)\n"
"v_mfma_f32_16x16x32_bf16 a[60:63], v[200:203], v[156:159], a[60:63]\n"
"v_mfma_f32_16x16x32_bf16 a[60:63], v[204:207], v[92:95], a[60:63]\n"
"v_mfma_f32_16x16x32_bf16 a[56:59], v[200:203], v[152:155], a[56:59]\n"
"v_mfma_f32_16x16x32_bf16 a[56:59], v[204:207], v[88:91], a[56:59]\n"
"buffer_load_dwordx4 v248, s[68:71], s88 offen lds\n"
"v_mfma_f32_16x16x32_bf16 a[52:55], v[200:203], v[148:151], a[52:55]\n"
"ds_read_b64_tr_b16 v[184:185], v229 offset:24704\n"
"v_mfma_f32_16x16x32_bf16 a[52:55], v[204:207], v[84:87], a[52:55]\n"
"ds_read_b64_tr_b16 v[186:187], v229 offset:30848\n"
"v_mfma_f32_16x16x32_bf16 a[48:51], v[200:203], v[144:147], a[48:51]\n"
"ds_read_b64_tr_b16 v[188:189], v229 offset:36992\n"
"v_mfma_f32_16x16x32_bf16 a[48:51], v[204:207], v[80:83], a[48:51]\n"
"s_add_u32 s88, s88, 81920\n"
"v_mfma_f32_16x16x32_bf16 a[64:67], v[208:211], v[144:147], a[64:67]\n"
"ds_read_b64_tr_b16 v[190:191], v229 offset:43136\n"
"v_mfma_f32_16x16x32_bf16 a[64:67], v[212:215], v[80:83], a[64:67]\n"
"ds_read_b64_tr_b16 v[198:199], v226 offset:43264\n"
"v_mfma_f32_16x16x32_bf16 a[68:71], v[208:211], v[148:151], a[68:71]\n"
"v_mfma_f32_16x16x32_bf16 a[68:71], v[212:215], v[84:87], a[68:71]\n"
"s_add_u32 m0, s86, 40960\n"
"v_mfma_f32_16x16x32_bf16 a[72:75], v[208:211], v[152:155], a[72:75]\n"
"ds_read_b64_tr_b16 v[192:193], v226 offset:24832\n"
"v_mfma_f32_16x16x32_bf16 a[72:75], v[212:215], v[88:91], a[72:75]\n"
"v_mfma_f32_16x16x32_bf16 a[76:79], v[208:211], v[156:159], a[76:79]\n"
"ds_read_b64_tr_b16 v[194:195], v226 offset:30976\n"
"v_mfma_f32_16x16x32_bf16 a[76:79], v[212:215], v[92:95], a[76:79]\n"
"ds_read_b64_tr_b16 v[196:197], v226 offset:37120\n"
"buffer_load_dwordx4 v244, s[68:71], s88 offen offset:0 lds\n"
"v_mfma_f32_16x16x32_bf16 a[92:95], v[216:219], v[156:159], a[92:95]\n"
"v_mfma_f32_16x16x32_bf16 a[92:95], v[220:223], v[92:95], a[92:95]\n"
"v_mfma_f32_16x16x32_bf16 a[88:91], v[216:219], v[152:155], a[88:91]\n"
"v_mfma_f32_16x16x32_bf16 a[88:91], v[220:223], v[88:91], a[88:91]\n"
"buffer_load_dwordx4 v245, s[68:71], s88 offen offset:1024 lds\n"
"v_mfma_f32_16x16x32_bf16 a[84:87], v[216:219], v[148:151], a[84:87]\n"
"ds_read_b64_tr_b16 v[200:201], v227 offset:24832\n"
"v_mfma_f32_16x16x32_bf16 a[84:87], v[220:223], v[84:87], a[84:87]\n"
"ds_read_b64_tr_b16 v[202:203], v227 offset:30976\n"
"v_mfma_f32_16x16x32_bf16 a[80:83], v[216:219], v[144:147], a[80:83]\n"
"v_mfma_f32_16x16x32_bf16 a[80:83], v[220:223], v[80:83], a[80:83]\n"
"ds_read_b64_tr_b16 v[204:205], v227 offset:37120\n"
"buffer_load_dwordx4 v246, s[68:71], s88 offen offset:2048 lds\n"
"s_waitcnt lgkmcnt(3)\n"
"v_mfma_f32_16x16x32_bf16 a[96:99], v[176:179], v[144:147], a[96:99]\n"
"ds_read_b64_tr_b16 v[206:207], v227 offset:43264\n"
"v_mfma_f32_16x16x32_bf16 a[96:99], v[180:183], v[80:83], a[96:99]\n"
"v_mfma_f32_16x16x32_bf16 a[100:103], v[176:179], v[148:151], a[100:103]\n"
"ds_read_b64_tr_b16 v[214:215], v228 offset:43264\n"
"v_mfma_f32_16x16x32_bf16 a[100:103], v[180:183], v[84:87], a[100:103]\n"
"ds_read_b64_tr_b16 v[208:209], v228 offset:24832\n"
"buffer_load_dwordx4 v247, s[68:71], s88 offen offset:3072 lds\n"
"v_mfma_f32_16x16x32_bf16 a[104:107], v[176:179], v[152:155], a[104:107]\n"
"v_mfma_f32_16x16x32_bf16 a[104:107], v[180:183], v[88:91], a[104:107]\n"
"ds_read_b64_tr_b16 v[210:211], v228 offset:30976\n"
"v_mfma_f32_16x16x32_bf16 a[108:111], v[176:179], v[156:159], a[108:111]\n"
"ds_read_b64_tr_b16 v[212:213], v228 offset:37120\n"
"v_mfma_f32_16x16x32_bf16 a[108:111], v[180:183], v[92:95], a[108:111]\n"
"s_add_u32 m0, s86, 45056\n"
"v_mfma_f32_16x16x32_bf16 a[124:127], v[184:187], v[156:159], a[124:127]\n"
"v_mfma_f32_16x16x32_bf16 a[124:127], v[188:191], v[92:95], a[124:127]\n"
"v_mfma_f32_16x16x32_bf16 a[120:123], v[184:187], v[152:155], a[120:123]\n"
"v_mfma_f32_16x16x32_bf16 a[120:123], v[188:191], v[88:91], a[120:123]\n"
"buffer_load_dwordx4 v248, s[68:71], s88 offen lds\n"
"v_mfma_f32_16x16x32_bf16 a[116:119], v[184:187], v[148:151], a[116:119]\n"
"ds_read_b64_tr_b16 v[216:217], v229 offset:24832\n"
"v_mfma_f32_16x16x32_bf16 a[116:119], v[188:191], v[84:87], a[116:119]\n"
"ds_read_b64_tr_b16 v[218:219], v229 offset:30976\n"
"v_mfma_f32_16x16x32_bf16 a[112:115], v[184:187], v[144:147], a[112:115]\n"
"v_mfma_f32_16x16x32_bf16 a[112:115], v[188:191], v[80:83], a[112:115]\n"
"ds_read_b64_tr_b16 v[220:221], v229 offset:37120\n"
"s_add_u32 s88, s88, 81920\n"
"v_mfma_f32_16x16x32_bf16 a[128:131], v[192:195], v[144:147], a[128:131]\n"
"ds_read_b64_tr_b16 v[222:223], v229 offset:43264\n"
"v_mfma_f32_16x16x32_bf16 a[128:131], v[196:199], v[80:83], a[128:131]\n"
"v_mfma_f32_16x16x32_bf16 a[132:135], v[192:195], v[148:151], a[132:135]\n"
"v_mfma_f32_16x16x32_bf16 a[132:135], v[196:199], v[84:87], a[132:135]\n"
"v_mfma_f32_16x16x32_bf16 a[136:139], v[192:195], v[152:155], a[136:139]\n"
"v_mfma_f32_16x16x32_bf16 a[136:139], v[196:199], v[88:91], a[136:139]\n"
"v_mfma_f32_16x16x32_bf16 a[140:143], v[192:195], v[156:159], a[140:143]\n"
"v_mfma_f32_16x16x32_bf16 a[140:143], v[196:199], v[92:95], a[140:143]\n"
"s_waitcnt lgkmcnt(0)\n"
"v_mfma_f32_16x16x32_bf16 a[156:159], v[200:203], v[156:159], a[156:159]\n"
"v_mfma_f32_16x16x32_bf16 a[156:159], v[204:207], v[92:95], a[156:159]\n"
"v_mfma_f32_16x16x32_bf16 a[152:155], v[200:203], v[152:155], a[152:155]\n"
"v_mfma_f32_16x16x32_bf16 a[152:155], v[204:207], v[88:91], a[152:155]\n"
"v_mfma_f32_16x16x32_bf16 a[148:151], v[200:203], v[148:151], a[148:151]\n"
"v_mfma_f32_16x16x32_bf16 a[148:151], v[204:207], v[84:87], a[148:151]\n"
"v_mfma_f32_16x16x32_bf16 a[144:147], v[200:203], v[144:147], a[144:147]\n"
"v_mfma_f32_16x16x32_bf16 a[144:147], v[204:207], v[80:83], a[144:147]\n"
"v_mfma_f32_16x16x32_bf16 a[160:163], v[208:211], v[144:147], a[160:163]\n"
"s_waitcnt lgkmcnt(0)\n"
"s_barrier\n"
"ds_read_b128 v[100:103], v97\n"
"v_mfma_f32_16x16x32_bf16 a[160:163], v[212:215], v[80:83], a[160:163]\n"
"v_mfma_f32_16x16x32_bf16 a[164:167], v[208:211], v[148:151], a[164:167]\n"
"v_mfma_f32_16x16x32_bf16 a[164:167], v[212:215], v[84:87], a[164:167]\n"
"v_mfma_f32_16x16x32_bf16 a[168:171], v[208:211], v[152:155], a[168:171]\n"
"v_mfma_f32_16x16x32_bf16 a[168:171], v[212:215], v[88:91], a[168:171]\n"
"v_mfma_f32_16x16x32_bf16 a[172:175], v[208:211], v[156:159], a[172:175]\n"
"v_mfma_f32_16x16x32_bf16 a[172:175], v[212:215], v[92:95], a[172:175]\n"
"v_mfma_f32_16x16x32_bf16 a[188:191], v[216:219], v[156:159], a[188:191]\n"
"v_mfma_f32_16x16x32_bf16 a[188:191], v[220:223], v[92:95], a[188:191]\n"
"v_mfma_f32_16x16x32_bf16 a[184:187], v[216:219], v[152:155], a[184:187]\n"
"v_mfma_f32_16x16x32_bf16 a[184:187], v[220:223], v[88:91], a[184:187]\n"
"v_mfma_f32_16x16x32_bf16 a[180:183], v[216:219], v[148:151], a[180:183]\n"
"v_mfma_f32_16x16x32_bf16 a[180:183], v[220:223], v[84:87], a[180:183]\n"
"v_mfma_f32_16x16x32_bf16 a[176:179], v[216:219], v[144:147], a[176:179]\n"
"v_mfma_f32_16x16x32_bf16 a[176:179], v[220:223], v[80:83], a[176:179]\n"
"s_waitcnt lgkmcnt(0)\n"
"v_or3_b32 v100, v100, v101, v102\n"
"v_or_b32 v100, v100, v103\n"
"s_nop 0\n"
"v_readfirstlane_b32 s25, v100\n"
"s_nop 1\n"
"s_cmp_lg_u32 s31, 0\n"
"s_cselect_b32 s25, 0, s25\n"
"s_cmp_lg_u32 s25, 0\n"
"s_cbranch_scc1 REDO_%=\n"
"s_lshl_b32 s83, s20, 3\n"
"s_add_u32 s83, s83, s21\n"
"s_lshl_b32 s83, s83, 2\n"
"s_add_u32 s83, s83, s22\n"
"s_mul_i32 s83, s83, 24576\n"
"s_add_u32 s83, s83, 8978432\n"
"v_accvgpr_read_b32 v176, a160\n"
"v_accvgpr_read_b32 v177, a161\n"
"v_accvgpr_read_b32 v178, a162\n"
"v_accvgpr_read_b32 v179, a163\n"
"v_accvgpr_read_b32 v180, a176\n"
"v_accvgpr_read_b32 v181, a177\n"
"v_accvgpr_read_b32 v182, a178\n"
"v_accvgpr_read_b32 v183, a179\n"
"v_pk_mul_f32 v[176:177], v[176:177], v[160:161] op_sel_hi:[1,0]\n"
"v_pk_mul_f32 v[178:179], v[178:179], v[160:161] op_sel_hi:[1,0]\n"
"v_pk_mul_f32 v[180:181], v[180:181], v[160:161] op_sel_hi:[1,0]\n"
"v_pk_mul_f32 v[182:183], v[182:183], v[160:161] op_sel_hi:[1,0]\n"
"v_cvt_pk_bf16_f32 v80, v176, v177\n"
"v_cvt_pk_bf16_f32 v81, v178, v179\n"
"v_cvt_pk_bf16_f32 v82, v180, v181\n"
"v_cvt_pk_bf16_f32 v83, v182, v183\n"
"v_accvgpr_read_b32 v184, a164\n"
"v_accvgpr_read_b32 v185, a165\n"
"v_accvgpr_read_b32 v186, a166\n"
"v_accvgpr_read_b32 v187, a167\n"
"v_accvgpr_read_b32 v188, a180\n"
"v_accvgpr_read_b32 v189, a181\n"
"v_accvgpr_read_b32 v190, a182\n"
"v_accvgpr_read_b32 v191, a183\n"
"v_pk_mul_f32 v[184:185], v[184:185], v[162:163] op_sel_hi:[1,0]\n"
"v_pk_mul_f32 v[186:187], v[186:187], v[162:163] op_sel_hi:[1,0]\n"
"v_pk_mul_f32 v[188:189], v[188:189], v[162:163] op_sel_hi:[1,0]\n"
"v_pk_mul_f32 v[190:191], v[190:191], v[162:163] op_sel_hi:[1,0]\n"
"v_cvt_pk_bf16_f32 v84, v184, v185\n"
"v_cvt_pk_bf16_f32 v85, v186, v187\n"
"v_cvt_pk_bf16_f32 v86, v188, v189\n"
"v_cvt_pk_bf16_f32 v87, v190, v191\n"
"v_accvgpr_read_b32 v192, a168\n"
"v_accvgpr_read_b32 v193, a169\n"
"v_accvgpr_read_b32 v194, a170\n"
"v_accvgpr_read_b32 v195, a171\n"
"v_accvgpr_read_b32 v196, a184\n"
"v_accvgpr_read_b32 v197, a185\n"
"v_accvgpr_read_b32 v198, a186\n"
"v_accvgpr_read_b32 v199, a187\n"
"v_pk_mul_f32 v[192:193], v[192:193], v[164:165] op_sel_hi:[1,0]\n"
"v_pk_mul_f32 v[194:195], v[194:195], v[164:165] op_sel_hi:[1,0]\n"
"v_pk_mul_f32 v[196:197], v[196:197], v[164:165] op_sel_hi:[1,0]\n"
"v_pk_mul_f32 v[198:199], v[198:199], v[164:165] op_sel_hi:[1,0]\n"
"v_cvt_pk_bf16_f32 v88, v192, v193\n"
"v_cvt_pk_bf16_f32 v89, v194, v195\n"
"v_cvt_pk_bf16_f32 v90, v196, v197\n"
"v_cvt_pk_bf16_f32 v91, v198, v199\n"
"v_accvgpr_read_b32 v200, a172\n"
"v_accvgpr_read_b32 v201, a173\n"
"v_accvgpr_read_b32 v202, a174\n"
"v_accvgpr_read_b32 v203, a175\n"
"v_accvgpr_read_b32 v204, a188\n"
"v_accvgpr_read_b32 v205, a189\n"
"v_accvgpr_read_b32 v206, a190\n"
"v_accvgpr_read_b32 v207, a191\n"
"v_pk_mul_f32 v[200:201], v[200:201], v[166:167] op_sel_hi:[1,0]\n"
"v_pk_mul_f32 v[202:203], v[202:203], v[166:167] op_sel_hi:[1,0]\n"
"v_pk_mul_f32 v[204:205], v[204:205], v[166:167] op_sel_hi:[1,0]\n"
"v_pk_mul_f32 v[206:207], v[206:207], v[166:167] op_sel_hi:[1,0]\n"
"v_cvt_pk_bf16_f32 v92, v200, v201\n"
"v_cvt_pk_bf16_f32 v93, v202, v203\n"
"v_cvt_pk_bf16_f32 v94, v204, v205\n"
"v_cvt_pk_bf16_f32 v95, v206, v207\n"
"s_add_u32 s92, s90, 0\n"
"buffer_load_dwordx4 a[192:195], v249, s[72:75], s92 offen offset:0 sc1\n"
"s_add_u32 s93, s91, 0\n"
"buffer_load_dwordx4 a[196:199], v249, s[72:75], s93 offen offset:0 sc1\n"
"s_add_u32 s92, s90, 0\n"
"buffer_load_dwordx2 a[200:201], v253, s[72:75], s92 offen offset:1024 sc1\n"
"buffer_load_dwordx2 a[202:203], v253, s[72:75], s92 offen offset:1536 sc1\n"
"s_add_u32 s93, s91, 0\n"
"buffer_load_dwordx2 a[204:205], v253, s[72:75], s93 offen offset:1024 sc1\n"
"buffer_load_dwordx2 a[206:207], v253, s[72:75], s93 offen offset:1536 sc1\n"
"s_add_u32 s92, s90, 8192\n"
"buffer_load_dwordx4 a[160:163], v249, s[72:75], s92 offen offset:0 sc1\n"
"s_add_u32 s93, s91, 8192\n"
"buffer_load_dwordx4 a[164:167], v249, s[72:75], s93 offen offset:0 sc1\n"
"s_add_u32 s92, s90, 8192\n"
"buffer_load_dwordx4 a[168:171], v249, s[72:75], s92 offen offset:1024 sc1\n"
"s_add_u32 s93, s91, 8192\n"
"buffer_load_dwordx4 a[172:175], v249, s[72:75], s93 offen offset:1024 sc1\n"
"s_add_u32 s92, s90, 8192\n"
"buffer_load_dwordx2 a[176:177], v253, s[72:75], s92 offen offset:2048 sc1\n"
"buffer_load_dwordx2 a[178:179], v253, s[72:75], s92 offen offset:2560 sc1\n"
"s_add_u32 s93, s91, 8192\n"
"buffer_load_dwordx2 a[180:181], v253, s[72:75], s93 offen offset:2048 sc1\n"
"buffer_load_dwordx2 a[182:183], v253, s[72:75], s93 offen offset:2560 sc1\n"
"s_add_u32 s92, s90, 8192\n"
"buffer_load_dwordx4 a[184:187], v249, s[72:75], s92 offen offset:3072 sc1\n"
"s_add_u32 s93, s91, 8192\n"
"buffer_load_dwordx4 a[188:191], v249, s[72:75], s93 offen offset:3072 sc1\n"
"s_add_u32 s87, s83, 20480\n"
"buffer_store_dwordx4 v[80:83], v249, s[72:75], s87 offen offset:0 sc1\n"
"s_add_u32 s87, s83, 20480\n"
"buffer_store_dwordx4 v[84:87], v249, s[72:75], s87 offen offset:1024 sc1\n"
"s_add_u32 s87, s83, 20480\n"
"buffer_store_dwordx4 v[88:91], v249, s[72:75], s87 offen offset:2048 sc1\n"
"s_add_u32 s87, s83, 20480\n"
"buffer_store_dwordx4 v[92:95], v249, s[72:75], s87 offen offset:3072 sc1\n"
"v_accvgpr_read_b32 v176, a0\n"
"v_accvgpr_read_b32 v177, a1\n"
"v_accvgpr_read_b32 v178, a2\n"
"v_accvgpr_read_b32 v179, a3\n"
"v_accvgpr_read_b32 v180, a16\n"
"v_accvgpr_read_b32 v181, a17\n"
"v_accvgpr_read_b32 v182, a18\n"
"v_accvgpr_read_b32 v183, a19\n"
"v_pk_mul_f32 v[176:177], v[176:177], v[160:161] op_sel_hi:[1,0]\n"
"v_pk_mul_f32 v[178:179], v[178:179], v[160:161] op_sel_hi:[1,0]\n"
"v_pk_mul_f32 v[180:181], v[180:181], v[160:161] op_sel_hi:[1,0]\n"
"v_pk_mul_f32 v[182:183], v[182:183], v[160:161] op_sel_hi:[1,0]\n"
"v_cvt_pk_bf16_f32 v0, v176, v177\n"
"v_cvt_pk_bf16_f32 v1, v178, v179\n"
"v_cvt_pk_bf16_f32 v2, v180, v181\n"
"v_cvt_pk_bf16_f32 v3, v182, v183\n"
"s_add_u32 s87, s83, 0\n"
"buffer_store_dwordx4 v[0:3], v249, s[72:75], s87 offen offset:0 sc1\n"
"v_accvgpr_read_b32 v184, a4\n"
"v_accvgpr_read_b32 v185, a5\n"
"v_accvgpr_read_b32 v186, a6\n"
"v_accvgpr_read_b32 v187, a7\n"
"v_accvgpr_read_b32 v188, a20\n"
"v_accvgpr_read_b32 v189, a21\n"
"v_accvgpr_read_b32 v190, a22\n"
"v_accvgpr_read_b32 v191, a23\n"
"v_pk_mul_f32 v[184:185], v[184:185], v[162:163] op_sel_hi:[1,0]\n"
"v_pk_mul_f32 v[186:187], v[186:187], v[162:163] op_sel_hi:[1,0]\n"
"v_pk_mul_f32 v[188:189], v[188:189], v[162:163] op_sel_hi:[1,0]\n"
"v_pk_mul_f32 v[190:191], v[190:191], v[162:163] op_sel_hi:[1,0]\n"
"v_cvt_pk_bf16_f32 v4, v184, v185\n"
"v_cvt_pk_bf16_f32 v5, v186, v187\n"
"v_cvt_pk_bf16_f32 v6, v188, v189\n"
"v_cvt_pk_bf16_f32 v7, v190, v191\n"
"s_add_u32 s87, s83, 0\n"
"buffer_store_dwordx4 v[4:7], v249, s[72:75], s87 offen offset:1024 sc1\n"
"v_accvgpr_read_b32 v192, a8\n"
"v_accvgpr_read_b32 v193, a9\n"
"v_accvgpr_read_b32 v194, a10\n"
"v_accvgpr_read_b32 v195, a11\n"
"v_accvgpr_read_b32 v196, a24\n"
"v_accvgpr_read_b32 v197, a25\n"
"v_accvgpr_read_b32 v198, a26\n"
"v_accvgpr_read_b32 v199, a27\n"
"v_pk_mul_f32 v[192:193], v[192:193], v[164:165] op_sel_hi:[1,0]\n"
"v_pk_mul_f32 v[194:195], v[194:195], v[164:165] op_sel_hi:[1,0]\n"
"v_pk_mul_f32 v[196:197], v[196:197], v[164:165] op_sel_hi:[1,0]\n"
"v_pk_mul_f32 v[198:199], v[198:199], v[164:165] op_sel_hi:[1,0]\n"
"v_cvt_pk_bf16_f32 v8, v192, v193\n"
"v_cvt_pk_bf16_f32 v9, v194, v195\n"
"v_cvt_pk_bf16_f32 v10, v196, v197\n"
"v_cvt_pk_bf16_f32 v11, v198, v199\n"
"s_add_u32 s87, s83, 0\n"
"buffer_store_dwordx4 v[8:11], v249, s[72:75], s87 offen offset:2048 sc1\n"
"v_accvgpr_read_b32 v200, a12\n"
"v_accvgpr_read_b32 v201, a13\n"
"v_accvgpr_read_b32 v202, a14\n"
"v_accvgpr_read_b32 v203, a15\n"
"v_accvgpr_read_b32 v204, a28\n"
"v_accvgpr_read_b32 v205, a29\n"
"v_accvgpr_read_b32 v206, a30\n"
"v_accvgpr_read_b32 v207, a31\n"
"v_pk_mul_f32 v[200:201], v[200:201], v[166:167] op_sel_hi:[1,0]\n"
"v_pk_mul_f32 v[202:203], v[202:203], v[166:167] op_sel_hi:[1,0]\n"
"v_pk_mul_f32 v[204:205], v[204:205], v[166:167] op_sel_hi:[1,0]\n"
"v_pk_mul_f32 v[206:207], v[206:207], v[166:167] op_sel_hi:[1,0]\n"
"v_cvt_pk_bf16_f32 v12, v200, v201\n"
"v_cvt_pk_bf16_f32 v13, v202, v203\n"
"v_cvt_pk_bf16_f32 v14, v204, v205\n"
"v_cvt_pk_bf16_f32 v15, v206, v207\n"
"s_add_u32 s87, s83, 0\n"
"buffer_store_dwordx4 v[12:15], v249, s[72:75], s87 offen offset:3072 sc1\n"
"v_accvgpr_read_b32 v176, a32\n"
"v_accvgpr_read_b32 v177, a33\n"
"v_accvgpr_read_b32 v178, a34\n"
"v_accvgpr_read_b32 v179, a35\n"
"v_accvgpr_read_b32 v180, a48\n"
"v_accvgpr_read_b32 v181, a49\n"
"v_accvgpr_read_b32 v182, a50\n"
"v_accvgpr_read_b32 v183, a51\n"
"v_pk_mul_f32 v[176:177], v[176:177], v[160:161] op_sel_hi:[1,0]\n"
"v_pk_mul_f32 v[178:179], v[178:179], v[160:161] op_sel_hi:[1,0]\n"
"v_pk_mul_f32 v[180:181], v[180:181], v[160:161] op_sel_hi:[1,0]\n"
"v_pk_mul_f32 v[182:183], v[182:183], v[160:161] op_sel_hi:[1,0]\n"
"v_cvt_pk_bf16_f32 v16, v176, v177\n"
"v_cvt_pk_bf16_f32 v17, v178, v179\n"
"v_cvt_pk_bf16_f32 v18, v180, v181\n"
"v_cvt_pk_bf16_f32 v19, v182, v183\n"
"s_add_u32 s87, s83, 4096\n"
"buffer_store_dwordx4 v[16:19], v249, s[72:75], s87 offen offset:0 sc1\n"
"v_accvgpr_read_b32 v184, a36\n"
"v_accvgpr_read_b32 v185, a37\n"
"v_accvgpr_read_b32 v186, a38\n"
"v_accvgpr_read_b32 v187, a39\n"
"v_accvgpr_read_b32 v188, a52\n"
"v_accvgpr_read_b32 v189, a53\n"
"v_accvgpr_read_b32 v190, a54\n"
"v_accvgpr_read_b32 v191, a55\n"
"v_pk_mul_f32 v[184:185], v[184:185], v[162:163] op_sel_hi:[1,0]\n"
"v_pk_mul_f32 v[186:187], v[186:187], v[162:163] op_sel_hi:[1,0]\n"
"v_pk_mul_f32 v[188:189], v[188:189], v[162:163] op_sel_hi:[1,0]\n"
"v_pk_mul_f32 v[190:191], v[190:191], v[162:163] op_sel_hi:[1,0]\n"
"v_cvt_pk_bf16_f32 v20, v184, v185\n"
"v_cvt_pk_bf16_f32 v21, v186, v187\n"
"v_cvt_pk_bf16_f32 v22, v188, v189\n"
"v_cvt_pk_bf16_f32 v23, v190, v191\n"
"s_add_u32 s87, s83, 4096\n"
"buffer_store_dwordx4 v[20:23], v249, s[72:75], s87 offen offset:1024 sc1\n"
"v_accvgpr_read_b32 v192, a40\n"
"v_accvgpr_read_b32 v193, a41\n"
"v_accvgpr_read_b32 v194, a42\n"
"v_accvgpr_read_b32 v195, a43\n"
"v_accvgpr_read_b32 v196, a56\n"
"v_accvgpr_read_b32 v197, a57\n"
"v_accvgpr_read_b32 v198, a58\n"
"v_accvgpr_read_b32 v199, a59\n"
"v_pk_mul_f32 v[192:193], v[192:193], v[164:165] op_sel_hi:[1,0]\n"
"v_pk_mul_f32 v[194:195], v[194:195], v[164:165] op_sel_hi:[1,0]\n"
"v_pk_mul_f32 v[196:197], v[196:197], v[164:165] op_sel_hi:[1,0]\n"
"v_pk_mul_f32 v[198:199], v[198:199], v[164:165] op_sel_hi:[1,0]\n"
"v_cvt_pk_bf16_f32 v24, v192, v193\n"
"v_cvt_pk_bf16_f32 v25, v194, v195\n"
"v_cvt_pk_bf16_f32 v26, v196, v197\n"
"v_cvt_pk_bf16_f32 v27, v198, v199\n"
"v_accvgpr_read_b32 v200, a44\n"
"v_accvgpr_read_b32 v201, a45\n"
"v_accvgpr_read_b32 v202, a46\n"
"v_accvgpr_read_b32 v203, a47\n"
"v_accvgpr_read_b32 v204, a60\n"
"v_accvgpr_read_b32 v205, a61\n"
"v_accvgpr_read_b32 v206, a62\n"
"v_accvgpr_read_b32 v207, a63\n"
"v_pk_mul_f32 v[200:201], v[200:201], v[166:167] op_sel_hi:[1,0]\n"
"v_pk_mul_f32 v[202:203], v[202:203], v[166:167] op_sel_hi:[1,0]\n"
"v_pk_mul_f32 v[204:205], v[204:205], v[166:167] op_sel_hi:[1,0]\n"
"v_pk_mul_f32 v[206:207], v[206:207], v[166:167] op_sel_hi:[1,0]\n"
"v_cvt_pk_bf16_f32 v28, v200, v201\n"
"v_cvt_pk_bf16_f32 v29, v202, v203\n"
"v_cvt_pk_bf16_f32 v30, v204, v205\n"
"v_cvt_pk_bf16_f32 v31, v206, v207\n"
"v_accvgpr_read_b32 v176, a64\n"
"v_accvgpr_read_b32 v177, a65\n"
"v_accvgpr_read_b32 v178, a66\n"
"v_accvgpr_read_b32 v179, a67\n"
"v_accvgpr_read_b32 v180, a80\n"
"v_accvgpr_read_b32 v181, a81\n"
"v_accvgpr_read_b32 v182, a82\n"
"v_accvgpr_read_b32 v183, a83\n"
"v_pk_mul_f32 v[176:177], v[176:177], v[160:161] op_sel_hi:[1,0]\n"
"v_pk_mul_f32 v[178:179], v[178:179], v[160:161] op_sel_hi:[1,0]\n"
"v_pk_mul_f32 v[180:181], v[180:181], v[160:161] op_sel_hi:[1,0]\n"
"v_pk_mul_f32 v[182:183], v[182:183], v[160:161] op_sel_hi:[1,0]\n"
"v_cvt_pk_bf16_f32 v32, v176, v177\n"
"v_cvt_pk_bf16_f32 v33, v178, v179\n"
"v_cvt_pk_bf16_f32 v34, v180, v181\n"
"v_cvt_pk_bf16_f32 v35, v182, v183\n"
"v_accvgpr_read_b32 v184, a68\n"
"v_accvgpr_read_b32 v185, a69\n"
"v_accvgpr_read_b32 v186, a70\n"
"v_accvgpr_read_b32 v187, a71\n"
"v_accvgpr_read_b32 v188, a84\n"
"v_accvgpr_read_b32 v189, a85\n"
"v_accvgpr_read_b32 v190, a86\n"
"v_accvgpr_read_b32 v191, a87\n"
"v_pk_mul_f32 v[184:185], v[184:185], v[162:163] op_sel_hi:[1,0]\n"
"v_pk_mul_f32 v[186:187], v[186:187], v[162:163] op_sel_hi:[1,0]\n"
"v_pk_mul_f32 v[188:189], v[188:189], v[162:163] op_sel_hi:[1,0]\n"
"v_pk_mul_f32 v[190:191], v[190:191], v[162:163] op_sel_hi:[1,0]\n"
"v_cvt_pk_bf16_f32 v36, v184, v185\n"
"v_cvt_pk_bf16_f32 v37, v186, v187\n"
"v_cvt_pk_bf16_f32 v38, v188, v189\n"
"v_cvt_pk_bf16_f32 v39, v190, v191\n"
"v_accvgpr_read_b32 v192, a72\n"
"v_accvgpr_read_b32 v193, a73\n"
"v_accvgpr_read_b32 v194, a74\n"
"v_accvgpr_read_b32 v195, a75\n"
"v_accvgpr_read_b32 v196, a88\n"
"v_accvgpr_read_b32 v197, a89\n"
"v_accvgpr_read_b32 v198, a90\n"
"v_accvgpr_read_b32 v199, a91\n"
"v_pk_mul_f32 v[192:193], v[192:193], v[164:165] op_sel_hi:[1,0]\n"
"v_pk_mul_f32 v[194:195], v[194:195], v[164:165] op_sel_hi:[1,0]\n"
"v_pk_mul_f32 v[196:197], v[196:197], v[164:165] op_sel_hi:[1,0]\n"
"v_pk_mul_f32 v[198:199], v[198:199], v[164:165] op_sel_hi:[1,0]\n"
"v_cvt_pk_bf16_f32 v40, v192, v193\n"
"v_cvt_pk_bf16_f32 v41, v194, v195\n"
"v_cvt_pk_bf16_f32 v42, v196, v197\n"
"v_cvt_pk_bf16_f32 v43, v198, v199\n"
"v_accvgpr_read_b32 v200, a76\n"
"v_accvgpr_read_b32 v201, a77\n"
"v_accvgpr_read_b32 v202, a78\n"
"v_accvgpr_read_b32 v203, a79\n"
"v_accvgpr_read_b32 v204, a92\n"
"v_accvgpr_read_b32 v205, a93\n"
"v_accvgpr_read_b32 v206, a94\n"
"v_accvgpr_read_b32 v207, a95\n"
"v_pk_mul_f32 v[200:201], v[200:201], v[166:167] op_sel_hi:[1,0]\n"
"v_pk_mul_f32 v[202:203], v[202:203], v[166:167] op_sel_hi:[1,0]\n"
"v_pk_mul_f32 v[204:205], v[204:205], v[166:167] op_sel_hi:[1,0]\n"
"v_pk_mul_f32 v[206:207], v[206:207], v[166:167] op_sel_hi:[1,0]\n"
"v_cvt_pk_bf16_f32 v44, v200, v201\n"
"v_cvt_pk_bf16_f32 v45, v202, v203\n"
"v_cvt_pk_bf16_f32 v46, v204, v205\n"
"v_cvt_pk_bf16_f32 v47, v206, v207\n"
"v_accvgpr_read_b32 v176, a96\n"
"v_accvgpr_read_b32 v177, a97\n"
"v_accvgpr_read_b32 v178, a98\n"
"v_accvgpr_read_b32 v179, a99\n"
"v_accvgpr_read_b32 v180, a112\n"
"v_accvgpr_read_b32 v181, a113\n"
"v_accvgpr_read_b32 v182, a114\n"
"v_accvgpr_read_b32 v183, a115\n"
"v_pk_mul_f32 v[176:177], v[176:177], v[160:161] op_sel_hi:[1,0]\n"
"v_pk_mul_f32 v[178:179], v[178:179], v[160:161] op_sel_hi:[1,0]\n"
"v_pk_mul_f32 v[180:181], v[180:181], v[160:161] op_sel_hi:[1,0]\n"
"v_pk_mul_f32 v[182:183], v[182:183], v[160:161] op_sel_hi:[1,0]\n"
"v_cvt_pk_bf16_f32 v48, v176, v177\n"
"v_cvt_pk_bf16_f32 v49, v178, v179\n"
"v_cvt_pk_bf16_f32 v50, v180, v181\n"
"v_cvt_pk_bf16_f32 v51, v182, v183\n"
"v_accvgpr_read_b32 v184, a100\n"
"v_accvgpr_read_b32 v185, a101\n"
"v_accvgpr_read_b32 v186, a102\n"
"v_accvgpr_read_b32 v187, a103\n"
"v_accvgpr_read_b32 v188, a116\n"
"v_accvgpr_read_b32 v189, a117\n"
"v_accvgpr_read_b32 v190, a118\n"
"v_accvgpr_read_b32 v191, a119\n"
"v_pk_mul_f32 v[184:185], v[184:185], v[162:163] op_sel_hi:[1,0]\n"
"v_pk_mul_f32 v[186:187], v[186:187], v[162:163] op_sel_hi:[1,0]\n"
"v_pk_mul_f32 v[188:189], v[188:189], v[162:163] op_sel_hi:[1,0]\n"
"v_pk_mul_f32 v[190:191], v[190:191], v[162:163] op_sel_hi:[1,0]\n"
"v_cvt_pk_bf16_f32 v52, v184, v185\n"
"v_cvt_pk_bf16_f32 v53, v186, v187\n"
"v_cvt_pk_bf16_f32 v54, v188, v189\n"
"v_cvt_pk_bf16_f32 v55, v190, v191\n"
"v_accvgpr_read_b32 v192, a104\n"
"v_accvgpr_read_b32 v193, a105\n"
"v_accvgpr_read_b32 v194, a106\n"
"v_accvgpr_read_b32 v195, a107\n"
"v_accvgpr_read_b32 v196, a120\n"
"v_accvgpr_read_b32 v197, a121\n"
"v_accvgpr_read_b32 v198, a122\n"
"v_accvgpr_read_b32 v199, a123\n"
"v_pk_mul_f32 v[192:193], v[192:193], v[164:165] op_sel_hi:[1,0]\n"
"v_pk_mul_f32 v[194:195], v[194:195], v[164:165] op_sel_hi:[1,0]\n"
"v_pk_mul_f32 v[196:197], v[196:197], v[164:165] op_sel_hi:[1,0]\n"
"v_pk_mul_f32 v[198:199], v[198:199], v[164:165] op_sel_hi:[1,0]\n"
"v_cvt_pk_bf16_f32 v56, v192, v193\n"
"v_cvt_pk_bf16_f32 v57, v194, v195\n"
"v_cvt_pk_bf16_f32 v58, v196, v197\n"
"v_cvt_pk_bf16_f32 v59, v198, v199\n"
"v_accvgpr_read_b32 v200, a108\n"
"v_accvgpr_read_b32 v201, a109\n"
"v_accvgpr_read_b32 v202, a110\n"
"v_accvgpr_read_b32 v203, a111\n"
"v_accvgpr_read_b32 v204, a124\n"
"v_accvgpr_read_b32 v205, a125\n"
"v_accvgpr_read_b32 v206, a126\n"
"v_accvgpr_read_b32 v207, a127\n"
"v_pk_mul_f32 v[200:201], v[200:201], v[166:167] op_sel_hi:[1,0]\n"
"v_pk_mul_f32 v[202:203], v[202:203], v[166:167] op_sel_hi:[1,0]\n"
"v_pk_mul_f32 v[204:205], v[204:205], v[166:167] op_sel_hi:[1,0]\n"
"v_pk_mul_f32 v[206:207], v[206:207], v[166:167] op_sel_hi:[1,0]\n"
"v_cvt_pk_bf16_f32 v60, v200, v201\n"
"v_cvt_pk_bf16_f32 v61, v202, v203\n"
"v_cvt_pk_bf16_f32 v62, v204, v205\n"
"v_cvt_pk_bf16_f32 v63, v206, v207\n"
"v_accvgpr_read_b32 v176, a128\n"
"v_accvgpr_read_b32 v177, a129\n"
"v_accvgpr_read_b32 v178, a130\n"
"v_accvgpr_read_b32 v179, a131\n"
"v_accvgpr_read_b32 v180, a144\n"
"v_accvgpr_read_b32 v181, a145\n"
"v_accvgpr_read_b32 v182, a146\n"
"v_accvgpr_read_b32 v183, a147\n"
"v_pk_mul_f32 v[176:177], v[176:177], v[160:161] op_sel_hi:[1,0]\n"
"v_pk_mul_f32 v[178:179], v[178:179], v[160:161] op_sel_hi:[1,0]\n"
"v_pk_mul_f32 v[180:181], v[180:181], v[160:161] op_sel_hi:[1,0]\n"
"v_pk_mul_f32 v[182:183], v[182:183], v[160:161] op_sel_hi:[1,0]\n"
"v_cvt_pk_bf16_f32 v64, v176, v177\n"
"v_cvt_pk_bf16_f32 v65, v178, v179\n"
"v_cvt_pk_bf16_f32 v66, v180, v181\n"
"v_cvt_pk_bf16_f32 v67, v182, v183\n"
"v_accvgpr_read_b32 v184, a132\n"
"v_accvgpr_read_b32 v185, a133\n"
"v_accvgpr_read_b32 v186, a134\n"
"v_accvgpr_read_b32 v187, a135\n"
"v_accvgpr_read_b32 v188, a148\n"
"v_accvgpr_read_b32 v189, a149\n"
"v_accvgpr_read_b32 v190, a150\n"
"v_accvgpr_read_b32 v191, a151\n"
"v_pk_mul_f32 v[184:185], v[184:185], v[162:163] op_sel_hi:[1,0]\n"
"v_pk_mul_f32 v[186:187], v[186:187], v[162:163] op_sel_hi:[1,0]\n"
"v_pk_mul_f32 v[188:189], v[188:189], v[162:163] op_sel_hi:[1,0]\n"
"v_pk_mul_f32 v[190:191], v[190:191], v[162:163] op_sel_hi:[1,0]\n"
"v_cvt_pk_bf16_f32 v68, v184, v185\n"
"v_cvt_pk_bf16_f32 v69, v186, v187\n"
"v_cvt_pk_bf16_f32 v70, v188, v189\n"
"v_cvt_pk_bf16_f32 v71, v190, v191\n"
"v_accvgpr_read_b32 v192, a136\n"
"v_accvgpr_read_b32 v193, a137\n"
"v_accvgpr_read_b32 v194, a138\n"
"v_accvgpr_read_b32 v195, a139\n"
"v_accvgpr_read_b32 v196, a152\n"
"v_accvgpr_read_b32 v197, a153\n"
"v_accvgpr_read_b32 v198, a154\n"
"v_accvgpr_read_b32 v199, a155\n"
"v_pk_mul_f32 v[192:193], v[192:193], v[164:165] op_sel_hi:[1,0]\n"
"v_pk_mul_f32 v[194:195], v[194:195], v[164:165] op_sel_hi:[1,0]\n"
"v_pk_mul_f32 v[196:197], v[196:197], v[164:165] op_sel_hi:[1,0]\n"
"v_pk_mul_f32 v[198:199], v[198:199], v[164:165] op_sel_hi:[1,0]\n"
"v_cvt_pk_bf16_f32 v72, v192, v193\n"
"v_cvt_pk_bf16_f32 v73, v194, v195\n"
"v_cvt_pk_bf16_f32 v74, v196, v197\n"
"v_cvt_pk_bf16_f32 v75, v198, v199\n"
"v_accvgpr_read_b32 v200, a140\n"
"v_accvgpr_read_b32 v201, a141\n"
"v_accvgpr_read_b32 v202, a142\n"
"v_accvgpr_read_b32 v203, a143\n"
"v_accvgpr_read_b32 v204, a156\n"
"v_accvgpr_read_b32 v205, a157\n"
"v_accvgpr_read_b32 v206, a158\n"
"v_accvgpr_read_b32 v207, a159\n"
"v_pk_mul_f32 v[200:201], v[200:201], v[166:167] op_sel_hi:[1,0]\n"
"v_pk_mul_f32 v[202:203], v[202:203], v[166:167] op_sel_hi:[1,0]\n"
"v_pk_mul_f32 v[204:205], v[204:205], v[166:167] op_sel_hi:[1,0]\n"
"v_pk_mul_f32 v[206:207], v[206:207], v[166:167] op_sel_hi:[1,0]\n"
"v_cvt_pk_bf16_f32 v76, v200, v201\n"
"v_cvt_pk_bf16_f32 v77, v202, v203\n"
"v_cvt_pk_bf16_f32 v78, v204, v205\n"
"v_cvt_pk_bf16_f32 v79, v206, v207\n"
"s_lshl_b32 s84, s23, 7\n"
"s_lshl_b32 s26, s21, 2\n"
"s_add_u32 s84, s84, s26\n"
"s_add_u32 s84, s84, s22\n"
"s_mul_i32 s84, s84, 24576\n"
"s_add_u32 s84, s84, 8978432\n"
"v_subrev_u32 v240, 98304, v250\n"
"v_subrev_u32 v241, 98304, v251\n"
"s_lshl_b32 s76, s21, 2\n"
"s_add_u32 s76, s76, s24\n"
"s_lshl_b32 s76, s76, 3\n"
"s_and_b32 s26, s23, 1\n"
"s_lshl_b32 s26, s26, 2\n"
"s_add_u32 s26, s26, s22\n"
"s_add_u32 s76, s76, s26\n"
"s_lshl_b32 s76, s76, 2\n"
"s_lshr_b32 s26, s23, 1\n"
"s_add_u32 s76, s76, s26\n"
"s_mul_i32 s76, s76, 10240\n"
"s_add_u32 s76, s76, 55115776\n"
"s_lshl_b32 s77, s21, 2\n"
"s_add_u32 s77, s77, s24\n"
"s_lshl_b32 s77, s77, 3\n"
"s_lshl_b32 s26, s22, 1\n"
"s_add_u32 s77, s77, s26\n"
"s_lshl_b32 s77, s77, 3\n"
"s_add_u32 s77, s77, s23\n"
"s_mul_i32 s77, s77, 10240\n"
"s_add_u32 s77, s77, 34144256\n"
"s_barrier\n"
"ds_read_b64_tr_b16 v[176:177], v250 offset:0\n"
"ds_read_b64_tr_b16 v[178:179], v250 offset:5120\n"
"ds_read_b64_tr_b16 v[180:181], v250 offset:10240\n"
"ds_read_b64_tr_b16 v[182:183], v250 offset:15360\n"
"ds_read_b64_tr_b16 v[184:185], v251 offset:0\n"
"ds_read_b64_tr_b16 v[186:187], v251 offset:5120\n"
"ds_read_b64_tr_b16 v[188:189], v251 offset:10240\n"
"ds_read_b64_tr_b16 v[190:191], v251 offset:15360\n"
"ds_read_b64_tr_b16 v[192:193], v250 offset:64\n"
"ds_read_b64_tr_b16 v[194:195], v250 offset:5184\n"
"ds_read_b64_tr_b16 v[196:197], v250 offset:10304\n"
"ds_read_b64_tr_b16 v[198:199], v250 offset:15424\n"
"ds_read_b64_tr_b16 v[200:201], v251 offset:64\n"
"ds_read_b64_tr_b16 v[202:203], v251 offset:5184\n"
"ds_read_b64_tr_b16 v[204:205], v251 offset:10304\n"
"ds_read_b64_tr_b16 v[206:207], v251 offset:15424\n"
"s_waitcnt vmcnt(25) lgkmcnt(0)\n"
"v_mfma_f32_16x16x32_bf16 v[96:99], v[176:179], a[192:195], 0\n"
"s_add_u32 s87, s83, 4096\n"
"s_waitcnt vmcnt(22)\n"
"v_mfma_f32_16x16x32_bf16 v[96:99], v[180:183], a[200:203], v[96:99]\n"
"buffer_store_dwordx4 v[24:27], v249, s[72:75], s87 offen offset:2048 sc1\n"
"v_mfma_f32_16x16x32_bf16 v[100:103], v[176:179], a[196:199], 0\n"
"ds_read_b64_tr_b16 v[208:209], v250 offset:128\n"
"s_waitcnt vmcnt(21)\n"
"v_mfma_f32_16x16x32_bf16 v[100:103], v[180:183], a[204:207], v[100:103]\n"
"ds_read_b64_tr_b16 v[210:211], v250 offset:5248\n"
"v_mfma_f32_16x16x32_bf16 v[104:107], v[184:187], a[192:195], 0\n"
"ds_read_b64_tr_b16 v[212:213], v250 offset:10368\n"
"v_mfma_f32_16x16x32_bf16 v[104:107], v[188:191], a[200:203], v[104:107]\n"
"ds_read_b64_tr_b16 v[214:215], v250 offset:15488\n"
"v_mfma_f32_16x16x32_bf16 v[108:111], v[184:187], a[196:199], 0\n"
"ds_read_b64_tr_b16 v[216:217], v251 offset:128\n"
"v_mfma_f32_16x16x32_bf16 v[108:111], v[188:191], a[204:207], v[108:111]\n"
"ds_read_b64_tr_b16 v[218:219], v251 offset:5248\n"
"v_mfma_f32_16x16x32_bf16 v[112:115], v[192:195], a[192:195], 0\n"
"ds_read_b64_tr_b16 v[220:221], v251 offset:10368\n"
"v_mfma_f32_16x16x32_bf16 v[112:115], v[196:199], a[200:203], v[112:115]\n"
"ds_read_b64_tr_b16 v[222:223], v251 offset:15488\n"
"v_mfma_f32_16x16x32_bf16 v[116:119], v[192:195], a[196:199], 0\n"
"ds_read_b64_tr_b16 v[176:177], v250 offset:192\n"
"v_mfma_f32_16x16x32_bf16 v[116:119], v[196:199], a[204:207], v[116:119]\n"
"ds_read_b64_tr_b16 v[178:179], v250 offset:5312\n"
"v_mfma_f32_16x16x32_bf16 v[120:123], v[200:203], a[192:195], 0\n"
"ds_read_b64_tr_b16 v[180:181], v250 offset:10432\n"
"v_mfma_f32_16x16x32_bf16 v[120:123], v[204:207], a[200:203], v[120:123]\n"
"ds_read_b64_tr_b16 v[182:183], v250 offset:15552\n"
"v_mfma_f32_16x16x32_bf16 v[124:127], v[200:203], a[196:199], 0\n"
"ds_read_b64_tr_b16 v[184:185], v251 offset:192\n"
"v_mfma_f32_16x16x32_bf16 v[124:127], v[204:207], a[204:207], v[124:127]\n"
"ds_read_b64_tr_b16 v[186:187], v251 offset:5312\n"
"s_waitcnt lgkmcnt(5)\n"
"v_mfma_f32_16x16x32_bf16 v[128:131], v[208:211], a[192:195], 0\n"
"ds_read_b64_tr_b16 v[188:189], v251 offset:10432\n"
"s_add_u32 s87, s83, 4096\n"
"v_mfma_f32_16x16x32_bf16 v[128:131], v[212:215], a[200:203], v[128:131]\n"
"ds_read_b64_tr_b16 v[190:191], v251 offset:15552\n"
"buffer_store_dwordx4 v[28:31], v249, s[72:75], s87 offen offset:3072 sc1\n"
"v_mfma_f32_16x16x32_bf16 v[132:135], v[208:211], a[196:199], 0\n"
"ds_read_b64_tr_b16 v[192:193], v250 offset:256\n"
"v_mfma_f32_16x16x32_bf16 v[132:135], v[212:215], a[204:207], v[132:135]\n"
"ds_read_b64_tr_b16 v[194:195], v250 offset:5376\n"
"v_mfma_f32_16x16x32_bf16 v[136:139], v[216:219], a[192:195], 0\n"
"ds_read_b64_tr_b16 v[196:197], v250 offset:10496\n"
"v_mfma_f32_16x16x32_bf16 v[136:139], v[220:223], a[200:203], v[136:139]\n"
"ds_read_b64_tr_b16 v[198:199], v250 offset:15616\n"
"v_mfma_f32_16x16x32_bf16 v[140:143], v[216:219], a[196:199], 0\n"
"ds_read_b64_tr_b16 v[200:201], v251 offset:256\n"
"v_mfma_f32_16x16x32_bf16 v[140:143], v[220:223], a[204:207], v[140:143]\n"
"ds_read_b64_tr_b16 v[202:203], v251 offset:5376\n"
"s_barrier\n"
"s_waitcnt lgkmcnt(5)\n"
"v_mfma_f32_16x16x32_bf16 v[144:147], v[176:179], a[192:195], 0\n"
"ds_read_b64_tr_b16 v[204:205], v251 offset:10496\n"
"v_mfma_f32_16x16x32_bf16 v[144:147], v[180:183], a[200:203], v[144:147]\n"
"ds_read_b64_tr_b16 v[206:207], v251 offset:15616\n"
"v_mfma_f32_16x16x32_bf16 v[148:151], v[176:179], a[196:199], 0\n"
"ds_read_b64_tr_b16 v[208:209], v250 offset:20480\n"
"v_mfma_f32_16x16x32_bf16 v[148:151], v[180:183], a[204:207], v[148:151]\n"
"ds_read_b64_tr_b16 v[210:211], v250 offset:25600\n"
"v_mfma_f32_16x16x32_bf16 v[152:155], v[184:187], a[192:195], 0\n"
"ds_read_b64_tr_b16 v[212:213], v250 offset:30720\n"
"v_mfma_f32_16x16x32_bf16 v[152:155], v[188:191], a[200:203], v[152:155]\n"
"ds_read_b64_tr_b16 v[214:215], v250 offset:35840\n"
"v_mfma_f32_16x16x32_bf16 v[156:159], v[184:187], a[196:199], 0\n"
"ds_read_b64_tr_b16 v[216:217], v251 offset:20480\n"
"v_mfma_f32_16x16x32_bf16 v[156:159], v[188:191], a[204:207], v[156:159]\n"
"ds_read_b64_tr_b16 v[218:219], v251 offset:25600\n"
"s_waitcnt lgkmcnt(5)\n"
"v_mfma_f32_16x16x32_bf16 v[160:163], v[192:195], a[192:195], 0\n"
"ds_read_b64_tr_b16 v[220:221], v251 offset:30720\n"
"s_add_u32 s87, s83, 8192\n"
"v_mfma_f32_16x16x32_bf16 v[160:163], v[196:199], a[200:203], v[160:163]\n"
"ds_read_b64_tr_b16 v[222:223], v251 offset:35840\n"
"buffer_store_dwordx4 v[32:35], v249, s[72:75], s87 offen offset:0 sc1\n"
"v_mfma_f32_16x16x32_bf16 v[164:167], v[192:195], a[196:199], 0\n"
"ds_read_b64_tr_b16 v[176:177], v250 offset:20544\n"
"v_mfma_f32_16x16x32_bf16 v[164:167], v[196:199], a[204:207], v[164:167]\n"
"ds_read_b64_tr_b16 v[178:179], v250 offset:25664\n"
"v_mfma_f32_16x16x32_bf16 v[168:171], v[200:203], a[192:195], 0\n"
"ds_read_b64_tr_b16 v[180:181], v250 offset:30784\n"
"v_mfma_f32_16x16x32_bf16 v[168:171], v[204:207], a[200:203], v[168:171]\n"
"ds_read_b64_tr_b16 v[182:183], v250 offset:35904\n"
"v_mfma_f32_16x16x32_bf16 v[172:175], v[200:203], a[196:199], 0\n"
"ds_read_b64_tr_b16 v[184:185], v251 offset:20544\n"
"v_mfma_f32_16x16x32_bf16 v[172:175], v[204:207], a[204:207], v[172:175]\n"
"ds_read_b64_tr_b16 v[186:187], v251 offset:25664\n"
"s_waitcnt lgkmcnt(5)\n"
"v_mfma_f32_16x16x32_bf16 v[96:99], v[208:211], a[208:211], v[96:99]\n"
"ds_read_b64_tr_b16 v[188:189], v251 offset:30784\n"
"v_mfma_f32_16x16x32_bf16 v[96:99], v[212:215], a[216:219], v[96:99]\n"
"ds_read_b64_tr_b16 v[190:191], v251 offset:35904\n"
"v_mfma_f32_16x16x32_bf16 v[100:103], v[208:211], a[212:215], v[100:103]\n"
"ds_read_b64_tr_b16 v[192:193], v250 offset:20608\n"
"v_mfma_f32_16x16x32_bf16 v[100:103], v[212:215], a[220:223], v[100:103]\n"
"ds_read_b64_tr_b16 v[194:195], v250 offset:25728\n"
"v_mfma_f32_16x16x32_bf16 v[104:107], v[216:219], a[208:211], v[104:107]\n"
"ds_read_b64_tr_b16 v[196:197], v250 offset:30848\n"
"v_mfma_f32_16x16x32_bf16 v[104:107], v[220:223], a[216:219], v[104:107]\n"
"ds_read_b64_tr_b16 v[198:199], v250 offset:35968\n"
"v_mfma_f32_16x16x32_bf16 v[108:111], v[216:219], a[212:215], v[108:111]\n"
"ds_read_b64_tr_b16 v[200:201], v251 offset:20608\n"
"v_mfma_f32_16x16x32_bf16 v[108:111], v[220:223], a[220:223], v[108:111]\n"
"ds_read_b64_tr_b16 v[202:203], v251 offset:25728\n"
"s_waitcnt lgkmcnt(5)\n"
"v_mfma_f32_16x16x32_bf16 v[112:115], v[176:179], a[208:211], v[112:115]\n"
"ds_read_b64_tr_b16 v[204:205], v251 offset:30848\n"
"v_mfma_f32_16x16x32_bf16 v[112:115], v[180:183], a[216:219], v[112:115]\n"
"ds_read_b64_tr_b16 v[206:207], v251 offset:35968\n"
"v_mfma_f32_16x16x32_bf16 v[116:119], v[176:179], a[212:215], v[116:119]\n"
"ds_read_b64_tr_b16 v[208:209], v250 offset:20672\n"
"s_add_u32 s87, s83, 8192\n"
"v_mfma_f32_16x16x32_bf16 v[116:119], v[180:183], a[220:223], v[116:119]\n"
"ds_read_b64_tr_b16 v[210:211], v250 offset:25792\n"
"buffer_store_dwordx4 v[36:39], v249, s[72:75], s87 offen offset:1024 sc1\n"
"v_mfma_f32_16x16x32_bf16 v[120:123], v[184:187], a[208:211], v[120:123]\n"
"ds_read_b64_tr_b16 v[212:213], v250 offset:30912\n"
"v_mfma_f32_16x16x32_bf16 v[120:123], v[188:191], a[216:219], v[120:123]\n"
"ds_read_b64_tr_b16 v[214:215], v250 offset:36032\n"
"v_mfma_f32_16x16x32_bf16 v[124:127], v[184:187], a[212:215], v[124:127]\n"
"ds_read_b64_tr_b16 v[216:217], v251 offset:20672\n"
"v_mfma_f32_16x16x32_bf16 v[124:127], v[188:191], a[220:223], v[124:127]\n"
"ds_read_b64_tr_b16 v[218:219], v251 offset:25792\n"
"s_waitcnt lgkmcnt(5)\n"
"v_mfma_f32_16x16x32_bf16 v[128:131], v[192:195], a[208:211], v[128:131]\n"
"ds_read_b64_tr_b16 v[220:221], v251 offset:30912\n"
"v_mfma_f32_16x16x32_bf16 v[128:131], v[196:199], a[216:219], v[128:131]\n"
"ds_read_b64_tr_b16 v[222:223], v251 offset:36032\n"
"v_mfma_f32_16x16x32_bf16 v[132:135], v[192:195], a[212:215], v[132:135]\n"
"ds_read_b64_tr_b16 v[176:177], v250 offset:20736\n"
"v_mfma_f32_16x16x32_bf16 v[132:135], v[196:199], a[220:223], v[132:135]\n"
"ds_read_b64_tr_b16 v[178:179], v250 offset:25856\n"
"v_mfma_f32_16x16x32_bf16 v[136:139], v[200:203], a[208:211], v[136:139]\n"
"ds_read_b64_tr_b16 v[180:181], v250 offset:30976\n"
"v_mfma_f32_16x16x32_bf16 v[136:139], v[204:207], a[216:219], v[136:139]\n"
"ds_read_b64_tr_b16 v[182:183], v250 offset:36096\n"
"v_mfma_f32_16x16x32_bf16 v[140:143], v[200:203], a[212:215], v[140:143]\n"
"ds_read_b64_tr_b16 v[184:185], v251 offset:20736\n"
"v_mfma_f32_16x16x32_bf16 v[140:143], v[204:207], a[220:223], v[140:143]\n"
"ds_read_b64_tr_b16 v[186:187], v251 offset:25856\n"
"s_barrier\n"
"s_waitcnt lgkmcnt(5)\n"
"v_mfma_f32_16x16x32_bf16 v[144:147], v[208:211], a[208:211], v[144:147]\n"
"ds_read_b64_tr_b16 v[188:189], v251 offset:30976\n"
"s_mov_b32 s80, s24\n"
"s_mul_i32 s79, s80, 245760\n"
"s_add_u32 s79, s79, s94\n"
"s_add_u32 m0, s86, 98304\n"
"v_mfma_f32_16x16x32_bf16 v[144:147], v[212:215], a[216:219], v[144:147]\n"
"ds_read_b64_tr_b16 v[190:191], v251 offset:36096\n"
"buffer_load_dwordx4 v244, s[64:67], s79 offen offset:0 lds\n"
"v_mfma_f32_16x16x32_bf16 v[148:151], v[208:211], a[212:215], v[148:151]\n"
"ds_read_b64_tr_b16 v[192:193], v250 offset:40960\n"
"v_mfma_f32_16x16x32_bf16 v[148:151], v[212:215], a[220:223], v[148:151]\n"
"ds_read_b64_tr_b16 v[194:195], v250 offset:46080\n"
"v_mfma_f32_16x16x32_bf16 v[152:155], v[216:219], a[208:211], v[152:155]\n"
"ds_read_b64_tr_b16 v[196:197], v250 offset:51200\n"
"v_mfma_f32_16x16x32_bf16 v[152:155], v[220:223], a[216:219], v[152:155]\n"
"ds_read_b64_tr_b16 v[198:199], v250 offset:56320\n"
"buffer_load_dwordx4 v245, s[64:67], s79 offen offset:1024 lds\n"
"v_mfma_f32_16x16x32_bf16 v[156:159], v[216:219], a[212:215], v[156:159]\n"
"ds_read_b64_tr_b16 v[200:201], v251 offset:40960\n"
"v_mfma_f32_16x16x32_bf16 v[156:159], v[220:223], a[220:223], v[156:159]\n"
"ds_read_b64_tr_b16 v[202:203], v251 offset:46080\n"
"s_waitcnt lgkmcnt(5)\n"
"v_mfma_f32_16x16x32_bf16 v[160:163], v[176:179], a[208:211], v[160:163]\n"
"ds_read_b64_tr_b16 v[204:205], v251 offset:51200\n"
"v_mfma_f32_16x16x32_bf16 v[160:163], v[180:183], a[216:219], v[160:163]\n"
"ds_read_b64_tr_b16 v[206:207], v251 offset:56320\n"
"buffer_load_dwordx4 v246, s[64:67], s79 offen offset:2048 lds\n"
"v_mfma_f32_16x16x32_bf16 v[164:167], v[176:179], a[212:215], v[164:167]\n"
"ds_read_b64_tr_b16 v[208:209], v250 offset:41024\n"
"v_mfma_f32_16x16x32_bf16 v[164:167], v[180:183], a[220:223], v[164:167]\n"
"ds_read_b64_tr_b16 v[210:211], v250 offset:46144\n"
"v_mfma_f32_16x16x32_bf16 v[168:171], v[184:187], a[208:211], v[168:171]\n"
"ds_read_b64_tr_b16 v[212:213], v250 offset:51264\n"
"v_mfma_f32_16x16x32_bf16 v[168:171], v[188:191], a[216:219], v[168:171]\n"
"ds_read_b64_tr_b16 v[214:215], v250 offset:56384\n"
"buffer_load_dwordx4 v247, s[64:67], s79 offen offset:3072 lds\n"
"v_mfma_f32_16x16x32_bf16 v[172:175], v[184:187], a[212:215], v[172:175]\n"
"ds_read_b64_tr_b16 v[216:217], v251 offset:41024\n"
"v_mfma_f32_16x16x32_bf16 v[172:175], v[188:191], a[220:223], v[172:175]\n"
"ds_read_b64_tr_b16 v[218:219], v251 offset:46144\n"
"s_waitcnt lgkmcnt(5)\n"
"v_mfma_f32_16x16x32_bf16 v[96:99], v[192:195], a[224:227], v[96:99]\n"
"ds_read_b64_tr_b16 v[220:221], v251 offset:51264\n"
"s_add_u32 m0, s86, 102400\n"
"v_mfma_f32_16x16x32_bf16 v[96:99], v[196:199], a[232:235], v[96:99]\n"
"ds_read_b64_tr_b16 v[222:223], v251 offset:56384\n"
"buffer_load_dwordx4 v248, s[64:67], s79 offen lds\n"
"s_add_u32 s79, s79, 81920\n"
"v_mfma_f32_16x16x32_bf16 v[100:103], v[192:195], a[228:231], v[100:103]\n"
"ds_read_b64_tr_b16 v[176:177], v250 offset:41088\n"
"v_mfma_f32_16x16x32_bf16 v[100:103], v[196:199], a[236:239], v[100:103]\n"
"ds_read_b64_tr_b16 v[178:179], v250 offset:46208\n"
"v_mfma_f32_16x16x32_bf16 v[104:107], v[200:203], a[224:227], v[104:107]\n"
"ds_read_b64_tr_b16 v[180:181], v250 offset:51328\n"
"s_add_u32 s87, s83, 8192\n"
"v_mfma_f32_16x16x32_bf16 v[104:107], v[204:207], a[232:235], v[104:107]\n"
"ds_read_b64_tr_b16 v[182:183], v250 offset:56448\n"
"buffer_store_dwordx4 v[40:43], v249, s[72:75], s87 offen offset:2048 sc1\n"
"v_mfma_f32_16x16x32_bf16 v[108:111], v[200:203], a[228:231], v[108:111]\n"
"ds_read_b64_tr_b16 v[184:185], v251 offset:41088\n"
"v_mfma_f32_16x16x32_bf16 v[108:111], v[204:207], a[236:239], v[108:111]\n"
"ds_read_b64_tr_b16 v[186:187], v251 offset:46208\n"
"s_waitcnt lgkmcnt(5)\n"
"v_mfma_f32_16x16x32_bf16 v[112:115], v[208:211], a[224:227], v[112:115]\n"
"ds_read_b64_tr_b16 v[188:189], v251 offset:51328\n"
"s_add_u32 s87, s83, 8192\n"
"v_mfma_f32_16x16x32_bf16 v[112:115], v[212:215], a[232:235], v[112:115]\n"
"ds_read_b64_tr_b16 v[190:191], v251 offset:56448\n"
"buffer_store_dwordx4 v[44:47], v249, s[72:75], s87 offen offset:3072 sc1\n"
"v_mfma_f32_16x16x32_bf16 v[116:119], v[208:211], a[228:231], v[116:119]\n"
"ds_read_b64_tr_b16 v[192:193], v250 offset:41152\n"
"v_mfma_f32_16x16x32_bf16 v[116:119], v[212:215], a[236:239], v[116:119]\n"
"ds_read_b64_tr_b16 v[194:195], v250 offset:46272\n"
"v_mfma_f32_16x16x32_bf16 v[120:123], v[216:219], a[224:227], v[120:123]\n"
"ds_read_b64_tr_b16 v[196:197], v250 offset:51392\n"
"v_mfma_f32_16x16x32_bf16 v[120:123], v[220:223], a[232:235], v[120:123]\n"
"ds_read_b64_tr_b16 v[198:199], v250 offset:56512\n"
"v_mfma_f32_16x16x32_bf16 v[124:127], v[216:219], a[228:231], v[124:127]\n"
"ds_read_b64_tr_b16 v[200:201], v251 offset:41152\n"
"v_mfma_f32_16x16x32_bf16 v[124:127], v[220:223], a[236:239], v[124:127]\n"
"ds_read_b64_tr_b16 v[202:203], v251 offset:46272\n"
"s_waitcnt lgkmcnt(5)\n"
"v_mfma_f32_16x16x32_bf16 v[128:131], v[176:179], a[224:227], v[128:131]\n"
"ds_read_b64_tr_b16 v[204:205], v251 offset:51392\n"
"v_mfma_f32_16x16x32_bf16 v[128:131], v[180:183], a[232:235], v[128:131]\n"
"ds_read_b64_tr_b16 v[206:207], v251 offset:56512\n"
"v_mfma_f32_16x16x32_bf16 v[132:135], v[176:179], a[228:231], v[132:135]\n"
"ds_read_b64_tr_b16 v[208:209], v250 offset:41216\n"
"s_add_u32 s87, s83, 12288\n"
"v_mfma_f32_16x16x32_bf16 v[132:135], v[180:183], a[236:239], v[132:135]\n"
"ds_read_b64_tr_b16 v[210:211], v250 offset:46336\n"
"buffer_store_dwordx4 v[48:51], v249, s[72:75], s87 offen offset:0 sc1\n"
"v_mfma_f32_16x16x32_bf16 v[136:139], v[184:187], a[224:227], v[136:139]\n"
"ds_read_b64_tr_b16 v[212:213], v250 offset:51456\n"
"v_mfma_f32_16x16x32_bf16 v[136:139], v[188:191], a[232:235], v[136:139]\n"
"ds_read_b64_tr_b16 v[214:215], v250 offset:56576\n"
"v_mfma_f32_16x16x32_bf16 v[140:143], v[184:187], a[228:231], v[140:143]\n"
"ds_read_b64_tr_b16 v[216:217], v251 offset:41216\n"
"v_mfma_f32_16x16x32_bf16 v[140:143], v[188:191], a[236:239], v[140:143]\n"
"ds_read_b64_tr_b16 v[218:219], v251 offset:46336\n"
"s_barrier\n"
"s_waitcnt lgkmcnt(5)\n"
"v_mfma_f32_16x16x32_bf16 v[144:147], v[192:195], a[224:227], v[144:147]\n"
"ds_read_b64_tr_b16 v[220:221], v251 offset:51456\n"
"s_add_u32 m0, s86, 118784\n"
"v_mfma_f32_16x16x32_bf16 v[144:147], v[196:199], a[232:235], v[144:147]\n"
"ds_read_b64_tr_b16 v[222:223], v251 offset:56576\n"
"buffer_load_dwordx4 v244, s[64:67], s79 offen offset:0 lds\n"
"v_mfma_f32_16x16x32_bf16 v[148:151], v[192:195], a[228:231], v[148:151]\n"
"ds_read_b64_tr_b16 v[176:177], v240 offset:0\n"
"v_mfma_f32_16x16x32_bf16 v[148:151], v[196:199], a[236:239], v[148:151]\n"
"ds_read_b64_tr_b16 v[178:179], v240 offset:5120\n"
"v_mfma_f32_16x16x32_bf16 v[152:155], v[200:203], a[224:227], v[152:155]\n"
"ds_read_b64_tr_b16 v[180:181], v240 offset:10240\n"
"v_mfma_f32_16x16x32_bf16 v[152:155], v[204:207], a[232:235], v[152:155]\n"
"ds_read_b64_tr_b16 v[182:183], v240 offset:15360\n"
"buffer_load_dwordx4 v245, s[64:67], s79 offen offset:1024 lds\n"
"v_mfma_f32_16x16x32_bf16 v[156:159], v[200:203], a[228:231], v[156:159]\n"
"ds_read_b64_tr_b16 v[184:185], v241 offset:0\n"
"v_mfma_f32_16x16x32_bf16 v[156:159], v[204:207], a[236:239], v[156:159]\n"
"ds_read_b64_tr_b16 v[186:187], v241 offset:5120\n"
"s_waitcnt lgkmcnt(5)\n"
"v_mfma_f32_16x16x32_bf16 v[160:163], v[208:211], a[224:227], v[160:163]\n"
"ds_read_b64_tr_b16 v[188:189], v241 offset:10240\n"
"v_mfma_f32_16x16x32_bf16 v[160:163], v[212:215], a[232:235], v[160:163]\n"
"ds_read_b64_tr_b16 v[190:191], v241 offset:15360\n"
"buffer_load_dwordx4 v246, s[64:67], s79 offen offset:2048 lds\n"
"v_mfma_f32_16x16x32_bf16 v[164:167], v[208:211], a[228:231], v[164:167]\n"
"ds_read_b64_tr_b16 v[192:193], v240 offset:64\n"
"v_mfma_f32_16x16x32_bf16 v[164:167], v[212:215], a[236:239], v[164:167]\n"
"ds_read_b64_tr_b16 v[194:195], v240 offset:5184\n"
"v_mfma_f32_16x16x32_bf16 v[168:171], v[216:219], a[224:227], v[168:171]\n"
"ds_read_b64_tr_b16 v[196:197], v240 offset:10304\n"
"v_mfma_f32_16x16x32_bf16 v[168:171], v[220:223], a[232:235], v[168:171]\n"
"ds_read_b64_tr_b16 v[198:199], v240 offset:15424\n"
"buffer_load_dwordx4 v247, s[64:67], s79 offen offset:3072 lds\n"
"v_mfma_f32_16x16x32_bf16 v[172:175], v[216:219], a[228:231], v[172:175]\n"
"ds_read_b64_tr_b16 v[200:201], v241 offset:64\n"
"v_mfma_f32_16x16x32_bf16 v[172:175], v[220:223], a[236:239], v[172:175]\n"
"ds_read_b64_tr_b16 v[202:203], v241 offset:5184\n"
"s_waitcnt lgkmcnt(5)\n"
"v_mfma_f32_16x16x32_bf16 v[96:99], v[176:179], a[240:243], v[96:99]\n"
"ds_read_b64_tr_b16 v[204:205], v241 offset:10304\n"
"s_add_u32 m0, s86, 122880\n"
"v_mfma_f32_16x16x32_bf16 v[96:99], v[180:183], a[248:251], v[96:99]\n"
"ds_read_b64_tr_b16 v[206:207], v241 offset:15424\n"
"buffer_load_dwordx4 v248, s[64:67], s79 offen lds\n"
"s_add_u32 s79, s79, 81920\n"
"v_mfma_f32_16x16x32_bf16 v[100:103], v[176:179], a[244:247], v[100:103]\n"
"ds_read_b64_tr_b16 v[208:209], v240 offset:128\n"
"v_mfma_f32_16x16x32_bf16 v[100:103], v[180:183], a[252:255], v[100:103]\n"
"ds_read_b64_tr_b16 v[210:211], v240 offset:5248\n"
"v_mfma_f32_16x16x32_bf16 v[104:107], v[184:187], a[240:243], v[104:107]\n"
"ds_read_b64_tr_b16 v[212:213], v240 offset:10368\n"
"s_add_u32 s87, s83, 12288\n"
"v_mfma_f32_16x16x32_bf16 v[104:107], v[188:191], a[248:251], v[104:107]\n"
"ds_read_b64_tr_b16 v[214:215], v240 offset:15488\n"
"buffer_store_dwordx4 v[52:55], v249, s[72:75], s87 offen offset:1024 sc1\n"
"v_mfma_f32_16x16x32_bf16 v[108:111], v[184:187], a[244:247], v[108:111]\n"
"ds_read_b64_tr_b16 v[216:217], v241 offset:128\n"
"v_mfma_f32_16x16x32_bf16 v[108:111], v[188:191], a[252:255], v[108:111]\n"
"ds_read_b64_tr_b16 v[218:219], v241 offset:5248\n"
"s_waitcnt lgkmcnt(5)\n"
"v_mfma_f32_16x16x32_bf16 v[112:115], v[192:195], a[240:243], v[112:115]\n"
"ds_read_b64_tr_b16 v[220:221], v241 offset:10368\n"
"v_mfma_f32_16x16x32_bf16 v[112:115], v[196:199], a[248:251], v[112:115]\n"
"ds_read_b64_tr_b16 v[222:223], v241 offset:15488\n"
"v_mfma_f32_16x16x32_bf16 v[116:119], v[192:195], a[244:247], v[116:119]\n"
"ds_read_b64_tr_b16 v[176:177], v240 offset:192\n"
"v_mfma_f32_16x16x32_bf16 v[116:119], v[196:199], a[252:255], v[116:119]\n"
"ds_read_b64_tr_b16 v[178:179], v240 offset:5312\n"
"v_mfma_f32_16x16x32_bf16 v[120:123], v[200:203], a[240:243], v[120:123]\n"
"ds_read_b64_tr_b16 v[180:181], v240 offset:10432\n"
"s_add_u32 s87, s83, 12288\n"
"v_mfma_f32_16x16x32_bf16 v[120:123], v[204:207], a[248:251], v[120:123]\n"
"ds_read_b64_tr_b16 v[182:183], v240 offset:15552\n"
"buffer_store_dwordx4 v[56:59], v249, s[72:75], s87 offen offset:2048 sc1\n"
"v_mfma_f32_16x16x32_bf16 v[124:127], v[200:203], a[244:247], v[124:127]\n"
"ds_read_b64_tr_b16 v[184:185], v241 offset:192\n"
"v_mfma_f32_16x16x32_bf16 v[124:127], v[204:207], a[252:255], v[124:127]\n"
"ds_read_b64_tr_b16 v[186:187], v241 offset:5312\n"
"s_waitcnt lgkmcnt(5)\n"
"v_mfma_f32_16x16x32_bf16 v[128:131], v[208:211], a[240:243], v[128:131]\n"
"ds_read_b64_tr_b16 v[188:189], v241 offset:10432\n"
"v_mfma_f32_16x16x32_bf16 v[128:131], v[212:215], a[248:251], v[128:131]\n"
"ds_read_b64_tr_b16 v[190:191], v241 offset:15552\n"
"v_mfma_f32_16x16x32_bf16 v[132:135], v[208:211], a[244:247], v[132:135]\n"
"ds_read_b64_tr_b16 v[192:193], v240 offset:256\n"
"v_mfma_f32_16x16x32_bf16 v[132:135], v[212:215], a[252:255], v[132:135]\n"
"ds_read_b64_tr_b16 v[194:195], v240 offset:5376\n"
"v_mfma_f32_16x16x32_bf16 v[136:139], v[216:219], a[240:243], v[136:139]\n"
"ds_read_b64_tr_b16 v[196:197], v240 offset:10496\n"
"v_mfma_f32_16x16x32_bf16 v[136:139], v[220:223], a[248:251], v[136:139]\n"
"ds_read_b64_tr_b16 v[198:199], v240 offset:15616\n"
"v_mfma_f32_16x16x32_bf16 v[140:143], v[216:219], a[244:247], v[140:143]\n"
"ds_read_b64_tr_b16 v[200:201], v241 offset:256\n"
"v_mfma_f32_16x16x32_bf16 v[140:143], v[220:223], a[252:255], v[140:143]\n"
"ds_read_b64_tr_b16 v[202:203], v241 offset:5376\n"
"s_barrier\n"
"s_waitcnt lgkmcnt(5)\n"
"v_mfma_f32_16x16x32_bf16 v[144:147], v[176:179], a[240:243], v[144:147]\n"
"ds_read_b64_tr_b16 v[204:205], v241 offset:10496\n"
"s_add_u32 m0, s86, 139264\n"
"v_mfma_f32_16x16x32_bf16 v[144:147], v[180:183], a[248:251], v[144:147]\n"
"ds_read_b64_tr_b16 v[206:207], v241 offset:15616\n"
"buffer_load_dwordx4 v244, s[64:67], s79 offen offset:0 lds\n"
"v_mfma_f32_16x16x32_bf16 v[148:151], v[176:179], a[244:247], v[148:151]\n"
"ds_read_b64_tr_b16 v[208:209], v240 offset:20480\n"
"v_mfma_f32_16x16x32_bf16 v[148:151], v[180:183], a[252:255], v[148:151]\n"
"ds_read_b64_tr_b16 v[210:211], v240 offset:25600\n"
"v_mfma_f32_16x16x32_bf16 v[152:155], v[184:187], a[240:243], v[152:155]\n"
"ds_read_b64_tr_b16 v[212:213], v240 offset:30720\n"
"s_add_u32 s87, s83, 12288\n"
"v_mfma_f32_16x16x32_bf16 v[152:155], v[188:191], a[248:251], v[152:155]\n"
"ds_read_b64_tr_b16 v[214:215], v240 offset:35840\n"
"buffer_store_dwordx4 v[60:63], v249, s[72:75], s87 offen offset:3072 sc1\n"
"v_mfma_f32_16x16x32_bf16 v[156:159], v[184:187], a[244:247], v[156:159]\n"
"ds_read_b64_tr_b16 v[216:217], v241 offset:20480\n"
"v_mfma_f32_16x16x32_bf16 v[156:159], v[188:191], a[252:255], v[156:159]\n"
"ds_read_b64_tr_b16 v[218:219], v241 offset:25600\n"
"s_waitcnt lgkmcnt(5)\n"
"v_mfma_f32_16x16x32_bf16 v[160:163], v[192:195], a[240:243], v[160:163]\n"
"ds_read_b64_tr_b16 v[220:221], v241 offset:30720\n"
"v_mfma_f32_16x16x32_bf16 v[160:163], v[196:199], a[248:251], v[160:163]\n"
"ds_read_b64_tr_b16 v[222:223], v241 offset:35840\n"
"buffer_load_dwordx4 v245, s[64:67], s79 offen offset:1024 lds\n"
"v_mfma_f32_16x16x32_bf16 v[164:167], v[192:195], a[244:247], v[164:167]\n"
"ds_read_b64_tr_b16 v[176:177], v240 offset:20544\n"
"v_mfma_f32_16x16x32_bf16 v[164:167], v[196:199], a[252:255], v[164:167]\n"
"ds_read_b64_tr_b16 v[178:179], v240 offset:25664\n"
"v_mfma_f32_16x16x32_bf16 v[168:171], v[200:203], a[240:243], v[168:171]\n"
"ds_read_b64_tr_b16 v[180:181], v240 offset:30784\n"
"v_mfma_f32_16x16x32_bf16 v[168:171], v[204:207], a[248:251], v[168:171]\n"
"ds_read_b64_tr_b16 v[182:183], v240 offset:35904\n"
"buffer_load_dwordx4 v246, s[64:67], s79 offen offset:2048 lds\n"
"v_mfma_f32_16x16x32_bf16 v[172:175], v[200:203], a[244:247], v[172:175]\n"
"ds_read_b64_tr_b16 v[184:185], v241 offset:20544\n"
"v_mfma_f32_16x16x32_bf16 v[172:175], v[204:207], a[252:255], v[172:175]\n"
"ds_read_b64_tr_b16 v[186:187], v241 offset:25664\n"
"s_waitcnt vmcnt(42) lgkmcnt(5)\n"
"v_mfma_f32_16x16x32_bf16 v[96:99], v[208:211], a[160:163], v[96:99]\n"
"ds_read_b64_tr_b16 v[188:189], v241 offset:30784\n"
"s_waitcnt vmcnt(40)\n"
"v_mfma_f32_16x16x32_bf16 v[96:99], v[212:215], a[168:171], v[96:99]\n"
"ds_read_b64_tr_b16 v[190:191], v241 offset:35904\n"
"buffer_load_dwordx4 v247, s[64:67], s79 offen offset:3072 lds\n"
"v_mfma_f32_16x16x32_bf16 v[100:103], v[208:211], a[164:167], v[100:103]\n"
"ds_read_b64_tr_b16 v[192:193], v240 offset:20608\n"
"s_waitcnt vmcnt(40)\n"
"v_mfma_f32_16x16x32_bf16 v[100:103], v[212:215], a[172:175], v[100:103]\n"
"ds_read_b64_tr_b16 v[194:195], v240 offset:25728\n"
"v_mfma_f32_16x16x32_bf16 v[104:107], v[216:219], a[160:163], v[104:107]\n"
"ds_read_b64_tr_b16 v[196:197], v240 offset:30848\n"
"s_add_u32 m0, s86, 143360\n"
"v_mfma_f32_16x16x32_bf16 v[104:107], v[220:223], a[168:171], v[104:107]\n"
"ds_read_b64_tr_b16 v[198:199], v240 offset:35968\n"
"buffer_load_dwordx4 v248, s[64:67], s79 offen lds\n"
"s_add_u32 s79, s79, 81920\n"
"v_mfma_f32_16x16x32_bf16 v[108:111], v[216:219], a[164:167], v[108:111]\n"
"ds_read_b64_tr_b16 v[200:201], v241 offset:20608\n"
"v_mfma_f32_16x16x32_bf16 v[108:111], v[220:223], a[172:175], v[108:111]\n"
"ds_read_b64_tr_b16 v[202:203], v241 offset:25728\n"
"s_waitcnt lgkmcnt(5)\n"
"v_mfma_f32_16x16x32_bf16 v[112:115], v[176:179], a[160:163], v[112:115]\n"
"ds_read_b64_tr_b16 v[204:205], v241 offset:30848\n"
"s_add_u32 s87, s83, 16384\n"
"v_mfma_f32_16x16x32_bf16 v[112:115], v[180:183], a[168:171], v[112:115]\n"
"ds_read_b64_tr_b16 v[206:207], v241 offset:35968\n"
"buffer_store_dwordx4 v[64:67], v249, s[72:75], s87 offen offset:0 sc1\n"
"v_mfma_f32_16x16x32_bf16 v[116:119], v[176:179], a[164:167], v[116:119]\n"
"ds_read_b64_tr_b16 v[208:209], v240 offset:20672\n"
"v_mfma_f32_16x16x32_bf16 v[116:119], v[180:183], a[172:175], v[116:119]\n"
"ds_read_b64_tr_b16 v[210:211], v240 offset:25792\n"
"v_mfma_f32_16x16x32_bf16 v[120:123], v[184:187], a[160:163], v[120:123]\n"
"ds_read_b64_tr_b16 v[212:213], v240 offset:30912\n"
"v_mfma_f32_16x16x32_bf16 v[120:123], v[188:191], a[168:171], v[120:123]\n"
"ds_read_b64_tr_b16 v[214:215], v240 offset:36032\n"
"v_mfma_f32_16x16x32_bf16 v[124:127], v[184:187], a[164:167], v[124:127]\n"
"ds_read_b64_tr_b16 v[216:217], v241 offset:20672\n"
"v_mfma_f32_16x16x32_bf16 v[124:127], v[188:191], a[172:175], v[124:127]\n"
"ds_read_b64_tr_b16 v[218:219], v241 offset:25792\n"
"s_waitcnt lgkmcnt(5)\n"
"v_mfma_f32_16x16x32_bf16 v[128:131], v[192:195], a[160:163], v[128:131]\n"
"ds_read_b64_tr_b16 v[220:221], v241 offset:30912\n"
"v_mfma_f32_16x16x32_bf16 v[128:131], v[196:199], a[168:171], v[128:131]\n"
"ds_read_b64_tr_b16 v[222:223], v241 offset:36032\n"
"v_mfma_f32_16x16x32_bf16 v[132:135], v[192:195], a[164:167], v[132:135]\n"
"ds_read_b64_tr_b16 v[176:177], v240 offset:20736\n"
"v_mfma_f32_16x16x32_bf16 v[132:135], v[196:199], a[172:175], v[132:135]\n"
"ds_read_b64_tr_b16 v[178:179], v240 offset:25856\n"
"v_mfma_f32_16x16x32_bf16 v[136:139], v[200:203], a[160:163], v[136:139]\n"
"ds_read_b64_tr_b16 v[180:181], v240 offset:30976\n"
"s_add_u32 s87, s83, 16384\n"
"v_mfma_f32_16x16x32_bf16 v[136:139], v[204:207], a[168:171], v[136:139]\n"
"ds_read_b64_tr_b16 v[182:183], v240 offset:36096\n"
"buffer_store_dwordx4 v[68:71], v249, s[72:75], s87 offen offset:1024 sc1\n"
"v_mfma_f32_16x16x32_bf16 v[140:143], v[200:203], a[164:167], v[140:143]\n"
"ds_read_b64_tr_b16 v[184:185], v241 offset:20736\n"
"v_mfma_f32_16x16x32_bf16 v[140:143], v[204:207], a[172:175], v[140:143]\n"
"ds_read_b64_tr_b16 v[186:187], v241 offset:25856\n"
"s_barrier\n"
"s_waitcnt lgkmcnt(5)\n"
"v_mfma_f32_16x16x32_bf16 v[144:147], v[208:211], a[160:163], v[144:147]\n"
"ds_read_b64_tr_b16 v[188:189], v241 offset:30976\n"
"s_cmp_le_u32 s24, 0\n"
"s_cselect_b32 s80, 1, 0\n"
"s_mul_i32 s79, s80, 245760\n"
"s_add_u32 s79, s79, s94\n"
"s_add_u32 m0, s86, 0\n"
"v_mfma_f32_16x16x32_bf16 v[144:147], v[212:215], a[168:171], v[144:147]\n"
"ds_read_b64_tr_b16 v[190:191], v241 offset:36096\n"
"buffer_load_dwordx4 v244, s[64:67], s79 offen offset:0 lds\n"
"v_mfma_f32_16x16x32_bf16 v[148:151], v[208:211], a[164:167], v[148:151]\n"
"ds_read_b64_tr_b16 v[192:193], v240 offset:40960\n"
"v_mfma_f32_16x16x32_bf16 v[148:151], v[212:215], a[172:175], v[148:151]\n"
"ds_read_b64_tr_b16 v[194:195], v240 offset:46080\n"
"v_mfma_f32_16x16x32_bf16 v[152:155], v[216:219], a[160:163], v[152:155]\n"
"ds_read_b64_tr_b16 v[196:197], v240 offset:51200\n"
"v_mfma_f32_16x16x32_bf16 v[152:155], v[220:223], a[168:171], v[152:155]\n"
"ds_read_b64_tr_b16 v[198:199], v240 offset:56320\n"
"buffer_load_dwordx4 v245, s[64:67], s79 offen offset:1024 lds\n"
"v_mfma_f32_16x16x32_bf16 v[156:159], v[216:219], a[164:167], v[156:159]\n"
"ds_read_b64_tr_b16 v[200:201], v241 offset:40960\n"
"v_mfma_f32_16x16x32_bf16 v[156:159], v[220:223], a[172:175], v[156:159]\n"
"ds_read_b64_tr_b16 v[202:203], v241 offset:46080\n"
"s_waitcnt lgkmcnt(5)\n"
"v_mfma_f32_16x16x32_bf16 v[160:163], v[176:179], a[160:163], v[160:163]\n"
"ds_read_b64_tr_b16 v[204:205], v241 offset:51200\n"
"v_mfma_f32_16x16x32_bf16 v[160:163], v[180:183], a[168:171], v[160:163]\n"
"ds_read_b64_tr_b16 v[206:207], v241 offset:56320\n"
"buffer_load_dwordx4 v246, s[64:67], s79 offen offset:2048 lds\n"
"v_mfma_f32_16x16x32_bf16 v[164:167], v[176:179], a[164:167], v[164:167]\n"
"ds_read_b64_tr_b16 v[208:209], v240 offset:41024\n"
"v_mfma_f32_16x16x32_bf16 v[164:167], v[180:183], a[172:175], v[164:167]\n"
"ds_read_b64_tr_b16 v[210:211], v240 offset:46144\n"
"v_mfma_f32_16x16x32_bf16 v[168:171], v[184:187], a[160:163], v[168:171]\n"
"ds_read_b64_tr_b16 v[212:213], v240 offset:51264\n"
"v_mfma_f32_16x16x32_bf16 v[168:171], v[188:191], a[168:171], v[168:171]\n"
"ds_read_b64_tr_b16 v[214:215], v240 offset:56384\n"
"buffer_load_dwordx4 v247, s[64:67], s79 offen offset:3072 lds\n"
"v_mfma_f32_16x16x32_bf16 v[172:175], v[184:187], a[164:167], v[172:175]\n"
"ds_read_b64_tr_b16 v[216:217], v241 offset:41024\n"
"v_mfma_f32_16x16x32_bf16 v[172:175], v[188:191], a[172:175], v[172:175]\n"
"ds_read_b64_tr_b16 v[218:219], v241 offset:46144\n"
"s_waitcnt vmcnt(45) lgkmcnt(5)\n"
"v_mfma_f32_16x16x32_bf16 v[96:99], v[192:195], a[176:179], v[96:99]\n"
"ds_read_b64_tr_b16 v[220:221], v241 offset:51264\n"
"s_add_u32 s87, s83, 16384\n"
"s_waitcnt vmcnt(42)\n"
"v_mfma_f32_16x16x32_bf16 v[96:99], v[196:199], a[184:187], v[96:99]\n"
"ds_read_b64_tr_b16 v[222:223], v241 offset:56384\n"
"buffer_store_dwordx4 v[72:75], v249, s[72:75], s87 offen offset:2048 sc1\n"
"v_mfma_f32_16x16x32_bf16 v[100:103], v[192:195], a[180:183], v[100:103]\n"
"ds_read_b64_tr_b16 v[176:177], v240 offset:41088\n"
"s_waitcnt vmcnt(42)\n"
"v_mfma_f32_16x16x32_bf16 v[100:103], v[196:199], a[188:191], v[100:103]\n"
"ds_read_b64_tr_b16 v[178:179], v240 offset:46208\n"
"v_mfma_f32_16x16x32_bf16 v[104:107], v[200:203], a[176:179], v[104:107]\n"
"ds_read_b64_tr_b16 v[180:181], v240 offset:51328\n"
"s_add_u32 m0, s86, 4096\n"
"v_mfma_f32_16x16x32_bf16 v[104:107], v[204:207], a[184:187], v[104:107]\n"
"ds_read_b64_tr_b16 v[182:183], v240 offset:56448\n"
"buffer_load_dwordx4 v248, s[64:67], s79 offen lds\n"
"s_add_u32 s79, s79, 81920\n"
"v_mfma_f32_16x16x32_bf16 v[108:111], v[200:203], a[180:183], v[108:111]\n"
"ds_read_b64_tr_b16 v[184:185], v241 offset:41088\n"
"v_mfma_f32_16x16x32_bf16 v[108:111], v[204:207], a[188:191], v[108:111]\n"
"ds_read_b64_tr_b16 v[186:187], v241 offset:46208\n"
"s_waitcnt vmcnt(24)\n"
"s_barrier\n"
"s_waitcnt lgkmcnt(5)\n"
"v_mfma_f32_16x16x32_bf16 v[112:115], v[208:211], a[176:179], v[112:115]\n"
"ds_read_b64_tr_b16 v[188:189], v241 offset:51328\n"
"s_add_u32 m0, s86, 20480\n"
"v_mfma_f32_16x16x32_bf16 v[112:115], v[212:215], a[184:187], v[112:115]\n"
"ds_read_b64_tr_b16 v[190:191], v241 offset:56448\n"
"buffer_load_dwordx4 v244, s[64:67], s79 offen offset:0 lds\n"
"v_mfma_f32_16x16x32_bf16 v[116:119], v[208:211], a[180:183], v[116:119]\n"
"ds_read_b64_tr_b16 v[192:193], v240 offset:41152\n"
"v_mfma_f32_16x16x32_bf16 v[116:119], v[212:215], a[188:191], v[116:119]\n"
"ds_read_b64_tr_b16 v[194:195], v240 offset:46272\n"
"v_cvt_pk_bf16_f32 v96, v96, v97\n"
"v_mfma_f32_16x16x32_bf16 v[120:123], v[216:219], a[176:179], v[120:123]\n"
"ds_read_b64_tr_b16 v[196:197], v240 offset:51392\n"
"v_cvt_pk_bf16_f32 v97, v98, v99\n"
"v_mfma_f32_16x16x32_bf16 v[120:123], v[220:223], a[184:187], v[120:123]\n"
"ds_read_b64_tr_b16 v[198:199], v240 offset:56512\n"
"v_cvt_pk_bf16_f32 v98, v104, v105\n"
"v_mfma_f32_16x16x32_bf16 v[124:127], v[216:219], a[180:183], v[124:127]\n"
"ds_read_b64_tr_b16 v[200:201], v241 offset:41152\n"
"v_cvt_pk_bf16_f32 v99, v106, v107\n"
"v_mfma_f32_16x16x32_bf16 v[124:127], v[220:223], a[188:191], v[124:127]\n"
"ds_read_b64_tr_b16 v[202:203], v241 offset:46272\n"
"v_cvt_pk_bf16_f32 v100, v100, v101\n"
"s_waitcnt lgkmcnt(5)\n"
"v_mfma_f32_16x16x32_bf16 v[128:131], v[176:179], a[176:179], v[128:131]\n"
"ds_read_b64_tr_b16 v[204:205], v241 offset:51392\n"
"v_cvt_pk_bf16_f32 v101, v102, v103\n"
"v_mfma_f32_16x16x32_bf16 v[128:131], v[180:183], a[184:187], v[128:131]\n"
"ds_read_b64_tr_b16 v[206:207], v241 offset:56512\n"
"v_cvt_pk_bf16_f32 v102, v108, v109\n"
"v_mfma_f32_16x16x32_bf16 v[132:135], v[176:179], a[180:183], v[132:135]\n"
"ds_read_b64_tr_b16 v[208:209], v240 offset:41216\n"
"v_cvt_pk_bf16_f32 v103, v110, v111\n"
"v_mfma_f32_16x16x32_bf16 v[132:135], v[180:183], a[188:191], v[132:135]\n"
"ds_read_b64_tr_b16 v[210:211], v240 offset:46336\n"
"v_cvt_pk_bf16_f32 v112, v112, v113\n"
"v_mfma_f32_16x16x32_bf16 v[136:139], v[184:187], a[176:179], v[136:139]\n"
"ds_read_b64_tr_b16 v[212:213], v240 offset:51456\n"
"v_cvt_pk_bf16_f32 v113, v114, v115\n"
"v_mfma_f32_16x16x32_bf16 v[136:139], v[188:191], a[184:187], v[136:139]\n"
"ds_read_b64_tr_b16 v[214:215], v240 offset:56576\n"
"v_cvt_pk_bf16_f32 v114, v120, v121\n"
"v_mfma_f32_16x16x32_bf16 v[140:143], v[184:187], a[180:183], v[140:143]\n"
"ds_read_b64_tr_b16 v[216:217], v241 offset:41216\n"
"v_cvt_pk_bf16_f32 v115, v122, v123\n"
"v_mfma_f32_16x16x32_bf16 v[140:143], v[188:191], a[188:191], v[140:143]\n"
"ds_read_b64_tr_b16 v[218:219], v241 offset:46336\n"
"v_cvt_pk_bf16_f32 v116, v116, v117\n"
"s_waitcnt lgkmcnt(5)\n"
"v_mfma_f32_16x16x32_bf16 v[144:147], v[192:195], a[176:179], v[144:147]\n"
"ds_read_b64_tr_b16 v[220:221], v241 offset:51456\n"
"v_cvt_pk_bf16_f32 v117, v118, v119\n"
"v_mfma_f32_16x16x32_bf16 v[144:147], v[196:199], a[184:187], v[144:147]\n"
"ds_read_b64_tr_b16 v[222:223], v241 offset:56576\n"
"v_cvt_pk_bf16_f32 v118, v124, v125\n"
"v_mfma_f32_16x16x32_bf16 v[148:151], v[192:195], a[180:183], v[148:151]\n"
"ds_read_b64_tr_b16 v[176:177], v250 offset:0\n"
"v_cvt_pk_bf16_f32 v119, v126, v127\n"
"v_mfma_f32_16x16x32_bf16 v[148:151], v[196:199], a[188:191], v[148:151]\n"
"ds_read_b64_tr_b16 v[178:179], v250 offset:5120\n"
"v_cvt_pk_bf16_f32 v128, v128, v129\n"
"v_mfma_f32_16x16x32_bf16 v[152:155], v[200:203], a[176:179], v[152:155]\n"
"ds_read_b64_tr_b16 v[180:181], v250 offset:10240\n"
"v_cvt_pk_bf16_f32 v129, v130, v131\n"
"v_mfma_f32_16x16x32_bf16 v[152:155], v[204:207], a[184:187], v[152:155]\n"
"ds_read_b64_tr_b16 v[182:183], v250 offset:15360\n"
"v_cvt_pk_bf16_f32 v132, v132, v133\n"
"v_mfma_f32_16x16x32_bf16 v[156:159], v[200:203], a[180:183], v[156:159]\n"
"ds_read_b64_tr_b16 v[184:185], v251 offset:0\n"
"v_cvt_pk_bf16_f32 v133, v134, v135\n"
"v_mfma_f32_16x16x32_bf16 v[156:159], v[204:207], a[188:191], v[156:159]\n"
"ds_read_b64_tr_b16 v[186:187], v251 offset:5120\n"
"v_cvt_pk_bf16_f32 v136, v136, v137\n"
"s_waitcnt lgkmcnt(5)\n"
"v_mfma_f32_16x16x32_bf16 v[160:163], v[208:211], a[176:179], v[160:163]\n"
"ds_read_b64_tr_b16 v[188:189], v251 offset:10240\n"
"v_cvt_pk_bf16_f32 v137, v138, v139\n"
"v_mfma_f32_16x16x32_bf16 v[160:163], v[212:215], a[184:187], v[160:163]\n"
"ds_read_b64_tr_b16 v[190:191], v251 offset:15360\n"
"v_cvt_pk_bf16_f32 v138, v144, v145\n"
"v_mfma_f32_16x16x32_bf16 v[164:167], v[208:211], a[180:183], v[164:167]\n"
"ds_read_b64_tr_b16 v[192:193], v250 offset:64\n"
"v_cvt_pk_bf16_f32 v139, v146, v147\n"
"v_mfma_f32_16x16x32_bf16 v[164:167], v[212:215], a[188:191], v[164:167]\n"
"ds_read_b64_tr_b16 v[194:195], v250 offset:5184\n"
"v_cvt_pk_bf16_f32 v140, v140, v141\n"
"v_mfma_f32_16x16x32_bf16 v[168:171], v[216:219], a[176:179], v[168:171]\n"
"ds_read_b64_tr_b16 v[196:197], v250 offset:10304\n"
"v_cvt_pk_bf16_f32 v141, v142, v143\n"
"v_mfma_f32_16x16x32_bf16 v[168:171], v[220:223], a[184:187], v[168:171]\n"
"ds_read_b64_tr_b16 v[198:199], v250 offset:15424\n"
"v_cvt_pk_bf16_f32 v142, v148, v149\n"
"v_mfma_f32_16x16x32_bf16 v[172:175], v[216:219], a[180:183], v[172:175]\n"
"ds_read_b64_tr_b16 v[200:201], v251 offset:64\n"
"v_cvt_pk_bf16_f32 v143, v150, v151\n"
"s_add_u32 s87, s83, 16384\n"
"v_mfma_f32_16x16x32_bf16 v[172:175], v[220:223], a[188:191], v[172:175]\n"
"v_cvt_pk_bf16_f32 v152, v152, v153\n"
"buffer_store_dwordx4 v[76:79], v249, s[72:75], s87 offen offset:3072 sc1\n"
"s_waitcnt lgkmcnt(4)\n"
"v_mfma_f32_16x16x32_bf16 a[0:3], v[176:179], v[0:3], 0\n"
"ds_read_b64_tr_b16 v[202:203], v251 offset:5184\n"
"v_cvt_pk_bf16_f32 v153, v154, v155\n"
"v_mfma_f32_16x16x32_bf16 a[0:3], v[180:183], v[16:19], a[0:3]\n"
"v_cvt_pk_bf16_f32 v154, v160, v161\n"
"v_mfma_f32_16x16x32_bf16 a[4:7], v[176:179], v[4:7], 0\n"
"ds_read_b64_tr_b16 v[204:205], v251 offset:10304\n"
"v_cvt_pk_bf16_f32 v155, v162, v163\n"
"v_mfma_f32_16x16x32_bf16 a[4:7], v[180:183], v[20:23], a[4:7]\n"
"v_cvt_pk_bf16_f32 v156, v156, v157\n"
"v_cvt_pk_bf16_f32 v157, v158, v159\n"
"v_mfma_f32_16x16x32_bf16 a[8:11], v[176:179], v[8:11], 0\n"
"ds_read_b64_tr_b16 v[206:207], v251 offset:15424\n"
"v_cvt_pk_bf16_f32 v158, v164, v165\n"
"v_mfma_f32_16x16x32_bf16 a[8:11], v[180:183], v[24:27], a[8:11]\n"
"v_cvt_pk_bf16_f32 v159, v166, v167\n"
"v_cvt_pk_bf16_f32 v168, v168, v169\n"
"v_mfma_f32_16x16x32_bf16 a[12:15], v[176:179], v[12:15], 0\n"
"ds_read_b64_tr_b16 v[208:209], v250 offset:128\n"
"v_cvt_pk_bf16_f32 v169, v170, v171\n"
"v_mfma_f32_16x16x32_bf16 a[12:15], v[180:183], v[28:31], a[12:15]\n"
"v_cvt_pk_bf16_f32 v172, v172, v173\n"
"buffer_load_dwordx4 v245, s[64:67], s79 offen offset:1024 lds\n"
"v_mfma_f32_16x16x32_bf16 a[16:19], v[184:187], v[0:3], 0\n"
"ds_read_b64_tr_b16 v[210:211], v250 offset:5248\n"
"v_cvt_pk_bf16_f32 v173, v174, v175\n"
"v_mfma_f32_16x16x32_bf16 a[16:19], v[188:191], v[16:19], a[16:19]\n"
"v_mfma_f32_16x16x32_bf16 a[20:23], v[184:187], v[4:7], 0\n"
"ds_read_b64_tr_b16 v[212:213], v250 offset:10368\n"
"v_mfma_f32_16x16x32_bf16 a[20:23], v[188:191], v[20:23], a[20:23]\n"
"buffer_load_dwordx4 v246, s[64:67], s79 offen offset:2048 lds\n"
"v_mfma_f32_16x16x32_bf16 a[24:27], v[184:187], v[8:11], 0\n"
"ds_read_b64_tr_b16 v[214:215], v250 offset:15488\n"
"v_mfma_f32_16x16x32_bf16 a[24:27], v[188:191], v[24:27], a[24:27]\n"
"v_mfma_f32_16x16x32_bf16 a[28:31], v[184:187], v[12:15], 0\n"
"ds_read_b64_tr_b16 v[216:217], v251 offset:128\n"
"v_mfma_f32_16x16x32_bf16 a[28:31], v[188:191], v[28:31], a[28:31]\n"
"buffer_load_dwordx4 v247, s[64:67], s79 offen offset:3072 lds\n"
"s_waitcnt lgkmcnt(3)\n"
"v_mfma_f32_16x16x32_bf16 a[32:35], v[192:195], v[0:3], 0\n"
"ds_read_b64_tr_b16 v[218:219], v251 offset:5248\n"
"v_mfma_f32_16x16x32_bf16 a[32:35], v[196:199], v[16:19], a[32:35]\n"
"v_mfma_f32_16x16x32_bf16 a[36:39], v[192:195], v[4:7], 0\n"
"ds_read_b64_tr_b16 v[220:221], v251 offset:10368\n"
"s_add_u32 m0, s86, 24576\n"
"v_mfma_f32_16x16x32_bf16 a[36:39], v[196:199], v[20:23], a[36:39]\n"
"buffer_load_dwordx4 v248, s[64:67], s79 offen lds\n"
"s_add_u32 s79, s79, 81920\n"
"s_waitcnt vmcnt(4)\n"
"s_barrier\n"
"s_cmp_lg_u32 s22, 0\n"
"s_cbranch_scc1 SIGSKIP3_%=\n"
"s_lshl_b32 s28, s21, 3\n"
"s_add_u32 s28, s28, s23\n"
"s_add_u32 s28, s28, 64\n"
"s_lshl_b32 s28, s28, 8\n"
"v_mov_b32 v255, s28\n"
"s_mov_b64 exec, 1\n"
"global_atomic_add v255, v254, s[54:55]\n"
"s_mov_b64 exec, -1\n"
"SIGSKIP3_%=:\n"
"v_mfma_f32_16x16x32_bf16 a[40:43], v[192:195], v[8:11], 0\n"
"ds_read_b64_tr_b16 v[222:223], v251 offset:15488\n"
"v_mfma_f32_16x16x32_bf16 a[40:43], v[196:199], v[24:27], a[40:43]\n"
"v_mfma_f32_16x16x32_bf16 a[44:47], v[192:195], v[12:15], 0\n"
"ds_read_b64_tr_b16 v[176:177], v250 offset:192\n"
"v_mfma_f32_16x16x32_bf16 a[44:47], v[196:199], v[28:31], a[44:47]\n"
"v_mfma_f32_16x16x32_bf16 a[48:51], v[200:203], v[0:3], 0\n"
"ds_read_b64_tr_b16 v[178:179], v250 offset:5312\n"
"v_mfma_f32_16x16x32_bf16 a[48:51], v[204:207], v[16:19], a[48:51]\n"
"v_mfma_f32_16x16x32_bf16 a[52:55], v[200:203], v[4:7], 0\n"
"ds_read_b64_tr_b16 v[180:181], v250 offset:10432\n"
"v_mfma_f32_16x16x32_bf16 a[52:55], v[204:207], v[20:23], a[52:55]\n"
"v_mfma_f32_16x16x32_bf16 a[56:59], v[200:203], v[8:11], 0\n"
"ds_read_b64_tr_b16 v[182:183], v250 offset:15552\n"
"v_mfma_f32_16x16x32_bf16 a[56:59], v[204:207], v[24:27], a[56:59]\n"
"v_mfma_f32_16x16x32_bf16 a[60:63], v[200:203], v[12:15], 0\n"
"ds_read_b64_tr_b16 v[184:185], v251 offset:192\n"
"v_mfma_f32_16x16x32_bf16 a[60:63], v[204:207], v[28:31], a[60:63]\n"
"v_mfma_f32_16x16x32_bf16 a[64:67], v[208:211], v[0:3], 0\n"
"ds_read_b64_tr_b16 v[186:187], v251 offset:5312\n"
"s_waitcnt lgkmcnt(3)\n"
"v_mfma_f32_16x16x32_bf16 a[64:67], v[212:215], v[16:19], a[64:67]\n"
"v_mfma_f32_16x16x32_bf16 a[68:71], v[208:211], v[4:7], 0\n"
"ds_read_b64_tr_b16 v[188:189], v251 offset:10432\n"
"v_mfma_f32_16x16x32_bf16 a[68:71], v[212:215], v[20:23], a[68:71]\n"
"v_mfma_f32_16x16x32_bf16 a[72:75], v[208:211], v[8:11], 0\n"
"ds_read_b64_tr_b16 v[190:191], v251 offset:15552\n"
"v_mfma_f32_16x16x32_bf16 a[72:75], v[212:215], v[24:27], a[72:75]\n"
"v_mfma_f32_16x16x32_bf16 a[76:79], v[208:211], v[12:15], 0\n"
"ds_read_b64_tr_b16 v[192:193], v250 offset:256\n"
"v_mfma_f32_16x16x32_bf16 a[76:79], v[212:215], v[28:31], a[76:79]\n"
"v_mfma_f32_16x16x32_bf16 a[80:83], v[216:219], v[0:3], 0\n"
"ds_read_b64_tr_b16 v[194:195], v250 offset:5376\n"
"v_mfma_f32_16x16x32_bf16 a[80:83], v[220:223], v[16:19], a[80:83]\n"
"v_mfma_f32_16x16x32_bf16 a[84:87], v[216:219], v[4:7], 0\n"
"ds_read_b64_tr_b16 v[196:197], v250 offset:10496\n"
"v_mfma_f32_16x16x32_bf16 a[84:87], v[220:223], v[20:23], a[84:87]\n"
"v_mfma_f32_16x16x32_bf16 a[88:91], v[216:219], v[8:11], 0\n"
"ds_read_b64_tr_b16 v[198:199], v250 offset:15616\n"
"v_mfma_f32_16x16x32_bf16 a[88:91], v[220:223], v[24:27], a[88:91]\n"
"v_mfma_f32_16x16x32_bf16 a[92:95], v[216:219], v[12:15], 0\n"
"ds_read_b64_tr_b16 v[200:201], v251 offset:256\n"
"v_mfma_f32_16x16x32_bf16 a[92:95], v[220:223], v[28:31], a[92:95]\n"
"s_barrier\n"
"v_mfma_f32_16x16x32_bf16 a[96:99], v[176:179], v[0:3], 0\n"
"ds_read_b64_tr_b16 v[202:203], v251 offset:5376\n"
"s_add_u32 m0, s86, 40960\n"
"s_waitcnt lgkmcnt(3)\n"
"v_mfma_f32_16x16x32_bf16 a[96:99], v[180:183], v[16:19], a[96:99]\n"
"buffer_load_dwordx4 v244, s[64:67], s79 offen offset:0 lds\n"
"s_lshl_b32 s28, s21, 3\n"
"s_add_u32 s28, s28, s23\n"
"s_add_u32 s28, s28, 64\n"
"s_lshl_b32 s28, s28, 8\n"
"v_mov_b32 v255, s28\n"
"s_mov_b32 s27, 8000\n"
"SPIN4_%=:\n"
"global_load_dword v252, v255, s[54:55] sc1\n"
"s_waitcnt vmcnt(0)\n"
"v_readfirstlane_b32 s28, v252\n"
"s_nop 1\n"
"s_cmp_ge_u32 s28, 4\n"
"s_cbranch_scc1 POLLOK4_%=\n"
"s_sleep 1\n"
"s_sub_u32 s27, s27, 1\n"
"s_cmp_lg_u32 s27, 0\n"
"s_cbranch_scc1 SPIN4_%=\n"
"POLLOK4_%=:\n"
"v_mfma_f32_16x16x32_bf16 a[100:103], v[176:179], v[4:7], 0\n"
"ds_read_b64_tr_b16 v[204:205], v251 offset:10496\n"
"v_mfma_f32_16x16x32_bf16 a[100:103], v[180:183], v[20:23], a[100:103]\n"
"v_mfma_f32_16x16x32_bf16 a[104:107], v[176:179], v[8:11], 0\n"
"ds_read_b64_tr_b16 v[206:207], v251 offset:15616\n"
"s_cmp_le_u32 s24, 0\n"
"s_cselect_b32 s95, 1, 0\n"
"s_mul_i32 s89, s95, 786432\n"
"s_add_u32 s89, s89, s84\n"
"v_mfma_f32_16x16x32_bf16 a[104:107], v[180:183], v[24:27], a[104:107]\n"
"buffer_load_dwordx4 a[160:163], v249, s[72:75], s89 offen offset:0 sc1\n"
"v_mfma_f32_16x16x32_bf16 a[108:111], v[176:179], v[12:15], 0\n"
"ds_read_b64_tr_b16 v[208:209], v250 offset:20480\n"
"v_mfma_f32_16x16x32_bf16 a[108:111], v[180:183], v[28:31], a[108:111]\n"
"v_mfma_f32_16x16x32_bf16 a[112:115], v[184:187], v[0:3], 0\n"
"ds_read_b64_tr_b16 v[210:211], v250 offset:25600\n"
"v_mfma_f32_16x16x32_bf16 a[112:115], v[188:191], v[16:19], a[112:115]\n"
"buffer_load_dwordx4 a[164:167], v249, s[72:75], s89 offen offset:1024 sc1\n"
"v_mfma_f32_16x16x32_bf16 a[116:119], v[184:187], v[4:7], 0\n"
"ds_read_b64_tr_b16 v[212:213], v250 offset:30720\n"
"v_mfma_f32_16x16x32_bf16 a[116:119], v[188:191], v[20:23], a[116:119]\n"
"v_mfma_f32_16x16x32_bf16 a[120:123], v[184:187], v[8:11], 0\n"
"ds_read_b64_tr_b16 v[214:215], v250 offset:35840\n"
"v_mfma_f32_16x16x32_bf16 a[120:123], v[188:191], v[24:27], a[120:123]\n"
"buffer_load_dwordx4 a[168:171], v249, s[72:75], s89 offen offset:2048 sc1\n"
"v_mfma_f32_16x16x32_bf16 a[124:127], v[184:187], v[12:15], 0\n"
"ds_read_b64_tr_b16 v[216:217], v251 offset:20480\n"
"v_mfma_f32_16x16x32_bf16 a[124:127], v[188:191], v[28:31], a[124:127]\n"
"v_mfma_f32_16x16x32_bf16 a[128:131], v[192:195], v[0:3], 0\n"
"ds_read_b64_tr_b16 v[218:219], v251 offset:25600\n"
"s_waitcnt lgkmcnt(3)\n"
"v_mfma_f32_16x16x32_bf16 a[128:131], v[196:199], v[16:19], a[128:131]\n"
"buffer_load_dwordx4 a[172:175], v249, s[72:75], s89 offen offset:3072 sc1\n"
"s_add_u32 s89, s89, 4096\n"
"v_mfma_f32_16x16x32_bf16 a[132:135], v[192:195], v[4:7], 0\n"
"ds_read_b64_tr_b16 v[220:221], v251 offset:30720\n"
"v_mfma_f32_16x16x32_bf16 a[132:135], v[196:199], v[20:23], a[132:135]\n"
"v_mfma_f32_16x16x32_bf16 a[136:139], v[192:195], v[8:11], 0\n"
"ds_read_b64_tr_b16 v[222:223], v251 offset:35840\n"
"v_mfma_f32_16x16x32_bf16 a[136:139], v[196:199], v[24:27], a[136:139]\n"
"buffer_load_dwordx4 a[176:179], v249, s[72:75], s89 offen offset:0 sc1\n"
"v_mfma_f32_16x16x32_bf16 a[140:143], v[192:195], v[12:15], 0\n"
"ds_read_b64_tr_b16 v[176:177], v250 offset:20544\n"
"v_mfma_f32_16x16x32_bf16 a[140:143], v[196:199], v[28:31], a[140:143]\n"
"v_mfma_f32_16x16x32_bf16 a[144:147], v[200:203], v[0:3], 0\n"
"ds_read_b64_tr_b16 v[178:179], v250 offset:25664\n"
"v_mfma_f32_16x16x32_bf16 a[144:147], v[204:207], v[16:19], a[144:147]\n"
"buffer_load_dwordx4 a[180:183], v249, s[72:75], s89 offen offset:1024 sc1\n"
"v_mfma_f32_16x16x32_bf16 a[148:151], v[200:203], v[4:7], 0\n"
"ds_read_b64_tr_b16 v[180:181], v250 offset:30784\n"
"v_mfma_f32_16x16x32_bf16 a[148:151], v[204:207], v[20:23], a[148:151]\n"
"v_mfma_f32_16x16x32_bf16 a[152:155], v[200:203], v[8:11], 0\n"
"ds_read_b64_tr_b16 v[182:183], v250 offset:35904\n"
"v_mfma_f32_16x16x32_bf16 a[152:155], v[204:207], v[24:27], a[152:155]\n"
"buffer_load_dwordx4 a[184:187], v249, s[72:75], s89 offen offset:2048 sc1\n"
"v_mfma_f32_16x16x32_bf16 a[156:159], v[200:203], v[12:15], 0\n"
"ds_read_b64_tr_b16 v[184:185], v251 offset:20544\n"
"v_mfma_f32_16x16x32_bf16 a[156:159], v[204:207], v[28:31], a[156:159]\n"
"v_mfma_f32_16x16x32_bf16 a[0:3], v[208:211], v[32:35], a[0:3]\n"
"ds_read_b64_tr_b16 v[186:187], v251 offset:25664\n"
"s_waitcnt lgkmcnt(3)\n"
"v_mfma_f32_16x16x32_bf16 a[0:3], v[212:215], v[48:51], a[0:3]\n"
"buffer_load_dwordx4 a[188:191], v249, s[72:75], s89 offen offset:3072 sc1\n"
"s_add_u32 s89, s89, 4096\n"
"v_mfma_f32_16x16x32_bf16 a[4:7], v[208:211], v[36:39], a[4:7]\n"
"ds_read_b64_tr_b16 v[188:189], v251 offset:30784\n"
"v_mfma_f32_16x16x32_bf16 a[4:7], v[212:215], v[52:55], a[4:7]\n"
"v_mfma_f32_16x16x32_bf16 a[8:11], v[208:211], v[40:43], a[8:11]\n"
"ds_read_b64_tr_b16 v[190:191], v251 offset:35904\n"
"v_mfma_f32_16x16x32_bf16 a[8:11], v[212:215], v[56:59], a[8:11]\n"
"buffer_load_dwordx4 a[192:195], v249, s[72:75], s89 offen offset:0 sc1\n"
"v_mfma_f32_16x16x32_bf16 a[12:15], v[208:211], v[44:47], a[12:15]\n"
"ds_read_b64_tr_b16 v[192:193], v250 offset:20608\n"
"v_mfma_f32_16x16x32_bf16 a[12:15], v[212:215], v[60:63], a[12:15]\n"
"v_mfma_f32_16x16x32_bf16 a[16:19], v[216:219], v[32:35], a[16:19]\n"
"ds_read_b64_tr_b16 v[194:195], v250 offset:25728\n"
"v_mfma_f32_16x16x32_bf16 a[16:19], v[220:223], v[48:51], a[16:19]\n"
"buffer_load_dwordx4 a[196:199], v249, s[72:75], s89 offen offset:1024 sc1\n"
"v_mfma_f32_16x16x32_bf16 a[20:23], v[216:219], v[36:39], a[20:23]\n"
"ds_read_b64_tr_b16 v[196:197], v250 offset:30848\n"
"v_mfma_f32_16x16x32_bf16 a[20:23], v[220:223], v[52:55], a[20:23]\n"
"v_mfma_f32_16x16x32_bf16 a[24:27], v[216:219], v[40:43], a[24:27]\n"
"ds_read_b64_tr_b16 v[198:199], v250 offset:35968\n"
"v_mfma_f32_16x16x32_bf16 a[24:27], v[220:223], v[56:59], a[24:27]\n"
"buffer_load_dwordx4 a[200:203], v249, s[72:75], s89 offen offset:2048 sc1\n"
"v_mfma_f32_16x16x32_bf16 a[28:31], v[216:219], v[44:47], a[28:31]\n"
"ds_read_b64_tr_b16 v[200:201], v251 offset:20608\n"
"v_mfma_f32_16x16x32_bf16 a[28:31], v[220:223], v[60:63], a[28:31]\n"
"v_mfma_f32_16x16x32_bf16 a[32:35], v[176:179], v[32:35], a[32:35]\n"
"ds_read_b64_tr_b16 v[202:203], v251 offset:25728\n"
"s_waitcnt lgkmcnt(3)\n"
"v_mfma_f32_16x16x32_bf16 a[32:35], v[180:183], v[48:51], a[32:35]\n"
"buffer_load_dwordx4 a[204:207], v249, s[72:75], s89 offen offset:3072 sc1\n"
"s_add_u32 s89, s89, 4096\n"
"v_mfma_f32_16x16x32_bf16 a[36:39], v[176:179], v[36:39], a[36:39]\n"
"ds_read_b64_tr_b16 v[204:205], v251 offset:30848\n"
"v_mfma_f32_16x16x32_bf16 a[36:39], v[180:183], v[52:55], a[36:39]\n"
"v_mfma_f32_16x16x32_bf16 a[40:43], v[176:179], v[40:43], a[40:43]\n"
"ds_read_b64_tr_b16 v[206:207], v251 offset:35968\n"
"v_mfma_f32_16x16x32_bf16 a[40:43], v[180:183], v[56:59], a[40:43]\n"
"buffer_load_dwordx4 a[208:211], v249, s[72:75], s89 offen offset:0 sc1\n"
"v_mfma_f32_16x16x32_bf16 a[44:47], v[176:179], v[44:47], a[44:47]\n"
"ds_read_b64_tr_b16 v[208:209], v250 offset:20672\n"
"v_mfma_f32_16x16x32_bf16 a[44:47], v[180:183], v[60:63], a[44:47]\n"
"v_mfma_f32_16x16x32_bf16 a[48:51], v[184:187], v[32:35], a[48:51]\n"
"ds_read_b64_tr_b16 v[210:211], v250 offset:25792\n"
"v_mfma_f32_16x16x32_bf16 a[48:51], v[188:191], v[48:51], a[48:51]\n"
"buffer_load_dwordx4 a[212:215], v249, s[72:75], s89 offen offset:1024 sc1\n"
"v_mfma_f32_16x16x32_bf16 a[52:55], v[184:187], v[36:39], a[52:55]\n"
"ds_read_b64_tr_b16 v[212:213], v250 offset:30912\n"
"v_mfma_f32_16x16x32_bf16 a[52:55], v[188:191], v[52:55], a[52:55]\n"
"v_mfma_f32_16x16x32_bf16 a[56:59], v[184:187], v[40:43], a[56:59]\n"
"ds_read_b64_tr_b16 v[214:215], v250 offset:36032\n"
"v_mfma_f32_16x16x32_bf16 a[56:59], v[188:191], v[56:59], a[56:59]\n"
"buffer_load_dwordx4 a[216:219], v249, s[72:75], s89 offen offset:2048 sc1\n"
"v_mfma_f32_16x16x32_bf16 a[60:63], v[184:187], v[44:47], a[60:63]\n"
"ds_read_b64_tr_b16 v[216:217], v251 offset:20672\n"
"v_mfma_f32_16x16x32_bf16 a[60:63], v[188:191], v[60:63], a[60:63]\n"
"v_mfma_f32_16x16x32_bf16 a[64:67], v[192:195], v[32:35], a[64:67]\n"
"ds_read_b64_tr_b16 v[218:219], v251 offset:25792\n"
"s_waitcnt lgkmcnt(3)\n"
"v_mfma_f32_16x16x32_bf16 a[64:67], v[196:199], v[48:51], a[64:67]\n"
"buffer_load_dwordx4 a[220:223], v249, s[72:75], s89 offen offset:3072 sc1\n"
"s_add_u32 s89, s89, 4096\n"
"v_mfma_f32_16x16x32_bf16 a[68:71], v[192:195], v[36:39], a[68:71]\n"
"ds_read_b64_tr_b16 v[220:221], v251 offset:30912\n"
"v_mfma_f32_16x16x32_bf16 a[68:71], v[196:199], v[52:55], a[68:71]\n"
"v_mfma_f32_16x16x32_bf16 a[72:75], v[192:195], v[40:43], a[72:75]\n"
"ds_read_b64_tr_b16 v[222:223], v251 offset:36032\n"
"v_mfma_f32_16x16x32_bf16 a[72:75], v[196:199], v[56:59], a[72:75]\n"
"buffer_load_dwordx4 v245, s[64:67], s79 offen offset:1024 lds\n"
"v_mfma_f32_16x16x32_bf16 a[76:79], v[192:195], v[44:47], a[76:79]\n"
"ds_read_b64_tr_b16 v[176:177], v250 offset:20736\n"
"v_mfma_f32_16x16x32_bf16 a[76:79], v[196:199], v[60:63], a[76:79]\n"
"v_mfma_f32_16x16x32_bf16 a[80:83], v[200:203], v[32:35], a[80:83]\n"
"ds_read_b64_tr_b16 v[178:179], v250 offset:25856\n"
"v_mfma_f32_16x16x32_bf16 a[80:83], v[204:207], v[48:51], a[80:83]\n"
"buffer_load_dwordx4 v246, s[64:67], s79 offen offset:2048 lds\n"
"v_mfma_f32_16x16x32_bf16 a[84:87], v[200:203], v[36:39], a[84:87]\n"
"ds_read_b64_tr_b16 v[180:181], v250 offset:30976\n"
"v_mfma_f32_16x16x32_bf16 a[84:87], v[204:207], v[52:55], a[84:87]\n"
"v_mfma_f32_16x16x32_bf16 a[88:91], v[200:203], v[40:43], a[88:91]\n"
"ds_read_b64_tr_b16 v[182:183], v250 offset:36096\n"
"v_mfma_f32_16x16x32_bf16 a[88:91], v[204:207], v[56:59], a[88:91]\n"
"buffer_load_dwordx4 v247, s[64:67], s79 offen offset:3072 lds\n"
"v_mfma_f32_16x16x32_bf16 a[92:95], v[200:203], v[44:47], a[92:95]\n"
"ds_read_b64_tr_b16 v[184:185], v251 offset:20736\n"
"v_mfma_f32_16x16x32_bf16 a[92:95], v[204:207], v[60:63], a[92:95]\n"
"s_barrier\n"
"v_mfma_f32_16x16x32_bf16 a[96:99], v[208:211], v[32:35], a[96:99]\n"
"ds_read_b64_tr_b16 v[186:187], v251 offset:25856\n"
"s_add_u32 m0, s86, 45056\n"
"s_waitcnt lgkmcnt(3)\n"
"v_mfma_f32_16x16x32_bf16 a[96:99], v[212:215], v[48:51], a[96:99]\n"
"buffer_load_dwordx4 v248, s[64:67], s79 offen lds\n"
"s_add_u32 s79, s79, 81920\n"
"v_mfma_f32_16x16x32_bf16 a[100:103], v[208:211], v[36:39], a[100:103]\n"
"ds_read_b64_tr_b16 v[188:189], v251 offset:30976\n"
"v_mfma_f32_16x16x32_bf16 a[100:103], v[212:215], v[52:55], a[100:103]\n"
"v_mfma_f32_16x16x32_bf16 a[104:107], v[208:211], v[40:43], a[104:107]\n"
"ds_read_b64_tr_b16 v[190:191], v251 offset:36096\n"
"v_mfma_f32_16x16x32_bf16 a[104:107], v[212:215], v[56:59], a[104:107]\n"
"buffer_load_dwordx4 a[224:227], v249, s[72:75], s89 offen offset:0 sc1\n"
"v_mfma_f32_16x16x32_bf16 a[108:111], v[208:211], v[44:47], a[108:111]\n"
"ds_read_b64_tr_b16 v[192:193], v250 offset:40960\n"
"v_mfma_f32_16x16x32_bf16 a[108:111], v[212:215], v[60:63], a[108:111]\n"
"v_mfma_f32_16x16x32_bf16 a[112:115], v[216:219], v[32:35], a[112:115]\n"
"ds_read_b64_tr_b16 v[194:195], v250 offset:46080\n"
"v_mfma_f32_16x16x32_bf16 a[112:115], v[220:223], v[48:51], a[112:115]\n"
"buffer_load_dwordx4 a[228:231], v249, s[72:75], s89 offen offset:1024 sc1\n"
"v_mfma_f32_16x16x32_bf16 a[116:119], v[216:219], v[36:39], a[116:119]\n"
"ds_read_b64_tr_b16 v[196:197], v250 offset:51200\n"
"v_mfma_f32_16x16x32_bf16 a[116:119], v[220:223], v[52:55], a[116:119]\n"
"v_mfma_f32_16x16x32_bf16 a[120:123], v[216:219], v[40:43], a[120:123]\n"
"ds_read_b64_tr_b16 v[198:199], v250 offset:56320\n"
"v_mfma_f32_16x16x32_bf16 a[120:123], v[220:223], v[56:59], a[120:123]\n"
"buffer_load_dwordx4 a[232:235], v249, s[72:75], s89 offen offset:2048 sc1\n"
"v_mfma_f32_16x16x32_bf16 a[124:127], v[216:219], v[44:47], a[124:127]\n"
"ds_read_b64_tr_b16 v[200:201], v251 offset:40960\n"
"v_mfma_f32_16x16x32_bf16 a[124:127], v[220:223], v[60:63], a[124:127]\n"
"v_mfma_f32_16x16x32_bf16 a[128:131], v[176:179], v[32:35], a[128:131]\n"
"ds_read_b64_tr_b16 v[202:203], v251 offset:46080\n"
"s_waitcnt lgkmcnt(3)\n"
"v_mfma_f32_16x16x32_bf16 a[128:131], v[180:183], v[48:51], a[128:131]\n"
"buffer_load_dwordx4 a[236:239], v249, s[72:75], s89 offen offset:3072 sc1\n"
"s_add_u32 s89, s89, 4096\n"
"v_mfma_f32_16x16x32_bf16 a[132:135], v[176:179], v[36:39], a[132:135]\n"
"ds_read_b64_tr_b16 v[204:205], v251 offset:51200\n"
"v_mfma_f32_16x16x32_bf16 a[132:135], v[180:183], v[52:55], a[132:135]\n"
"v_mfma_f32_16x16x32_bf16 a[136:139], v[176:179], v[40:43], a[136:139]\n"
"ds_read_b64_tr_b16 v[206:207], v251 offset:56320\n"
"v_mfma_f32_16x16x32_bf16 a[136:139], v[180:183], v[56:59], a[136:139]\n"
"buffer_load_dwordx4 a[240:243], v249, s[72:75], s89 offen offset:0 sc1\n"
"v_mfma_f32_16x16x32_bf16 a[140:143], v[176:179], v[44:47], a[140:143]\n"
"ds_read_b64_tr_b16 v[208:209], v250 offset:41024\n"
"v_mfma_f32_16x16x32_bf16 a[140:143], v[180:183], v[60:63], a[140:143]\n"
"v_mfma_f32_16x16x32_bf16 a[144:147], v[184:187], v[32:35], a[144:147]\n"
"ds_read_b64_tr_b16 v[210:211], v250 offset:46144\n"
"v_mfma_f32_16x16x32_bf16 a[144:147], v[188:191], v[48:51], a[144:147]\n"
"buffer_load_dwordx4 a[244:247], v249, s[72:75], s89 offen offset:1024 sc1\n"
"v_mfma_f32_16x16x32_bf16 a[148:151], v[184:187], v[36:39], a[148:151]\n"
"ds_read_b64_tr_b16 v[212:213], v250 offset:51264\n"
"v_mfma_f32_16x16x32_bf16 a[148:151], v[188:191], v[52:55], a[148:151]\n"
"v_mfma_f32_16x16x32_bf16 a[152:155], v[184:187], v[40:43], a[152:155]\n"
"ds_read_b64_tr_b16 v[214:215], v250 offset:56384\n"
"v_mfma_f32_16x16x32_bf16 a[152:155], v[188:191], v[56:59], a[152:155]\n"
"buffer_load_dwordx4 a[248:251], v249, s[72:75], s89 offen offset:2048 sc1\n"
"v_mfma_f32_16x16x32_bf16 a[156:159], v[184:187], v[44:47], a[156:159]\n"
"ds_read_b64_tr_b16 v[216:217], v251 offset:41024\n"
"v_mfma_f32_16x16x32_bf16 a[156:159], v[188:191], v[60:63], a[156:159]\n"
"v_mfma_f32_16x16x32_bf16 a[0:3], v[192:195], v[64:67], a[0:3]\n"
"ds_read_b64_tr_b16 v[218:219], v251 offset:46144\n"
"s_waitcnt lgkmcnt(3)\n"
"v_mfma_f32_16x16x32_bf16 a[0:3], v[196:199], v[80:83], a[0:3]\n"
"buffer_load_dwordx4 a[252:255], v249, s[72:75], s89 offen offset:3072 sc1\n"
"s_add_u32 s89, s89, 4096\n"
"v_mfma_f32_16x16x32_bf16 a[4:7], v[192:195], v[68:71], a[4:7]\n"
"ds_read_b64_tr_b16 v[220:221], v251 offset:51264\n"
"v_mfma_f32_16x16x32_bf16 a[4:7], v[196:199], v[84:87], a[4:7]\n"
"v_mfma_f32_16x16x32_bf16 a[8:11], v[192:195], v[72:75], a[8:11]\n"
"ds_read_b64_tr_b16 v[222:223], v251 offset:56384\n"
"s_cmp_le_u32 s24, 1\n"
"s_cselect_b32 s80, 2, 1\n"
"s_mul_i32 s79, s80, 245760\n"
"s_add_u32 s79, s79, s94\n"
"s_add_u32 m0, s86, 98304\n"
"v_mfma_f32_16x16x32_bf16 a[8:11], v[196:199], v[88:91], a[8:11]\n"
"buffer_load_dwordx4 v244, s[64:67], s79 offen offset:0 lds\n"
"v_mfma_f32_16x16x32_bf16 a[12:15], v[192:195], v[76:79], a[12:15]\n"
"ds_read_b64_tr_b16 v[176:177], v250 offset:41088\n"
"v_mfma_f32_16x16x32_bf16 a[12:15], v[196:199], v[92:95], a[12:15]\n"
"v_mfma_f32_16x16x32_bf16 a[16:19], v[200:203], v[64:67], a[16:19]\n"
"ds_read_b64_tr_b16 v[178:179], v250 offset:46208\n"
"v_mfma_f32_16x16x32_bf16 a[16:19], v[204:207], v[80:83], a[16:19]\n"
"buffer_load_dwordx4 v245, s[64:67], s79 offen offset:1024 lds\n"
"v_mfma_f32_16x16x32_bf16 a[20:23], v[200:203], v[68:71], a[20:23]\n"
"ds_read_b64_tr_b16 v[180:181], v250 offset:51328\n"
"v_mfma_f32_16x16x32_bf16 a[20:23], v[204:207], v[84:87], a[20:23]\n"
"v_mfma_f32_16x16x32_bf16 a[24:27], v[200:203], v[72:75], a[24:27]\n"
"ds_read_b64_tr_b16 v[182:183], v250 offset:56448\n"
"v_mfma_f32_16x16x32_bf16 a[24:27], v[204:207], v[88:91], a[24:27]\n"
"buffer_load_dwordx4 v246, s[64:67], s79 offen offset:2048 lds\n"
"v_mfma_f32_16x16x32_bf16 a[28:31], v[200:203], v[76:79], a[28:31]\n"
"ds_read_b64_tr_b16 v[184:185], v251 offset:41088\n"
"v_mfma_f32_16x16x32_bf16 a[28:31], v[204:207], v[92:95], a[28:31]\n"
"v_mfma_f32_16x16x32_bf16 a[32:35], v[208:211], v[64:67], a[32:35]\n"
"ds_read_b64_tr_b16 v[186:187], v251 offset:46208\n"
"s_waitcnt lgkmcnt(3)\n"
"v_mfma_f32_16x16x32_bf16 a[32:35], v[212:215], v[80:83], a[32:35]\n"
"buffer_load_dwordx4 v247, s[64:67], s79 offen offset:3072 lds\n"
"v_mfma_f32_16x16x32_bf16 a[36:39], v[208:211], v[68:71], a[36:39]\n"
"ds_read_b64_tr_b16 v[188:189], v251 offset:51328\n"
"v_mfma_f32_16x16x32_bf16 a[36:39], v[212:215], v[84:87], a[36:39]\n"
"v_mfma_f32_16x16x32_bf16 a[40:43], v[208:211], v[72:75], a[40:43]\n"
"ds_read_b64_tr_b16 v[190:191], v251 offset:56448\n"
"s_add_u32 m0, s86, 102400\n"
"v_mfma_f32_16x16x32_bf16 a[40:43], v[212:215], v[88:91], a[40:43]\n"
"buffer_load_dwordx4 v248, s[64:67], s79 offen lds\n"
"s_add_u32 s79, s79, 81920\n"
"v_mfma_f32_16x16x32_bf16 a[44:47], v[208:211], v[76:79], a[44:47]\n"
"ds_read_b64_tr_b16 v[192:193], v250 offset:41152\n"
"v_mfma_f32_16x16x32_bf16 a[44:47], v[212:215], v[92:95], a[44:47]\n"
"v_mfma_f32_16x16x32_bf16 a[48:51], v[216:219], v[64:67], a[48:51]\n"
"ds_read_b64_tr_b16 v[194:195], v250 offset:46272\n"
"s_add_u32 s81, s76, 0\n"
"v_mfma_f32_16x16x32_bf16 a[48:51], v[220:223], v[80:83], a[48:51]\n"
"buffer_store_dwordx4 v[96:99], v249, s[72:75], s81 offen offset:0 sc1\n"
"v_mfma_f32_16x16x32_bf16 a[52:55], v[216:219], v[68:71], a[52:55]\n"
"ds_read_b64_tr_b16 v[196:197], v250 offset:51392\n"
"v_mfma_f32_16x16x32_bf16 a[52:55], v[220:223], v[84:87], a[52:55]\n"
"v_mfma_f32_16x16x32_bf16 a[56:59], v[216:219], v[72:75], a[56:59]\n"
"ds_read_b64_tr_b16 v[198:199], v250 offset:56512\n"
"s_add_u32 s81, s76, 5120\n"
"v_mfma_f32_16x16x32_bf16 a[56:59], v[220:223], v[88:91], a[56:59]\n"
"buffer_store_dwordx4 v[100:103], v249, s[72:75], s81 offen offset:0 sc1\n"
"v_mfma_f32_16x16x32_bf16 a[60:63], v[216:219], v[76:79], a[60:63]\n"
"ds_read_b64_tr_b16 v[200:201], v251 offset:41152\n"
"v_mfma_f32_16x16x32_bf16 a[60:63], v[220:223], v[92:95], a[60:63]\n"
"v_mfma_f32_16x16x32_bf16 a[64:67], v[176:179], v[64:67], a[64:67]\n"
"ds_read_b64_tr_b16 v[202:203], v251 offset:46272\n"
"s_add_u32 s81, s76, 0\n"
"s_waitcnt lgkmcnt(3)\n"
"v_mfma_f32_16x16x32_bf16 a[64:67], v[180:183], v[80:83], a[64:67]\n"
"buffer_store_dwordx4 v[112:115], v249, s[72:75], s81 offen offset:1024 sc1\n"
"v_mfma_f32_16x16x32_bf16 a[68:71], v[176:179], v[68:71], a[68:71]\n"
"ds_read_b64_tr_b16 v[204:205], v251 offset:51392\n"
"v_mfma_f32_16x16x32_bf16 a[68:71], v[180:183], v[84:87], a[68:71]\n"
"v_mfma_f32_16x16x32_bf16 a[72:75], v[176:179], v[72:75], a[72:75]\n"
"ds_read_b64_tr_b16 v[206:207], v251 offset:56512\n"
"v_mfma_f32_16x16x32_bf16 a[72:75], v[180:183], v[88:91], a[72:75]\n"
"v_mfma_f32_16x16x32_bf16 a[76:79], v[176:179], v[76:79], a[76:79]\n"
"ds_read_b64_tr_b16 v[208:209], v250 offset:41216\n"
"v_mfma_f32_16x16x32_bf16 a[76:79], v[180:183], v[92:95], a[76:79]\n"
"v_mfma_f32_16x16x32_bf16 a[80:83], v[184:187], v[64:67], a[80:83]\n"
"ds_read_b64_tr_b16 v[210:211], v250 offset:46336\n"
"v_mfma_f32_16x16x32_bf16 a[80:83], v[188:191], v[80:83], a[80:83]\n"
"v_mfma_f32_16x16x32_bf16 a[84:87], v[184:187], v[68:71], a[84:87]\n"
"ds_read_b64_tr_b16 v[212:213], v250 offset:51456\n"
"v_mfma_f32_16x16x32_bf16 a[84:87], v[188:191], v[84:87], a[84:87]\n"
"v_mfma_f32_16x16x32_bf16 a[88:91], v[184:187], v[72:75], a[88:91]\n"
"ds_read_b64_tr_b16 v[214:215], v250 offset:56576\n"
"v_mfma_f32_16x16x32_bf16 a[88:91], v[188:191], v[88:91], a[88:91]\n"
"v_mfma_f32_16x16x32_bf16 a[92:95], v[184:187], v[76:79], a[92:95]\n"
"ds_read_b64_tr_b16 v[216:217], v251 offset:41216\n"
"s_add_u32 s81, s76, 5120\n"
"v_mfma_f32_16x16x32_bf16 a[92:95], v[188:191], v[92:95], a[92:95]\n"
"buffer_store_dwordx4 v[116:119], v249, s[72:75], s81 offen offset:1024 sc1\n"
"s_barrier\n"
"v_mfma_f32_16x16x32_bf16 a[96:99], v[192:195], v[64:67], a[96:99]\n"
"ds_read_b64_tr_b16 v[218:219], v251 offset:46336\n"
"s_waitcnt lgkmcnt(3)\n"
"v_mfma_f32_16x16x32_bf16 a[96:99], v[196:199], v[80:83], a[96:99]\n"
"v_mfma_f32_16x16x32_bf16 a[100:103], v[192:195], v[68:71], a[100:103]\n"
"ds_read_b64_tr_b16 v[220:221], v251 offset:51456\n"
"s_add_u32 m0, s86, 118784\n"
"v_mfma_f32_16x16x32_bf16 a[100:103], v[196:199], v[84:87], a[100:103]\n"
"buffer_load_dwordx4 v244, s[64:67], s79 offen offset:0 lds\n"
"v_mfma_f32_16x16x32_bf16 a[104:107], v[192:195], v[72:75], a[104:107]\n"
"ds_read_b64_tr_b16 v[222:223], v251 offset:56576\n"
"v_mfma_f32_16x16x32_bf16 a[104:107], v[196:199], v[88:91], a[104:107]\n"
"v_mfma_f32_16x16x32_bf16 a[108:111], v[192:195], v[76:79], a[108:111]\n"
"ds_read_b64_tr_b16 v[176:177], v240 offset:0\n"
"v_mfma_f32_16x16x32_bf16 a[108:111], v[196:199], v[92:95], a[108:111]\n"
"buffer_load_dwordx4 v245, s[64:67], s79 offen offset:1024 lds\n"
"v_mfma_f32_16x16x32_bf16 a[112:115], v[200:203], v[64:67], a[112:115]\n"
"ds_read_b64_tr_b16 v[178:179], v240 offset:5120\n"
"v_mfma_f32_16x16x32_bf16 a[112:115], v[204:207], v[80:83], a[112:115]\n"
"v_mfma_f32_16x16x32_bf16 a[116:119], v[200:203], v[68:71], a[116:119]\n"
"ds_read_b64_tr_b16 v[180:181], v240 offset:10240\n"
"v_mfma_f32_16x16x32_bf16 a[116:119], v[204:207], v[84:87], a[116:119]\n"
"buffer_load_dwordx4 v246, s[64:67], s79 offen offset:2048 lds\n"
"v_mfma_f32_16x16x32_bf16 a[120:123], v[200:203], v[72:75], a[120:123]\n"
"ds_read_b64_tr_b16 v[182:183], v240 offset:15360\n"
"v_mfma_f32_16x16x32_bf16 a[120:123], v[204:207], v[88:91], a[120:123]\n"
"v_mfma_f32_16x16x32_bf16 a[124:127], v[200:203], v[76:79], a[124:127]\n"
"ds_read_b64_tr_b16 v[184:185], v241 offset:0\n"
"v_mfma_f32_16x16x32_bf16 a[124:127], v[204:207], v[92:95], a[124:127]\n"
"buffer_load_dwordx4 v247, s[64:67], s79 offen offset:3072 lds\n"
"v_mfma_f32_16x16x32_bf16 a[128:131], v[208:211], v[64:67], a[128:131]\n"
"ds_read_b64_tr_b16 v[186:187], v241 offset:5120\n"
"s_waitcnt lgkmcnt(3)\n"
"v_mfma_f32_16x16x32_bf16 a[128:131], v[212:215], v[80:83], a[128:131]\n"
"v_mfma_f32_16x16x32_bf16 a[132:135], v[208:211], v[68:71], a[132:135]\n"
"ds_read_b64_tr_b16 v[188:189], v241 offset:10240\n"
"s_add_u32 m0, s86, 122880\n"
"v_mfma_f32_16x16x32_bf16 a[132:135], v[212:215], v[84:87], a[132:135]\n"
"buffer_load_dwordx4 v248, s[64:67], s79 offen lds\n"
"s_add_u32 s79, s79, 81920\n"
"v_mfma_f32_16x16x32_bf16 a[136:139], v[208:211], v[72:75], a[136:139]\n"
"ds_read_b64_tr_b16 v[190:191], v241 offset:15360\n"
"v_mfma_f32_16x16x32_bf16 a[136:139], v[212:215], v[88:91], a[136:139]\n"
"v_mfma_f32_16x16x32_bf16 a[140:143], v[208:211], v[76:79], a[140:143]\n"
"ds_read_b64_tr_b16 v[192:193], v240 offset:64\n"
"s_add_u32 s81, s76, 0\n"
"v_mfma_f32_16x16x32_bf16 a[140:143], v[212:215], v[92:95], a[140:143]\n"
"buffer_store_dwordx2 v[128:129], v253, s[72:75], s81 offen offset:2048 sc1\n"
"v_mfma_f32_16x16x32_bf16 a[144:147], v[216:219], v[64:67], a[144:147]\n"
"ds_read_b64_tr_b16 v[194:195], v240 offset:5184\n"
"v_mfma_f32_16x16x32_bf16 a[144:147], v[220:223], v[80:83], a[144:147]\n"
"v_mfma_f32_16x16x32_bf16 a[148:151], v[216:219], v[68:71], a[148:151]\n"
"ds_read_b64_tr_b16 v[196:197], v240 offset:10304\n"
"v_mfma_f32_16x16x32_bf16 a[148:151], v[220:223], v[84:87], a[148:151]\n"
"v_mfma_f32_16x16x32_bf16 a[152:155], v[216:219], v[72:75], a[152:155]\n"
"ds_read_b64_tr_b16 v[198:199], v240 offset:15424\n"
"s_add_u32 s81, s76, 5120\n"
"v_mfma_f32_16x16x32_bf16 a[152:155], v[220:223], v[88:91], a[152:155]\n"
"buffer_store_dwordx2 v[132:133], v253, s[72:75], s81 offen offset:2048 sc1\n"
"v_mfma_f32_16x16x32_bf16 a[156:159], v[216:219], v[76:79], a[156:159]\n"
"ds_read_b64_tr_b16 v[200:201], v241 offset:64\n"
"v_mfma_f32_16x16x32_bf16 a[156:159], v[220:223], v[92:95], a[156:159]\n"
"s_waitcnt vmcnt(43)\n"
"v_mfma_f32_16x16x32_bf16 a[0:3], v[176:179], a[160:163], a[0:3]\n"
"ds_read_b64_tr_b16 v[202:203], v241 offset:5184\n"
"s_waitcnt vmcnt(39) lgkmcnt(3)\n"
"v_mfma_f32_16x16x32_bf16 a[0:3], v[180:183], a[176:179], a[0:3]\n"
"v_mfma_f32_16x16x32_bf16 a[4:7], v[176:179], a[164:167], a[4:7]\n"
"ds_read_b64_tr_b16 v[204:205], v241 offset:10304\n"
"s_waitcnt vmcnt(38)\n"
"v_mfma_f32_16x16x32_bf16 a[4:7], v[180:183], a[180:183], a[4:7]\n"
"v_mfma_f32_16x16x32_bf16 a[8:11], v[176:179], a[168:171], a[8:11]\n"
"ds_read_b64_tr_b16 v[206:207], v241 offset:15424\n"
"s_waitcnt vmcnt(37)\n"
"v_mfma_f32_16x16x32_bf16 a[8:11], v[180:183], a[184:187], a[8:11]\n"
"v_mfma_f32_16x16x32_bf16 a[12:15], v[176:179], a[172:175], a[12:15]\n"
"ds_read_b64_tr_b16 v[208:209], v240 offset:128\n"
"s_waitcnt vmcnt(36)\n"
"v_mfma_f32_16x16x32_bf16 a[12:15], v[180:183], a[188:191], a[12:15]\n"
"v_mfma_f32_16x16x32_bf16 a[16:19], v[184:187], a[160:163], a[16:19]\n"
"ds_read_b64_tr_b16 v[210:211], v240 offset:5248\n"
"v_mfma_f32_16x16x32_bf16 a[16:19], v[188:191], a[176:179], a[16:19]\n"
"v_mfma_f32_16x16x32_bf16 a[20:23], v[184:187], a[164:167], a[20:23]\n"
"ds_read_b64_tr_b16 v[212:213], v240 offset:10368\n"
"v_mfma_f32_16x16x32_bf16 a[20:23], v[188:191], a[180:183], a[20:23]\n"
"v_mfma_f32_16x16x32_bf16 a[24:27], v[184:187], a[168:171], a[24:27]\n"
"ds_read_b64_tr_b16 v[214:215], v240 offset:15488\n"
"s_cmp_le_u32 s24, 1\n"
"s_cselect_b32 s95, 2, 1\n"
"s_mul_i32 s89, s95, 786432\n"
"s_add_u32 s89, s89, s84\n"
"v_mfma_f32_16x16x32_bf16 a[24:27], v[188:191], a[184:187], a[24:27]\n"
"buffer_load_dwordx4 v[0:3], v249, s[72:75], s89 offen offset:0 sc1\n"
"v_mfma_f32_16x16x32_bf16 a[28:31], v[184:187], a[172:175], a[28:31]\n"
"ds_read_b64_tr_b16 v[216:217], v241 offset:128\n"
"v_mfma_f32_16x16x32_bf16 a[28:31], v[188:191], a[188:191], a[28:31]\n"
"v_mfma_f32_16x16x32_bf16 a[32:35], v[192:195], a[160:163], a[32:35]\n"
"ds_read_b64_tr_b16 v[218:219], v241 offset:5248\n"
"s_waitcnt lgkmcnt(3)\n"
"v_mfma_f32_16x16x32_bf16 a[32:35], v[196:199], a[176:179], a[32:35]\n"
"buffer_load_dwordx4 v[4:7], v249, s[72:75], s89 offen offset:1024 sc1\n"
"v_mfma_f32_16x16x32_bf16 a[36:39], v[192:195], a[164:167], a[36:39]\n"
"ds_read_b64_tr_b16 v[220:221], v241 offset:10368\n"
"v_mfma_f32_16x16x32_bf16 a[36:39], v[196:199], a[180:183], a[36:39]\n"
"v_mfma_f32_16x16x32_bf16 a[40:43], v[192:195], a[168:171], a[40:43]\n"
"ds_read_b64_tr_b16 v[222:223], v241 offset:15488\n"
"v_mfma_f32_16x16x32_bf16 a[40:43], v[196:199], a[184:187], a[40:43]\n"
"buffer_load_dwordx4 v[8:11], v249, s[72:75], s89 offen offset:2048 sc1\n"
"v_mfma_f32_16x16x32_bf16 a[44:47], v[192:195], a[172:175], a[44:47]\n"
"ds_read_b64_tr_b16 v[176:177], v240 offset:192\n"
"v_mfma_f32_16x16x32_bf16 a[44:47], v[196:199], a[188:191], a[44:47]\n"
"v_mfma_f32_16x16x32_bf16 a[48:51], v[200:203], a[160:163], a[48:51]\n"
"ds_read_b64_tr_b16 v[178:179], v240 offset:5312\n"
"v_mfma_f32_16x16x32_bf16 a[48:51], v[204:207], a[176:179], a[48:51]\n"
"buffer_load_dwordx4 v[12:15], v249, s[72:75], s89 offen offset:3072 sc1\n"
"s_add_u32 s89, s89, 4096\n"
"v_mfma_f32_16x16x32_bf16 a[52:55], v[200:203], a[164:167], a[52:55]\n"
"ds_read_b64_tr_b16 v[180:181], v240 offset:10432\n"
"v_mfma_f32_16x16x32_bf16 a[52:55], v[204:207], a[180:183], a[52:55]\n"
"v_mfma_f32_16x16x32_bf16 a[56:59], v[200:203], a[168:171], a[56:59]\n"
"ds_read_b64_tr_b16 v[182:183], v240 offset:15552\n"
"v_mfma_f32_16x16x32_bf16 a[56:59], v[204:207], a[184:187], a[56:59]\n"
"buffer_load_dwordx4 v[16:19], v249, s[72:75], s89 offen offset:0 sc1\n"
"v_mfma_f32_16x16x32_bf16 a[60:63], v[200:203], a[172:175], a[60:63]\n"
"ds_read_b64_tr_b16 v[184:185], v241 offset:192\n"
"v_mfma_f32_16x16x32_bf16 a[60:63], v[204:207], a[188:191], a[60:63]\n"
"v_mfma_f32_16x16x32_bf16 a[64:67], v[208:211], a[160:163], a[64:67]\n"
"ds_read_b64_tr_b16 v[186:187], v241 offset:5312\n"
"s_waitcnt lgkmcnt(3)\n"
"v_mfma_f32_16x16x32_bf16 a[64:67], v[212:215], a[176:179], a[64:67]\n"
"buffer_load_dwordx4 v[20:23], v249, s[72:75], s89 offen offset:1024 sc1\n"
"v_mfma_f32_16x16x32_bf16 a[68:71], v[208:211], a[164:167], a[68:71]\n"
"ds_read_b64_tr_b16 v[188:189], v241 offset:10432\n"
"v_mfma_f32_16x16x32_bf16 a[68:71], v[212:215], a[180:183], a[68:71]\n"
"v_mfma_f32_16x16x32_bf16 a[72:75], v[208:211], a[168:171], a[72:75]\n"
"ds_read_b64_tr_b16 v[190:191], v241 offset:15552\n"
"v_mfma_f32_16x16x32_bf16 a[72:75], v[212:215], a[184:187], a[72:75]\n"
"buffer_load_dwordx4 v[24:27], v249, s[72:75], s89 offen offset:2048 sc1\n"
"v_mfma_f32_16x16x32_bf16 a[76:79], v[208:211], a[172:175], a[76:79]\n"
"ds_read_b64_tr_b16 v[192:193], v240 offset:256\n"
"v_mfma_f32_16x16x32_bf16 a[76:79], v[212:215], a[188:191], a[76:79]\n"
"v_mfma_f32_16x16x32_bf16 a[80:83], v[216:219], a[160:163], a[80:83]\n"
"ds_read_b64_tr_b16 v[194:195], v240 offset:5376\n"
"v_mfma_f32_16x16x32_bf16 a[80:83], v[220:223], a[176:179], a[80:83]\n"
"buffer_load_dwordx4 v[28:31], v249, s[72:75], s89 offen offset:3072 sc1\n"
"s_add_u32 s89, s89, 4096\n"
"v_mfma_f32_16x16x32_bf16 a[84:87], v[216:219], a[164:167], a[84:87]\n"
"ds_read_b64_tr_b16 v[196:197], v240 offset:10496\n"
"v_mfma_f32_16x16x32_bf16 a[84:87], v[220:223], a[180:183], a[84:87]\n"
"v_mfma_f32_16x16x32_bf16 a[88:91], v[216:219], a[168:171], a[88:91]\n"
"ds_read_b64_tr_b16 v[198:199], v240 offset:15616\n"
"v_mfma_f32_16x16x32_bf16 a[88:91], v[220:223], a[184:187], a[88:91]\n"
"buffer_load_dwordx4 v[32:35], v249, s[72:75], s89 offen offset:0 sc1\n"
"v_mfma_f32_16x16x32_bf16 a[92:95], v[216:219], a[172:175], a[92:95]\n"
"ds_read_b64_tr_b16 v[200:201], v241 offset:256\n"
"v_mfma_f32_16x16x32_bf16 a[92:95], v[220:223], a[188:191], a[92:95]\n"
"s_barrier\n"
"v_mfma_f32_16x16x32_bf16 a[96:99], v[176:179], a[160:163], a[96:99]\n"
"ds_read_b64_tr_b16 v[202:203], v241 offset:5376\n"
"s_waitcnt lgkmcnt(3)\n"
"v_mfma_f32_16x16x32_bf16 a[96:99], v[180:183], a[176:179], a[96:99]\n"
"buffer_load_dwordx4 v[36:39], v249, s[72:75], s89 offen offset:1024 sc1\n"
"v_mfma_f32_16x16x32_bf16 a[100:103], v[176:179], a[164:167], a[100:103]\n"
"ds_read_b64_tr_b16 v[204:205], v241 offset:10496\n"
"v_mfma_f32_16x16x32_bf16 a[100:103], v[180:183], a[180:183], a[100:103]\n"
"v_mfma_f32_16x16x32_bf16 a[104:107], v[176:179], a[168:171], a[104:107]\n"
"ds_read_b64_tr_b16 v[206:207], v241 offset:15616\n"
"v_mfma_f32_16x16x32_bf16 a[104:107], v[180:183], a[184:187], a[104:107]\n"
"buffer_load_dwordx4 v[40:43], v249, s[72:75], s89 offen offset:2048 sc1\n"
"v_mfma_f32_16x16x32_bf16 a[108:111], v[176:179], a[172:175], a[108:111]\n"
"ds_read_b64_tr_b16 v[208:209], v240 offset:20480\n"
"v_mfma_f32_16x16x32_bf16 a[108:111], v[180:183], a[188:191], a[108:111]\n"
"v_mfma_f32_16x16x32_bf16 a[112:115], v[184:187], a[160:163], a[112:115]\n"
"ds_read_b64_tr_b16 v[210:211], v240 offset:25600\n"
"v_mfma_f32_16x16x32_bf16 a[112:115], v[188:191], a[176:179], a[112:115]\n"
"buffer_load_dwordx4 v[44:47], v249, s[72:75], s89 offen offset:3072 sc1\n"
"s_add_u32 s89, s89, 4096\n"
"v_mfma_f32_16x16x32_bf16 a[116:119], v[184:187], a[164:167], a[116:119]\n"
"ds_read_b64_tr_b16 v[212:213], v240 offset:30720\n"
"v_mfma_f32_16x16x32_bf16 a[116:119], v[188:191], a[180:183], a[116:119]\n"
"v_mfma_f32_16x16x32_bf16 a[120:123], v[184:187], a[168:171], a[120:123]\n"
"ds_read_b64_tr_b16 v[214:215], v240 offset:35840\n"
"v_mfma_f32_16x16x32_bf16 a[120:123], v[188:191], a[184:187], a[120:123]\n"
"buffer_load_dwordx4 v[48:51], v249, s[72:75], s89 offen offset:0 sc1\n"
"v_mfma_f32_16x16x32_bf16 a[124:127], v[184:187], a[172:175], a[124:127]\n"
"ds_read_b64_tr_b16 v[216:217], v241 offset:20480\n"
"v_mfma_f32_16x16x32_bf16 a[124:127], v[188:191], a[188:191], a[124:127]\n"
"v_mfma_f32_16x16x32_bf16 a[128:131], v[192:195], a[160:163], a[128:131]\n"
"ds_read_b64_tr_b16 v[218:219], v241 offset:25600\n"
"s_waitcnt lgkmcnt(3)\n"
"v_mfma_f32_16x16x32_bf16 a[128:131], v[196:199], a[176:179], a[128:131]\n"
"buffer_load_dwordx4 v[52:55], v249, s[72:75], s89 offen offset:1024 sc1\n"
"v_mfma_f32_16x16x32_bf16 a[132:135], v[192:195], a[164:167], a[132:135]\n"
"ds_read_b64_tr_b16 v[220:221], v241 offset:30720\n"
"v_mfma_f32_16x16x32_bf16 a[132:135], v[196:199], a[180:183], a[132:135]\n"
"v_mfma_f32_16x16x32_bf16 a[136:139], v[192:195], a[168:171], a[136:139]\n"
"ds_read_b64_tr_b16 v[222:223], v241 offset:35840\n"
"v_mfma_f32_16x16x32_bf16 a[136:139], v[196:199], a[184:187], a[136:139]\n"
"buffer_load_dwordx4 v[56:59], v249, s[72:75], s89 offen offset:2048 sc1\n"
"v_mfma_f32_16x16x32_bf16 a[140:143], v[192:195], a[172:175], a[140:143]\n"
"ds_read_b64_tr_b16 v[176:177], v240 offset:20544\n"
"v_mfma_f32_16x16x32_bf16 a[140:143], v[196:199], a[188:191], a[140:143]\n"
"v_mfma_f32_16x16x32_bf16 a[144:147], v[200:203], a[160:163], a[144:147]\n"
"ds_read_b64_tr_b16 v[178:179], v240 offset:25664\n"
"v_mfma_f32_16x16x32_bf16 a[144:147], v[204:207], a[176:179], a[144:147]\n"
"buffer_load_dwordx4 v[60:63], v249, s[72:75], s89 offen offset:3072 sc1\n"
"s_add_u32 s89, s89, 4096\n"
"v_mfma_f32_16x16x32_bf16 a[148:151], v[200:203], a[164:167], a[148:151]\n"
"ds_read_b64_tr_b16 v[180:181], v240 offset:30784\n"
"v_mfma_f32_16x16x32_bf16 a[148:151], v[204:207], a[180:183], a[148:151]\n"
"v_mfma_f32_16x16x32_bf16 a[152:155], v[200:203], a[168:171], a[152:155]\n"
"ds_read_b64_tr_b16 v[182:183], v240 offset:35904\n"
"s_add_u32 s81, s76, 2560\n"
"v_mfma_f32_16x16x32_bf16 a[152:155], v[204:207], a[184:187], a[152:155]\n"
"buffer_store_dwordx4 v[136:139], v249, s[72:75], s81 offen offset:0 sc1\n"
"v_mfma_f32_16x16x32_bf16 a[156:159], v[200:203], a[172:175], a[156:159]\n"
"ds_read_b64_tr_b16 v[184:185], v241 offset:20544\n"
"v_mfma_f32_16x16x32_bf16 a[156:159], v[204:207], a[188:191], a[156:159]\n"
"s_waitcnt vmcnt(52)\n"
"v_mfma_f32_16x16x32_bf16 a[0:3], v[208:211], a[192:195], a[0:3]\n"
"ds_read_b64_tr_b16 v[186:187], v241 offset:25664\n"
"s_add_u32 s81, s76, 7680\n"
"s_waitcnt vmcnt(48) lgkmcnt(3)\n"
"v_mfma_f32_16x16x32_bf16 a[0:3], v[212:215], a[208:211], a[0:3]\n"
"buffer_store_dwordx4 v[140:143], v249, s[72:75], s81 offen offset:0 sc1\n"
"v_mfma_f32_16x16x32_bf16 a[4:7], v[208:211], a[196:199], a[4:7]\n"
"ds_read_b64_tr_b16 v[188:189], v241 offset:30784\n"
"s_waitcnt vmcnt(48)\n"
"v_mfma_f32_16x16x32_bf16 a[4:7], v[212:215], a[212:215], a[4:7]\n"
"v_mfma_f32_16x16x32_bf16 a[8:11], v[208:211], a[200:203], a[8:11]\n"
"ds_read_b64_tr_b16 v[190:191], v241 offset:35904\n"
"s_add_u32 s81, s76, 2560\n"
"s_waitcnt vmcnt(47)\n"
"v_mfma_f32_16x16x32_bf16 a[8:11], v[212:215], a[216:219], a[8:11]\n"
"buffer_store_dwordx4 v[152:155], v249, s[72:75], s81 offen offset:1024 sc1\n"
"v_mfma_f32_16x16x32_bf16 a[12:15], v[208:211], a[204:207], a[12:15]\n"
"ds_read_b64_tr_b16 v[192:193], v240 offset:20608\n"
"s_waitcnt vmcnt(47)\n"
"v_mfma_f32_16x16x32_bf16 a[12:15], v[212:215], a[220:223], a[12:15]\n"
"v_mfma_f32_16x16x32_bf16 a[16:19], v[216:219], a[192:195], a[16:19]\n"
"ds_read_b64_tr_b16 v[194:195], v240 offset:25728\n"
"s_add_u32 s81, s76, 7680\n"
"v_mfma_f32_16x16x32_bf16 a[16:19], v[220:223], a[208:211], a[16:19]\n"
"buffer_store_dwordx4 v[156:159], v249, s[72:75], s81 offen offset:1024 sc1\n"
"v_mfma_f32_16x16x32_bf16 a[20:23], v[216:219], a[196:199], a[20:23]\n"
"ds_read_b64_tr_b16 v[196:197], v240 offset:30848\n"
"v_mfma_f32_16x16x32_bf16 a[20:23], v[220:223], a[212:215], a[20:23]\n"
"v_mfma_f32_16x16x32_bf16 a[24:27], v[216:219], a[200:203], a[24:27]\n"
"ds_read_b64_tr_b16 v[198:199], v240 offset:35968\n"
"s_add_u32 s81, s76, 2560\n"
"v_mfma_f32_16x16x32_bf16 a[24:27], v[220:223], a[216:219], a[24:27]\n"
"buffer_store_dwordx2 v[168:169], v253, s[72:75], s81 offen offset:2048 sc1\n"
"v_mfma_f32_16x16x32_bf16 a[28:31], v[216:219], a[204:207], a[28:31]\n"
"ds_read_b64_tr_b16 v[200:201], v241 offset:20608\n"
"v_mfma_f32_16x16x32_bf16 a[28:31], v[220:223], a[220:223], a[28:31]\n"
"v_mfma_f32_16x16x32_bf16 a[32:35], v[176:179], a[192:195], a[32:35]\n"
"ds_read_b64_tr_b16 v[202:203], v241 offset:25728\n"
"s_add_u32 s81, s76, 7680\n"
"s_waitcnt lgkmcnt(3)\n"
"v_mfma_f32_16x16x32_bf16 a[32:35], v[180:183], a[208:211], a[32:35]\n"
"buffer_store_dwordx2 v[172:173], v253, s[72:75], s81 offen offset:2048 sc1\n"
"v_mfma_f32_16x16x32_bf16 a[36:39], v[176:179], a[196:199], a[36:39]\n"
"ds_read_b64_tr_b16 v[204:205], v241 offset:30848\n"
"v_mfma_f32_16x16x32_bf16 a[36:39], v[180:183], a[212:215], a[36:39]\n"
"v_mfma_f32_16x16x32_bf16 a[40:43], v[176:179], a[200:203], a[40:43]\n"
"ds_read_b64_tr_b16 v[206:207], v241 offset:35968\n"
"s_add_u32 m0, s86, 139264\n"
"v_mfma_f32_16x16x32_bf16 a[40:43], v[180:183], a[216:219], a[40:43]\n"
"buffer_load_dwordx4 v244, s[64:67], s79 offen offset:0 lds\n"
"v_mfma_f32_16x16x32_bf16 a[44:47], v[176:179], a[204:207], a[44:47]\n"
"ds_read_b64_tr_b16 v[208:209], v240 offset:20672\n"
"v_mfma_f32_16x16x32_bf16 a[44:47], v[180:183], a[220:223], a[44:47]\n"
"v_mfma_f32_16x16x32_bf16 a[48:51], v[184:187], a[192:195], a[48:51]\n"
"ds_read_b64_tr_b16 v[210:211], v240 offset:25792\n"
"v_mfma_f32_16x16x32_bf16 a[48:51], v[188:191], a[208:211], a[48:51]\n"
"buffer_load_dwordx4 v245, s[64:67], s79 offen offset:1024 lds\n"
"v_mfma_f32_16x16x32_bf16 a[52:55], v[184:187], a[196:199], a[52:55]\n"
"ds_read_b64_tr_b16 v[212:213], v240 offset:30912\n"
"v_mfma_f32_16x16x32_bf16 a[52:55], v[188:191], a[212:215], a[52:55]\n"
"v_mfma_f32_16x16x32_bf16 a[56:59], v[184:187], a[200:203], a[56:59]\n"
"ds_read_b64_tr_b16 v[214:215], v240 offset:36032\n"
"v_mfma_f32_16x16x32_bf16 a[56:59], v[188:191], a[216:219], a[56:59]\n"
"buffer_load_dwordx4 v246, s[64:67], s79 offen offset:2048 lds\n"
"v_mfma_f32_16x16x32_bf16 a[60:63], v[184:187], a[204:207], a[60:63]\n"
"ds_read_b64_tr_b16 v[216:217], v241 offset:20672\n"
"v_mfma_f32_16x16x32_bf16 a[60:63], v[188:191], a[220:223], a[60:63]\n"
"v_mfma_f32_16x16x32_bf16 a[64:67], v[192:195], a[192:195], a[64:67]\n"
"ds_read_b64_tr_b16 v[218:219], v241 offset:25792\n"
"s_waitcnt lgkmcnt(3)\n"
"v_mfma_f32_16x16x32_bf16 a[64:67], v[196:199], a[208:211], a[64:67]\n"
"buffer_load_dwordx4 v247, s[64:67], s79 offen offset:3072 lds\n"
"v_mfma_f32_16x16x32_bf16 a[68:71], v[192:195], a[196:199], a[68:71]\n"
"ds_read_b64_tr_b16 v[220:221], v241 offset:30912\n"
"v_mfma_f32_16x16x32_bf16 a[68:71], v[196:199], a[212:215], a[68:71]\n"
"v_mfma_f32_16x16x32_bf16 a[72:75], v[192:195], a[200:203], a[72:75]\n"
"ds_read_b64_tr_b16 v[222:223], v241 offset:36032\n"
"s_add_u32 m0, s86, 143360\n"
"v_mfma_f32_16x16x32_bf16 a[72:75], v[196:199], a[216:219], a[72:75]\n"
"buffer_load_dwordx4 v248, s[64:67], s79 offen lds\n"
"s_add_u32 s79, s79, 81920\n"
"v_mfma_f32_16x16x32_bf16 a[76:79], v[192:195], a[204:207], a[76:79]\n"
"ds_read_b64_tr_b16 v[176:177], v240 offset:20736\n"
"v_mfma_f32_16x16x32_bf16 a[76:79], v[196:199], a[220:223], a[76:79]\n"
"v_mfma_f32_16x16x32_bf16 a[80:83], v[200:203], a[192:195], a[80:83]\n"
"ds_read_b64_tr_b16 v[178:179], v240 offset:25856\n"
"v_mfma_f32_16x16x32_bf16 a[80:83], v[204:207], a[208:211], a[80:83]\n"
"buffer_load_dwordx4 v[64:67], v249, s[72:75], s89 offen offset:0 sc1\n"
"v_mfma_f32_16x16x32_bf16 a[84:87], v[200:203], a[196:199], a[84:87]\n"
"ds_read_b64_tr_b16 v[180:181], v240 offset:30976\n"
"v_mfma_f32_16x16x32_bf16 a[84:87], v[204:207], a[212:215], a[84:87]\n"
"v_mfma_f32_16x16x32_bf16 a[88:91], v[200:203], a[200:203], a[88:91]\n"
"ds_read_b64_tr_b16 v[182:183], v240 offset:36096\n"
"v_mfma_f32_16x16x32_bf16 a[88:91], v[204:207], a[216:219], a[88:91]\n"
"buffer_load_dwordx4 v[68:71], v249, s[72:75], s89 offen offset:1024 sc1\n"
"v_mfma_f32_16x16x32_bf16 a[92:95], v[200:203], a[204:207], a[92:95]\n"
"ds_read_b64_tr_b16 v[184:185], v241 offset:20736\n"
"v_mfma_f32_16x16x32_bf16 a[92:95], v[204:207], a[220:223], a[92:95]\n"
"s_waitcnt vmcnt(7)\n"
"s_barrier\n"
"s_cmp_lg_u32 s22, 0\n"
"s_cbranch_scc1 SIGSKIP5_%=\n"
"s_lshl_b32 s28, s21, 2\n"
"s_add_u32 s28, s28, s24\n"
"s_add_u32 s28, s28, 128\n"
"s_lshl_b32 s28, s28, 8\n"
"v_mov_b32 v255, s28\n"
"s_mov_b64 exec, 1\n"
"global_atomic_add v255, v254, s[54:55]\n"
"s_mov_b64 exec, -1\n"
"SIGSKIP5_%=:\n"
"v_mfma_f32_16x16x32_bf16 a[96:99], v[208:211], a[192:195], a[96:99]\n"
"ds_read_b64_tr_b16 v[186:187], v241 offset:25856\n"
"s_waitcnt lgkmcnt(3)\n"
"v_mfma_f32_16x16x32_bf16 a[96:99], v[212:215], a[208:211], a[96:99]\n"
"buffer_load_dwordx4 v[72:75], v249, s[72:75], s89 offen offset:2048 sc1\n"
"v_mfma_f32_16x16x32_bf16 a[100:103], v[208:211], a[196:199], a[100:103]\n"
"ds_read_b64_tr_b16 v[188:189], v241 offset:30976\n"
"v_mfma_f32_16x16x32_bf16 a[100:103], v[212:215], a[212:215], a[100:103]\n"
"v_mfma_f32_16x16x32_bf16 a[104:107], v[208:211], a[200:203], a[104:107]\n"
"ds_read_b64_tr_b16 v[190:191], v241 offset:36096\n"
"v_mfma_f32_16x16x32_bf16 a[104:107], v[212:215], a[216:219], a[104:107]\n"
"buffer_load_dwordx4 v[76:79], v249, s[72:75], s89 offen offset:3072 sc1\n"
"s_add_u32 s89, s89, 4096\n"
"v_mfma_f32_16x16x32_bf16 a[108:111], v[208:211], a[204:207], a[108:111]\n"
"ds_read_b64_tr_b16 v[192:193], v240 offset:40960\n"
"v_mfma_f32_16x16x32_bf16 a[108:111], v[212:215], a[220:223], a[108:111]\n"
"v_mfma_f32_16x16x32_bf16 a[112:115], v[216:219], a[192:195], a[112:115]\n"
"ds_read_b64_tr_b16 v[194:195], v240 offset:46080\n"
"v_mfma_f32_16x16x32_bf16 a[112:115], v[220:223], a[208:211], a[112:115]\n"
"buffer_load_dwordx4 v[80:83], v249, s[72:75], s89 offen offset:0 sc1\n"
"v_mfma_f32_16x16x32_bf16 a[116:119], v[216:219], a[196:199], a[116:119]\n"
"ds_read_b64_tr_b16 v[196:197], v240 offset:51200\n"
"v_mfma_f32_16x16x32_bf16 a[116:119], v[220:223], a[212:215], a[116:119]\n"
"v_mfma_f32_16x16x32_bf16 a[120:123], v[216:219], a[200:203], a[120:123]\n"
"ds_read_b64_tr_b16 v[198:199], v240 offset:56320\n"
"v_mfma_f32_16x16x32_bf16 a[120:123], v[220:223], a[216:219], a[120:123]\n"
"buffer_load_dwordx4 v[84:87], v249, s[72:75], s89 offen offset:1024 sc1\n"
"v_mfma_f32_16x16x32_bf16 a[124:127], v[216:219], a[204:207], a[124:127]\n"
"ds_read_b64_tr_b16 v[200:201], v241 offset:40960\n"
"v_mfma_f32_16x16x32_bf16 a[124:127], v[220:223], a[220:223], a[124:127]\n"
"v_mfma_f32_16x16x32_bf16 a[128:131], v[176:179], a[192:195], a[128:131]\n"
"ds_read_b64_tr_b16 v[202:203], v241 offset:46080\n"
"s_waitcnt lgkmcnt(3)\n"
"v_mfma_f32_16x16x32_bf16 a[128:131], v[180:183], a[208:211], a[128:131]\n"
"buffer_load_dwordx4 v[88:91], v249, s[72:75], s89 offen offset:2048 sc1\n"
"v_mfma_f32_16x16x32_bf16 a[132:135], v[176:179], a[196:199], a[132:135]\n"
"ds_read_b64_tr_b16 v[204:205], v241 offset:51200\n"
"v_mfma_f32_16x16x32_bf16 a[132:135], v[180:183], a[212:215], a[132:135]\n"
"v_mfma_f32_16x16x32_bf16 a[136:139], v[176:179], a[200:203], a[136:139]\n"
"ds_read_b64_tr_b16 v[206:207], v241 offset:56320\n"
"v_mfma_f32_16x16x32_bf16 a[136:139], v[180:183], a[216:219], a[136:139]\n"
"buffer_load_dwordx4 v[92:95], v249, s[72:75], s89 offen offset:3072 sc1\n"
"s_add_u32 s89, s89, 4096\n"
"v_mfma_f32_16x16x32_bf16 a[140:143], v[176:179], a[204:207], a[140:143]\n"
"ds_read_b64_tr_b16 v[208:209], v240 offset:41024\n"
"v_mfma_f32_16x16x32_bf16 a[140:143], v[180:183], a[220:223], a[140:143]\n"
"v_mfma_f32_16x16x32_bf16 a[144:147], v[184:187], a[192:195], a[144:147]\n"
"ds_read_b64_tr_b16 v[210:211], v240 offset:46144\n"
"s_cmp_le_u32 s24, 2\n"
"s_cselect_b32 s80, 3, 2\n"
"s_mul_i32 s79, s80, 245760\n"
"s_add_u32 s79, s79, s94\n"
"s_add_u32 m0, s86, 0\n"
"v_mfma_f32_16x16x32_bf16 a[144:147], v[188:191], a[208:211], a[144:147]\n"
"buffer_load_dwordx4 v244, s[64:67], s79 offen offset:0 lds\n"
"v_mfma_f32_16x16x32_bf16 a[148:151], v[184:187], a[196:199], a[148:151]\n"
"ds_read_b64_tr_b16 v[212:213], v240 offset:51264\n"
"v_mfma_f32_16x16x32_bf16 a[148:151], v[188:191], a[212:215], a[148:151]\n"
"v_mfma_f32_16x16x32_bf16 a[152:155], v[184:187], a[200:203], a[152:155]\n"
"ds_read_b64_tr_b16 v[214:215], v240 offset:56384\n"
"v_mfma_f32_16x16x32_bf16 a[152:155], v[188:191], a[216:219], a[152:155]\n"
"buffer_load_dwordx4 v245, s[64:67], s79 offen offset:1024 lds\n"
"v_mfma_f32_16x16x32_bf16 a[156:159], v[184:187], a[204:207], a[156:159]\n"
"ds_read_b64_tr_b16 v[216:217], v241 offset:41024\n"
"v_mfma_f32_16x16x32_bf16 a[156:159], v[188:191], a[220:223], a[156:159]\n"
"v_mfma_f32_16x16x32_bf16 a[0:3], v[192:195], a[224:227], a[0:3]\n"
"ds_read_b64_tr_b16 v[218:219], v241 offset:46144\n"
"s_lshl_b32 s28, s21, 2\n"
"s_add_u32 s28, s28, s24\n"
"s_add_u32 s28, s28, 128\n"
"s_lshl_b32 s28, s28, 8\n"
"v_mov_b32 v255, s28\n"
"global_load_dword v252, v255, s[54:55] sc1\n"
"s_waitcnt lgkmcnt(3)\n"
"v_mfma_f32_16x16x32_bf16 a[0:3], v[196:199], a[240:243], a[0:3]\n"
"buffer_load_dwordx4 v246, s[64:67], s79 offen offset:2048 lds\n"
"v_mfma_f32_16x16x32_bf16 a[4:7], v[192:195], a[228:231], a[4:7]\n"
"ds_read_b64_tr_b16 v[220:221], v241 offset:51264\n"
"v_mfma_f32_16x16x32_bf16 a[4:7], v[196:199], a[244:247], a[4:7]\n"
"v_mfma_f32_16x16x32_bf16 a[8:11], v[192:195], a[232:235], a[8:11]\n"
"ds_read_b64_tr_b16 v[222:223], v241 offset:56384\n"
"v_mfma_f32_16x16x32_bf16 a[8:11], v[196:199], a[248:251], a[8:11]\n"
"buffer_load_dwordx4 v247, s[64:67], s79 offen offset:3072 lds\n"
"v_mfma_f32_16x16x32_bf16 a[12:15], v[192:195], a[236:239], a[12:15]\n"
"ds_read_b64_tr_b16 v[176:177], v240 offset:41088\n"
"v_mfma_f32_16x16x32_bf16 a[12:15], v[196:199], a[252:255], a[12:15]\n"
"v_mfma_f32_16x16x32_bf16 a[16:19], v[200:203], a[224:227], a[16:19]\n"
"ds_read_b64_tr_b16 v[178:179], v240 offset:46208\n"
"s_add_u32 m0, s86, 4096\n"
"v_mfma_f32_16x16x32_bf16 a[16:19], v[204:207], a[240:243], a[16:19]\n"
"buffer_load_dwordx4 v248, s[64:67], s79 offen lds\n"
"s_add_u32 s79, s79, 81920\n"
"v_mfma_f32_16x16x32_bf16 a[20:23], v[200:203], a[228:231], a[20:23]\n"
"ds_read_b64_tr_b16 v[180:181], v240 offset:51328\n"
"v_mfma_f32_16x16x32_bf16 a[20:23], v[204:207], a[244:247], a[20:23]\n"
"v_mfma_f32_16x16x32_bf16 a[24:27], v[200:203], a[232:235], a[24:27]\n"
"ds_read_b64_tr_b16 v[182:183], v240 offset:56448\n"
"v_mfma_f32_16x16x32_bf16 a[24:27], v[204:207], a[248:251], a[24:27]\n"
"v_mfma_f32_16x16x32_bf16 a[28:31], v[200:203], a[236:239], a[28:31]\n"
"ds_read_b64_tr_b16 v[184:185], v241 offset:41088\n"
"v_mfma_f32_16x16x32_bf16 a[28:31], v[204:207], a[252:255], a[28:31]\n"
"v_mfma_f32_16x16x32_bf16 a[32:35], v[208:211], a[224:227], a[32:35]\n"
"ds_read_b64_tr_b16 v[186:187], v241 offset:46208\n"
"s_waitcnt lgkmcnt(3)\n"
"v_mfma_f32_16x16x32_bf16 a[32:35], v[212:215], a[240:243], a[32:35]\n"
"v_mfma_f32_16x16x32_bf16 a[36:39], v[208:211], a[228:231], a[36:39]\n"
"ds_read_b64_tr_b16 v[188:189], v241 offset:51328\n"
"v_mfma_f32_16x16x32_bf16 a[36:39], v[212:215], a[244:247], a[36:39]\n"
"v_mfma_f32_16x16x32_bf16 a[40:43], v[208:211], a[232:235], a[40:43]\n"
"ds_read_b64_tr_b16 v[190:191], v241 offset:56448\n"
"v_mfma_f32_16x16x32_bf16 a[40:43], v[212:215], a[248:251], a[40:43]\n"
"v_mfma_f32_16x16x32_bf16 a[44:47], v[208:211], a[236:239], a[44:47]\n"
"ds_read_b64_tr_b16 v[192:193], v240 offset:41152\n"
"v_mfma_f32_16x16x32_bf16 a[44:47], v[212:215], a[252:255], a[44:47]\n"
"v_mfma_f32_16x16x32_bf16 a[48:51], v[216:219], a[224:227], a[48:51]\n"
"ds_read_b64_tr_b16 v[194:195], v240 offset:46272\n"
"v_mfma_f32_16x16x32_bf16 a[48:51], v[220:223], a[240:243], a[48:51]\n"
"v_mfma_f32_16x16x32_bf16 a[52:55], v[216:219], a[228:231], a[52:55]\n"
"ds_read_b64_tr_b16 v[196:197], v240 offset:51392\n"
"v_mfma_f32_16x16x32_bf16 a[52:55], v[220:223], a[244:247], a[52:55]\n"
"v_mfma_f32_16x16x32_bf16 a[56:59], v[216:219], a[232:235], a[56:59]\n"
"ds_read_b64_tr_b16 v[198:199], v240 offset:56512\n"
"v_mfma_f32_16x16x32_bf16 a[56:59], v[220:223], a[248:251], a[56:59]\n"
"v_mfma_f32_16x16x32_bf16 a[60:63], v[216:219], a[236:239], a[60:63]\n"
"ds_read_b64_tr_b16 v[200:201], v241 offset:41152\n"
"v_mfma_f32_16x16x32_bf16 a[60:63], v[220:223], a[252:255], a[60:63]\n"
"v_mfma_f32_16x16x32_bf16 a[64:67], v[176:179], a[224:227], a[64:67]\n"
"ds_read_b64_tr_b16 v[202:203], v241 offset:46272\n"
"s_waitcnt lgkmcnt(3)\n"
"v_mfma_f32_16x16x32_bf16 a[64:67], v[180:183], a[240:243], a[64:67]\n"
"v_mfma_f32_16x16x32_bf16 a[68:71], v[176:179], a[228:231], a[68:71]\n"
"ds_read_b64_tr_b16 v[204:205], v241 offset:51392\n"
"v_mfma_f32_16x16x32_bf16 a[68:71], v[180:183], a[244:247], a[68:71]\n"
"v_mfma_f32_16x16x32_bf16 a[72:75], v[176:179], a[232:235], a[72:75]\n"
"ds_read_b64_tr_b16 v[206:207], v241 offset:56512\n"
"v_mfma_f32_16x16x32_bf16 a[72:75], v[180:183], a[248:251], a[72:75]\n"
"v_mfma_f32_16x16x32_bf16 a[76:79], v[176:179], a[236:239], a[76:79]\n"
"ds_read_b64_tr_b16 v[208:209], v240 offset:41216\n"
"v_mfma_f32_16x16x32_bf16 a[76:79], v[180:183], a[252:255], a[76:79]\n"
"v_mfma_f32_16x16x32_bf16 a[80:83], v[184:187], a[224:227], a[80:83]\n"
"ds_read_b64_tr_b16 v[210:211], v240 offset:46336\n"
"v_mfma_f32_16x16x32_bf16 a[80:83], v[188:191], a[240:243], a[80:83]\n"
"v_mfma_f32_16x16x32_bf16 a[84:87], v[184:187], a[228:231], a[84:87]\n"
"ds_read_b64_tr_b16 v[212:213], v240 offset:51456\n"
"v_mfma_f32_16x16x32_bf16 a[84:87], v[188:191], a[244:247], a[84:87]\n"
"v_mfma_f32_16x16x32_bf16 a[88:91], v[184:187], a[232:235], a[88:91]\n"
"ds_read_b64_tr_b16 v[214:215], v240 offset:56576\n"
"v_mfma_f32_16x16x32_bf16 a[88:91], v[188:191], a[248:251], a[88:91]\n"
"v_mfma_f32_16x16x32_bf16 a[92:95], v[184:187], a[236:239], a[92:95]\n"
"ds_read_b64_tr_b16 v[216:217], v241 offset:41216\n"
"v_mfma_f32_16x16x32_bf16 a[92:95], v[188:191], a[252:255], a[92:95]\n"
"s_barrier\n"
"v_mfma_f32_16x16x32_bf16 a[96:99], v[192:195], a[224:227], a[96:99]\n"
"ds_read_b64_tr_b16 v[218:219], v241 offset:46336\n"
"s_add_u32 m0, s86, 20480\n"
"s_waitcnt lgkmcnt(3)\n"
"v_mfma_f32_16x16x32_bf16 a[96:99], v[196:199], a[240:243], a[96:99]\n"
"buffer_load_dwordx4 v244, s[64:67], s79 offen offset:0 lds\n"
"v_mfma_f32_16x16x32_bf16 a[100:103], v[192:195], a[228:231], a[100:103]\n"
"ds_read_b64_tr_b16 v[220:221], v241 offset:51456\n"
"v_mfma_f32_16x16x32_bf16 a[100:103], v[196:199], a[244:247], a[100:103]\n"
"v_mfma_f32_16x16x32_bf16 a[104:107], v[192:195], a[232:235], a[104:107]\n"
"ds_read_b64_tr_b16 v[222:223], v241 offset:56576\n"
"v_mfma_f32_16x16x32_bf16 a[104:107], v[196:199], a[248:251], a[104:107]\n"
"buffer_load_dwordx4 v245, s[64:67], s79 offen offset:1024 lds\n"
"v_mfma_f32_16x16x32_bf16 a[108:111], v[192:195], a[236:239], a[108:111]\n"
"ds_read_b64_tr_b16 v[176:177], v250 offset:0\n"
"v_mfma_f32_16x16x32_bf16 a[108:111], v[196:199], a[252:255], a[108:111]\n"
"v_mfma_f32_16x16x32_bf16 a[112:115], v[200:203], a[224:227], a[112:115]\n"
"ds_read_b64_tr_b16 v[178:179], v250 offset:5120\n"
"v_mfma_f32_16x16x32_bf16 a[112:115], v[204:207], a[240:243], a[112:115]\n"
"buffer_load_dwordx4 v246, s[64:67], s79 offen offset:2048 lds\n"
"v_mfma_f32_16x16x32_bf16 a[116:119], v[200:203], a[228:231], a[116:119]\n"
"ds_read_b64_tr_b16 v[180:181], v250 offset:10240\n"
"v_mfma_f32_16x16x32_bf16 a[116:119], v[204:207], a[244:247], a[116:119]\n"
"v_mfma_f32_16x16x32_bf16 a[120:123], v[200:203], a[232:235], a[120:123]\n"
"ds_read_b64_tr_b16 v[182:183], v250 offset:15360\n"
"s_waitcnt vmcnt(6)\n"
"v_readfirstlane_b32 s28, v252\n"
"s_nop 1\n"
"s_cmp_ge_u32 s28, 8\n"
"s_cbranch_scc1 POLLOK6_%=\n"
"s_lshl_b32 s28, s21, 2\n"
"s_add_u32 s28, s28, s24\n"
"s_add_u32 s28, s28, 128\n"
"s_lshl_b32 s28, s28, 8\n"
"v_mov_b32 v255, s28\n"
"s_mov_b32 s27, 8000\n"
"SPIN6_%=:\n"
"global_load_dword v252, v255, s[54:55] sc1\n"
"s_waitcnt vmcnt(0)\n"
"v_readfirstlane_b32 s28, v252\n"
"s_nop 1\n"
"s_cmp_ge_u32 s28, 8\n"
"s_cbranch_scc1 POLLOK6_%=\n"
"s_sleep 1\n"
"s_sub_u32 s27, s27, 1\n"
"s_cmp_lg_u32 s27, 0\n"
"s_cbranch_scc1 SPIN6_%=\n"
"POLLOK6_%=:\n"
"v_mfma_f32_16x16x32_bf16 a[120:123], v[204:207], a[248:251], a[120:123]\n"
"buffer_load_dwordx4 v247, s[64:67], s79 offen offset:3072 lds\n"
"v_mfma_f32_16x16x32_bf16 a[124:127], v[200:203], a[236:239], a[124:127]\n"
"ds_read_b64_tr_b16 v[184:185], v251 offset:0\n"
"v_mfma_f32_16x16x32_bf16 a[124:127], v[204:207], a[252:255], a[124:127]\n"
"v_mfma_f32_16x16x32_bf16 a[128:131], v[208:211], a[224:227], a[128:131]\n"
"ds_read_b64_tr_b16 v[186:187], v251 offset:5120\n"
"s_lshl_b32 s78, s21, 2\n"
"s_add_u32 s78, s78, s24\n"
"s_lshl_b32 s78, s78, 3\n"
"s_add_u32 s78, s78, s23\n"
"s_mul_i32 s78, s78, 40960\n"
"s_and_b32 s26, s22, 1\n"
"s_lshl_b32 s26, s26, 1\n"
"s_lshr_b32 s27, s22, 1\n"
"s_add_u32 s26, s26, s27\n"
"s_mul_i32 s26, s26, 2560\n"
"s_add_u32 s78, s78, s26\n"
"s_add_u32 s78, s78, 55115776\n"
"s_add_u32 m0, s86, 24576\n"
"s_waitcnt lgkmcnt(3)\n"
"v_mfma_f32_16x16x32_bf16 a[128:131], v[212:215], a[240:243], a[128:131]\n"
"buffer_load_dwordx4 v248, s[64:67], s79 offen lds\n"
"s_add_u32 s79, s79, 81920\n"
"v_mfma_f32_16x16x32_bf16 a[132:135], v[208:211], a[228:231], a[132:135]\n"
"ds_read_b64_tr_b16 v[188:189], v251 offset:10240\n"
"v_mfma_f32_16x16x32_bf16 a[132:135], v[212:215], a[244:247], a[132:135]\n"
"v_mfma_f32_16x16x32_bf16 a[136:139], v[208:211], a[232:235], a[136:139]\n"
"ds_read_b64_tr_b16 v[190:191], v251 offset:15360\n"
"v_mfma_f32_16x16x32_bf16 a[136:139], v[212:215], a[248:251], a[136:139]\n"
"buffer_load_dwordx4 v[96:99], v249, s[72:75], s78 offen sc1\n"
"v_mfma_f32_16x16x32_bf16 a[140:143], v[208:211], a[236:239], a[140:143]\n"
"ds_read_b64_tr_b16 v[192:193], v250 offset:64\n"
"v_mfma_f32_16x16x32_bf16 a[140:143], v[212:215], a[252:255], a[140:143]\n"
"v_mfma_f32_16x16x32_bf16 a[144:147], v[216:219], a[224:227], a[144:147]\n"
"ds_read_b64_tr_b16 v[194:195], v250 offset:5184\n"
"v_mfma_f32_16x16x32_bf16 a[144:147], v[220:223], a[240:243], a[144:147]\n"
"buffer_load_dwordx4 v[100:103], v249, s[72:75], s78 offen offset:1024 sc1\n"
"v_mfma_f32_16x16x32_bf16 a[148:151], v[216:219], a[228:231], a[148:151]\n"
"ds_read_b64_tr_b16 v[196:197], v250 offset:10304\n"
"v_mfma_f32_16x16x32_bf16 a[148:151], v[220:223], a[244:247], a[148:151]\n"
"v_mfma_f32_16x16x32_bf16 a[152:155], v[216:219], a[232:235], a[152:155]\n"
"ds_read_b64_tr_b16 v[198:199], v250 offset:15424\n"
"v_mfma_f32_16x16x32_bf16 a[152:155], v[220:223], a[248:251], a[152:155]\n"
"buffer_load_dwordx2 v[104:105], v253, s[72:75], s78 offen offset:2048 sc1\n"
"v_mfma_f32_16x16x32_bf16 a[156:159], v[216:219], a[236:239], a[156:159]\n"
"ds_read_b64_tr_b16 v[200:201], v251 offset:64\n"
"v_mfma_f32_16x16x32_bf16 a[156:159], v[220:223], a[252:255], a[156:159]\n"
"v_mfma_f32_16x16x32_bf16 a[0:3], v[176:179], v[0:3], a[0:3]\n"
"ds_read_b64_tr_b16 v[202:203], v251 offset:5184\n"
"s_add_u32 s78, s78, 10240\n"
"s_waitcnt lgkmcnt(3)\n"
"v_mfma_f32_16x16x32_bf16 a[0:3], v[180:183], v[16:19], a[0:3]\n"
"buffer_load_dwordx4 v[106:109], v249, s[72:75], s78 offen sc1\n"
"v_mfma_f32_16x16x32_bf16 a[4:7], v[176:179], v[4:7], a[4:7]\n"
"ds_read_b64_tr_b16 v[204:205], v251 offset:10304\n"
"v_mfma_f32_16x16x32_bf16 a[4:7], v[180:183], v[20:23], a[4:7]\n"
"v_mfma_f32_16x16x32_bf16 a[8:11], v[176:179], v[8:11], a[8:11]\n"
"ds_read_b64_tr_b16 v[206:207], v251 offset:15424\n"
"v_mfma_f32_16x16x32_bf16 a[8:11], v[180:183], v[24:27], a[8:11]\n"
"buffer_load_dwordx4 v[110:113], v249, s[72:75], s78 offen offset:1024 sc1\n"
"v_mfma_f32_16x16x32_bf16 a[12:15], v[176:179], v[12:15], a[12:15]\n"
"ds_read_b64_tr_b16 v[208:209], v250 offset:128\n"
"v_mfma_f32_16x16x32_bf16 a[12:15], v[180:183], v[28:31], a[12:15]\n"
"v_mfma_f32_16x16x32_bf16 a[16:19], v[184:187], v[0:3], a[16:19]\n"
"ds_read_b64_tr_b16 v[210:211], v250 offset:5248\n"
"v_mfma_f32_16x16x32_bf16 a[16:19], v[188:191], v[16:19], a[16:19]\n"
"buffer_load_dwordx2 v[114:115], v253, s[72:75], s78 offen offset:2048 sc1\n"
"v_mfma_f32_16x16x32_bf16 a[20:23], v[184:187], v[4:7], a[20:23]\n"
"ds_read_b64_tr_b16 v[212:213], v250 offset:10368\n"
"v_mfma_f32_16x16x32_bf16 a[20:23], v[188:191], v[20:23], a[20:23]\n"
"v_mfma_f32_16x16x32_bf16 a[24:27], v[184:187], v[8:11], a[24:27]\n"
"ds_read_b64_tr_b16 v[214:215], v250 offset:15488\n"
"s_cmp_le_u32 s24, 2\n"
"s_cselect_b32 s95, 3, 2\n"
"s_mul_i32 s89, s95, 786432\n"
"s_add_u32 s89, s89, s84\n"
"v_mfma_f32_16x16x32_bf16 a[24:27], v[188:191], v[24:27], a[24:27]\n"
"buffer_load_dwordx4 a[160:163], v249, s[72:75], s89 offen offset:0 sc1\n"
"v_mfma_f32_16x16x32_bf16 a[28:31], v[184:187], v[12:15], a[28:31]\n"
"ds_read_b64_tr_b16 v[216:217], v251 offset:128\n"
"v_mfma_f32_16x16x32_bf16 a[28:31], v[188:191], v[28:31], a[28:31]\n"
"v_mfma_f32_16x16x32_bf16 a[32:35], v[192:195], v[0:3], a[32:35]\n"
"ds_read_b64_tr_b16 v[218:219], v251 offset:5248\n"
"s_waitcnt lgkmcnt(3)\n"
"v_mfma_f32_16x16x32_bf16 a[32:35], v[196:199], v[16:19], a[32:35]\n"
"buffer_load_dwordx4 a[164:167], v249, s[72:75], s89 offen offset:1024 sc1\n"
"v_mfma_f32_16x16x32_bf16 a[36:39], v[192:195], v[4:7], a[36:39]\n"
"ds_read_b64_tr_b16 v[220:221], v251 offset:10368\n"
"v_mfma_f32_16x16x32_bf16 a[36:39], v[196:199], v[20:23], a[36:39]\n"
"v_mfma_f32_16x16x32_bf16 a[40:43], v[192:195], v[8:11], a[40:43]\n"
"ds_read_b64_tr_b16 v[222:223], v251 offset:15488\n"
"v_mfma_f32_16x16x32_bf16 a[40:43], v[196:199], v[24:27], a[40:43]\n"
"buffer_load_dwordx4 a[168:171], v249, s[72:75], s89 offen offset:2048 sc1\n"
"v_mfma_f32_16x16x32_bf16 a[44:47], v[192:195], v[12:15], a[44:47]\n"
"ds_read_b64_tr_b16 v[176:177], v250 offset:192\n"
"v_mfma_f32_16x16x32_bf16 a[44:47], v[196:199], v[28:31], a[44:47]\n"
"v_mfma_f32_16x16x32_bf16 a[48:51], v[200:203], v[0:3], a[48:51]\n"
"ds_read_b64_tr_b16 v[178:179], v250 offset:5312\n"
"v_mfma_f32_16x16x32_bf16 a[48:51], v[204:207], v[16:19], a[48:51]\n"
"buffer_load_dwordx4 a[172:175], v249, s[72:75], s89 offen offset:3072 sc1\n"
"s_add_u32 s89, s89, 4096\n"
"v_mfma_f32_16x16x32_bf16 a[52:55], v[200:203], v[4:7], a[52:55]\n"
"ds_read_b64_tr_b16 v[180:181], v250 offset:10432\n"
"v_mfma_f32_16x16x32_bf16 a[52:55], v[204:207], v[20:23], a[52:55]\n"
"v_mfma_f32_16x16x32_bf16 a[56:59], v[200:203], v[8:11], a[56:59]\n"
"ds_read_b64_tr_b16 v[182:183], v250 offset:15552\n"
"v_mfma_f32_16x16x32_bf16 a[56:59], v[204:207], v[24:27], a[56:59]\n"
"buffer_load_dwordx4 a[176:179], v249, s[72:75], s89 offen offset:0 sc1\n"
"v_mfma_f32_16x16x32_bf16 a[60:63], v[200:203], v[12:15], a[60:63]\n"
"ds_read_b64_tr_b16 v[184:185], v251 offset:192\n"
"v_mfma_f32_16x16x32_bf16 a[60:63], v[204:207], v[28:31], a[60:63]\n"
"v_mfma_f32_16x16x32_bf16 a[64:67], v[208:211], v[0:3], a[64:67]\n"
"ds_read_b64_tr_b16 v[186:187], v251 offset:5312\n"
"s_waitcnt lgkmcnt(3)\n"
"v_mfma_f32_16x16x32_bf16 a[64:67], v[212:215], v[16:19], a[64:67]\n"
"buffer_load_dwordx4 a[180:183], v249, s[72:75], s89 offen offset:1024 sc1\n"
"v_mfma_f32_16x16x32_bf16 a[68:71], v[208:211], v[4:7], a[68:71]\n"
"ds_read_b64_tr_b16 v[188:189], v251 offset:10432\n"
"v_mfma_f32_16x16x32_bf16 a[68:71], v[212:215], v[20:23], a[68:71]\n"
"v_mfma_f32_16x16x32_bf16 a[72:75], v[208:211], v[8:11], a[72:75]\n"
"ds_read_b64_tr_b16 v[190:191], v251 offset:15552\n"
"v_mfma_f32_16x16x32_bf16 a[72:75], v[212:215], v[24:27], a[72:75]\n"
"buffer_load_dwordx4 a[184:187], v249, s[72:75], s89 offen offset:2048 sc1\n"
"v_mfma_f32_16x16x32_bf16 a[76:79], v[208:211], v[12:15], a[76:79]\n"
"ds_read_b64_tr_b16 v[192:193], v250 offset:256\n"
"v_mfma_f32_16x16x32_bf16 a[76:79], v[212:215], v[28:31], a[76:79]\n"
"v_mfma_f32_16x16x32_bf16 a[80:83], v[216:219], v[0:3], a[80:83]\n"
"ds_read_b64_tr_b16 v[194:195], v250 offset:5376\n"
"v_mfma_f32_16x16x32_bf16 a[80:83], v[220:223], v[16:19], a[80:83]\n"
"buffer_load_dwordx4 a[188:191], v249, s[72:75], s89 offen offset:3072 sc1\n"
"s_add_u32 s89, s89, 4096\n"
"v_mfma_f32_16x16x32_bf16 a[84:87], v[216:219], v[4:7], a[84:87]\n"
"ds_read_b64_tr_b16 v[196:197], v250 offset:10496\n"
"v_mfma_f32_16x16x32_bf16 a[84:87], v[220:223], v[20:23], a[84:87]\n"
"v_mfma_f32_16x16x32_bf16 a[88:91], v[216:219], v[8:11], a[88:91]\n"
"ds_read_b64_tr_b16 v[198:199], v250 offset:15616\n"
"v_mfma_f32_16x16x32_bf16 a[88:91], v[220:223], v[24:27], a[88:91]\n"
"buffer_load_dwordx4 a[192:195], v249, s[72:75], s89 offen offset:0 sc1\n"
"v_mfma_f32_16x16x32_bf16 a[92:95], v[216:219], v[12:15], a[92:95]\n"
"ds_read_b64_tr_b16 v[200:201], v251 offset:256\n"
"v_mfma_f32_16x16x32_bf16 a[92:95], v[220:223], v[28:31], a[92:95]\n"
"s_barrier\n"
"v_mfma_f32_16x16x32_bf16 a[96:99], v[176:179], v[0:3], a[96:99]\n"
"ds_read_b64_tr_b16 v[202:203], v251 offset:5376\n"
"s_add_u32 m0, s86, 40960\n"
"s_waitcnt lgkmcnt(3)\n"
"v_mfma_f32_16x16x32_bf16 a[96:99], v[180:183], v[16:19], a[96:99]\n"
"buffer_load_dwordx4 v244, s[64:67], s79 offen offset:0 lds\n"
"v_mfma_f32_16x16x32_bf16 a[100:103], v[176:179], v[4:7], a[100:103]\n"
"ds_read_b64_tr_b16 v[204:205], v251 offset:10496\n"
"v_mfma_f32_16x16x32_bf16 a[100:103], v[180:183], v[20:23], a[100:103]\n"
"v_mfma_f32_16x16x32_bf16 a[104:107], v[176:179], v[8:11], a[104:107]\n"
"ds_read_b64_tr_b16 v[206:207], v251 offset:15616\n"
"v_mfma_f32_16x16x32_bf16 a[104:107], v[180:183], v[24:27], a[104:107]\n"
"buffer_load_dwordx4 v245, s[64:67], s79 offen offset:1024 lds\n"
"v_mfma_f32_16x16x32_bf16 a[108:111], v[176:179], v[12:15], a[108:111]\n"
"ds_read_b64_tr_b16 v[208:209], v250 offset:20480\n"
"v_mfma_f32_16x16x32_bf16 a[108:111], v[180:183], v[28:31], a[108:111]\n"
"v_mfma_f32_16x16x32_bf16 a[112:115], v[184:187], v[0:3], a[112:115]\n"
"ds_read_b64_tr_b16 v[210:211], v250 offset:25600\n"
"v_mfma_f32_16x16x32_bf16 a[112:115], v[188:191], v[16:19], a[112:115]\n"
"buffer_load_dwordx4 v246, s[64:67], s79 offen offset:2048 lds\n"
"v_mfma_f32_16x16x32_bf16 a[116:119], v[184:187], v[4:7], a[116:119]\n"
"ds_read_b64_tr_b16 v[212:213], v250 offset:30720\n"
"v_mfma_f32_16x16x32_bf16 a[116:119], v[188:191], v[20:23], a[116:119]\n"
"v_mfma_f32_16x16x32_bf16 a[120:123], v[184:187], v[8:11], a[120:123]\n"
"ds_read_b64_tr_b16 v[214:215], v250 offset:35840\n"
"v_mfma_f32_16x16x32_bf16 a[120:123], v[188:191], v[24:27], a[120:123]\n"
"buffer_load_dwordx4 v247, s[64:67], s79 offen offset:3072 lds\n"
"v_mfma_f32_16x16x32_bf16 a[124:127], v[184:187], v[12:15], a[124:127]\n"
"ds_read_b64_tr_b16 v[216:217], v251 offset:20480\n"
"v_mfma_f32_16x16x32_bf16 a[124:127], v[188:191], v[28:31], a[124:127]\n"
"v_mfma_f32_16x16x32_bf16 a[128:131], v[192:195], v[0:3], a[128:131]\n"
"ds_read_b64_tr_b16 v[218:219], v251 offset:25600\n"
"s_add_u32 m0, s86, 45056\n"
"s_waitcnt lgkmcnt(3)\n"
"v_mfma_f32_16x16x32_bf16 a[128:131], v[196:199], v[16:19], a[128:131]\n"
"buffer_load_dwordx4 v248, s[64:67], s79 offen lds\n"
"s_add_u32 s79, s79, 81920\n"
"v_mfma_f32_16x16x32_bf16 a[132:135], v[192:195], v[4:7], a[132:135]\n"
"ds_read_b64_tr_b16 v[220:221], v251 offset:30720\n"
"v_mfma_f32_16x16x32_bf16 a[132:135], v[196:199], v[20:23], a[132:135]\n"
"v_mfma_f32_16x16x32_bf16 a[136:139], v[192:195], v[8:11], a[136:139]\n"
"ds_read_b64_tr_b16 v[222:223], v251 offset:35840\n"
"v_mfma_f32_16x16x32_bf16 a[136:139], v[196:199], v[24:27], a[136:139]\n"
"buffer_load_dwordx4 a[196:199], v249, s[72:75], s89 offen offset:1024 sc1\n"
"v_mfma_f32_16x16x32_bf16 a[140:143], v[192:195], v[12:15], a[140:143]\n"
"ds_read_b64_tr_b16 v[176:177], v250 offset:20544\n"
"v_mfma_f32_16x16x32_bf16 a[140:143], v[196:199], v[28:31], a[140:143]\n"
"v_mfma_f32_16x16x32_bf16 a[144:147], v[200:203], v[0:3], a[144:147]\n"
"ds_read_b64_tr_b16 v[178:179], v250 offset:25664\n"
"v_mfma_f32_16x16x32_bf16 a[144:147], v[204:207], v[16:19], a[144:147]\n"
"buffer_load_dwordx4 a[200:203], v249, s[72:75], s89 offen offset:2048 sc1\n"
"v_mfma_f32_16x16x32_bf16 a[148:151], v[200:203], v[4:7], a[148:151]\n"
"ds_read_b64_tr_b16 v[180:181], v250 offset:30784\n"
"v_mfma_f32_16x16x32_bf16 a[148:151], v[204:207], v[20:23], a[148:151]\n"
"v_mfma_f32_16x16x32_bf16 a[152:155], v[200:203], v[8:11], a[152:155]\n"
"ds_read_b64_tr_b16 v[182:183], v250 offset:35904\n"
"v_mfma_f32_16x16x32_bf16 a[152:155], v[204:207], v[24:27], a[152:155]\n"
"buffer_load_dwordx4 a[204:207], v249, s[72:75], s89 offen offset:3072 sc1\n"
"s_add_u32 s89, s89, 4096\n"
"v_mfma_f32_16x16x32_bf16 a[156:159], v[200:203], v[12:15], a[156:159]\n"
"ds_read_b64_tr_b16 v[184:185], v251 offset:20544\n"
"v_mfma_f32_16x16x32_bf16 a[156:159], v[204:207], v[28:31], a[156:159]\n"
"v_mfma_f32_16x16x32_bf16 a[0:3], v[208:211], v[32:35], a[0:3]\n"
"ds_read_b64_tr_b16 v[186:187], v251 offset:25664\n"
"s_waitcnt lgkmcnt(3)\n"
"v_mfma_f32_16x16x32_bf16 a[0:3], v[212:215], v[48:51], a[0:3]\n"
"buffer_load_dwordx4 a[208:211], v249, s[72:75], s89 offen offset:0 sc1\n"
"v_mfma_f32_16x16x32_bf16 a[4:7], v[208:211], v[36:39], a[4:7]\n"
"ds_read_b64_tr_b16 v[188:189], v251 offset:30784\n"
"v_mfma_f32_16x16x32_bf16 a[4:7], v[212:215], v[52:55], a[4:7]\n"
"v_mfma_f32_16x16x32_bf16 a[8:11], v[208:211], v[40:43], a[8:11]\n"
"ds_read_b64_tr_b16 v[190:191], v251 offset:35904\n"
"v_mfma_f32_16x16x32_bf16 a[8:11], v[212:215], v[56:59], a[8:11]\n"
"buffer_load_dwordx4 a[212:215], v249, s[72:75], s89 offen offset:1024 sc1\n"
"v_mfma_f32_16x16x32_bf16 a[12:15], v[208:211], v[44:47], a[12:15]\n"
"ds_read_b64_tr_b16 v[192:193], v250 offset:20608\n"
"v_mfma_f32_16x16x32_bf16 a[12:15], v[212:215], v[60:63], a[12:15]\n"
"v_mfma_f32_16x16x32_bf16 a[16:19], v[216:219], v[32:35], a[16:19]\n"
"ds_read_b64_tr_b16 v[194:195], v250 offset:25728\n"
"v_mfma_f32_16x16x32_bf16 a[16:19], v[220:223], v[48:51], a[16:19]\n"
"buffer_load_dwordx4 a[216:219], v249, s[72:75], s89 offen offset:2048 sc1\n"
"v_mfma_f32_16x16x32_bf16 a[20:23], v[216:219], v[36:39], a[20:23]\n"
"ds_read_b64_tr_b16 v[196:197], v250 offset:30848\n"
"v_mfma_f32_16x16x32_bf16 a[20:23], v[220:223], v[52:55], a[20:23]\n"
"v_mfma_f32_16x16x32_bf16 a[24:27], v[216:219], v[40:43], a[24:27]\n"
"ds_read_b64_tr_b16 v[198:199], v250 offset:35968\n"
"v_mfma_f32_16x16x32_bf16 a[24:27], v[220:223], v[56:59], a[24:27]\n"
"buffer_load_dwordx4 a[220:223], v249, s[72:75], s89 offen offset:3072 sc1\n"
"s_add_u32 s89, s89, 4096\n"
"v_mfma_f32_16x16x32_bf16 a[28:31], v[216:219], v[44:47], a[28:31]\n"
"ds_read_b64_tr_b16 v[200:201], v251 offset:20608\n"
"v_mfma_f32_16x16x32_bf16 a[28:31], v[220:223], v[60:63], a[28:31]\n"
"v_mfma_f32_16x16x32_bf16 a[32:35], v[176:179], v[32:35], a[32:35]\n"
"ds_read_b64_tr_b16 v[202:203], v251 offset:25728\n"
"s_waitcnt lgkmcnt(3)\n"
"v_mfma_f32_16x16x32_bf16 a[32:35], v[180:183], v[48:51], a[32:35]\n"
"buffer_load_dwordx4 a[224:227], v249, s[72:75], s89 offen offset:0 sc1\n"
"v_mfma_f32_16x16x32_bf16 a[36:39], v[176:179], v[36:39], a[36:39]\n"
"ds_read_b64_tr_b16 v[204:205], v251 offset:30848\n"
"v_mfma_f32_16x16x32_bf16 a[36:39], v[180:183], v[52:55], a[36:39]\n"
"v_mfma_f32_16x16x32_bf16 a[40:43], v[176:179], v[40:43], a[40:43]\n"
"ds_read_b64_tr_b16 v[206:207], v251 offset:35968\n"
"v_mfma_f32_16x16x32_bf16 a[40:43], v[180:183], v[56:59], a[40:43]\n"
"buffer_load_dwordx4 a[228:231], v249, s[72:75], s89 offen offset:1024 sc1\n"
"v_mfma_f32_16x16x32_bf16 a[44:47], v[176:179], v[44:47], a[44:47]\n"
"ds_read_b64_tr_b16 v[208:209], v250 offset:20672\n"
"v_mfma_f32_16x16x32_bf16 a[44:47], v[180:183], v[60:63], a[44:47]\n"
"v_mfma_f32_16x16x32_bf16 a[48:51], v[184:187], v[32:35], a[48:51]\n"
"ds_read_b64_tr_b16 v[210:211], v250 offset:25792\n"
"v_mfma_f32_16x16x32_bf16 a[48:51], v[188:191], v[48:51], a[48:51]\n"
"buffer_load_dwordx4 a[232:235], v249, s[72:75], s89 offen offset:2048 sc1\n"
"v_mfma_f32_16x16x32_bf16 a[52:55], v[184:187], v[36:39], a[52:55]\n"
"ds_read_b64_tr_b16 v[212:213], v250 offset:30912\n"
"v_mfma_f32_16x16x32_bf16 a[52:55], v[188:191], v[52:55], a[52:55]\n"
"v_mfma_f32_16x16x32_bf16 a[56:59], v[184:187], v[40:43], a[56:59]\n"
"ds_read_b64_tr_b16 v[214:215], v250 offset:36032\n"
"v_mfma_f32_16x16x32_bf16 a[56:59], v[188:191], v[56:59], a[56:59]\n"
"buffer_load_dwordx4 a[236:239], v249, s[72:75], s89 offen offset:3072 sc1\n"
"s_add_u32 s89, s89, 4096\n"
"v_mfma_f32_16x16x32_bf16 a[60:63], v[184:187], v[44:47], a[60:63]\n"
"ds_read_b64_tr_b16 v[216:217], v251 offset:20672\n"
"v_mfma_f32_16x16x32_bf16 a[60:63], v[188:191], v[60:63], a[60:63]\n"
"v_mfma_f32_16x16x32_bf16 a[64:67], v[192:195], v[32:35], a[64:67]\n"
"ds_read_b64_tr_b16 v[218:219], v251 offset:25792\n"
"s_waitcnt lgkmcnt(3)\n"
"v_mfma_f32_16x16x32_bf16 a[64:67], v[196:199], v[48:51], a[64:67]\n"
"buffer_load_dwordx4 a[240:243], v249, s[72:75], s89 offen offset:0 sc1\n"
"v_mfma_f32_16x16x32_bf16 a[68:71], v[192:195], v[36:39], a[68:71]\n"
"ds_read_b64_tr_b16 v[220:221], v251 offset:30912\n"
"v_mfma_f32_16x16x32_bf16 a[68:71], v[196:199], v[52:55], a[68:71]\n"
"v_mfma_f32_16x16x32_bf16 a[72:75], v[192:195], v[40:43], a[72:75]\n"
"ds_read_b64_tr_b16 v[222:223], v251 offset:36032\n"
"v_mfma_f32_16x16x32_bf16 a[72:75], v[196:199], v[56:59], a[72:75]\n"
"buffer_load_dwordx4 a[244:247], v249, s[72:75], s89 offen offset:1024 sc1\n"
"v_mfma_f32_16x16x32_bf16 a[76:79], v[192:195], v[44:47], a[76:79]\n"
"ds_read_b64_tr_b16 v[176:177], v250 offset:20736\n"
"v_mfma_f32_16x16x32_bf16 a[76:79], v[196:199], v[60:63], a[76:79]\n"
"v_mfma_f32_16x16x32_bf16 a[80:83], v[200:203], v[32:35], a[80:83]\n"
"ds_read_b64_tr_b16 v[178:179], v250 offset:25856\n"
"v_mfma_f32_16x16x32_bf16 a[80:83], v[204:207], v[48:51], a[80:83]\n"
"buffer_load_dwordx4 a[248:251], v249, s[72:75], s89 offen offset:2048 sc1\n"
"v_mfma_f32_16x16x32_bf16 a[84:87], v[200:203], v[36:39], a[84:87]\n"
"ds_read_b64_tr_b16 v[180:181], v250 offset:30976\n"
"v_mfma_f32_16x16x32_bf16 a[84:87], v[204:207], v[52:55], a[84:87]\n"
"v_mfma_f32_16x16x32_bf16 a[88:91], v[200:203], v[40:43], a[88:91]\n"
"ds_read_b64_tr_b16 v[182:183], v250 offset:36096\n"
"v_mfma_f32_16x16x32_bf16 a[88:91], v[204:207], v[56:59], a[88:91]\n"
"buffer_load_dwordx4 a[252:255], v249, s[72:75], s89 offen offset:3072 sc1\n"
"s_add_u32 s89, s89, 4096\n"
"v_mfma_f32_16x16x32_bf16 a[92:95], v[200:203], v[44:47], a[92:95]\n"
"ds_read_b64_tr_b16 v[184:185], v251 offset:20736\n"
"v_mfma_f32_16x16x32_bf16 a[92:95], v[204:207], v[60:63], a[92:95]\n"
"s_barrier\n"
"v_mfma_f32_16x16x32_bf16 a[96:99], v[208:211], v[32:35], a[96:99]\n"
"ds_read_b64_tr_b16 v[186:187], v251 offset:25856\n"
"s_add_u32 s78, s78, 10240\n"
"s_waitcnt lgkmcnt(3)\n"
"v_mfma_f32_16x16x32_bf16 a[96:99], v[212:215], v[48:51], a[96:99]\n"
"buffer_load_dwordx4 v[116:119], v249, s[72:75], s78 offen sc1\n"
"v_mfma_f32_16x16x32_bf16 a[100:103], v[208:211], v[36:39], a[100:103]\n"
"ds_read_b64_tr_b16 v[188:189], v251 offset:30976\n"
"v_mfma_f32_16x16x32_bf16 a[100:103], v[212:215], v[52:55], a[100:103]\n"
"v_mfma_f32_16x16x32_bf16 a[104:107], v[208:211], v[40:43], a[104:107]\n"
"ds_read_b64_tr_b16 v[190:191], v251 offset:36096\n"
"v_mfma_f32_16x16x32_bf16 a[104:107], v[212:215], v[56:59], a[104:107]\n"
"buffer_load_dwordx4 v[120:123], v249, s[72:75], s78 offen offset:1024 sc1\n"
"v_mfma_f32_16x16x32_bf16 a[108:111], v[208:211], v[44:47], a[108:111]\n"
"ds_read_b64_tr_b16 v[192:193], v250 offset:40960\n"
"v_mfma_f32_16x16x32_bf16 a[108:111], v[212:215], v[60:63], a[108:111]\n"
"v_mfma_f32_16x16x32_bf16 a[112:115], v[216:219], v[32:35], a[112:115]\n"
"ds_read_b64_tr_b16 v[194:195], v250 offset:46080\n"
"v_mfma_f32_16x16x32_bf16 a[112:115], v[220:223], v[48:51], a[112:115]\n"
"buffer_load_dwordx2 v[124:125], v253, s[72:75], s78 offen offset:2048 sc1\n"
"v_mfma_f32_16x16x32_bf16 a[116:119], v[216:219], v[36:39], a[116:119]\n"
"ds_read_b64_tr_b16 v[196:197], v250 offset:51200\n"
"v_mfma_f32_16x16x32_bf16 a[116:119], v[220:223], v[52:55], a[116:119]\n"
"v_mfma_f32_16x16x32_bf16 a[120:123], v[216:219], v[40:43], a[120:123]\n"
"ds_read_b64_tr_b16 v[198:199], v250 offset:56320\n"
"s_add_u32 s78, s78, 10240\n"
"v_mfma_f32_16x16x32_bf16 a[120:123], v[220:223], v[56:59], a[120:123]\n"
"buffer_load_dwordx4 v[126:129], v249, s[72:75], s78 offen sc1\n"
"v_mfma_f32_16x16x32_bf16 a[124:127], v[216:219], v[44:47], a[124:127]\n"
"ds_read_b64_tr_b16 v[200:201], v251 offset:40960\n"
"v_mfma_f32_16x16x32_bf16 a[124:127], v[220:223], v[60:63], a[124:127]\n"
"v_mfma_f32_16x16x32_bf16 a[128:131], v[176:179], v[32:35], a[128:131]\n"
"ds_read_b64_tr_b16 v[202:203], v251 offset:46080\n"
"s_waitcnt lgkmcnt(3)\n"
"v_mfma_f32_16x16x32_bf16 a[128:131], v[180:183], v[48:51], a[128:131]\n"
"buffer_load_dwordx4 v[130:133], v249, s[72:75], s78 offen offset:1024 sc1\n"
"v_mfma_f32_16x16x32_bf16 a[132:135], v[176:179], v[36:39], a[132:135]\n"
"ds_read_b64_tr_b16 v[204:205], v251 offset:51200\n"
"v_mfma_f32_16x16x32_bf16 a[132:135], v[180:183], v[52:55], a[132:135]\n"
"v_mfma_f32_16x16x32_bf16 a[136:139], v[176:179], v[40:43], a[136:139]\n"
"ds_read_b64_tr_b16 v[206:207], v251 offset:56320\n"
"v_mfma_f32_16x16x32_bf16 a[136:139], v[180:183], v[56:59], a[136:139]\n"
"buffer_load_dwordx2 v[134:135], v253, s[72:75], s78 offen offset:2048 sc1\n"
"v_mfma_f32_16x16x32_bf16 a[140:143], v[176:179], v[44:47], a[140:143]\n"
"ds_read_b64_tr_b16 v[208:209], v250 offset:41024\n"
"v_mfma_f32_16x16x32_bf16 a[140:143], v[180:183], v[60:63], a[140:143]\n"
"v_mfma_f32_16x16x32_bf16 a[144:147], v[184:187], v[32:35], a[144:147]\n"
"ds_read_b64_tr_b16 v[210:211], v250 offset:46144\n"
"v_mfma_f32_16x16x32_bf16 a[144:147], v[188:191], v[48:51], a[144:147]\n"
"v_mfma_f32_16x16x32_bf16 a[148:151], v[184:187], v[36:39], a[148:151]\n"
"ds_read_b64_tr_b16 v[212:213], v250 offset:51264\n"
"v_mfma_f32_16x16x32_bf16 a[148:151], v[188:191], v[52:55], a[148:151]\n"
"v_mfma_f32_16x16x32_bf16 a[152:155], v[184:187], v[40:43], a[152:155]\n"
"ds_read_b64_tr_b16 v[214:215], v250 offset:56384\n"
"v_mfma_f32_16x16x32_bf16 a[152:155], v[188:191], v[56:59], a[152:155]\n"
"v_mfma_f32_16x16x32_bf16 a[156:159], v[184:187], v[44:47], a[156:159]\n"
"ds_read_b64_tr_b16 v[216:217], v251 offset:41024\n"
"v_mfma_f32_16x16x32_bf16 a[156:159], v[188:191], v[60:63], a[156:159]\n"
"v_mfma_f32_16x16x32_bf16 a[0:3], v[192:195], v[64:67], a[0:3]\n"
"ds_read_b64_tr_b16 v[218:219], v251 offset:46144\n"
"s_waitcnt lgkmcnt(3)\n"
"v_mfma_f32_16x16x32_bf16 a[0:3], v[196:199], v[80:83], a[0:3]\n"
"v_mfma_f32_16x16x32_bf16 a[4:7], v[192:195], v[68:71], a[4:7]\n"
"ds_read_b64_tr_b16 v[220:221], v251 offset:51264\n"
"v_mfma_f32_16x16x32_bf16 a[4:7], v[196:199], v[84:87], a[4:7]\n"
"v_mfma_f32_16x16x32_bf16 a[8:11], v[192:195], v[72:75], a[8:11]\n"
"ds_read_b64_tr_b16 v[222:223], v251 offset:56384\n"
"v_mfma_f32_16x16x32_bf16 a[8:11], v[196:199], v[88:91], a[8:11]\n"
"v_mfma_f32_16x16x32_bf16 a[12:15], v[192:195], v[76:79], a[12:15]\n"
"ds_read_b64_tr_b16 v[176:177], v250 offset:41088\n"
"v_mfma_f32_16x16x32_bf16 a[12:15], v[196:199], v[92:95], a[12:15]\n"
"v_mfma_f32_16x16x32_bf16 a[16:19], v[200:203], v[64:67], a[16:19]\n"
"ds_read_b64_tr_b16 v[178:179], v250 offset:46208\n"
"v_mfma_f32_16x16x32_bf16 a[16:19], v[204:207], v[80:83], a[16:19]\n"
"v_mfma_f32_16x16x32_bf16 a[20:23], v[200:203], v[68:71], a[20:23]\n"
"ds_read_b64_tr_b16 v[180:181], v250 offset:51328\n"
"v_mfma_f32_16x16x32_bf16 a[20:23], v[204:207], v[84:87], a[20:23]\n"
"v_mfma_f32_16x16x32_bf16 a[24:27], v[200:203], v[72:75], a[24:27]\n"
"ds_read_b64_tr_b16 v[182:183], v250 offset:56448\n"
"v_mfma_f32_16x16x32_bf16 a[24:27], v[204:207], v[88:91], a[24:27]\n"
"v_mfma_f32_16x16x32_bf16 a[28:31], v[200:203], v[76:79], a[28:31]\n"
"ds_read_b64_tr_b16 v[184:185], v251 offset:41088\n"
"v_mfma_f32_16x16x32_bf16 a[28:31], v[204:207], v[92:95], a[28:31]\n"
"v_mfma_f32_16x16x32_bf16 a[32:35], v[208:211], v[64:67], a[32:35]\n"
"ds_read_b64_tr_b16 v[186:187], v251 offset:46208\n"
"s_waitcnt lgkmcnt(3)\n"
"v_mfma_f32_16x16x32_bf16 a[32:35], v[212:215], v[80:83], a[32:35]\n"
"v_mfma_f32_16x16x32_bf16 a[36:39], v[208:211], v[68:71], a[36:39]\n"
"ds_read_b64_tr_b16 v[188:189], v251 offset:51328\n"
"v_mfma_f32_16x16x32_bf16 a[36:39], v[212:215], v[84:87], a[36:39]\n"
"v_mfma_f32_16x16x32_bf16 a[40:43], v[208:211], v[72:75], a[40:43]\n"
"ds_read_b64_tr_b16 v[190:191], v251 offset:56448\n"
"v_mfma_f32_16x16x32_bf16 a[40:43], v[212:215], v[88:91], a[40:43]\n"
"v_mfma_f32_16x16x32_bf16 a[44:47], v[208:211], v[76:79], a[44:47]\n"
"ds_read_b64_tr_b16 v[192:193], v250 offset:41152\n"
"v_mfma_f32_16x16x32_bf16 a[44:47], v[212:215], v[92:95], a[44:47]\n"
"v_mfma_f32_16x16x32_bf16 a[48:51], v[216:219], v[64:67], a[48:51]\n"
"ds_read_b64_tr_b16 v[194:195], v250 offset:46272\n"
"v_mfma_f32_16x16x32_bf16 a[48:51], v[220:223], v[80:83], a[48:51]\n"
"v_mfma_f32_16x16x32_bf16 a[52:55], v[216:219], v[68:71], a[52:55]\n"
"ds_read_b64_tr_b16 v[196:197], v250 offset:51392\n"
"v_mfma_f32_16x16x32_bf16 a[52:55], v[220:223], v[84:87], a[52:55]\n"
"v_mfma_f32_16x16x32_bf16 a[56:59], v[216:219], v[72:75], a[56:59]\n"
"ds_read_b64_tr_b16 v[198:199], v250 offset:56512\n"
"v_mfma_f32_16x16x32_bf16 a[56:59], v[220:223], v[88:91], a[56:59]\n"
"v_mfma_f32_16x16x32_bf16 a[60:63], v[216:219], v[76:79], a[60:63]\n"
"ds_read_b64_tr_b16 v[200:201], v251 offset:41152\n"
"v_mfma_f32_16x16x32_bf16 a[60:63], v[220:223], v[92:95], a[60:63]\n"
"v_mfma_f32_16x16x32_bf16 a[64:67], v[176:179], v[64:67], a[64:67]\n"
"ds_read_b64_tr_b16 v[202:203], v251 offset:46272\n"
"s_waitcnt lgkmcnt(3)\n"
"v_mfma_f32_16x16x32_bf16 a[64:67], v[180:183], v[80:83], a[64:67]\n"
"v_mfma_f32_16x16x32_bf16 a[68:71], v[176:179], v[68:71], a[68:71]\n"
"ds_read_b64_tr_b16 v[204:205], v251 offset:51392\n"
"v_mfma_f32_16x16x32_bf16 a[68:71], v[180:183], v[84:87], a[68:71]\n"
"v_mfma_f32_16x16x32_bf16 a[72:75], v[176:179], v[72:75], a[72:75]\n"
"ds_read_b64_tr_b16 v[206:207], v251 offset:56512\n"
"v_mfma_f32_16x16x32_bf16 a[72:75], v[180:183], v[88:91], a[72:75]\n"
"v_mfma_f32_16x16x32_bf16 a[76:79], v[176:179], v[76:79], a[76:79]\n"
"ds_read_b64_tr_b16 v[208:209], v250 offset:41216\n"
"v_mfma_f32_16x16x32_bf16 a[76:79], v[180:183], v[92:95], a[76:79]\n"
"v_mfma_f32_16x16x32_bf16 a[80:83], v[184:187], v[64:67], a[80:83]\n"
"ds_read_b64_tr_b16 v[210:211], v250 offset:46336\n"
"v_mfma_f32_16x16x32_bf16 a[80:83], v[188:191], v[80:83], a[80:83]\n"
"v_mfma_f32_16x16x32_bf16 a[84:87], v[184:187], v[68:71], a[84:87]\n"
"ds_read_b64_tr_b16 v[212:213], v250 offset:51456\n"
"v_mfma_f32_16x16x32_bf16 a[84:87], v[188:191], v[84:87], a[84:87]\n"
"v_mfma_f32_16x16x32_bf16 a[88:91], v[184:187], v[72:75], a[88:91]\n"
"ds_read_b64_tr_b16 v[214:215], v250 offset:56576\n"
"v_mfma_f32_16x16x32_bf16 a[88:91], v[188:191], v[88:91], a[88:91]\n"
"v_mfma_f32_16x16x32_bf16 a[92:95], v[184:187], v[76:79], a[92:95]\n"
"ds_read_b64_tr_b16 v[216:217], v251 offset:41216\n"
"v_mfma_f32_16x16x32_bf16 a[92:95], v[188:191], v[92:95], a[92:95]\n"
"s_waitcnt vmcnt(21)\n"
"s_barrier\n"
"v_mfma_f32_16x16x32_bf16 a[96:99], v[192:195], v[64:67], a[96:99]\n"
"ds_read_b64_tr_b16 v[218:219], v251 offset:46336\n"
"s_waitcnt lgkmcnt(3)\n"
"v_mfma_f32_16x16x32_bf16 a[96:99], v[196:199], v[80:83], a[96:99]\n"
"v_mfma_f32_16x16x32_bf16 a[100:103], v[192:195], v[68:71], a[100:103]\n"
"ds_read_b64_tr_b16 v[220:221], v251 offset:51456\n"
"v_mfma_f32_16x16x32_bf16 a[100:103], v[196:199], v[84:87], a[100:103]\n"
"v_mfma_f32_16x16x32_bf16 a[104:107], v[192:195], v[72:75], a[104:107]\n"
"ds_read_b64_tr_b16 v[222:223], v251 offset:56576\n"
"v_mfma_f32_16x16x32_bf16 a[104:107], v[196:199], v[88:91], a[104:107]\n"
"v_mfma_f32_16x16x32_bf16 a[108:111], v[192:195], v[76:79], a[108:111]\n"
"ds_read_b64_tr_b16 v[176:177], v240 offset:0\n"
"v_mfma_f32_16x16x32_bf16 a[108:111], v[196:199], v[92:95], a[108:111]\n"
"v_mfma_f32_16x16x32_bf16 a[112:115], v[200:203], v[64:67], a[112:115]\n"
"ds_read_b64_tr_b16 v[178:179], v240 offset:5120\n"
"v_mfma_f32_16x16x32_bf16 a[112:115], v[204:207], v[80:83], a[112:115]\n"
"v_mfma_f32_16x16x32_bf16 a[116:119], v[200:203], v[68:71], a[116:119]\n"
"ds_read_b64_tr_b16 v[180:181], v240 offset:10240\n"
"v_mfma_f32_16x16x32_bf16 a[116:119], v[204:207], v[84:87], a[116:119]\n"
"v_mfma_f32_16x16x32_bf16 a[120:123], v[200:203], v[72:75], a[120:123]\n"
"ds_read_b64_tr_b16 v[182:183], v240 offset:15360\n"
"v_mfma_f32_16x16x32_bf16 a[120:123], v[204:207], v[88:91], a[120:123]\n"
"ds_read_b64_tr_b16 v[184:185], v240 offset:20480\n"
"v_mfma_f32_16x16x32_bf16 a[124:127], v[200:203], v[76:79], a[124:127]\n"
"ds_read_b64_tr_b16 v[186:187], v240 offset:25600\n"
"v_mfma_f32_16x16x32_bf16 a[124:127], v[204:207], v[92:95], a[124:127]\n"
"ds_read_b64_tr_b16 v[188:189], v240 offset:30720\n"
"v_mfma_f32_16x16x32_bf16 a[128:131], v[208:211], v[64:67], a[128:131]\n"
"ds_read_b64_tr_b16 v[190:191], v240 offset:35840\n"
"s_waitcnt lgkmcnt(5)\n"
"v_mfma_f32_16x16x32_bf16 a[128:131], v[212:215], v[80:83], a[128:131]\n"
"ds_read_b64_tr_b16 v[192:193], v240 offset:40960\n"
"v_mfma_f32_16x16x32_bf16 a[132:135], v[208:211], v[68:71], a[132:135]\n"
"ds_read_b64_tr_b16 v[194:195], v240 offset:46080\n"
"v_mfma_f32_16x16x32_bf16 a[132:135], v[212:215], v[84:87], a[132:135]\n"
"ds_read_b64_tr_b16 v[196:197], v240 offset:51200\n"
"v_mfma_f32_16x16x32_bf16 a[136:139], v[208:211], v[72:75], a[136:139]\n"
"ds_read_b64_tr_b16 v[198:199], v240 offset:56320\n"
"v_mfma_f32_16x16x32_bf16 a[136:139], v[212:215], v[88:91], a[136:139]\n"
"v_mfma_f32_16x16x32_bf16 a[140:143], v[208:211], v[76:79], a[140:143]\n"
"v_mfma_f32_16x16x32_bf16 a[140:143], v[212:215], v[92:95], a[140:143]\n"
"v_mfma_f32_16x16x32_bf16 a[144:147], v[216:219], v[64:67], a[144:147]\n"
"v_mfma_f32_16x16x32_bf16 a[144:147], v[220:223], v[80:83], a[144:147]\n"
"v_mfma_f32_16x16x32_bf16 a[148:151], v[216:219], v[68:71], a[148:151]\n"
"ds_read_b64_tr_b16 v[200:201], v241 offset:0\n"
"v_mfma_f32_16x16x32_bf16 a[148:151], v[220:223], v[84:87], a[148:151]\n"
"v_mfma_f32_16x16x32_bf16 a[152:155], v[216:219], v[72:75], a[152:155]\n"
"ds_read_b64_tr_b16 v[202:203], v241 offset:5120\n"
"v_mfma_f32_16x16x32_bf16 a[152:155], v[220:223], v[88:91], a[152:155]\n"
"v_mfma_f32_16x16x32_bf16 a[156:159], v[216:219], v[76:79], a[156:159]\n"
"ds_read_b64_tr_b16 v[204:205], v241 offset:10240\n"
"v_mfma_f32_16x16x32_bf16 a[156:159], v[220:223], v[92:95], a[156:159]\n"
"v_mfma_f32_16x16x32_bf16 a[0:3], v[176:179], a[160:163], a[0:3]\n"
"ds_read_b64_tr_b16 v[206:207], v241 offset:15360\n"
"s_waitcnt lgkmcnt(4)\n"
"v_mfma_f32_16x16x32_bf16 a[0:3], v[180:183], a[176:179], a[0:3]\n"
"v_mfma_f32_16x16x32_bf16 a[0:3], v[184:187], a[192:195], a[0:3]\n"
"ds_read_b64_tr_b16 v[208:209], v241 offset:20480\n"
"v_mbcnt_lo_u32_b32 v92, -1, 0\n"
"v_mbcnt_hi_u32_b32 v92, -1, v92\n"
"v_and_b32 v93, 15, v92\n"
"v_mul_u32_u24 v93, 1280, v93\n"
"v_lshrrev_b32 v92, 4, v92\n"
"v_lshl_add_u32 v93, v92, 3, v93\n"
"s_lshl_b32 s25, s21, 8\n"
"s_lshl_b32 s26, s23, 5\n"
"s_add_u32 s25, s25, s26\n"
"s_and_b32 s26, s22, 1\n"
"s_lshl_b32 s26, s26, 4\n"
"s_add_u32 s25, s25, s26\n"
"s_mul_i32 s25, s25, 1280\n"
"s_mul_i32 s26, s24, 320\n"
"s_add_u32 s25, s25, s26\n"
"s_lshr_b32 s26, s22, 1\n"
"s_mul_i32 s26, s26, 160\n"
"s_add_u32 s25, s25, s26\n"
"v_add_u32 v90, s25, v93\n"
"s_waitcnt vmcnt(17)\n"
"v_mfma_f32_16x16x32_bf16 a[0:3], v[188:191], a[208:211], a[0:3]\n"
"s_waitcnt vmcnt(13)\n"
"v_mfma_f32_16x16x32_bf16 a[0:3], v[192:195], a[224:227], a[0:3]\n"
"ds_read_b64_tr_b16 v[210:211], v241 offset:25600\n"
"s_waitcnt vmcnt(9)\n"
"v_mfma_f32_16x16x32_bf16 a[0:3], v[196:199], a[240:243], a[0:3]\n"
"ds_read_b64_tr_b16 v[212:213], v241 offset:30720\n"
"v_mfma_f32_16x16x32_bf16 a[4:7], v[176:179], a[164:167], a[4:7]\n"
"ds_read_b64_tr_b16 v[214:215], v241 offset:35840\n"
"v_mfma_f32_16x16x32_bf16 a[4:7], v[180:183], a[180:183], a[4:7]\n"
"ds_read_b64_tr_b16 v[216:217], v241 offset:40960\n"
"global_load_dwordx2 v[80:81], v90, s[36:37] offset:0\n"
"v_mfma_f32_16x16x32_bf16 a[4:7], v[184:187], a[196:199], a[4:7]\n"
"ds_read_b64_tr_b16 v[218:219], v241 offset:46080\n"
"v_mfma_f32_16x16x32_bf16 a[4:7], v[188:191], a[212:215], a[4:7]\n"
"ds_read_b64_tr_b16 v[220:221], v241 offset:51200\n"
"v_mfma_f32_16x16x32_bf16 a[4:7], v[192:195], a[228:231], a[4:7]\n"
"ds_read_b64_tr_b16 v[222:223], v241 offset:56320\n"
"v_accvgpr_read_b32 v32, a0\n"
"s_waitcnt vmcnt(9)\n"
"v_mfma_f32_16x16x32_bf16 a[4:7], v[196:199], a[244:247], a[4:7]\n"
"global_load_dwordx2 v[82:83], v90, s[36:37] offset:32\n"
"v_mfma_f32_16x16x32_bf16 a[8:11], v[176:179], a[168:171], a[8:11]\n"
"v_accvgpr_read_b32 v33, a1\n"
"v_accvgpr_read_b32 v34, a2\n"
"v_mfma_f32_16x16x32_bf16 a[8:11], v[180:183], a[184:187], a[8:11]\n"
"v_accvgpr_read_b32 v35, a3\n"
"v_cvt_pk_bf16_f32 v0, v32, v33\n"
"v_mfma_f32_16x16x32_bf16 a[8:11], v[184:187], a[200:203], a[8:11]\n"
"v_cvt_pk_bf16_f32 v1, v34, v35\n"
"v_mfma_f32_16x16x32_bf16 a[8:11], v[188:191], a[216:219], a[8:11]\n"
"global_load_dwordx2 v[84:85], v90, s[36:37] offset:64\n"
"v_mfma_f32_16x16x32_bf16 a[8:11], v[192:195], a[232:235], a[8:11]\n"
"v_accvgpr_read_b32 v36, a4\n"
"v_accvgpr_read_b32 v37, a5\n"
"s_waitcnt vmcnt(10)\n"
"v_mfma_f32_16x16x32_bf16 a[8:11], v[196:199], a[248:251], a[8:11]\n"
"v_accvgpr_read_b32 v38, a6\n"
"v_accvgpr_read_b32 v39, a7\n"
"v_mfma_f32_16x16x32_bf16 a[12:15], v[176:179], a[172:175], a[12:15]\n"
"v_cvt_pk_bf16_f32 v4, v36, v37\n"
"v_mfma_f32_16x16x32_bf16 a[12:15], v[180:183], a[188:191], a[12:15]\n"
"global_load_dwordx2 v[86:87], v90, s[36:37] offset:96\n"
"v_mfma_f32_16x16x32_bf16 a[12:15], v[184:187], a[204:207], a[12:15]\n"
"ds_read_b64_tr_b16 v[176:177], v240 offset:64\n"
"v_cvt_pk_bf16_f32 v5, v38, v39\n"
"v_mfma_f32_16x16x32_bf16 a[12:15], v[188:191], a[220:223], a[12:15]\n"
"v_mfma_f32_16x16x32_bf16 a[12:15], v[192:195], a[236:239], a[12:15]\n"
"ds_read_b64_tr_b16 v[178:179], v240 offset:5184\n"
"v_accvgpr_read_b32 v32, a8\n"
"s_waitcnt vmcnt(10)\n"
"v_mfma_f32_16x16x32_bf16 a[12:15], v[196:199], a[252:255], a[12:15]\n"
"global_load_dwordx2 v[88:89], v90, s[36:37] offset:128\n"
"s_waitcnt lgkmcnt(2)\n"
"v_mfma_f32_16x16x32_bf16 a[16:19], v[200:203], a[160:163], a[16:19]\n"
"ds_read_b64_tr_b16 v[180:181], v240 offset:10304\n"
"v_accvgpr_read_b32 v33, a9\n"
"v_mfma_f32_16x16x32_bf16 a[16:19], v[204:207], a[176:179], a[16:19]\n"
"v_accvgpr_read_b32 v34, a10\n"
"v_accvgpr_read_b32 v35, a11\n"
"v_mfma_f32_16x16x32_bf16 a[16:19], v[208:211], a[192:195], a[16:19]\n"
"ds_read_b64_tr_b16 v[182:183], v240 offset:15424\n"
"v_cvt_pk_bf16_f32 v8, v32, v33\n"
"v_mfma_f32_16x16x32_bf16 a[16:19], v[212:215], a[208:211], a[16:19]\n"
"v_cvt_pk_bf16_f32 v9, v34, v35\n"
"v_mfma_f32_16x16x32_bf16 a[16:19], v[216:219], a[224:227], a[16:19]\n"
"ds_read_b64_tr_b16 v[184:185], v240 offset:20544\n"
"v_accvgpr_read_b32 v36, a12\n"
"v_mfma_f32_16x16x32_bf16 a[16:19], v[220:223], a[240:243], a[16:19]\n"
"v_accvgpr_read_b32 v37, a13\n"
"v_accvgpr_read_b32 v38, a14\n"
"v_mfma_f32_16x16x32_bf16 a[20:23], v[200:203], a[164:167], a[20:23]\n"
"ds_read_b64_tr_b16 v[186:187], v240 offset:25664\n"
"v_accvgpr_read_b32 v39, a15\n"
"v_mfma_f32_16x16x32_bf16 a[20:23], v[204:207], a[180:183], a[20:23]\n"
"ds_read_b64_tr_b16 v[188:189], v240 offset:30784\n"
"v_cvt_pk_bf16_f32 v12, v36, v37\n"
"v_mfma_f32_16x16x32_bf16 a[20:23], v[208:211], a[196:199], a[20:23]\n"
"ds_read_b64_tr_b16 v[190:191], v240 offset:35904\n"
"v_cvt_pk_bf16_f32 v13, v38, v39\n"
"v_mfma_f32_16x16x32_bf16 a[20:23], v[212:215], a[212:215], a[20:23]\n"
"ds_read_b64_tr_b16 v[192:193], v240 offset:41024\n"
"v_mfma_f32_16x16x32_bf16 a[20:23], v[216:219], a[228:231], a[20:23]\n"
"ds_read_b64_tr_b16 v[194:195], v240 offset:46144\n"
"v_accvgpr_read_b32 v32, a16\n"
"v_mfma_f32_16x16x32_bf16 a[20:23], v[220:223], a[244:247], a[20:23]\n"
"ds_read_b64_tr_b16 v[196:197], v240 offset:51264\n"
"v_accvgpr_read_b32 v33, a17\n"
"v_mfma_f32_16x16x32_bf16 a[24:27], v[200:203], a[168:171], a[24:27]\n"
"ds_read_b64_tr_b16 v[198:199], v240 offset:56384\n"
"v_accvgpr_read_b32 v34, a18\n"
"v_mfma_f32_16x16x32_bf16 a[24:27], v[204:207], a[184:187], a[24:27]\n"
"v_accvgpr_read_b32 v35, a19\n"
"v_cvt_pk_bf16_f32 v2, v32, v33\n"
"v_mfma_f32_16x16x32_bf16 a[24:27], v[208:211], a[200:203], a[24:27]\n"
"v_cvt_pk_bf16_f32 v3, v34, v35\n"
"v_mfma_f32_16x16x32_bf16 a[24:27], v[212:215], a[216:219], a[24:27]\n"
"s_add_u32 s81, s77, 0\n"
"v_mfma_f32_16x16x32_bf16 a[24:27], v[216:219], a[232:235], a[24:27]\n"
"buffer_store_dwordx4 v[0:3], v249, s[72:75], s81 offen offset:0 sc1\n"
"v_mfma_f32_16x16x32_bf16 a[24:27], v[220:223], a[248:251], a[24:27]\n"
"v_accvgpr_read_b32 v36, a20\n"
"v_accvgpr_read_b32 v37, a21\n"
"v_mfma_f32_16x16x32_bf16 a[28:31], v[200:203], a[172:175], a[28:31]\n"
"v_accvgpr_read_b32 v38, a22\n"
"v_accvgpr_read_b32 v39, a23\n"
"v_mfma_f32_16x16x32_bf16 a[28:31], v[204:207], a[188:191], a[28:31]\n"
"v_cvt_pk_bf16_f32 v6, v36, v37\n"
"v_cvt_pk_bf16_f32 v7, v38, v39\n"
"v_mfma_f32_16x16x32_bf16 a[28:31], v[208:211], a[204:207], a[28:31]\n"
"ds_read_b64_tr_b16 v[200:201], v241 offset:64\n"
"s_add_u32 s81, s77, 5120\n"
"v_mfma_f32_16x16x32_bf16 a[28:31], v[212:215], a[220:223], a[28:31]\n"
"buffer_store_dwordx4 v[4:7], v249, s[72:75], s81 offen offset:0 sc1\n"
"v_mfma_f32_16x16x32_bf16 a[28:31], v[216:219], a[236:239], a[28:31]\n"
"ds_read_b64_tr_b16 v[202:203], v241 offset:5184\n"
"v_accvgpr_read_b32 v32, a24\n"
"v_mfma_f32_16x16x32_bf16 a[28:31], v[220:223], a[252:255], a[28:31]\n"
"v_accvgpr_read_b32 v33, a25\n"
"v_accvgpr_read_b32 v34, a26\n"
"s_waitcnt lgkmcnt(2)\n"
"v_mfma_f32_16x16x32_bf16 a[32:35], v[176:179], a[160:163], a[32:35]\n"
"ds_read_b64_tr_b16 v[204:205], v241 offset:10304\n"
"v_accvgpr_read_b32 v35, a27\n"
"v_mfma_f32_16x16x32_bf16 a[32:35], v[180:183], a[176:179], a[32:35]\n"
"v_cvt_pk_bf16_f32 v10, v32, v33\n"
"v_cvt_pk_bf16_f32 v11, v34, v35\n"
"v_mfma_f32_16x16x32_bf16 a[32:35], v[184:187], a[192:195], a[32:35]\n"
"ds_read_b64_tr_b16 v[206:207], v241 offset:15424\n"
"s_add_u32 s81, s77, 81920\n"
"v_mfma_f32_16x16x32_bf16 a[32:35], v[188:191], a[208:211], a[32:35]\n"
"buffer_store_dwordx4 v[8:11], v249, s[72:75], s81 offen offset:0 sc1\n"
"v_mfma_f32_16x16x32_bf16 a[32:35], v[192:195], a[224:227], a[32:35]\n"
"ds_read_b64_tr_b16 v[208:209], v241 offset:20544\n"
"v_accvgpr_read_b32 v36, a28\n"
"v_mfma_f32_16x16x32_bf16 a[32:35], v[196:199], a[240:243], a[32:35]\n"
"v_accvgpr_read_b32 v37, a29\n"
"v_accvgpr_read_b32 v38, a30\n"
"v_mfma_f32_16x16x32_bf16 a[36:39], v[176:179], a[164:167], a[36:39]\n"
"ds_read_b64_tr_b16 v[210:211], v241 offset:25664\n"
"v_accvgpr_read_b32 v39, a31\n"
"v_mfma_f32_16x16x32_bf16 a[36:39], v[180:183], a[180:183], a[36:39]\n"
"ds_read_b64_tr_b16 v[212:213], v241 offset:30784\n"
"v_cvt_pk_bf16_f32 v14, v36, v37\n"
"v_mfma_f32_16x16x32_bf16 a[36:39], v[184:187], a[196:199], a[36:39]\n"
"ds_read_b64_tr_b16 v[214:215], v241 offset:35904\n"
"v_cvt_pk_bf16_f32 v15, v38, v39\n"
"v_mfma_f32_16x16x32_bf16 a[36:39], v[188:191], a[212:215], a[36:39]\n"
"ds_read_b64_tr_b16 v[216:217], v241 offset:41024\n"
"s_add_u32 s81, s77, 87040\n"
"v_mfma_f32_16x16x32_bf16 a[36:39], v[192:195], a[228:231], a[36:39]\n"
"ds_read_b64_tr_b16 v[218:219], v241 offset:46144\n"
"v_accvgpr_read_b32 v32, a32\n"
"v_mfma_f32_16x16x32_bf16 a[36:39], v[196:199], a[244:247], a[36:39]\n"
"ds_read_b64_tr_b16 v[220:221], v241 offset:51264\n"
"v_accvgpr_read_b32 v33, a33\n"
"v_mfma_f32_16x16x32_bf16 a[40:43], v[176:179], a[168:171], a[40:43]\n"
"ds_read_b64_tr_b16 v[222:223], v241 offset:56384\n"
"v_accvgpr_read_b32 v34, a34\n"
"v_mfma_f32_16x16x32_bf16 a[40:43], v[180:183], a[184:187], a[40:43]\n"
"buffer_store_dwordx4 v[12:15], v249, s[72:75], s81 offen offset:0 sc1\n"
"v_mfma_f32_16x16x32_bf16 a[40:43], v[184:187], a[200:203], a[40:43]\n"
"v_accvgpr_read_b32 v35, a35\n"
"v_cvt_pk_bf16_f32 v16, v32, v33\n"
"v_mfma_f32_16x16x32_bf16 a[40:43], v[188:191], a[216:219], a[40:43]\n"
"v_cvt_pk_bf16_f32 v17, v34, v35\n"
"v_mfma_f32_16x16x32_bf16 a[40:43], v[192:195], a[232:235], a[40:43]\n"
"v_accvgpr_read_b32 v36, a36\n"
"v_accvgpr_read_b32 v37, a37\n"
"v_mfma_f32_16x16x32_bf16 a[40:43], v[196:199], a[248:251], a[40:43]\n"
"v_accvgpr_read_b32 v38, a38\n"
"v_accvgpr_read_b32 v39, a39\n"
"v_mfma_f32_16x16x32_bf16 a[44:47], v[176:179], a[172:175], a[44:47]\n"
"v_cvt_pk_bf16_f32 v20, v36, v37\n"
"v_mfma_f32_16x16x32_bf16 a[44:47], v[180:183], a[188:191], a[44:47]\n"
"v_cvt_pk_bf16_f32 v21, v38, v39\n"
"v_mfma_f32_16x16x32_bf16 a[44:47], v[184:187], a[204:207], a[44:47]\n"
"ds_read_b64_tr_b16 v[176:177], v240 offset:128\n"
"v_mfma_f32_16x16x32_bf16 a[44:47], v[188:191], a[220:223], a[44:47]\n"
"v_mfma_f32_16x16x32_bf16 a[44:47], v[192:195], a[236:239], a[44:47]\n"
"ds_read_b64_tr_b16 v[178:179], v240 offset:5248\n"
"v_accvgpr_read_b32 v32, a40\n"
"v_mfma_f32_16x16x32_bf16 a[44:47], v[196:199], a[252:255], a[44:47]\n"
"v_accvgpr_read_b32 v33, a41\n"
"v_accvgpr_read_b32 v34, a42\n"
"s_waitcnt lgkmcnt(2)\n"
"v_mfma_f32_16x16x32_bf16 a[48:51], v[200:203], a[160:163], a[48:51]\n"
"ds_read_b64_tr_b16 v[180:181], v240 offset:10368\n"
"v_accvgpr_read_b32 v35, a43\n"
"v_mfma_f32_16x16x32_bf16 a[48:51], v[204:207], a[176:179], a[48:51]\n"
"v_cvt_pk_bf16_f32 v24, v32, v33\n"
"v_cvt_pk_bf16_f32 v25, v34, v35\n"
"v_mfma_f32_16x16x32_bf16 a[48:51], v[208:211], a[192:195], a[48:51]\n"
"ds_read_b64_tr_b16 v[182:183], v240 offset:15488\n"
"v_mfma_f32_16x16x32_bf16 a[48:51], v[212:215], a[208:211], a[48:51]\n"
"v_mfma_f32_16x16x32_bf16 a[48:51], v[216:219], a[224:227], a[48:51]\n"
"ds_read_b64_tr_b16 v[184:185], v240 offset:20608\n"
"v_accvgpr_read_b32 v36, a44\n"
"v_mfma_f32_16x16x32_bf16 a[48:51], v[220:223], a[240:243], a[48:51]\n"
"v_accvgpr_read_b32 v37, a45\n"
"v_accvgpr_read_b32 v38, a46\n"
"v_mfma_f32_16x16x32_bf16 a[52:55], v[200:203], a[164:167], a[52:55]\n"
"ds_read_b64_tr_b16 v[186:187], v240 offset:25728\n"
"v_accvgpr_read_b32 v39, a47\n"
"v_mfma_f32_16x16x32_bf16 a[52:55], v[204:207], a[180:183], a[52:55]\n"
"ds_read_b64_tr_b16 v[188:189], v240 offset:30848\n"
"v_cvt_pk_bf16_f32 v28, v36, v37\n"
"v_mfma_f32_16x16x32_bf16 a[52:55], v[208:211], a[196:199], a[52:55]\n"
"ds_read_b64_tr_b16 v[190:191], v240 offset:35968\n"
"v_cvt_pk_bf16_f32 v29, v38, v39\n"
"v_mfma_f32_16x16x32_bf16 a[52:55], v[212:215], a[212:215], a[52:55]\n"
"ds_read_b64_tr_b16 v[192:193], v240 offset:41088\n"
"v_mfma_f32_16x16x32_bf16 a[52:55], v[216:219], a[228:231], a[52:55]\n"
"ds_read_b64_tr_b16 v[194:195], v240 offset:46208\n"
"v_accvgpr_read_b32 v32, a48\n"
"v_mfma_f32_16x16x32_bf16 a[52:55], v[220:223], a[244:247], a[52:55]\n"
"ds_read_b64_tr_b16 v[196:197], v240 offset:51328\n"
"v_accvgpr_read_b32 v33, a49\n"
"v_mfma_f32_16x16x32_bf16 a[56:59], v[200:203], a[168:171], a[56:59]\n"
"ds_read_b64_tr_b16 v[198:199], v240 offset:56448\n"
"v_accvgpr_read_b32 v34, a50\n"
"v_mfma_f32_16x16x32_bf16 a[56:59], v[204:207], a[184:187], a[56:59]\n"
"v_accvgpr_read_b32 v35, a51\n"
"v_cvt_pk_bf16_f32 v18, v32, v33\n"
"v_mfma_f32_16x16x32_bf16 a[56:59], v[208:211], a[200:203], a[56:59]\n"
"v_cvt_pk_bf16_f32 v19, v34, v35\n"
"v_mfma_f32_16x16x32_bf16 a[56:59], v[212:215], a[216:219], a[56:59]\n"
"s_add_u32 s81, s77, 0\n"
"v_mfma_f32_16x16x32_bf16 a[56:59], v[216:219], a[232:235], a[56:59]\n"
"buffer_store_dwordx4 v[16:19], v249, s[72:75], s81 offen offset:1024 sc1\n"
"v_mfma_f32_16x16x32_bf16 a[56:59], v[220:223], a[248:251], a[56:59]\n"
"v_accvgpr_read_b32 v36, a52\n"
"v_accvgpr_read_b32 v37, a53\n"
"v_mfma_f32_16x16x32_bf16 a[60:63], v[200:203], a[172:175], a[60:63]\n"
"v_accvgpr_read_b32 v38, a54\n"
"v_accvgpr_read_b32 v39, a55\n"
"v_mfma_f32_16x16x32_bf16 a[60:63], v[204:207], a[188:191], a[60:63]\n"
"v_cvt_pk_bf16_f32 v22, v36, v37\n"
"v_cvt_pk_bf16_f32 v23, v38, v39\n"
"v_mfma_f32_16x16x32_bf16 a[60:63], v[208:211], a[204:207], a[60:63]\n"
"ds_read_b64_tr_b16 v[200:201], v241 offset:128\n"
"s_add_u32 s81, s77, 5120\n"
"v_mfma_f32_16x16x32_bf16 a[60:63], v[212:215], a[220:223], a[60:63]\n"
"buffer_store_dwordx4 v[20:23], v249, s[72:75], s81 offen offset:1024 sc1\n"
"v_mfma_f32_16x16x32_bf16 a[60:63], v[216:219], a[236:239], a[60:63]\n"
"ds_read_b64_tr_b16 v[202:203], v241 offset:5248\n"
"v_accvgpr_read_b32 v32, a56\n"
"v_mfma_f32_16x16x32_bf16 a[60:63], v[220:223], a[252:255], a[60:63]\n"
"v_accvgpr_read_b32 v33, a57\n"
"v_accvgpr_read_b32 v34, a58\n"
"s_waitcnt lgkmcnt(2)\n"
"v_mfma_f32_16x16x32_bf16 a[64:67], v[176:179], a[160:163], a[64:67]\n"
"ds_read_b64_tr_b16 v[204:205], v241 offset:10368\n"
"v_accvgpr_read_b32 v35, a59\n"
"v_mfma_f32_16x16x32_bf16 a[64:67], v[180:183], a[176:179], a[64:67]\n"
"v_cvt_pk_bf16_f32 v26, v32, v33\n"
"v_cvt_pk_bf16_f32 v27, v34, v35\n"
"v_mfma_f32_16x16x32_bf16 a[64:67], v[184:187], a[192:195], a[64:67]\n"
"ds_read_b64_tr_b16 v[206:207], v241 offset:15488\n"
"s_add_u32 s81, s77, 81920\n"
"v_mfma_f32_16x16x32_bf16 a[64:67], v[188:191], a[208:211], a[64:67]\n"
"buffer_store_dwordx4 v[24:27], v249, s[72:75], s81 offen offset:1024 sc1\n"
"v_mfma_f32_16x16x32_bf16 a[64:67], v[192:195], a[224:227], a[64:67]\n"
"ds_read_b64_tr_b16 v[208:209], v241 offset:20608\n"
"v_accvgpr_read_b32 v36, a60\n"
"v_mfma_f32_16x16x32_bf16 a[64:67], v[196:199], a[240:243], a[64:67]\n"
"v_accvgpr_read_b32 v37, a61\n"
"v_accvgpr_read_b32 v38, a62\n"
"v_mfma_f32_16x16x32_bf16 a[68:71], v[176:179], a[164:167], a[68:71]\n"
"ds_read_b64_tr_b16 v[210:211], v241 offset:25728\n"
"v_accvgpr_read_b32 v39, a63\n"
"v_mfma_f32_16x16x32_bf16 a[68:71], v[180:183], a[180:183], a[68:71]\n"
"ds_read_b64_tr_b16 v[212:213], v241 offset:30848\n"
"v_cvt_pk_bf16_f32 v30, v36, v37\n"
"v_mfma_f32_16x16x32_bf16 a[68:71], v[184:187], a[196:199], a[68:71]\n"
"ds_read_b64_tr_b16 v[214:215], v241 offset:35968\n"
"v_cvt_pk_bf16_f32 v31, v38, v39\n"
"v_mfma_f32_16x16x32_bf16 a[68:71], v[188:191], a[212:215], a[68:71]\n"
"ds_read_b64_tr_b16 v[216:217], v241 offset:41088\n"
"s_add_u32 s81, s77, 87040\n"
"v_mfma_f32_16x16x32_bf16 a[68:71], v[192:195], a[228:231], a[68:71]\n"
"ds_read_b64_tr_b16 v[218:219], v241 offset:46208\n"
"v_accvgpr_read_b32 v32, a64\n"
"v_mfma_f32_16x16x32_bf16 a[68:71], v[196:199], a[244:247], a[68:71]\n"
"ds_read_b64_tr_b16 v[220:221], v241 offset:51328\n"
"v_accvgpr_read_b32 v33, a65\n"
"v_mfma_f32_16x16x32_bf16 a[72:75], v[176:179], a[168:171], a[72:75]\n"
"ds_read_b64_tr_b16 v[222:223], v241 offset:56448\n"
"v_accvgpr_read_b32 v34, a66\n"
"v_mfma_f32_16x16x32_bf16 a[72:75], v[180:183], a[184:187], a[72:75]\n"
"buffer_store_dwordx4 v[28:31], v249, s[72:75], s81 offen offset:1024 sc1\n"
"v_mfma_f32_16x16x32_bf16 a[72:75], v[184:187], a[200:203], a[72:75]\n"
"v_accvgpr_read_b32 v35, a67\n"
"v_cvt_pk_bf16_f32 v0, v32, v33\n"
"v_mfma_f32_16x16x32_bf16 a[72:75], v[188:191], a[216:219], a[72:75]\n"
"v_cvt_pk_bf16_f32 v1, v34, v35\n"
"v_mfma_f32_16x16x32_bf16 a[72:75], v[192:195], a[232:235], a[72:75]\n"
"s_add_u32 s81, s77, 0\n"
"v_accvgpr_read_b32 v36, a68\n"
"v_mfma_f32_16x16x32_bf16 a[72:75], v[196:199], a[248:251], a[72:75]\n"
"buffer_store_dwordx2 v[0:1], v253, s[72:75], s81 offen offset:2048 sc1\n"
"v_mfma_f32_16x16x32_bf16 a[76:79], v[176:179], a[172:175], a[76:79]\n"
"v_accvgpr_read_b32 v37, a69\n"
"v_accvgpr_read_b32 v38, a70\n"
"v_mfma_f32_16x16x32_bf16 a[76:79], v[180:183], a[188:191], a[76:79]\n"
"v_accvgpr_read_b32 v39, a71\n"
"v_cvt_pk_bf16_f32 v4, v36, v37\n"
"v_mfma_f32_16x16x32_bf16 a[76:79], v[184:187], a[204:207], a[76:79]\n"
"ds_read_b64_tr_b16 v[176:177], v240 offset:192\n"
"v_cvt_pk_bf16_f32 v5, v38, v39\n"
"v_mfma_f32_16x16x32_bf16 a[76:79], v[188:191], a[220:223], a[76:79]\n"
"s_add_u32 s81, s77, 5120\n"
"v_mfma_f32_16x16x32_bf16 a[76:79], v[192:195], a[236:239], a[76:79]\n"
"ds_read_b64_tr_b16 v[178:179], v240 offset:5312\n"
"v_accvgpr_read_b32 v32, a72\n"
"v_mfma_f32_16x16x32_bf16 a[76:79], v[196:199], a[252:255], a[76:79]\n"
"buffer_store_dwordx2 v[4:5], v253, s[72:75], s81 offen offset:2048 sc1\n"
"s_waitcnt lgkmcnt(2)\n"
"v_mfma_f32_16x16x32_bf16 a[80:83], v[200:203], a[160:163], a[80:83]\n"
"ds_read_b64_tr_b16 v[180:181], v240 offset:10432\n"
"v_accvgpr_read_b32 v33, a73\n"
"v_mfma_f32_16x16x32_bf16 a[80:83], v[204:207], a[176:179], a[80:83]\n"
"v_accvgpr_read_b32 v34, a74\n"
"v_accvgpr_read_b32 v35, a75\n"
"v_mfma_f32_16x16x32_bf16 a[80:83], v[208:211], a[192:195], a[80:83]\n"
"ds_read_b64_tr_b16 v[182:183], v240 offset:15552\n"
"v_cvt_pk_bf16_f32 v8, v32, v33\n"
"v_mfma_f32_16x16x32_bf16 a[80:83], v[212:215], a[208:211], a[80:83]\n"
"v_cvt_pk_bf16_f32 v9, v34, v35\n"
"v_mfma_f32_16x16x32_bf16 a[80:83], v[216:219], a[224:227], a[80:83]\n"
"ds_read_b64_tr_b16 v[184:185], v240 offset:20672\n"
"s_add_u32 s81, s77, 81920\n"
"v_mfma_f32_16x16x32_bf16 a[80:83], v[220:223], a[240:243], a[80:83]\n"
"buffer_store_dwordx2 v[8:9], v253, s[72:75], s81 offen offset:2048 sc1\n"
"v_mfma_f32_16x16x32_bf16 a[84:87], v[200:203], a[164:167], a[84:87]\n"
"ds_read_b64_tr_b16 v[186:187], v240 offset:25792\n"
"v_accvgpr_read_b32 v36, a76\n"
"v_mfma_f32_16x16x32_bf16 a[84:87], v[204:207], a[180:183], a[84:87]\n"
"ds_read_b64_tr_b16 v[188:189], v240 offset:30912\n"
"v_accvgpr_read_b32 v37, a77\n"
"v_mfma_f32_16x16x32_bf16 a[84:87], v[208:211], a[196:199], a[84:87]\n"
"ds_read_b64_tr_b16 v[190:191], v240 offset:36032\n"
"v_accvgpr_read_b32 v38, a78\n"
"v_mfma_f32_16x16x32_bf16 a[84:87], v[212:215], a[212:215], a[84:87]\n"
"ds_read_b64_tr_b16 v[192:193], v240 offset:41152\n"
"v_accvgpr_read_b32 v39, a79\n"
"v_mfma_f32_16x16x32_bf16 a[84:87], v[216:219], a[228:231], a[84:87]\n"
"ds_read_b64_tr_b16 v[194:195], v240 offset:46272\n"
"v_cvt_pk_bf16_f32 v12, v36, v37\n"
"v_mfma_f32_16x16x32_bf16 a[84:87], v[220:223], a[244:247], a[84:87]\n"
"ds_read_b64_tr_b16 v[196:197], v240 offset:51392\n"
"v_cvt_pk_bf16_f32 v13, v38, v39\n"
"v_mfma_f32_16x16x32_bf16 a[88:91], v[200:203], a[168:171], a[88:91]\n"
"ds_read_b64_tr_b16 v[198:199], v240 offset:56512\n"
"s_add_u32 s81, s77, 87040\n"
"v_mfma_f32_16x16x32_bf16 a[88:91], v[204:207], a[184:187], a[88:91]\n"
"buffer_store_dwordx2 v[12:13], v253, s[72:75], s81 offen offset:2048 sc1\n"
"v_mfma_f32_16x16x32_bf16 a[88:91], v[208:211], a[200:203], a[88:91]\n"
"v_accvgpr_read_b32 v32, a80\n"
"v_accvgpr_read_b32 v33, a81\n"
"v_mfma_f32_16x16x32_bf16 a[88:91], v[212:215], a[216:219], a[88:91]\n"
"v_accvgpr_read_b32 v34, a82\n"
"v_accvgpr_read_b32 v35, a83\n"
"v_mfma_f32_16x16x32_bf16 a[88:91], v[216:219], a[232:235], a[88:91]\n"
"v_cvt_pk_bf16_f32 v16, v32, v33\n"
"v_cvt_pk_bf16_f32 v17, v34, v35\n"
"v_mfma_f32_16x16x32_bf16 a[88:91], v[220:223], a[248:251], a[88:91]\n"
"v_accvgpr_read_b32 v36, a84\n"
"v_accvgpr_read_b32 v37, a85\n"
"v_mfma_f32_16x16x32_bf16 a[92:95], v[200:203], a[172:175], a[92:95]\n"
"v_accvgpr_read_b32 v38, a86\n"
"v_accvgpr_read_b32 v39, a87\n"
"v_mfma_f32_16x16x32_bf16 a[92:95], v[204:207], a[188:191], a[92:95]\n"
"v_cvt_pk_bf16_f32 v20, v36, v37\n"
"v_cvt_pk_bf16_f32 v21, v38, v39\n"
"v_mfma_f32_16x16x32_bf16 a[92:95], v[208:211], a[204:207], a[92:95]\n"
"ds_read_b64_tr_b16 v[200:201], v241 offset:192\n"
"v_mfma_f32_16x16x32_bf16 a[92:95], v[212:215], a[220:223], a[92:95]\n"
"v_mfma_f32_16x16x32_bf16 a[92:95], v[216:219], a[236:239], a[92:95]\n"
"ds_read_b64_tr_b16 v[202:203], v241 offset:5312\n"
"v_accvgpr_read_b32 v32, a88\n"
"v_mfma_f32_16x16x32_bf16 a[92:95], v[220:223], a[252:255], a[92:95]\n"
"v_accvgpr_read_b32 v33, a89\n"
"v_accvgpr_read_b32 v34, a90\n"
"s_waitcnt lgkmcnt(2)\n"
"v_mfma_f32_16x16x32_bf16 a[96:99], v[176:179], a[160:163], a[96:99]\n"
"ds_read_b64_tr_b16 v[204:205], v241 offset:10432\n"
"v_accvgpr_read_b32 v35, a91\n"
"v_mfma_f32_16x16x32_bf16 a[96:99], v[180:183], a[176:179], a[96:99]\n"
"v_cvt_pk_bf16_f32 v24, v32, v33\n"
"v_cvt_pk_bf16_f32 v25, v34, v35\n"
"v_mfma_f32_16x16x32_bf16 a[96:99], v[184:187], a[192:195], a[96:99]\n"
"ds_read_b64_tr_b16 v[206:207], v241 offset:15552\n"
"v_mfma_f32_16x16x32_bf16 a[96:99], v[188:191], a[208:211], a[96:99]\n"
"v_mfma_f32_16x16x32_bf16 a[96:99], v[192:195], a[224:227], a[96:99]\n"
"ds_read_b64_tr_b16 v[208:209], v241 offset:20672\n"
"v_accvgpr_read_b32 v36, a92\n"
"v_mfma_f32_16x16x32_bf16 a[96:99], v[196:199], a[240:243], a[96:99]\n"
"v_accvgpr_read_b32 v37, a93\n"
"v_accvgpr_read_b32 v38, a94\n"
"v_mfma_f32_16x16x32_bf16 a[100:103], v[176:179], a[164:167], a[100:103]\n"
"ds_read_b64_tr_b16 v[210:211], v241 offset:25792\n"
"v_accvgpr_read_b32 v39, a95\n"
"v_mfma_f32_16x16x32_bf16 a[100:103], v[180:183], a[180:183], a[100:103]\n"
"ds_read_b64_tr_b16 v[212:213], v241 offset:30912\n"
"v_cvt_pk_bf16_f32 v28, v36, v37\n"
"v_mfma_f32_16x16x32_bf16 a[100:103], v[184:187], a[196:199], a[100:103]\n"
"ds_read_b64_tr_b16 v[214:215], v241 offset:36032\n"
"v_cvt_pk_bf16_f32 v29, v38, v39\n"
"v_mfma_f32_16x16x32_bf16 a[100:103], v[188:191], a[212:215], a[100:103]\n"
"ds_read_b64_tr_b16 v[216:217], v241 offset:41152\n"
"v_mfma_f32_16x16x32_bf16 a[100:103], v[192:195], a[228:231], a[100:103]\n"
"ds_read_b64_tr_b16 v[218:219], v241 offset:46272\n"
"v_accvgpr_read_b32 v32, a96\n"
"v_mfma_f32_16x16x32_bf16 a[100:103], v[196:199], a[244:247], a[100:103]\n"
"ds_read_b64_tr_b16 v[220:221], v241 offset:51392\n"
"v_accvgpr_read_b32 v33, a97\n"
"v_mfma_f32_16x16x32_bf16 a[104:107], v[176:179], a[168:171], a[104:107]\n"
"ds_read_b64_tr_b16 v[222:223], v241 offset:56512\n"
"v_accvgpr_read_b32 v34, a98\n"
"v_mfma_f32_16x16x32_bf16 a[104:107], v[180:183], a[184:187], a[104:107]\n"
"v_accvgpr_read_b32 v35, a99\n"
"v_cvt_pk_bf16_f32 v18, v32, v33\n"
"v_mfma_f32_16x16x32_bf16 a[104:107], v[184:187], a[200:203], a[104:107]\n"
"v_cvt_pk_bf16_f32 v19, v34, v35\n"
"v_mfma_f32_16x16x32_bf16 a[104:107], v[188:191], a[216:219], a[104:107]\n"
"s_add_u32 s81, s77, 2560\n"
"v_mfma_f32_16x16x32_bf16 a[104:107], v[192:195], a[232:235], a[104:107]\n"
"buffer_store_dwordx4 v[16:19], v249, s[72:75], s81 offen offset:0 sc1\n"
"v_mfma_f32_16x16x32_bf16 a[104:107], v[196:199], a[248:251], a[104:107]\n"
"v_accvgpr_read_b32 v36, a100\n"
"v_accvgpr_read_b32 v37, a101\n"
"v_mfma_f32_16x16x32_bf16 a[108:111], v[176:179], a[172:175], a[108:111]\n"
"v_accvgpr_read_b32 v38, a102\n"
"v_accvgpr_read_b32 v39, a103\n"
"v_mfma_f32_16x16x32_bf16 a[108:111], v[180:183], a[188:191], a[108:111]\n"
"v_cvt_pk_bf16_f32 v22, v36, v37\n"
"v_cvt_pk_bf16_f32 v23, v38, v39\n"
"v_mfma_f32_16x16x32_bf16 a[108:111], v[184:187], a[204:207], a[108:111]\n"
"ds_read_b64_tr_b16 v[176:177], v240 offset:256\n"
"s_add_u32 s81, s77, 7680\n"
"v_mfma_f32_16x16x32_bf16 a[108:111], v[188:191], a[220:223], a[108:111]\n"
"buffer_store_dwordx4 v[20:23], v249, s[72:75], s81 offen offset:0 sc1\n"
"v_mfma_f32_16x16x32_bf16 a[108:111], v[192:195], a[236:239], a[108:111]\n"
"ds_read_b64_tr_b16 v[178:179], v240 offset:5376\n"
"v_accvgpr_read_b32 v32, a104\n"
"v_mfma_f32_16x16x32_bf16 a[108:111], v[196:199], a[252:255], a[108:111]\n"
"v_accvgpr_read_b32 v33, a105\n"
"v_accvgpr_read_b32 v34, a106\n"
"s_waitcnt lgkmcnt(2)\n"
"v_mfma_f32_16x16x32_bf16 a[112:115], v[200:203], a[160:163], a[112:115]\n"
"ds_read_b64_tr_b16 v[180:181], v240 offset:10496\n"
"v_accvgpr_read_b32 v35, a107\n"
"v_mfma_f32_16x16x32_bf16 a[112:115], v[204:207], a[176:179], a[112:115]\n"
"v_cvt_pk_bf16_f32 v26, v32, v33\n"
"v_cvt_pk_bf16_f32 v27, v34, v35\n"
"v_mfma_f32_16x16x32_bf16 a[112:115], v[208:211], a[192:195], a[112:115]\n"
"ds_read_b64_tr_b16 v[182:183], v240 offset:15616\n"
"s_add_u32 s81, s77, 84480\n"
"v_mfma_f32_16x16x32_bf16 a[112:115], v[212:215], a[208:211], a[112:115]\n"
"buffer_store_dwordx4 v[24:27], v249, s[72:75], s81 offen offset:0 sc1\n"
"v_mfma_f32_16x16x32_bf16 a[112:115], v[216:219], a[224:227], a[112:115]\n"
"ds_read_b64_tr_b16 v[184:185], v240 offset:20736\n"
"v_accvgpr_read_b32 v36, a108\n"
"v_mfma_f32_16x16x32_bf16 a[112:115], v[220:223], a[240:243], a[112:115]\n"
"v_accvgpr_read_b32 v37, a109\n"
"v_accvgpr_read_b32 v38, a110\n"
"v_mfma_f32_16x16x32_bf16 a[116:119], v[200:203], a[164:167], a[116:119]\n"
"ds_read_b64_tr_b16 v[186:187], v240 offset:25856\n"
"v_accvgpr_read_b32 v39, a111\n"
"v_mfma_f32_16x16x32_bf16 a[116:119], v[204:207], a[180:183], a[116:119]\n"
"ds_read_b64_tr_b16 v[188:189], v240 offset:30976\n"
"v_cvt_pk_bf16_f32 v30, v36, v37\n"
"v_mfma_f32_16x16x32_bf16 a[116:119], v[208:211], a[196:199], a[116:119]\n"
"ds_read_b64_tr_b16 v[190:191], v240 offset:36096\n"
"v_cvt_pk_bf16_f32 v31, v38, v39\n"
"v_mfma_f32_16x16x32_bf16 a[116:119], v[212:215], a[212:215], a[116:119]\n"
"ds_read_b64_tr_b16 v[192:193], v240 offset:41216\n"
"s_add_u32 s81, s77, 89600\n"
"v_mfma_f32_16x16x32_bf16 a[116:119], v[216:219], a[228:231], a[116:119]\n"
"ds_read_b64_tr_b16 v[194:195], v240 offset:46336\n"
"v_accvgpr_read_b32 v32, a112\n"
"v_mfma_f32_16x16x32_bf16 a[116:119], v[220:223], a[244:247], a[116:119]\n"
"ds_read_b64_tr_b16 v[196:197], v240 offset:51456\n"
"v_accvgpr_read_b32 v33, a113\n"
"v_mfma_f32_16x16x32_bf16 a[120:123], v[200:203], a[168:171], a[120:123]\n"
"ds_read_b64_tr_b16 v[198:199], v240 offset:56576\n"
"v_accvgpr_read_b32 v34, a114\n"
"v_mfma_f32_16x16x32_bf16 a[120:123], v[204:207], a[184:187], a[120:123]\n"
"buffer_store_dwordx4 v[28:31], v249, s[72:75], s81 offen offset:0 sc1\n"
"v_mfma_f32_16x16x32_bf16 a[120:123], v[208:211], a[200:203], a[120:123]\n"
"v_accvgpr_read_b32 v35, a115\n"
"v_cvt_pk_bf16_f32 v0, v32, v33\n"
"v_mfma_f32_16x16x32_bf16 a[120:123], v[212:215], a[216:219], a[120:123]\n"
"v_cvt_pk_bf16_f32 v1, v34, v35\n"
"v_mfma_f32_16x16x32_bf16 a[120:123], v[216:219], a[232:235], a[120:123]\n"
"v_accvgpr_read_b32 v36, a116\n"
"v_accvgpr_read_b32 v37, a117\n"
"v_mfma_f32_16x16x32_bf16 a[120:123], v[220:223], a[248:251], a[120:123]\n"
"v_accvgpr_read_b32 v38, a118\n"
"v_accvgpr_read_b32 v39, a119\n"
"v_mfma_f32_16x16x32_bf16 a[124:127], v[200:203], a[172:175], a[124:127]\n"
"v_cvt_pk_bf16_f32 v4, v36, v37\n"
"v_mfma_f32_16x16x32_bf16 a[124:127], v[204:207], a[188:191], a[124:127]\n"
"v_cvt_pk_bf16_f32 v5, v38, v39\n"
"v_mfma_f32_16x16x32_bf16 a[124:127], v[208:211], a[204:207], a[124:127]\n"
"ds_read_b64_tr_b16 v[200:201], v241 offset:256\n"
"v_mfma_f32_16x16x32_bf16 a[124:127], v[212:215], a[220:223], a[124:127]\n"
"v_mfma_f32_16x16x32_bf16 a[124:127], v[216:219], a[236:239], a[124:127]\n"
"ds_read_b64_tr_b16 v[202:203], v241 offset:5376\n"
"v_accvgpr_read_b32 v32, a120\n"
"v_mfma_f32_16x16x32_bf16 a[124:127], v[220:223], a[252:255], a[124:127]\n"
"v_accvgpr_read_b32 v33, a121\n"
"v_accvgpr_read_b32 v34, a122\n"
"s_waitcnt lgkmcnt(2)\n"
"v_mfma_f32_16x16x32_bf16 a[128:131], v[176:179], a[160:163], a[128:131]\n"
"ds_read_b64_tr_b16 v[204:205], v241 offset:10496\n"
"v_accvgpr_read_b32 v35, a123\n"
"v_mfma_f32_16x16x32_bf16 a[128:131], v[180:183], a[176:179], a[128:131]\n"
"v_cvt_pk_bf16_f32 v8, v32, v33\n"
"v_cvt_pk_bf16_f32 v9, v34, v35\n"
"v_mfma_f32_16x16x32_bf16 a[128:131], v[184:187], a[192:195], a[128:131]\n"
"ds_read_b64_tr_b16 v[206:207], v241 offset:15616\n"
"v_mfma_f32_16x16x32_bf16 a[128:131], v[188:191], a[208:211], a[128:131]\n"
"v_mfma_f32_16x16x32_bf16 a[128:131], v[192:195], a[224:227], a[128:131]\n"
"ds_read_b64_tr_b16 v[208:209], v241 offset:20736\n"
"v_accvgpr_read_b32 v36, a124\n"
"v_mfma_f32_16x16x32_bf16 a[128:131], v[196:199], a[240:243], a[128:131]\n"
"v_accvgpr_read_b32 v37, a125\n"
"v_accvgpr_read_b32 v38, a126\n"
"v_mfma_f32_16x16x32_bf16 a[132:135], v[176:179], a[164:167], a[132:135]\n"
"ds_read_b64_tr_b16 v[210:211], v241 offset:25856\n"
"v_accvgpr_read_b32 v39, a127\n"
"v_mfma_f32_16x16x32_bf16 a[132:135], v[180:183], a[180:183], a[132:135]\n"
"ds_read_b64_tr_b16 v[212:213], v241 offset:30976\n"
"v_cvt_pk_bf16_f32 v12, v36, v37\n"
"v_mfma_f32_16x16x32_bf16 a[132:135], v[184:187], a[196:199], a[132:135]\n"
"ds_read_b64_tr_b16 v[214:215], v241 offset:36096\n"
"v_cvt_pk_bf16_f32 v13, v38, v39\n"
"v_mfma_f32_16x16x32_bf16 a[132:135], v[188:191], a[212:215], a[132:135]\n"
"ds_read_b64_tr_b16 v[216:217], v241 offset:41216\n"
"v_mfma_f32_16x16x32_bf16 a[132:135], v[192:195], a[228:231], a[132:135]\n"
"ds_read_b64_tr_b16 v[218:219], v241 offset:46336\n"
"v_accvgpr_read_b32 v32, a128\n"
"v_mfma_f32_16x16x32_bf16 a[132:135], v[196:199], a[244:247], a[132:135]\n"
"ds_read_b64_tr_b16 v[220:221], v241 offset:51456\n"
"v_accvgpr_read_b32 v33, a129\n"
"v_mfma_f32_16x16x32_bf16 a[136:139], v[176:179], a[168:171], a[136:139]\n"
"ds_read_b64_tr_b16 v[222:223], v241 offset:56576\n"
"v_accvgpr_read_b32 v34, a130\n"
"v_mfma_f32_16x16x32_bf16 a[136:139], v[180:183], a[184:187], a[136:139]\n"
"v_accvgpr_read_b32 v35, a131\n"
"v_cvt_pk_bf16_f32 v2, v32, v33\n"
"v_mfma_f32_16x16x32_bf16 a[136:139], v[184:187], a[200:203], a[136:139]\n"
"v_cvt_pk_bf16_f32 v3, v34, v35\n"
"v_mfma_f32_16x16x32_bf16 a[136:139], v[188:191], a[216:219], a[136:139]\n"
"s_add_u32 s81, s77, 2560\n"
"v_mfma_f32_16x16x32_bf16 a[136:139], v[192:195], a[232:235], a[136:139]\n"
"buffer_store_dwordx4 v[0:3], v249, s[72:75], s81 offen offset:1024 sc1\n"
"v_mfma_f32_16x16x32_bf16 a[136:139], v[196:199], a[248:251], a[136:139]\n"
"v_accvgpr_read_b32 v36, a132\n"
"v_accvgpr_read_b32 v37, a133\n"
"v_mfma_f32_16x16x32_bf16 a[140:143], v[176:179], a[172:175], a[140:143]\n"
"v_accvgpr_read_b32 v38, a134\n"
"v_accvgpr_read_b32 v39, a135\n"
"v_mfma_f32_16x16x32_bf16 a[140:143], v[180:183], a[188:191], a[140:143]\n"
"v_cvt_pk_bf16_f32 v6, v36, v37\n"
"v_cvt_pk_bf16_f32 v7, v38, v39\n"
"v_mfma_f32_16x16x32_bf16 a[140:143], v[184:187], a[204:207], a[140:143]\n"
"s_add_u32 s81, s77, 7680\n"
"v_mfma_f32_16x16x32_bf16 a[140:143], v[188:191], a[220:223], a[140:143]\n"
"buffer_store_dwordx4 v[4:7], v249, s[72:75], s81 offen offset:1024 sc1\n"
"v_mfma_f32_16x16x32_bf16 a[140:143], v[192:195], a[236:239], a[140:143]\n"
"v_accvgpr_read_b32 v32, a136\n"
"v_accvgpr_read_b32 v33, a137\n"
"v_mfma_f32_16x16x32_bf16 a[140:143], v[196:199], a[252:255], a[140:143]\n"
"v_accvgpr_read_b32 v34, a138\n"
"v_accvgpr_read_b32 v35, a139\n"
"s_waitcnt lgkmcnt(0)\n"
"v_mfma_f32_16x16x32_bf16 a[144:147], v[200:203], a[160:163], a[144:147]\n"
"v_cvt_pk_bf16_f32 v10, v32, v33\n"
"v_mfma_f32_16x16x32_bf16 a[144:147], v[204:207], a[176:179], a[144:147]\n"
"v_cvt_pk_bf16_f32 v11, v34, v35\n"
"v_mfma_f32_16x16x32_bf16 a[144:147], v[208:211], a[192:195], a[144:147]\n"
"s_add_u32 s81, s77, 84480\n"
"v_mfma_f32_16x16x32_bf16 a[144:147], v[212:215], a[208:211], a[144:147]\n"
"buffer_store_dwordx4 v[8:11], v249, s[72:75], s81 offen offset:1024 sc1\n"
"v_mfma_f32_16x16x32_bf16 a[144:147], v[216:219], a[224:227], a[144:147]\n"
"v_accvgpr_read_b32 v36, a140\n"
"v_accvgpr_read_b32 v37, a141\n"
"v_mfma_f32_16x16x32_bf16 a[144:147], v[220:223], a[240:243], a[144:147]\n"
"v_accvgpr_read_b32 v38, a142\n"
"v_accvgpr_read_b32 v39, a143\n"
"v_mfma_f32_16x16x32_bf16 a[148:151], v[200:203], a[164:167], a[148:151]\n"
"v_cvt_pk_bf16_f32 v14, v36, v37\n"
"v_mfma_f32_16x16x32_bf16 a[148:151], v[204:207], a[180:183], a[148:151]\n"
"v_cvt_pk_bf16_f32 v15, v38, v39\n"
"v_mfma_f32_16x16x32_bf16 a[148:151], v[208:211], a[196:199], a[148:151]\n"
"s_add_u32 s81, s77, 89600\n"
"v_mfma_f32_16x16x32_bf16 a[148:151], v[212:215], a[212:215], a[148:151]\n"
"buffer_store_dwordx4 v[12:15], v249, s[72:75], s81 offen offset:1024 sc1\n"
"v_mfma_f32_16x16x32_bf16 a[148:151], v[216:219], a[228:231], a[148:151]\n"
"v_accvgpr_read_b32 v32, a144\n"
"v_accvgpr_read_b32 v33, a145\n"
"v_mfma_f32_16x16x32_bf16 a[148:151], v[220:223], a[244:247], a[148:151]\n"
"v_accvgpr_read_b32 v34, a146\n"
"v_accvgpr_read_b32 v35, a147\n"
"v_mfma_f32_16x16x32_bf16 a[152:155], v[200:203], a[168:171], a[152:155]\n"
"v_cvt_pk_bf16_f32 v16, v32, v33\n"
"v_mfma_f32_16x16x32_bf16 a[152:155], v[204:207], a[184:187], a[152:155]\n"
"v_cvt_pk_bf16_f32 v17, v34, v35\n"
"v_mfma_f32_16x16x32_bf16 a[152:155], v[208:211], a[200:203], a[152:155]\n"
"s_add_u32 s81, s77, 2560\n"
"v_mfma_f32_16x16x32_bf16 a[152:155], v[212:215], a[216:219], a[152:155]\n"
"buffer_store_dwordx2 v[16:17], v253, s[72:75], s81 offen offset:2048 sc1\n"
"v_mfma_f32_16x16x32_bf16 a[152:155], v[216:219], a[232:235], a[152:155]\n"
"v_mfma_f32_16x16x32_bf16 a[152:155], v[220:223], a[248:251], a[152:155]\n"
"v_mfma_f32_16x16x32_bf16 a[156:159], v[200:203], a[172:175], a[156:159]\n"
"v_mfma_f32_16x16x32_bf16 a[156:159], v[204:207], a[188:191], a[156:159]\n"
"v_mfma_f32_16x16x32_bf16 a[156:159], v[208:211], a[204:207], a[156:159]\n"
"v_mfma_f32_16x16x32_bf16 a[156:159], v[212:215], a[220:223], a[156:159]\n"
"v_mfma_f32_16x16x32_bf16 a[156:159], v[216:219], a[236:239], a[156:159]\n"
"v_mfma_f32_16x16x32_bf16 a[156:159], v[220:223], a[252:255], a[156:159]\n"
"s_nop 7\n"
"v_accvgpr_read_b32 v36, a148\n"
"v_accvgpr_read_b32 v37, a149\n"
"v_accvgpr_read_b32 v38, a150\n"
"v_accvgpr_read_b32 v39, a151\n"
"v_cvt_pk_bf16_f32 v20, v36, v37\n"
"v_cvt_pk_bf16_f32 v21, v38, v39\n"
"s_add_u32 s81, s77, 7680\n"
"buffer_store_dwordx2 v[20:21], v253, s[72:75], s81 offen offset:2048 sc1\n"
"v_accvgpr_read_b32 v32, a152\n"
"v_accvgpr_read_b32 v33, a153\n"
"v_accvgpr_read_b32 v34, a154\n"
"v_accvgpr_read_b32 v35, a155\n"
"v_cvt_pk_bf16_f32 v24, v32, v33\n"
"v_cvt_pk_bf16_f32 v25, v34, v35\n"
"s_add_u32 s81, s77, 84480\n"
"buffer_store_dwordx2 v[24:25], v253, s[72:75], s81 offen offset:2048 sc1\n"
"v_accvgpr_read_b32 v36, a156\n"
"v_accvgpr_read_b32 v37, a157\n"
"v_accvgpr_read_b32 v38, a158\n"
"v_accvgpr_read_b32 v39, a159\n"
"v_cvt_pk_bf16_f32 v28, v36, v37\n"
"v_cvt_pk_bf16_f32 v29, v38, v39\n"
"s_add_u32 s81, s77, 89600\n"
"buffer_store_dwordx2 v[28:29], v253, s[72:75], s81 offen offset:2048 sc1\n"
"s_waitcnt vmcnt(24)\n"
"v_lshlrev_b32 v176, 16, v96\n"
"v_and_b32 v177, 0xffff0000, v96\n"
"v_lshlrev_b32 v178, 16, v97\n"
"v_and_b32 v179, 0xffff0000, v97\n"
"v_lshlrev_b32 v180, 16, v98\n"
"v_and_b32 v181, 0xffff0000, v98\n"
"v_lshlrev_b32 v182, 16, v99\n"
"v_and_b32 v183, 0xffff0000, v99\n"
"v_lshlrev_b32 v184, 16, v100\n"
"v_and_b32 v185, 0xffff0000, v100\n"
"v_lshlrev_b32 v186, 16, v101\n"
"v_and_b32 v187, 0xffff0000, v101\n"
"v_lshlrev_b32 v188, 16, v102\n"
"v_and_b32 v189, 0xffff0000, v102\n"
"v_lshlrev_b32 v190, 16, v103\n"
"v_and_b32 v191, 0xffff0000, v103\n"
"v_lshlrev_b32 v192, 16, v104\n"
"v_and_b32 v193, 0xffff0000, v104\n"
"v_lshlrev_b32 v194, 16, v105\n"
"v_and_b32 v195, 0xffff0000, v105\n"
"v_lshlrev_b32 v216, 16, v106\n"
"v_and_b32 v217, 0xffff0000, v106\n"
"v_add_f32 v176, v176, v216\n"
"v_add_f32 v177, v177, v217\n"
"v_lshlrev_b32 v218, 16, v107\n"
"v_and_b32 v219, 0xffff0000, v107\n"
"v_add_f32 v178, v178, v218\n"
"v_add_f32 v179, v179, v219\n"
"v_lshlrev_b32 v216, 16, v108\n"
"v_and_b32 v217, 0xffff0000, v108\n"
"v_add_f32 v180, v180, v216\n"
"v_add_f32 v181, v181, v217\n"
"v_lshlrev_b32 v218, 16, v109\n"
"v_and_b32 v219, 0xffff0000, v109\n"
"v_add_f32 v182, v182, v218\n"
"v_add_f32 v183, v183, v219\n"
"v_lshlrev_b32 v216, 16, v110\n"
"v_and_b32 v217, 0xffff0000, v110\n"
"v_add_f32 v184, v184, v216\n"
"v_add_f32 v185, v185, v217\n"
"v_lshlrev_b32 v218, 16, v111\n"
"v_and_b32 v219, 0xffff0000, v111\n"
"v_add_f32 v186, v186, v218\n"
"v_add_f32 v187, v187, v219\n"
"v_lshlrev_b32 v216, 16, v112\n"
"v_and_b32 v217, 0xffff0000, v112\n"
"v_add_f32 v188, v188, v216\n"
"v_add_f32 v189, v189, v217\n"
"v_lshlrev_b32 v218, 16, v113\n"
"v_and_b32 v219, 0xffff0000, v113\n"
"v_add_f32 v190, v190, v218\n"
"v_add_f32 v191, v191, v219\n"
"v_lshlrev_b32 v216, 16, v114\n"
"v_and_b32 v217, 0xffff0000, v114\n"
"v_add_f32 v192, v192, v216\n"
"v_add_f32 v193, v193, v217\n"
"v_lshlrev_b32 v218, 16, v115\n"
"v_and_b32 v219, 0xffff0000, v115\n"
"v_add_f32 v194, v194, v218\n"
"v_add_f32 v195, v195, v219\n"
"v_lshlrev_b32 v216, 16, v116\n"
"v_and_b32 v217, 0xffff0000, v116\n"
"v_add_f32 v176, v176, v216\n"
"v_add_f32 v177, v177, v217\n"
"v_lshlrev_b32 v218, 16, v117\n"
"v_and_b32 v219, 0xffff0000, v117\n"
"v_add_f32 v178, v178, v218\n"
"v_add_f32 v179, v179, v219\n"
"v_lshlrev_b32 v216, 16, v118\n"
"v_and_b32 v217, 0xffff0000, v118\n"
"v_add_f32 v180, v180, v216\n"
"v_add_f32 v181, v181, v217\n"
"v_lshlrev_b32 v218, 16, v119\n"
"v_and_b32 v219, 0xffff0000, v119\n"
"v_add_f32 v182, v182, v218\n"
"v_add_f32 v183, v183, v219\n"
"v_lshlrev_b32 v216, 16, v120\n"
"v_and_b32 v217, 0xffff0000, v120\n"
"v_add_f32 v184, v184, v216\n"
"v_add_f32 v185, v185, v217\n"
"v_lshlrev_b32 v218, 16, v121\n"
"v_and_b32 v219, 0xffff0000, v121\n"
"v_add_f32 v186, v186, v218\n"
"v_add_f32 v187, v187, v219\n"
"v_lshlrev_b32 v216, 16, v122\n"
"v_and_b32 v217, 0xffff0000, v122\n"
"v_add_f32 v188, v188, v216\n"
"v_add_f32 v189, v189, v217\n"
"v_lshlrev_b32 v218, 16, v123\n"
"v_and_b32 v219, 0xffff0000, v123\n"
"v_add_f32 v190, v190, v218\n"
"v_add_f32 v191, v191, v219\n"
"v_lshlrev_b32 v216, 16, v124\n"
"v_and_b32 v217, 0xffff0000, v124\n"
"v_add_f32 v192, v192, v216\n"
"v_add_f32 v193, v193, v217\n"
"v_lshlrev_b32 v218, 16, v125\n"
"v_and_b32 v219, 0xffff0000, v125\n"
"v_add_f32 v194, v194, v218\n"
"v_add_f32 v195, v195, v219\n"
"v_lshlrev_b32 v216, 16, v126\n"
"v_and_b32 v217, 0xffff0000, v126\n"
"v_add_f32 v176, v176, v216\n"
"v_add_f32 v177, v177, v217\n"
"v_lshlrev_b32 v218, 16, v127\n"
"v_and_b32 v219, 0xffff0000, v127\n"
"v_add_f32 v178, v178, v218\n"
"v_add_f32 v179, v179, v219\n"
"v_lshlrev_b32 v216, 16, v128\n"
"v_and_b32 v217, 0xffff0000, v128\n"
"v_add_f32 v180, v180, v216\n"
"v_add_f32 v181, v181, v217\n"
"v_lshlrev_b32 v218, 16, v129\n"
"v_and_b32 v219, 0xffff0000, v129\n"
"v_add_f32 v182, v182, v218\n"
"v_add_f32 v183, v183, v219\n"
"v_lshlrev_b32 v216, 16, v130\n"
"v_and_b32 v217, 0xffff0000, v130\n"
"v_add_f32 v184, v184, v216\n"
"v_add_f32 v185, v185, v217\n"
"v_lshlrev_b32 v218, 16, v131\n"
"v_and_b32 v219, 0xffff0000, v131\n"
"v_add_f32 v186, v186, v218\n"
"v_add_f32 v187, v187, v219\n"
"v_lshlrev_b32 v216, 16, v132\n"
"v_and_b32 v217, 0xffff0000, v132\n"
"v_add_f32 v188, v188, v216\n"
"v_add_f32 v189, v189, v217\n"
"v_lshlrev_b32 v218, 16, v133\n"
"v_and_b32 v219, 0xffff0000, v133\n"
"v_add_f32 v190, v190, v218\n"
"v_add_f32 v191, v191, v219\n"
"v_lshlrev_b32 v216, 16, v134\n"
"v_and_b32 v217, 0xffff0000, v134\n"
"v_add_f32 v192, v192, v216\n"
"v_add_f32 v193, v193, v217\n"
"v_lshlrev_b32 v218, 16, v135\n"
"v_and_b32 v219, 0xffff0000, v135\n"
"v_add_f32 v194, v194, v218\n"
"v_add_f32 v195, v195, v219\n"
"s_mov_b32 s27, 0xbfb8aa3b\n"
"v_mul_f32 v216, s27, v176\n"
"v_mul_f32 v217, s27, v177\n"
"v_mul_f32 v218, s27, v178\n"
"v_mul_f32 v219, s27, v179\n"
"v_exp_f32 v216, v216\n"
"v_exp_f32 v217, v217\n"
"v_exp_f32 v218, v218\n"
"v_exp_f32 v219, v219\n"
"s_nop 0\n"
"v_add_f32 v216, 1.0, v216\n"
"v_add_f32 v217, 1.0, v217\n"
"v_add_f32 v218, 1.0, v218\n"
"v_add_f32 v219, 1.0, v219\n"
"v_rcp_f32 v216, v216\n"
"v_rcp_f32 v217, v217\n"
"v_rcp_f32 v218, v218\n"
"v_rcp_f32 v219, v219\n"
"s_nop 0\n"
"v_mul_f32 v176, v176, v216\n"
"v_mul_f32 v177, v177, v217\n"
"v_mul_f32 v178, v178, v218\n"
"v_mul_f32 v179, v179, v219\n"
"v_mul_f32 v216, s27, v180\n"
"v_mul_f32 v217, s27, v181\n"
"v_mul_f32 v218, s27, v182\n"
"v_mul_f32 v219, s27, v183\n"
"v_exp_f32 v216, v216\n"
"v_exp_f32 v217, v217\n"
"v_exp_f32 v218, v218\n"
"v_exp_f32 v219, v219\n"
"s_nop 0\n"
"v_add_f32 v216, 1.0, v216\n"
"v_add_f32 v217, 1.0, v217\n"
"v_add_f32 v218, 1.0, v218\n"
"v_add_f32 v219, 1.0, v219\n"
"v_rcp_f32 v216, v216\n"
"v_rcp_f32 v217, v217\n"
"v_rcp_f32 v218, v218\n"
"v_rcp_f32 v219, v219\n"
"s_nop 0\n"
"v_mul_f32 v180, v180, v216\n"
"v_mul_f32 v181, v181, v217\n"
"v_mul_f32 v182, v182, v218\n"
"v_mul_f32 v183, v183, v219\n"
"v_mul_f32 v216, s27, v184\n"
"v_mul_f32 v217, s27, v185\n"
"v_mul_f32 v218, s27, v186\n"
"v_mul_f32 v219, s27, v187\n"
"v_exp_f32 v216, v216\n"
"v_exp_f32 v217, v217\n"
"v_exp_f32 v218, v218\n"
"v_exp_f32 v219, v219\n"
"s_nop 0\n"
"v_add_f32 v216, 1.0, v216\n"
"v_add_f32 v217, 1.0, v217\n"
"v_add_f32 v218, 1.0, v218\n"
"v_add_f32 v219, 1.0, v219\n"
"v_rcp_f32 v216, v216\n"
"v_rcp_f32 v217, v217\n"
"v_rcp_f32 v218, v218\n"
"v_rcp_f32 v219, v219\n"
"s_nop 0\n"
"v_mul_f32 v184, v184, v216\n"
"v_mul_f32 v185, v185, v217\n"
"v_mul_f32 v186, v186, v218\n"
"v_mul_f32 v187, v187, v219\n"
"v_mul_f32 v216, s27, v188\n"
"v_mul_f32 v217, s27, v189\n"
"v_mul_f32 v218, s27, v190\n"
"v_mul_f32 v219, s27, v191\n"
"v_exp_f32 v216, v216\n"
"v_exp_f32 v217, v217\n"
"v_exp_f32 v218, v218\n"
"v_exp_f32 v219, v219\n"
"s_nop 0\n"
"v_add_f32 v216, 1.0, v216\n"
"v_add_f32 v217, 1.0, v217\n"
"v_add_f32 v218, 1.0, v218\n"
"v_add_f32 v219, 1.0, v219\n"
"v_rcp_f32 v216, v216\n"
"v_rcp_f32 v217, v217\n"
"v_rcp_f32 v218, v218\n"
"v_rcp_f32 v219, v219\n"
"s_nop 0\n"
"v_mul_f32 v188, v188, v216\n"
"v_mul_f32 v189, v189, v217\n"
"v_mul_f32 v190, v190, v218\n"
"v_mul_f32 v191, v191, v219\n"
"v_mul_f32 v216, s27, v192\n"
"v_mul_f32 v217, s27, v193\n"
"v_mul_f32 v218, s27, v194\n"
"v_mul_f32 v219, s27, v195\n"
"v_exp_f32 v216, v216\n"
"v_exp_f32 v217, v217\n"
"v_exp_f32 v218, v218\n"
"v_exp_f32 v219, v219\n"
"s_nop 0\n"
"v_add_f32 v216, 1.0, v216\n"
"v_add_f32 v217, 1.0, v217\n"
"v_add_f32 v218, 1.0, v218\n"
"v_add_f32 v219, 1.0, v219\n"
"v_rcp_f32 v216, v216\n"
"v_rcp_f32 v217, v217\n"
"v_rcp_f32 v218, v218\n"
"v_rcp_f32 v219, v219\n"
"s_nop 0\n"
"v_mul_f32 v192, v192, v216\n"
"v_mul_f32 v193, v193, v217\n"
"v_mul_f32 v194, v194, v218\n"
"v_mul_f32 v195, v195, v219\n"
"s_lshl_b32 s78, s21, 2\n"
"s_add_u32 s78, s78, s24\n"
"s_lshl_b32 s78, s78, 3\n"
"s_add_u32 s78, s78, s23\n"
"s_mul_i32 s78, s78, 81920\n"
"s_and_b32 s26, s22, 1\n"
"s_lshl_b32 s26, s26, 1\n"
"s_lshr_b32 s27, s22, 1\n"
"s_add_u32 s26, s26, s27\n"
"s_mul_i32 s26, s26, 2560\n"
"s_add_u32 s78, s78, s26\n"
"s_add_u32 s78, s78, 34144256\n"
"s_waitcnt vmcnt(0)\n"
"s_barrier\n"
"s_cmp_lg_u32 s22, 0\n"
"s_cbranch_scc1 SIGSKIP7_%=\n"
"s_lshl_b32 s28, s21, 2\n"
"s_add_u32 s28, s28, s24\n"
"s_add_u32 s28, s28, 160\n"
"s_lshl_b32 s28, s28, 8\n"
"v_mov_b32 v255, s28\n"
"s_mov_b64 exec, 1\n"
"global_atomic_add v255, v254, s[54:55]\n"
"s_mov_b64 exec, -1\n"
"SIGSKIP7_%=:\n"
"s_lshl_b32 s28, s21, 2\n"
"s_add_u32 s28, s28, s24\n"
"s_add_u32 s28, s28, 160\n"
"s_lshl_b32 s28, s28, 8\n"
"v_mov_b32 v255, s28\n"
"s_mov_b32 s27, 8000\n"
"SPIN8_%=:\n"
"global_load_dword v252, v255, s[54:55] sc1\n"
"s_waitcnt vmcnt(0)\n"
"v_readfirstlane_b32 s28, v252\n"
"s_nop 1\n"
"s_cmp_ge_u32 s28, 8\n"
"s_cbranch_scc1 POLLOK8_%=\n"
"s_sleep 1\n"
"s_sub_u32 s27, s27, 1\n"
"s_cmp_lg_u32 s27, 0\n"
"s_cbranch_scc1 SPIN8_%=\n"
"POLLOK8_%=:\n"
"buffer_load_dwordx4 v[96:99], v249, s[72:75], s78 offen sc1\n"
"buffer_load_dwordx4 v[100:103], v249, s[72:75], s78 offen offset:1024 sc1\n"
"buffer_load_dwordx2 v[104:105], v253, s[72:75], s78 offen offset:2048 sc1\n"
"s_add_u32 s78, s78, 10240\n"
"buffer_load_dwordx4 v[106:109], v249, s[72:75], s78 offen sc1\n"
"buffer_load_dwordx4 v[110:113], v249, s[72:75], s78 offen offset:1024 sc1\n"
"buffer_load_dwordx2 v[114:115], v253, s[72:75], s78 offen offset:2048 sc1\n"
"s_add_u32 s78, s78, 10240\n"
"buffer_load_dwordx4 v[116:119], v249, s[72:75], s78 offen sc1\n"
"buffer_load_dwordx4 v[120:123], v249, s[72:75], s78 offen offset:1024 sc1\n"
"buffer_load_dwordx2 v[124:125], v253, s[72:75], s78 offen offset:2048 sc1\n"
"s_add_u32 s78, s78, 10240\n"
"buffer_load_dwordx4 v[126:129], v249, s[72:75], s78 offen sc1\n"
"buffer_load_dwordx4 v[130:133], v249, s[72:75], s78 offen offset:1024 sc1\n"
"buffer_load_dwordx2 v[134:135], v253, s[72:75], s78 offen offset:2048 sc1\n"
"s_add_u32 s78, s78, 10240\n"
"buffer_load_dwordx4 v[136:139], v249, s[72:75], s78 offen sc1\n"
"buffer_load_dwordx4 v[140:143], v249, s[72:75], s78 offen offset:1024 sc1\n"
"buffer_load_dwordx2 v[144:145], v253, s[72:75], s78 offen offset:2048 sc1\n"
"s_add_u32 s78, s78, 10240\n"
"buffer_load_dwordx4 v[146:149], v249, s[72:75], s78 offen sc1\n"
"buffer_load_dwordx4 v[150:153], v249, s[72:75], s78 offen offset:1024 sc1\n"
"buffer_load_dwordx2 v[154:155], v253, s[72:75], s78 offen offset:2048 sc1\n"
"s_add_u32 s78, s78, 10240\n"
"buffer_load_dwordx4 v[156:159], v249, s[72:75], s78 offen sc1\n"
"buffer_load_dwordx4 v[160:163], v249, s[72:75], s78 offen offset:1024 sc1\n"
"buffer_load_dwordx2 v[164:165], v253, s[72:75], s78 offen offset:2048 sc1\n"
"s_add_u32 s78, s78, 10240\n"
"buffer_load_dwordx4 v[166:169], v249, s[72:75], s78 offen sc1\n"
"buffer_load_dwordx4 v[170:173], v249, s[72:75], s78 offen offset:1024 sc1\n"
"buffer_load_dwordx2 v[174:175], v253, s[72:75], s78 offen offset:2048 sc1\n"
"s_waitcnt vmcnt(21)\n"
"v_lshlrev_b32 v196, 16, v96\n"
"v_and_b32 v197, 0xffff0000, v96\n"
"v_lshlrev_b32 v198, 16, v97\n"
"v_and_b32 v199, 0xffff0000, v97\n"
"v_lshlrev_b32 v200, 16, v98\n"
"v_and_b32 v201, 0xffff0000, v98\n"
"v_lshlrev_b32 v202, 16, v99\n"
"v_and_b32 v203, 0xffff0000, v99\n"
"v_lshlrev_b32 v204, 16, v100\n"
"v_and_b32 v205, 0xffff0000, v100\n"
"v_lshlrev_b32 v206, 16, v101\n"
"v_and_b32 v207, 0xffff0000, v101\n"
"v_lshlrev_b32 v208, 16, v102\n"
"v_and_b32 v209, 0xffff0000, v102\n"
"v_lshlrev_b32 v210, 16, v103\n"
"v_and_b32 v211, 0xffff0000, v103\n"
"v_lshlrev_b32 v212, 16, v104\n"
"v_and_b32 v213, 0xffff0000, v104\n"
"v_lshlrev_b32 v214, 16, v105\n"
"v_and_b32 v215, 0xffff0000, v105\n"
"s_waitcnt vmcnt(18)\n"
"v_lshlrev_b32 v216, 16, v106\n"
"v_and_b32 v217, 0xffff0000, v106\n"
"v_add_f32 v196, v196, v216\n"
"v_add_f32 v197, v197, v217\n"
"v_lshlrev_b32 v218, 16, v107\n"
"v_and_b32 v219, 0xffff0000, v107\n"
"v_add_f32 v198, v198, v218\n"
"v_add_f32 v199, v199, v219\n"
"v_lshlrev_b32 v216, 16, v108\n"
"v_and_b32 v217, 0xffff0000, v108\n"
"v_add_f32 v200, v200, v216\n"
"v_add_f32 v201, v201, v217\n"
"v_lshlrev_b32 v218, 16, v109\n"
"v_and_b32 v219, 0xffff0000, v109\n"
"v_add_f32 v202, v202, v218\n"
"v_add_f32 v203, v203, v219\n"
"v_lshlrev_b32 v216, 16, v110\n"
"v_and_b32 v217, 0xffff0000, v110\n"
"v_add_f32 v204, v204, v216\n"
"v_add_f32 v205, v205, v217\n"
"v_lshlrev_b32 v218, 16, v111\n"
"v_and_b32 v219, 0xffff0000, v111\n"
"v_add_f32 v206, v206, v218\n"
"v_add_f32 v207, v207, v219\n"
"v_lshlrev_b32 v216, 16, v112\n"
"v_and_b32 v217, 0xffff0000, v112\n"
"v_add_f32 v208, v208, v216\n"
"v_add_f32 v209, v209, v217\n"
"v_lshlrev_b32 v218, 16, v113\n"
"v_and_b32 v219, 0xffff0000, v113\n"
"v_add_f32 v210, v210, v218\n"
"v_add_f32 v211, v211, v219\n"
"v_lshlrev_b32 v216, 16, v114\n"
"v_and_b32 v217, 0xffff0000, v114\n"
"v_add_f32 v212, v212, v216\n"
"v_add_f32 v213, v213, v217\n"
"v_lshlrev_b32 v218, 16, v115\n"
"v_and_b32 v219, 0xffff0000, v115\n"
"v_add_f32 v214, v214, v218\n"
"v_add_f32 v215, v215, v219\n"
"s_waitcnt vmcnt(15)\n"
"v_lshlrev_b32 v216, 16, v116\n"
"v_and_b32 v217, 0xffff0000, v116\n"
"v_add_f32 v196, v196, v216\n"
"v_add_f32 v197, v197, v217\n"
"v_lshlrev_b32 v218, 16, v117\n"
"v_and_b32 v219, 0xffff0000, v117\n"
"v_add_f32 v198, v198, v218\n"
"v_add_f32 v199, v199, v219\n"
"v_lshlrev_b32 v216, 16, v118\n"
"v_and_b32 v217, 0xffff0000, v118\n"
"v_add_f32 v200, v200, v216\n"
"v_add_f32 v201, v201, v217\n"
"v_lshlrev_b32 v218, 16, v119\n"
"v_and_b32 v219, 0xffff0000, v119\n"
"v_add_f32 v202, v202, v218\n"
"v_add_f32 v203, v203, v219\n"
"v_lshlrev_b32 v216, 16, v120\n"
"v_and_b32 v217, 0xffff0000, v120\n"
"v_add_f32 v204, v204, v216\n"
"v_add_f32 v205, v205, v217\n"
"v_lshlrev_b32 v218, 16, v121\n"
"v_and_b32 v219, 0xffff0000, v121\n"
"v_add_f32 v206, v206, v218\n"
"v_add_f32 v207, v207, v219\n"
"v_lshlrev_b32 v216, 16, v122\n"
"v_and_b32 v217, 0xffff0000, v122\n"
"v_add_f32 v208, v208, v216\n"
"v_add_f32 v209, v209, v217\n"
"v_lshlrev_b32 v218, 16, v123\n"
"v_and_b32 v219, 0xffff0000, v123\n"
"v_add_f32 v210, v210, v218\n"
"v_add_f32 v211, v211, v219\n"
"v_lshlrev_b32 v216, 16, v124\n"
"v_and_b32 v217, 0xffff0000, v124\n"
"v_add_f32 v212, v212, v216\n"
"v_add_f32 v213, v213, v217\n"
"v_lshlrev_b32 v218, 16, v125\n"
"v_and_b32 v219, 0xffff0000, v125\n"
"v_add_f32 v214, v214, v218\n"
"v_add_f32 v215, v215, v219\n"
"s_waitcnt vmcnt(12)\n"
"v_lshlrev_b32 v216, 16, v126\n"
"v_and_b32 v217, 0xffff0000, v126\n"
"v_add_f32 v196, v196, v216\n"
"v_add_f32 v197, v197, v217\n"
"v_lshlrev_b32 v218, 16, v127\n"
"v_and_b32 v219, 0xffff0000, v127\n"
"v_add_f32 v198, v198, v218\n"
"v_add_f32 v199, v199, v219\n"
"v_lshlrev_b32 v216, 16, v128\n"
"v_and_b32 v217, 0xffff0000, v128\n"
"v_add_f32 v200, v200, v216\n"
"v_add_f32 v201, v201, v217\n"
"v_lshlrev_b32 v218, 16, v129\n"
"v_and_b32 v219, 0xffff0000, v129\n"
"v_add_f32 v202, v202, v218\n"
"v_add_f32 v203, v203, v219\n"
"v_lshlrev_b32 v216, 16, v130\n"
"v_and_b32 v217, 0xffff0000, v130\n"
"v_add_f32 v204, v204, v216\n"
"v_add_f32 v205, v205, v217\n"
"v_lshlrev_b32 v218, 16, v131\n"
"v_and_b32 v219, 0xffff0000, v131\n"
"v_add_f32 v206, v206, v218\n"
"v_add_f32 v207, v207, v219\n"
"v_lshlrev_b32 v216, 16, v132\n"
"v_and_b32 v217, 0xffff0000, v132\n"
"v_add_f32 v208, v208, v216\n"
"v_add_f32 v209, v209, v217\n"
"v_lshlrev_b32 v218, 16, v133\n"
"v_and_b32 v219, 0xffff0000, v133\n"
"v_add_f32 v210, v210, v218\n"
"v_add_f32 v211, v211, v219\n"
"v_lshlrev_b32 v216, 16, v134\n"
"v_and_b32 v217, 0xffff0000, v134\n"
"v_add_f32 v212, v212, v216\n"
"v_add_f32 v213, v213, v217\n"
"v_lshlrev_b32 v218, 16, v135\n"
"v_and_b32 v219, 0xffff0000, v135\n"
"v_add_f32 v214, v214, v218\n"
"v_add_f32 v215, v215, v219\n"
"s_waitcnt vmcnt(9)\n"
"v_lshlrev_b32 v216, 16, v136\n"
"v_and_b32 v217, 0xffff0000, v136\n"
"v_add_f32 v196, v196, v216\n"
"v_add_f32 v197, v197, v217\n"
"v_lshlrev_b32 v218, 16, v137\n"
"v_and_b32 v219, 0xffff0000, v137\n"
"v_add_f32 v198, v198, v218\n"
"v_add_f32 v199, v199, v219\n"
"v_lshlrev_b32 v216, 16, v138\n"
"v_and_b32 v217, 0xffff0000, v138\n"
"v_add_f32 v200, v200, v216\n"
"v_add_f32 v201, v201, v217\n"
"v_lshlrev_b32 v218, 16, v139\n"
"v_and_b32 v219, 0xffff0000, v139\n"
"v_add_f32 v202, v202, v218\n"
"v_add_f32 v203, v203, v219\n"
"v_lshlrev_b32 v216, 16, v140\n"
"v_and_b32 v217, 0xffff0000, v140\n"
"v_add_f32 v204, v204, v216\n"
"v_add_f32 v205, v205, v217\n"
"v_lshlrev_b32 v218, 16, v141\n"
"v_and_b32 v219, 0xffff0000, v141\n"
"v_add_f32 v206, v206, v218\n"
"v_add_f32 v207, v207, v219\n"
"v_lshlrev_b32 v216, 16, v142\n"
"v_and_b32 v217, 0xffff0000, v142\n"
"v_add_f32 v208, v208, v216\n"
"v_add_f32 v209, v209, v217\n"
"v_lshlrev_b32 v218, 16, v143\n"
"v_and_b32 v219, 0xffff0000, v143\n"
"v_add_f32 v210, v210, v218\n"
"v_add_f32 v211, v211, v219\n"
"v_lshlrev_b32 v216, 16, v144\n"
"v_and_b32 v217, 0xffff0000, v144\n"
"v_add_f32 v212, v212, v216\n"
"v_add_f32 v213, v213, v217\n"
"v_lshlrev_b32 v218, 16, v145\n"
"v_and_b32 v219, 0xffff0000, v145\n"
"v_add_f32 v214, v214, v218\n"
"v_add_f32 v215, v215, v219\n"
"s_waitcnt vmcnt(6)\n"
"v_lshlrev_b32 v216, 16, v146\n"
"v_and_b32 v217, 0xffff0000, v146\n"
"v_add_f32 v196, v196, v216\n"
"v_add_f32 v197, v197, v217\n"
"v_lshlrev_b32 v218, 16, v147\n"
"v_and_b32 v219, 0xffff0000, v147\n"
"v_add_f32 v198, v198, v218\n"
"v_add_f32 v199, v199, v219\n"
"v_lshlrev_b32 v216, 16, v148\n"
"v_and_b32 v217, 0xffff0000, v148\n"
"v_add_f32 v200, v200, v216\n"
"v_add_f32 v201, v201, v217\n"
"v_lshlrev_b32 v218, 16, v149\n"
"v_and_b32 v219, 0xffff0000, v149\n"
"v_add_f32 v202, v202, v218\n"
"v_add_f32 v203, v203, v219\n"
"v_lshlrev_b32 v216, 16, v150\n"
"v_and_b32 v217, 0xffff0000, v150\n"
"v_add_f32 v204, v204, v216\n"
"v_add_f32 v205, v205, v217\n"
"v_lshlrev_b32 v218, 16, v151\n"
"v_and_b32 v219, 0xffff0000, v151\n"
"v_add_f32 v206, v206, v218\n"
"v_add_f32 v207, v207, v219\n"
"v_lshlrev_b32 v216, 16, v152\n"
"v_and_b32 v217, 0xffff0000, v152\n"
"v_add_f32 v208, v208, v216\n"
"v_add_f32 v209, v209, v217\n"
"v_lshlrev_b32 v218, 16, v153\n"
"v_and_b32 v219, 0xffff0000, v153\n"
"v_add_f32 v210, v210, v218\n"
"v_add_f32 v211, v211, v219\n"
"v_lshlrev_b32 v216, 16, v154\n"
"v_and_b32 v217, 0xffff0000, v154\n"
"v_add_f32 v212, v212, v216\n"
"v_add_f32 v213, v213, v217\n"
"v_lshlrev_b32 v218, 16, v155\n"
"v_and_b32 v219, 0xffff0000, v155\n"
"v_add_f32 v214, v214, v218\n"
"v_add_f32 v215, v215, v219\n"
"s_waitcnt vmcnt(3)\n"
"v_lshlrev_b32 v216, 16, v156\n"
"v_and_b32 v217, 0xffff0000, v156\n"
"v_add_f32 v196, v196, v216\n"
"v_add_f32 v197, v197, v217\n"
"v_lshlrev_b32 v218, 16, v157\n"
"v_and_b32 v219, 0xffff0000, v157\n"
"v_add_f32 v198, v198, v218\n"
"v_add_f32 v199, v199, v219\n"
"v_lshlrev_b32 v216, 16, v158\n"
"v_and_b32 v217, 0xffff0000, v158\n"
"v_add_f32 v200, v200, v216\n"
"v_add_f32 v201, v201, v217\n"
"v_lshlrev_b32 v218, 16, v159\n"
"v_and_b32 v219, 0xffff0000, v159\n"
"v_add_f32 v202, v202, v218\n"
"v_add_f32 v203, v203, v219\n"
"v_lshlrev_b32 v216, 16, v160\n"
"v_and_b32 v217, 0xffff0000, v160\n"
"v_add_f32 v204, v204, v216\n"
"v_add_f32 v205, v205, v217\n"
"v_lshlrev_b32 v218, 16, v161\n"
"v_and_b32 v219, 0xffff0000, v161\n"
"v_add_f32 v206, v206, v218\n"
"v_add_f32 v207, v207, v219\n"
"v_lshlrev_b32 v216, 16, v162\n"
"v_and_b32 v217, 0xffff0000, v162\n"
"v_add_f32 v208, v208, v216\n"
"v_add_f32 v209, v209, v217\n"
"v_lshlrev_b32 v218, 16, v163\n"
"v_and_b32 v219, 0xffff0000, v163\n"
"v_add_f32 v210, v210, v218\n"
"v_add_f32 v211, v211, v219\n"
"v_lshlrev_b32 v216, 16, v164\n"
"v_and_b32 v217, 0xffff0000, v164\n"
"v_add_f32 v212, v212, v216\n"
"v_add_f32 v213, v213, v217\n"
"v_lshlrev_b32 v218, 16, v165\n"
"v_and_b32 v219, 0xffff0000, v165\n"
"v_add_f32 v214, v214, v218\n"
"v_add_f32 v215, v215, v219\n"
"s_waitcnt vmcnt(0)\n"
"v_lshlrev_b32 v216, 16, v166\n"
"v_and_b32 v217, 0xffff0000, v166\n"
"v_add_f32 v196, v196, v216\n"
"v_add_f32 v197, v197, v217\n"
"v_lshlrev_b32 v218, 16, v167\n"
"v_and_b32 v219, 0xffff0000, v167\n"
"v_add_f32 v198, v198, v218\n"
"v_add_f32 v199, v199, v219\n"
"v_lshlrev_b32 v216, 16, v168\n"
"v_and_b32 v217, 0xffff0000, v168\n"
"v_add_f32 v200, v200, v216\n"
"v_add_f32 v201, v201, v217\n"
"v_lshlrev_b32 v218, 16, v169\n"
"v_and_b32 v219, 0xffff0000, v169\n"
"v_add_f32 v202, v202, v218\n"
"v_add_f32 v203, v203, v219\n"
"v_lshlrev_b32 v216, 16, v170\n"
"v_and_b32 v217, 0xffff0000, v170\n"
"v_add_f32 v204, v204, v216\n"
"v_add_f32 v205, v205, v217\n"
"v_lshlrev_b32 v218, 16, v171\n"
"v_and_b32 v219, 0xffff0000, v171\n"
"v_add_f32 v206, v206, v218\n"
"v_add_f32 v207, v207, v219\n"
"v_lshlrev_b32 v216, 16, v172\n"
"v_and_b32 v217, 0xffff0000, v172\n"
"v_add_f32 v208, v208, v216\n"
"v_add_f32 v209, v209, v217\n"
"v_lshlrev_b32 v218, 16, v173\n"
"v_and_b32 v219, 0xffff0000, v173\n"
"v_add_f32 v210, v210, v218\n"
"v_add_f32 v211, v211, v219\n"
"v_lshlrev_b32 v216, 16, v174\n"
"v_and_b32 v217, 0xffff0000, v174\n"
"v_add_f32 v212, v212, v216\n"
"v_add_f32 v213, v213, v217\n"
"v_lshlrev_b32 v218, 16, v175\n"
"v_and_b32 v219, 0xffff0000, v175\n"
"v_add_f32 v214, v214, v218\n"
"v_add_f32 v215, v215, v219\n"
"v_lshlrev_b32 v216, 16, v80\n"
"v_and_b32 v217, 0xffff0000, v80\n"
"v_lshlrev_b32 v218, 16, v81\n"
"v_and_b32 v219, 0xffff0000, v81\n"
"v_fma_f32 v216, v196, v176, v216\n"
"v_fma_f32 v217, v197, v177, v217\n"
"v_fma_f32 v218, v198, v178, v218\n"
"v_fma_f32 v219, v199, v179, v219\n"
"v_cvt_pk_bf16_f32 v224, v216, v217\n"
"v_cvt_pk_bf16_f32 v225, v218, v219\n"
"global_store_dwordx2 v90, v[224:225], s[52:53] offset:0\n"
"v_lshlrev_b32 v216, 16, v82\n"
"v_and_b32 v217, 0xffff0000, v82\n"
"v_lshlrev_b32 v218, 16, v83\n"
"v_and_b32 v219, 0xffff0000, v83\n"
"v_fma_f32 v216, v200, v180, v216\n"
"v_fma_f32 v217, v201, v181, v217\n"
"v_fma_f32 v218, v202, v182, v218\n"
"v_fma_f32 v219, v203, v183, v219\n"
"v_cvt_pk_bf16_f32 v226, v216, v217\n"
"v_cvt_pk_bf16_f32 v227, v218, v219\n"
"global_store_dwordx2 v90, v[226:227], s[52:53] offset:32\n"
"v_lshlrev_b32 v216, 16, v84\n"
"v_and_b32 v217, 0xffff0000, v84\n"
"v_lshlrev_b32 v218, 16, v85\n"
"v_and_b32 v219, 0xffff0000, v85\n"
"v_fma_f32 v216, v204, v184, v216\n"
"v_fma_f32 v217, v205, v185, v217\n"
"v_fma_f32 v218, v206, v186, v218\n"
"v_fma_f32 v219, v207, v187, v219\n"
"v_cvt_pk_bf16_f32 v228, v216, v217\n"
"v_cvt_pk_bf16_f32 v229, v218, v219\n"
"global_store_dwordx2 v90, v[228:229], s[52:53] offset:64\n"
"v_lshlrev_b32 v216, 16, v86\n"
"v_and_b32 v217, 0xffff0000, v86\n"
"v_lshlrev_b32 v218, 16, v87\n"
"v_and_b32 v219, 0xffff0000, v87\n"
"v_fma_f32 v216, v208, v188, v216\n"
"v_fma_f32 v217, v209, v189, v217\n"
"v_fma_f32 v218, v210, v190, v218\n"
"v_fma_f32 v219, v211, v191, v219\n"
"v_cvt_pk_bf16_f32 v230, v216, v217\n"
"v_cvt_pk_bf16_f32 v231, v218, v219\n"
"global_store_dwordx2 v90, v[230:231], s[52:53] offset:96\n"
"v_lshlrev_b32 v216, 16, v88\n"
"v_and_b32 v217, 0xffff0000, v88\n"
"v_lshlrev_b32 v218, 16, v89\n"
"v_and_b32 v219, 0xffff0000, v89\n"
"v_fma_f32 v216, v212, v192, v216\n"
"v_fma_f32 v217, v213, v193, v217\n"
"v_fma_f32 v218, v214, v194, v218\n"
"v_fma_f32 v219, v215, v195, v219\n"
"v_cvt_pk_bf16_f32 v232, v216, v217\n"
"v_cvt_pk_bf16_f32 v233, v218, v219\n"
"global_store_dwordx2 v90, v[232:233], s[52:53] offset:128\n"
"s_waitcnt vmcnt(0)\n"
"s_branch END_%=\n"
"REDO_%=:\n"
"s_waitcnt vmcnt(0) lgkmcnt(0)\n"
"s_mov_b32 s31, 1\n"
"s_mul_i32 s25, s22, 3072\n"
"s_add_u32 s76, s25, 0\n"
"s_add_u32 s80, s25, 49152\n"
"s_add_u32 s77, s25, 12288\n"
"s_add_u32 s81, s25, 61440\n"
"s_add_u32 s78, s25, 24576\n"
"s_add_u32 s82, s25, 73728\n"
"s_add_u32 s79, s25, 36864\n"
"s_add_u32 s83, s25, 86016\n"
"s_sub_u32 s88, s88, 245760\n"
"v_mov_b32 v240, 0xff800000\n"
"v_mov_b32 v241, 0xff800000\n"
"v_mov_b32 v242, 0xff800000\n"
"v_mov_b32 v243, 0xff800000\n"
"s_mov_b32 s84, 0\n"
"s_mov_b32 s30, 64\n"
"REDOLOOP_%=:\n"
"s_barrier\n"
"s_mov_b32 m0, s76\n"
"s_nop 0\n"
"buffer_load_dwordx4 v230, s[56:59], s84 offen offset:0 lds\n"
"buffer_load_dwordx4 v231, s[56:59], s84 offen offset:1024 lds\n"
"buffer_load_dwordx4 v232, s[56:59], s84 offen offset:2048 lds\n"
"s_waitcnt vmcnt(0)\n"
"s_barrier\n"
"ds_read_b128 v[176:179], v224 offset:0\n"
"ds_read_b128 v[180:183], v225 offset:0\n"
"ds_read_b128 v[184:187], v224 offset:128\n"
"ds_read_b128 v[188:191], v225 offset:128\n"
"ds_read_b128 v[192:195], v224 offset:256\n"
"ds_read_b128 v[196:199], v225 offset:256\n"
"ds_read_b128 v[200:203], v224 offset:6144\n"
"ds_read_b128 v[204:207], v225 offset:6144\n"
"ds_read_b128 v[208:211], v224 offset:6272\n"
"ds_read_b128 v[212:215], v225 offset:6272\n"
"ds_read_b128 v[216:219], v224 offset:6400\n"
"ds_read_b128 v[220:223], v225 offset:6400\n"
"s_waitcnt lgkmcnt(0)\n"
"v_mfma_f32_16x16x32_bf16 v[96:99], v[176:179], v[0:3], 0\n"
"v_mfma_f32_16x16x32_bf16 v[96:99], v[180:183], v[16:19], v[96:99]\n"
"v_mfma_f32_16x16x32_bf16 v[96:99], v[184:187], v[32:35], v[96:99]\n"
"v_mfma_f32_16x16x32_bf16 v[96:99], v[188:191], v[48:51], v[96:99]\n"
"v_mfma_f32_16x16x32_bf16 v[96:99], v[192:195], v[64:67], v[96:99]\n"
"v_mfma_f32_16x16x32_bf16 v[96:99], v[196:199], a[192:195], v[96:99]\n"
"v_mfma_f32_16x16x32_bf16 v[100:103], v[176:179], v[4:7], 0\n"
"v_mfma_f32_16x16x32_bf16 v[100:103], v[180:183], v[20:23], v[100:103]\n"
"v_mfma_f32_16x16x32_bf16 v[100:103], v[184:187], v[36:39], v[100:103]\n"
"v_mfma_f32_16x16x32_bf16 v[100:103], v[188:191], v[52:55], v[100:103]\n"
"v_mfma_f32_16x16x32_bf16 v[100:103], v[192:195], v[68:71], v[100:103]\n"
"v_mfma_f32_16x16x32_bf16 v[100:103], v[196:199], a[196:199], v[100:103]\n"
"v_mfma_f32_16x16x32_bf16 v[104:107], v[176:179], v[8:11], 0\n"
"v_mfma_f32_16x16x32_bf16 v[104:107], v[180:183], v[24:27], v[104:107]\n"
"v_mfma_f32_16x16x32_bf16 v[104:107], v[184:187], v[40:43], v[104:107]\n"
"v_mfma_f32_16x16x32_bf16 v[104:107], v[188:191], v[56:59], v[104:107]\n"
"v_mfma_f32_16x16x32_bf16 v[104:107], v[192:195], v[72:75], v[104:107]\n"
"v_mfma_f32_16x16x32_bf16 v[104:107], v[196:199], a[200:203], v[104:107]\n"
"v_mfma_f32_16x16x32_bf16 v[108:111], v[176:179], v[12:15], 0\n"
"v_mfma_f32_16x16x32_bf16 v[108:111], v[180:183], v[28:31], v[108:111]\n"
"v_mfma_f32_16x16x32_bf16 v[108:111], v[184:187], v[44:47], v[108:111]\n"
"v_mfma_f32_16x16x32_bf16 v[108:111], v[188:191], v[60:63], v[108:111]\n"
"v_mfma_f32_16x16x32_bf16 v[108:111], v[192:195], v[76:79], v[108:111]\n"
"v_mfma_f32_16x16x32_bf16 v[108:111], v[196:199], a[204:207], v[108:111]\n"
"v_mfma_f32_16x16x32_bf16 v[112:115], v[200:203], v[0:3], 0\n"
"v_mfma_f32_16x16x32_bf16 v[112:115], v[204:207], v[16:19], v[112:115]\n"
"v_mfma_f32_16x16x32_bf16 v[112:115], v[208:211], v[32:35], v[112:115]\n"
"v_mfma_f32_16x16x32_bf16 v[112:115], v[212:215], v[48:51], v[112:115]\n"
"v_mfma_f32_16x16x32_bf16 v[112:115], v[216:219], v[64:67], v[112:115]\n"
"v_mfma_f32_16x16x32_bf16 v[112:115], v[220:223], a[192:195], v[112:115]\n"
"v_mfma_f32_16x16x32_bf16 v[116:119], v[200:203], v[4:7], 0\n"
"v_mfma_f32_16x16x32_bf16 v[116:119], v[204:207], v[20:23], v[116:119]\n"
"v_mfma_f32_16x16x32_bf16 v[116:119], v[208:211], v[36:39], v[116:119]\n"
"v_mfma_f32_16x16x32_bf16 v[116:119], v[212:215], v[52:55], v[116:119]\n"
"v_mfma_f32_16x16x32_bf16 v[116:119], v[216:219], v[68:71], v[116:119]\n"
"v_mfma_f32_16x16x32_bf16 v[116:119], v[220:223], a[196:199], v[116:119]\n"
"v_mfma_f32_16x16x32_bf16 v[120:123], v[200:203], v[8:11], 0\n"
"v_mfma_f32_16x16x32_bf16 v[120:123], v[204:207], v[24:27], v[120:123]\n"
"v_mfma_f32_16x16x32_bf16 v[120:123], v[208:211], v[40:43], v[120:123]\n"
"v_mfma_f32_16x16x32_bf16 v[120:123], v[212:215], v[56:59], v[120:123]\n"
"v_mfma_f32_16x16x32_bf16 v[120:123], v[216:219], v[72:75], v[120:123]\n"
"v_mfma_f32_16x16x32_bf16 v[120:123], v[220:223], a[200:203], v[120:123]\n"
"v_mfma_f32_16x16x32_bf16 v[124:127], v[200:203], v[12:15], 0\n"
"v_mfma_f32_16x16x32_bf16 v[124:127], v[204:207], v[28:31], v[124:127]\n"
"v_mfma_f32_16x16x32_bf16 v[124:127], v[208:211], v[44:47], v[124:127]\n"
"v_mfma_f32_16x16x32_bf16 v[124:127], v[212:215], v[60:63], v[124:127]\n"
"v_mfma_f32_16x16x32_bf16 v[124:127], v[216:219], v[76:79], v[124:127]\n"
"v_mfma_f32_16x16x32_bf16 v[124:127], v[220:223], a[204:207], v[124:127]\n"
"s_nop 7\n"
"s_nop 7\n"
"v_max3_f32 v240, v240, v96, v97\n"
"v_max3_f32 v240, v240, v98, v99\n"
"v_max3_f32 v240, v240, v112, v113\n"
"v_max3_f32 v240, v240, v114, v115\n"
"v_max3_f32 v241, v241, v100, v101\n"
"v_max3_f32 v241, v241, v102, v103\n"
"v_max3_f32 v241, v241, v116, v117\n"
"v_max3_f32 v241, v241, v118, v119\n"
"v_max3_f32 v242, v242, v104, v105\n"
"v_max3_f32 v242, v242, v106, v107\n"
"v_max3_f32 v242, v242, v120, v121\n"
"v_max3_f32 v242, v242, v122, v123\n"
"v_max3_f32 v243, v243, v108, v109\n"
"v_max3_f32 v243, v243, v110, v111\n"
"v_max3_f32 v243, v243, v124, v125\n"
"v_max3_f32 v243, v243, v126, v127\n"
"s_add_u32 s84, s84, 12288\n"
"s_sub_u32 s30, s30, 1\n"
"s_cmp_lg_u32 s30, 0\n"
"s_cbranch_scc1 REDOLOOP_%=\n"
"v_mov_b32 v176, v240\n"
"s_nop 1\n"
"v_permlane16_swap_b32 v240, v176\n"
"s_nop 1\n"
"v_max_f32 v240, v240, v176\n"
"v_mov_b32 v176, v240\n"
"s_nop 1\n"
"v_permlane32_swap_b32 v240, v176\n"
"s_nop 1\n"
"v_max_f32 v240, v240, v176\n"
"v_mov_b32 v176, v241\n"
"s_nop 1\n"
"v_permlane16_swap_b32 v241, v176\n"
"s_nop 1\n"
"v_max_f32 v241, v241, v176\n"
"v_mov_b32 v176, v241\n"
"s_nop 1\n"
"v_permlane32_swap_b32 v241, v176\n"
"s_nop 1\n"
"v_max_f32 v241, v241, v176\n"
"v_mov_b32 v176, v242\n"
"s_nop 1\n"
"v_permlane16_swap_b32 v242, v176\n"
"s_nop 1\n"
"v_max_f32 v242, v242, v176\n"
"v_mov_b32 v176, v242\n"
"s_nop 1\n"
"v_permlane32_swap_b32 v242, v176\n"
"s_nop 1\n"
"v_max_f32 v242, v242, v176\n"
"v_mov_b32 v176, v243\n"
"s_nop 1\n"
"v_permlane16_swap_b32 v243, v176\n"
"s_nop 1\n"
"v_max_f32 v243, v243, v176\n"
"v_mov_b32 v176, v243\n"
"s_nop 1\n"
"v_permlane32_swap_b32 v243, v176\n"
"s_nop 1\n"
"v_max_f32 v243, v243, v176\n"
"s_barrier\n"
"s_mov_b32 s84, 0\n"
"s_mov_b32 s85, 0\n"
"s_mov_b32 m0, s76\n"
"s_nop 0\n"
"buffer_load_dwordx4 v230, s[56:59], s84 offen offset:0 lds\n"
"buffer_load_dwordx4 v231, s[56:59], s84 offen offset:1024 lds\n"
"buffer_load_dwordx4 v232, s[56:59], s84 offen offset:2048 lds\n"
"s_add_u32 s84, s84, 12288\n"
"s_mov_b32 m0, s77\n"
"s_nop 0\n"
"buffer_load_dwordx4 v230, s[56:59], s84 offen offset:0 lds\n"
"buffer_load_dwordx4 v231, s[56:59], s84 offen offset:1024 lds\n"
"buffer_load_dwordx4 v232, s[56:59], s84 offen offset:2048 lds\n"
"s_add_u32 s84, s84, 12288\n"
"s_mov_b32 m0, s78\n"
"s_nop 0\n"
"buffer_load_dwordx4 v230, s[56:59], s84 offen offset:0 lds\n"
"buffer_load_dwordx4 v231, s[56:59], s84 offen offset:1024 lds\n"
"buffer_load_dwordx4 v232, s[56:59], s84 offen offset:2048 lds\n"
"s_mov_b32 m0, s80\n"
"s_nop 0\n"
"buffer_load_dwordx4 v233, s[60:63], s85 offen offset:0 lds\n"
"buffer_load_dwordx4 v234, s[60:63], s85 offen offset:1024 lds\n"
"buffer_load_dwordx4 v235, s[60:63], s85 offen offset:2048 lds\n"
"s_add_u32 s85, s85, 12288\n"
"s_mov_b32 m0, s81\n"
"s_nop 0\n"
"buffer_load_dwordx4 v233, s[60:63], s85 offen offset:0 lds\n"
"buffer_load_dwordx4 v234, s[60:63], s85 offen offset:1024 lds\n"
"buffer_load_dwordx4 v235, s[60:63], s85 offen offset:2048 lds\n"
"s_waitcnt vmcnt(0)\n"
"s_branch RESTART_%=\n"
"END_%=:\n"
"KEND_%=:\n"
  :: "s"(ka), "s"(bid), "s"(wid), "s"((unsigned)(size_t)lds), "s"(did) : "memory", "vcc", "scc", "v0", "v1", "v2", "v3", "v4", "v5", "v6", "v7", "v8", "v9", "v10", "v11", "v12", "v13", "v14", "v15", "v16", "v17", "v18", "v19", "v20", "v21", "v22", "v23", "v24", "v25", "v26", "v27", "v28", "v29", "v30", "v31", "v32", "v33", "v34", "v35", "v36", "v37", "v38", "v39", "v40", "v41", "v42", "v43", "v44", "v45", "v46", "v47", "v48", "v49", "v50", "v51", "v52", "v53", "v54", "v55", "v56", "v57", "v58", "v59", "v60", "v61", "v62", "v63", "v64", "v65", "v66", "v67", "v68", "v69", "v70", "v71", "v72", "v73", "v74", "v75", "v76", "v77", "v78", "v79", "v80", "v81", "v82", "v83", "v84", "v85", "v86", "v87", "v88", "v89", "v90", "v91", "v92", "v93", "v94", "v95", "v96", "v97", "v98", "v99", "v100", "v101", "v102", "v103", "v104", "v105", "v106", "v107", "v108", "v109", "v110", "v111", "v112", "v113", "v114", "v115", "v116", "v117", "v118", "v119", "v120", "v121", "v122", "v123", "v124", "v125", "v126", "v127", "v128", "v129", "v130", "v131", "v132", "v133", "v134", "v135", "v136", "v137", "v138", "v139", "v140", "v141", "v142", "v143", "v144", "v145", "v146", "v147", "v148", "v149", "v150", "v151", "v152", "v153", "v154", "v155", "v156", "v157", "v158", "v159", "v160", "v161", "v162", "v163", "v164", "v165", "v166", "v167", "v168", "v169", "v170", "v171", "v172", "v173", "v174", "v175", "v176", "v177", "v178", "v179", "v180", "v181", "v182", "v183", "v184", "v185", "v186", "v187", "v188", "v189", "v190", "v191", "v192", "v193", "v194", "v195", "v196", "v197", "v198", "v199", "v200", "v201", "v202", "v203", "v204", "v205", "v206", "v207", "v208", "v209", "v210", "v211", "v212", "v213", "v214", "v215", "v216", "v217", "v218", "v219", "v220", "v221", "v222", "v223", "v224", "v225", "v226", "v227", "v228", "v229", "v230", "v231", "v232", "v233", "v234", "v235", "v236", "v237", "v238", "v239", "v240", "v241", "v242", "v243", "v244", "v245", "v246", "v247", "v248", "v249", "v250", "v251", "v252", "v253", "v254", "v255", "a0", "a1", "a2", "a3", "a4", "a5", "a6", "a7", "a8", "a9", "a10", "a11", "a12", "a13", "a14", "a15", "a16", "a17", "a18", "a19", "a20", "a21", "a22", "a23", "a24", "a25", "a26", "a27", "a28", "a29", "a30", "a31", "a32", "a33", "a34", "a35", "a36", "a37", "a38", "a39", "a40", "a41", "a42", "a43", "a44", "a45", "a46", "a47", "a48", "a49", "a50", "a51", "a52", "a53", "a54", "a55", "a56", "a57", "a58", "a59", "a60", "a61", "a62", "a63", "a64", "a65", "a66", "a67", "a68", "a69", "a70", "a71", "a72", "a73", "a74", "a75", "a76", "a77", "a78", "a79", "a80", "a81", "a82", "a83", "a84", "a85", "a86", "a87", "a88", "a89", "a90", "a91", "a92", "a93", "a94", "a95", "a96", "a97", "a98", "a99", "a100", "a101", "a102", "a103", "a104", "a105", "a106", "a107", "a108", "a109", "a110", "a111", "a112", "a113", "a114", "a115", "a116", "a117", "a118", "a119", "a120", "a121", "a122", "a123", "a124", "a125", "a126", "a127", "a128", "a129", "a130", "a131", "a132", "a133", "a134", "a135", "a136", "a137", "a138", "a139", "a140", "a141", "a142", "a143", "a144", "a145", "a146", "a147", "a148", "a149", "a150", "a151", "a152", "a153", "a154", "a155", "a156", "a157", "a158", "a159", "a160", "a161", "a162", "a163", "a164", "a165", "a166", "a167", "a168", "a169", "a170", "a171", "a172", "a173", "a174", "a175", "a176", "a177", "a178", "a179", "a180", "a181", "a182", "a183", "a184", "a185", "a186", "a187", "a188", "a189", "a190", "a191", "a192", "a193", "a194", "a195", "a196", "a197", "a198", "a199", "a200", "a201", "a202", "a203", "a204", "a205", "a206", "a207", "a208", "a209", "a210", "a211", "a212", "a213", "a214", "a215", "a216", "a217", "a218", "a219", "a220", "a221", "a222", "a223", "a224", "a225", "a226", "a227", "a228", "a229", "a230", "a231", "a232", "a233", "a234", "a235", "a236", "a237", "a238", "a239", "a240", "a241", "a242", "a243", "a244", "a245", "a246", "a247", "a248", "a249", "a250", "a251", "a252", "a253", "a254", "a255", "s20", "s21", "s22", "s23", "s24", "s25", "s26", "s27", "s28", "s29", "s30", "s31", "s36", "s37", "s38", "s39", "s40", "s41", "s42", "s43", "s44", "s45", "s46", "s47", "s48", "s49", "s50", "s51", "s52", "s53", "s54", "s55", "s56", "s57", "s58", "s59", "s60", "s61", "s62", "s63", "s64", "s65", "s66", "s67", "s68", "s69", "s70", "s71", "s72", "s73", "s74", "s75", "s76", "s77", "s78", "s79", "s80", "s81", "s82", "s83", "s84", "s85", "s86", "s87", "s88", "s89", "s90", "s91", "s92", "s93", "s94", "s95", "s96", "s97", "s98", "s99", "s100", "s101");
}
extern "C" void kernel_launch(void* const* d_in, const int* in_sizes, int n_in, void* d_out, int out_size, void* d_ws, size_t ws_size, hipStream_t stream) {
  (void)in_sizes; (void)n_in; (void)out_size; (void)ws_size;
  hipLaunchKernelGGL(fused_block_kernel, dim3(256), dim3(256), 0, stream, (const void*)d_in[0], (const void*)d_in[1], (const void*)d_in[2], (const void*)d_in[3],
                     (const void*)d_in[4], (const void*)d_in[6], (const void*)d_in[7], (const void*)d_in[8], d_out, d_ws);
}
```
